```python
import torch

def _softmax(x, axis=-1):
    return torch.softmax(x, dim=axis)

def _gelu(x):
    return torch.nn.functional.gelu(x, approximate='tanh')

def _silu(x):
    return torch.nn.functional.silu(x)

def _sigmoid(x):
    return torch.sigmoid(x)

def _layernorm(x, axis=-1, eps=1e-05):
    m = x.mean(dim=axis, keepdim=True)
    v = x.var(dim=axis, keepdim=True, unbiased=False)
    return (x - m) / torch.sqrt(v + eps)

def _scatter_add(tgt, idx, src, axis=0):
    return tgt.clone().index_add_(axis, idx, src)

def _segment_sum(src, seg, n):
    out = torch.zeros((n,) + src.shape[1:], dtype=src.dtype)
    return out.index_add_(0, seg, src)

def _pad(x, pw):
    flat = tuple((v for pair in reversed(pw) for v in pair))
    return torch.nn.functional.pad(x, flat)
kernel_name = 'gated_mix__linear_attn_block__memory_aug_recurrent_mem_tr__prompt'

def setup_inputs():

    _w = lambda a: (lambda t: t.to(torch.bfloat16) if t.is_floating_point() else t)(torch.as_tensor(a))

    def _A_setup():
        B, S, D = (1, 2048, 448)
        return {'x': _w((lambda *s: torch.randn(*s, dtype=torch.bfloat16) * 0.05)(B, S, D)), 'wqkv': _w((lambda *s: torch.randn(*s, dtype=torch.bfloat16) * 1.6)(D, 3 * D)), 'wo': _w((lambda *s: torch.randn(*s, dtype=torch.bfloat16) * 0.1)(D, D))}
    _A_in = _A_setup()

    def _B_setup():
        B, S, Dm = (1, 2048, 448)
        H, Dh = (16, 256)
        M, seg = (8, 32)
        seg = min(seg, S)
        S = S // seg * seg
        return {'x': _w((lambda *s: torch.randn(*s, dtype=torch.bfloat16) * 0.05)(B, S, Dm)), 'mem0': _w((lambda *s: torch.randn(*s, dtype=torch.bfloat16) * 0.8)(B, M, Dm)), 'Wq': _w((lambda *s: torch.randn(*s, dtype=torch.bfloat16) * 3.2)(Dm, H, Dh) * Dm ** (-0.5)), 'Wk': _w((lambda *s: torch.randn(*s, dtype=torch.bfloat16) * 3.2)(Dm, H, Dh) * Dm ** (-0.5)), 'Wv': _w((lambda *s: torch.randn(*s, dtype=torch.bfloat16) * 0.8)(Dm, H, Dh) * Dm ** (-0.5)), 'Wo': _w((lambda *s: torch.randn(*s, dtype=torch.bfloat16) * 0.8)(H, Dh, Dm) * (H * Dh) ** (-0.5)), 'seg_len': seg}
    _B_in = _B_setup()
    return {'A_x': _A_in['x'], 'A_wqkv': _A_in['wqkv'], 'A_wo': _A_in['wo'], 'B_x': _B_in['x'], 'B_mem0': _B_in['mem0'], 'B_Wq': _B_in['Wq'], 'B_Wk': _B_in['Wk'], 'B_Wv': _B_in['Wv'], 'B_Wo': _B_in['Wo'], 'B_seg_len': _B_in['seg_len'], 'gate_w': _w((lambda *s: torch.randn(*s, dtype=torch.bfloat16) * 0.05)(448) * 16.0)}

def reference(A_x, A_wqkv, A_wo, B_x, B_mem0, B_Wq, B_Wk, B_Wv, B_Wo, B_seg_len, gate_w):

    def _A_ref(x, wqkv, wo):
        B, S, D = x.shape
        H = 16
        Dh = D // H
        qkv = (lambda x, *d: x.permute(*d))((x @ wqkv).reshape(B, S, 3, H, Dh), 2, 0, 3, 1, 4)
        q, k, v = (qkv[0], qkv[1], qkv[2])
        phi = lambda t: torch.where(t > 0, t + 1, torch.exp(t)) + 1e-06
        pq, pk = (phi(q), phi(k))
        kv = torch.einsum('bhsd,bhse->bhde', pk, v)
        z = torch.einsum('bhsd->bhd', pk)
        num = torch.einsum('bhqd,bhde->bhqe', pq, kv)
        den = torch.einsum('bhqd,bhd->bhq', pq, z)[..., None] + 1e-06
        o = num / den
        return (lambda x, *d: x.permute(*d))(o, 0, 2, 1, 3).reshape(B, S, D) @ wo

    def _B_ref(x, mem0, Wq, Wk, Wv, Wo, seg_len):
        B, S, Dm = x.shape
        H, Dh = (Wq.shape[1], Wq.shape[2])
        M = mem0.shape[1]
        scale = Dh ** (-0.5)
        n_seg = S // seg_len
        L = M + seg_len
        idx = torch.arange(L)
        mask = (idx[:, None] < M) | (idx[:, None] >= idx[None, :])
        mem = mem0
        outs = []
        for s in range(n_seg):
            xs = x[:, s * seg_len:(s + 1) * seg_len, :].clone()
            h = torch.concatenate([mem, xs], axis=1)
            q = torch.einsum('btd,dhe->bhte', h, Wq)
            k = torch.einsum('btd,dhe->bhte', h, Wk)
            v = torch.einsum('btd,dhe->bhte', h, Wv)
            scores = torch.einsum('bhqe,bhke->bhqk', q, k) * scale
            scores = torch.where(mask[None, None], scores, -1000000000.0)
            p = _softmax(scores, axis=-1)
            o = torch.einsum('bhqk,bhke->bhqe', p, v)
            o = torch.einsum('bhte,hed->btd', o, Wo)
            mem = o[:, :M, :].clone()
            outs.append(o[:, M:, :])
        return torch.concatenate(outs, axis=1)
    x = A_x
    g = _sigmoid((x * gate_w).sum(-1, keepdims=True))
    a = _A_ref(x, A_wqkv, A_wo)
    b = _B_ref(x, B_mem0, B_Wq, B_Wk, B_Wv, B_Wo, B_seg_len)
    return g * a + (1 - g) * b
```

```cpp
#include <hip/hip_runtime.h>
#include <hip/hip_bf16.h>

typedef unsigned v4u __attribute__((ext_vector_type(4)));
typedef unsigned v2u __attribute__((ext_vector_type(2)));
typedef float v4f __attribute__((ext_vector_type(4)));
typedef float v2f __attribute__((ext_vector_type(2)));
typedef short v4s __attribute__((ext_vector_type(4)));
typedef __bf16 v8bf __attribute__((ext_vector_type(8)));
typedef __bf16 v2bf __attribute__((ext_vector_type(2)));
typedef __amdgpu_buffer_rsrc_t rsrc_t;
#define LAS __attribute__((address_space(3)))
#define DEV __device__ __forceinline__

constexpr int NSEG = 64;
constexpr int SC1 = 16;
constexpr int OOB = 0x7ff00000;
constexpr unsigned SENT = 0xFFFFFFFFu;
constexpr int MB = 1 << 20;
constexpr int O_CRIT = 0;
constexpr int O_AFR = 16 * MB, O_BFR = 23 * MB, O_XFR = 30 * MB, O_LW = 32 * MB, O_LWO = 34 * MB;
constexpr int O_KVP = 35 * MB, O_KVF = 36 * MB, O_AOUT = 37 * MB, O_FLG = 41 * MB;
constexpr int O_KX = 48 * MB, O_VX = 80 * MB, O_KM = 112 * MB, O_U = 120 * MB, WS_USED = 128 * MB;
constexpr int F_KS = 0, F_VS = 64, F_PA = 128, F_PC = 192, F_L1 = 256, F_L2 = 272, F_L3 = 288, F_ABORT = 304, F_COUNT = 320;
constexpr int O_TAB = O_FLG + F_COUNT * 256;
constexpr int O_DBG = O_FLG + 256 * 1024;
constexpr unsigned SPIN_CAP = 1u << 18;
constexpr float CA = 1.4426950408889634f / 16.0f;
constexpr float MASKV = -1.0e9f * 1.4426950408889634f;

struct Params {
  const unsigned short* x; const unsigned short* wqkv; const unsigned short* awo; const unsigned short* mem0;
  const unsigned short* wq; const unsigned short* wk; const unsigned short* wv; const unsigned short* wo; const unsigned short* gw;
  unsigned short* out; char* ws; unsigned mask; unsigned pad;
};

DEV rsrc_t mkrs(const void* p, unsigned bytes) { return __builtin_amdgcn_make_buffer_rsrc((void*)p, 0, bytes, 0x00020000); }
template <int AUX> DEV v4u ld16(rsrc_t r, int v, int s) { return __builtin_amdgcn_raw_buffer_load_b128(r, v, s, AUX); }
template <int AUX> DEV v2u ld8(rsrc_t r, int v, int s) { return __builtin_amdgcn_raw_buffer_load_b64(r, v, s, AUX); }
template <int AUX> DEV unsigned ld4(rsrc_t r, int v, int s) { return __builtin_amdgcn_raw_buffer_load_b32(r, v, s, AUX); }
template <int AUX> DEV void st16(v4u d, rsrc_t r, int v, int s) { __builtin_amdgcn_raw_buffer_store_b128(d, r, v, s, AUX); }
template <int AUX> DEV void st8(v2u d, rsrc_t r, int v, int s) { __builtin_amdgcn_raw_buffer_store_b64(d, r, v, s, AUX); }
template <int AUX> DEV void st4(unsigned d, rsrc_t r, int v, int s) { __builtin_amdgcn_raw_buffer_store_b32(d, r, v, s, AUX); }
DEV v4f mfma32(v4u a, v4u b, v4f c) { return __builtin_amdgcn_mfma_f32_16x16x32_bf16(__builtin_bit_cast(v8bf, a), __builtin_bit_cast(v8bf, b), c, 0, 0, 0); }
DEV v4f mfma16(v2u a, v2u b, v4f c) { return __builtin_amdgcn_mfma_f32_16x16x16bf16_1k(__builtin_bit_cast(v4s, a), __builtin_bit_cast(v4s, b), c, 0, 0, 0); }
DEV unsigned pk(float lo, float hi) { v2f f = {lo, hi}; return __builtin_bit_cast(unsigned, __builtin_convertvector(f, v2bf)); }
DEV v4u pk8(v4f a, v4f b) { return v4u{pk(a.x, a.y), pk(a.z, a.w), pk(b.x, b.y), pk(b.z, b.w)}; }
template <int CTRL> DEV unsigned dppu(unsigned x) { return (unsigned)__builtin_amdgcn_mov_dpp((int)x, CTRL, 0xf, 0xf, true); }
template <int CTRL> DEV float dppf(float x) { return __uint_as_float(dppu<CTRL>(__float_as_uint(x))); }
constexpr int XOR1 = 0xB1, XOR2 = 0x4E, ROR8 = 0x128, HMIR = 0x141;
DEV v4u ror8(v4u a) { return v4u{dppu<ROR8>(a.x), dppu<ROR8>(a.y), dppu<ROR8>(a.z), dppu<ROR8>(a.w)}; }
DEV unsigned mx4(v4u a) { return max(max(a.x, a.y), max(a.z, a.w)); }
DEV unsigned san(unsigned x) { return min(x, 0xFFFFFFFEu); }
DEV float ex2(float x) { return __builtin_amdgcn_exp2f(x); }
DEV void fence_c() { asm volatile("" ::: "memory"); }
DEV void drain() { asm volatile("s_waitcnt vmcnt(0)" ::: "memory"); }
DEV void bar() { asm volatile("s_waitcnt lgkmcnt(0)\n\ts_barrier" ::: "memory"); }
DEV int rfl(int x) { return __builtin_amdgcn_readfirstlane(x); }
DEV float xmax(float x) {
  auto s = __builtin_amdgcn_permlane16_swap(__float_as_uint(x), __float_as_uint(x), false, false);
  x = fmaxf(__uint_as_float(s[0]), __uint_as_float(s[1]));
  auto t = __builtin_amdgcn_permlane32_swap(__float_as_uint(x), __float_as_uint(x), false, false);
  return fmaxf(__uint_as_float(t[0]), __uint_as_float(t[1]));
}
DEV float xsum(float x) {
  auto s = __builtin_amdgcn_permlane16_swap(__float_as_uint(x), __float_as_uint(x), false, false);
  x = __uint_as_float(s[0]) + __uint_as_float(s[1]);
  auto t = __builtin_amdgcn_permlane32_swap(__float_as_uint(x), __float_as_uint(x), false, false);
  return __uint_as_float(t[0]) + __uint_as_float(t[1]);
}
DEV bool spin_fail(unsigned& n, rsrc_t r) {
  ++n;
  if (n > SPIN_CAP) { st4<SC1>(1u, r, O_FLG + F_ABORT * 256, 0); return true; }
  return (n & 255u) == 0u && rfl((int)ld4<SC1>(r, O_FLG + F_ABORT * 256, 0)) != 0;
}
DEV bool wait_prog(rsrc_t r, int voff, int need, int& have) {
  unsigned n = 0;
  while (have < need) {
    fence_c();
    int v = voff == OOB ? 0x7fffffff : 0;
    v |= (int)ld4<SC1>(r, voff, 0);
#pragma unroll
    for (int m = 32; m >= 1; m >>= 1) v = min(v, __shfl_xor(v, m));
    have = rfl(v);
    if (have >= need) break;
    __builtin_amdgcn_s_sleep(4);
    if (spin_fail(n, r)) return false;
  }
  fence_c();
  return true;
}
DEV int m_piece(int rep, int p) { return (2 * (rep >> 1) + (p >= 28)) * 32768 + ((rep & 1) << 14) + (p >= 28 ? p - 28 : p) * 256; }
DEV int m_frag(int rep, int ks, int g, int r) { return m_piece(rep, (2 * ks + (g >> 1)) * 2 + (r >> 2)) + (r & 3) * 32 + (g & 1) * 16; }
DEV int s_piece(int hf, int p) { return (4 + (p >> 5)) * 32768 + (hf << 14) + (p & 31) * 256; }
DEV int e_piece(int hf, int h) { return 6 * 32768 + (hf << 14) + h * 512; }

__global__ void __launch_bounds__(256, 1) k_prep(Params p) {
  __shared__ __attribute__((aligned(16))) char sm[256 * 144 + 256];
  const int b = blockIdx.x, t = threadIdx.x, l = t & 63, w = rfl(t >> 6), g = l >> 4, n = l & 15;
  const int x = b & 7, j = b >> 3;
  const rsrc_t ws = mkrs(p.ws, WS_USED);
  {
    const rsrc_t rx = mkrs(p.x, 2048 * 448 * 2);
    for (int q = w; q < 7; q += 4) {
      const int f = b * 7 + q, ks = f % 14, tile = f / 14;
      st16<0>(ld16<0>(rx, ((tile * 16 + n) * 448 + ks * 32 + g * 8) * 2, 0), ws, O_XFR + f * 1024 + l * 16, 0);
      unsigned e[8];
      if (f < 1344) {
        const int tau = f / 14, u = tau & 1, h = (tau >> 1) & 15, which = tau >> 5, d = 16 * u + n;
#pragma unroll
        for (int i = 0; i < 8; ++i) e[i] = d < 28 ? p.wqkv[(ks * 32 + g * 8 + i) * 1344 + which * 448 + h * 28 + d] : 0u;
        st16<0>(v4u{e[0] | e[1] << 16, e[2] | e[3] << 16, e[4] | e[5] << 16, e[6] | e[7] << 16}, ws, O_LW + f * 1024 + l * 16, 0);
      } else {
        const int f2 = f - 1344, h = f2 & 15, ct = f2 >> 4;
#pragma unroll
        for (int i = 0; i < 8; ++i) { const int ee = 16 * (i >> 2) + 4 * g + (i & 3); e[i] = ee < 28 ? p.awo[(h * 28 + ee) * 448 + ct * 16 + n] : 0u; }
        st16<0>(v4u{e[0] | e[1] << 16, e[2] | e[3] << 16, e[4] | e[5] << 16, e[6] | e[7] << 16}, ws, O_LWO + f2 * 1024 + l * 16, 0);
      }
    }
  }
  if (j < 28) {
    const int mat = x * 4 + j / 7, grp = j % 7, isB = mat & 1, h = mat >> 1;
    v4u bop[4][8];
    if (!isB) {
      const rsrc_t rq = mkrs(p.wq, 448 * 4096 * 2);
#pragma unroll
      for (int tt = 0; tt < 4; ++tt) {
        const int it = 4 * grp + tt, i = 32 * (it >> 1) + 8 * (n >> 2) + 4 * (it & 1) + (n & 3);
#pragma unroll
        for (int es = 0; es < 8; ++es) bop[tt][es] = ld16<0>(rq, (i * 4096 + h * 256 + g * 8) * 2 + es * 64, 0);
      }
    } else {
      const rsrc_t ro = mkrs(p.wo, 4096 * 448 * 2);
#pragma unroll
      for (int q = 0; q < 8; ++q) {
        const int c = q * 256 + t, e = c >> 3, ch = c & 7;
        *(LAS v4u*)((LAS char*)sm + e * 144 + ch * 16) = ld16<0>(ro, ((h * 256 + e) * 448 + grp * 64 + ch * 8) * 2, 0);
      }
      __syncthreads();
#pragma unroll
      for (int tt = 0; tt < 4; ++tt)
#pragma unroll
        for (int es = 0; es < 8; ++es) {
          LAS char* a = (LAS char*)sm + (32 * es + 8 * g + (n >> 2)) * 144 + (16 * tt + 4 * (n & 3)) * 2;
          const v2u lo = __builtin_bit_cast(v2u, __builtin_amdgcn_ds_read_tr16_b64_v4i16((LAS v4s*)a));
          const v2u hi = __builtin_bit_cast(v2u, __builtin_amdgcn_ds_read_tr16_b64_v4i16((LAS v4s*)(a + 4 * 144)));
          bop[tt][es] = v4u{lo.x, lo.y, hi.x, hi.y};
        }
    }
    const rsrc_t ra = mkrs(isB ? p.wv : p.wk, 448 * 4096 * 2);
    const float sc = isB ? 1.0f : CA;
    const int obase = (isB ? O_BFR : O_AFR) + (h * 28 + 4 * grp) * 14 * 1024 + l * 16;
    const int arow = ((8 * (n >> 2) + (n & 3)) * 4096 + h * 256 + g * 8) * 2;
    for (int ks = w; ks < 14; ks += 4) {
      v4u aop[2][8];
#pragma unroll
      for (int u = 0; u < 2; ++u)
#pragma unroll
        for (int es = 0; es < 8; ++es) aop[u][es] = ld16<0>(ra, arow + es * 64, (ks * 32 + 4 * u) * 8192);
      v4f acc[2][4];
#pragma unroll
      for (int u = 0; u < 2; ++u)
#pragma unroll
        for (int tt = 0; tt < 4; ++tt) {
          acc[u][tt] = v4f{0, 0, 0, 0};
#pragma unroll
          for (int es = 0; es < 8; ++es) acc[u][tt] = mfma32(aop[u][es], bop[tt][es], acc[u][tt]);
        }
#pragma unroll
      for (int tt = 0; tt < 4; ++tt) st16<0>(pk8(acc[0][tt] * sc, acc[1][tt] * sc), ws, obase, (tt * 14 + ks) * 1024);
    }
  } else {
    const int i = x * 4 + (j - 28), reg = O_CRIT + i * 2 * 262144;
    unsigned best[4] = {~0u, ~0u, ~0u, ~0u};
    for (int rd = 0; rd < 3; ++rd)
#pragma unroll
      for (int c = 0; c < 4; ++c) {
        const int off = l == 0 ? reg + 7 * 32768 + c * 8192 + (w * 3 + rd) * 256 : OOB;
        unsigned long long t0, t1;
        asm volatile("s_waitcnt vmcnt(0)\n\ts_memtime %0\n\ts_waitcnt lgkmcnt(0)" : "=s"(t0) :: "memory");
        st4<SC1>(0u, ws, off, 0);
        asm volatile("s_waitcnt vmcnt(0)\n\ts_memtime %0\n\ts_waitcnt lgkmcnt(0)" : "=s"(t1) :: "memory");
        best[c] = min(best[c], (unsigned)(t1 - t0));
      }
    LAS unsigned* sb = (LAS unsigned*)sm;
    if (l == 0) { sb[w * 4] = best[0]; sb[w * 4 + 1] = best[1]; sb[w * 4 + 2] = best[2]; sb[w * 4 + 3] = best[3]; }
    __syncthreads();
    int bc = 0; unsigned bv = ~0u;
#pragma unroll
    for (int c = 0; c < 4; ++c) { const unsigned v = min(min(sb[c], sb[4 + c]), min(sb[8 + c], sb[12 + c])); if (v < bv) { bv = v; bc = c; } }
    const int xm = (((bc & 1) ^ (x >> 2)) << 13) | (((bc >> 1) ^ (x & 1)) << 14);
    if (t < 2) st4<0>((unsigned)xm, ws, O_TAB + (2 * i + t) * 4, 0);
    if (t < 10) st4<0>(0u, ws, O_FLG + (i * 10 + t) * 256, 0);
    const v4u sv = {SENT, SENT, SENT, SENT};
    const rsrc_t rm = mkrs(p.mem0, 8 * 448 * 2);
    for (int ss = 0; ss < 2; ++ss) {
      const int so = reg + ss * 262144;
      for (int c = t; c < 4 * 56 * 8; c += 256) {
        const int rep = c / 448, pc = (c % 448) >> 3, ch = c & 7;
        v4u v = sv;
        if (i == 0 && ss == 0) { v = ld16<0>(rm, ((4 * (pc & 1) + (ch >> 1)) * 448 + (pc >> 1) * 16 + (ch & 1) * 8) * 2, 0); v = v4u{san(v.x), san(v.y), san(v.z), san(v.w)}; }
        st16<0>(v, ws, (m_piece(rep, pc) + ch * 16) ^ xm, so);
      }
      for (int c = t; c < 2 * 64 * 8; c += 256) st16<0>(sv, ws, (s_piece(c >> 9, (c >> 3) & 63) + (c & 7) * 16) ^ xm, so);
      for (int c = t; c < 2 * 16 * 18; c += 256) st16<0>(sv, ws, (e_piece(c / 288, (c % 288) / 18) + (c % 18) * 16) ^ xm, so);
    }
  }
}

DEV int lk2ks(int w, int lk) { return 7 * (w >> 1) + ((w & 1) ? (lk < 3 ? 4 + lk : 3) : lk); }

DEV void role_A(const Params& p, LAS char* sm, int rep, int idx) {
  const int t = threadIdx.x, l = t & 63, w = rfl(t >> 6), g = l >> 4, n = l & 15, r = n & 7, odd = w & 1;
  const rsrc_t ws = mkrs(p.ws, WS_USED);
  v4u W[8][3], W3[4];
#pragma unroll
  for (int lt = 0; lt < 8; ++lt) {
    const int T = 8 * idx + (lt ^ (4 * odd));
#pragma unroll
    for (int lk = 0; lk < 3; ++lk) W[lt][lk] = ld16<0>(ws, l * 16, O_AFR + (T * 14 + lk2ks(w, lk)) * 1024);
    if (lt < 4) W3[lt] = ld16<0>(ws, l * 16, O_AFR + (T * 14 + lk2ks(w, 3)) * 1024);
  }
  const int tab = (int)ld4<0>(ws, O_TAB + l * 4, 0);
  const int oMa = m_frag(rep, lk2ks(w, n >> 3), g, r), oMb = m_frag(rep, lk2ks(w, 2 + (n >> 3)), g, r);
  const int gl = (n >> 3) ^ odd;
  const int oS0 = m_frag(rep, (4 * idx + 2 * gl) % 14, g, r), oS1 = m_frag(rep, (4 * idx + 2 * gl + 1) % 14, g, r);
  const bool mid = idx % 7 == 3;
  const int G = l >> 5, hf = (l >> 4) & 1, rl = (l >> 2) & 3, pos = l & 3;
  const int gam = 2 * idx + G, hd = gam / 7, gi = gam % 7, jj = (gi + (hd & 1)) >> 1, pi = pos ^ jj;
  const int rdE = (G * 64 + (pi >> 1) * 16 + 4 * hf + rl + 8 * G) * 16 + (pi & 1) * 8;
  const int rdO = ((G ^ 1) * 64 + (pi >> 1) * 16 + 4 * hf + rl + 8 * (G ^ 1)) * 16 + (pi & 1) * 8;
  const int oSt = (G == 0 || mid) ? s_piece(hf, hd * 4 + jj) + rl * 32 + pos * 8 : OOB;
  const float mrg = mid ? 0.0f : 1.0f;
  LAS char* sS = sm; LAS unsigned* cnt = (LAS unsigned*)(sm + 8192); LAS char* sK0 = sm + 8192 + 256;
  if (w == 0) cnt[l] = 0;
  const int P = 4 * idx + w, ph = P / 14, pp = P % 14;
  const int oKM = O_KM + (((ph >> 2) * 2 + (ph & 1)) * 14 + pp) * 1024 + (g * 16 + ((ph >> 1) & 1) * 8 + r) * 16 + (n >> 3) * 8;
  bar();
  for (int s = 0; s < NSEG; ++s) {
    const int xm = __builtin_amdgcn_readlane(tab, s), so = O_CRIT + s * 262144;
    v4u Ma, Mb; unsigned sp = 0;
    for (;;) {
      fence_c();
      Ma = ld16<SC1>(ws, oMa ^ xm, so); Mb = ld16<SC1>(ws, oMb ^ xm, so);
      if (__all(max(mx4(Ma), mx4(Mb)) != SENT)) break;
      if (spin_fail(sp, ws)) return;
    }
    fence_c();
    v4u L0 = ld16<SC1>(ws, oS0 ^ xm, so), L1 = ld16<SC1>(ws, oS1 ^ xm, so);
    const v4u Mar = ror8(Ma), Mbr = ror8(Mb);
    v4f acc[8];
#pragma unroll
    for (int lt = 0; lt < 8; ++lt) {
      acc[lt] = mfma32(W[lt][0], Ma, v4f{0, 0, 0, 0});
      acc[lt] = mfma32(W[lt][1], Mar, acc[lt]);
      acc[lt] = mfma32(W[lt][2], Mb, acc[lt]);
      if (lt < 4) acc[lt] = mfma32(W3[lt], Mbr, acc[lt]);
    }
    if (s < NSEG - 1) {
      while (!__all(max(mx4(L0), mx4(L1)) != SENT)) {
        if (spin_fail(sp, ws)) return;
        fence_c();
        L0 = ld16<SC1>(ws, oS0 ^ xm, so); L1 = ld16<SC1>(ws, oS1 ^ xm, so);
      }
      v4f s0 = mfma32(pk8(acc[0], acc[1]), L0, v4f{0, 0, 0, 0}); s0 = mfma32(pk8(acc[2], acc[3]), L1, s0);
      v4f s1 = mfma32(pk8(acc[4], acc[5]), L0, v4f{0, 0, 0, 0}); s1 = mfma32(pk8(acc[6], acc[7]), L1, s1);
      *(LAS v4f*)(sS + (w * 2) * 1024 + l * 16) = s0;
      *(LAS v4f*)(sS + (w * 2 + 1) * 1024 + l * 16) = s1;
      fence_c();
      const unsigned old = __hip_atomic_fetch_add(cnt + l, 1u, __ATOMIC_RELAXED, __HIP_MEMORY_SCOPE_WORKGROUP);
      fence_c();
      const v2f a0 = *(LAS v2f*)(sS + rdE), a1 = *(LAS v2f*)(sS + 2048 + rdO), a2 = *(LAS v2f*)(sS + 4096 + rdE), a3 = *(LAS v2f*)(sS + 6144 + rdO);
      const float u0 = (a0.x + a1.x) + (a2.x + a3.x), u1 = (a0.y + a1.y) + (a2.y + a3.y);
      auto q0 = __builtin_amdgcn_permlane32_swap(__float_as_uint(u0), __float_as_uint(u0), false, false);
      auto q1 = __builtin_amdgcn_permlane32_swap(__float_as_uint(u1), __float_as_uint(u1), false, false);
      const float o0 = mid ? u0 : u0 + __uint_as_float(q0[1]), o1 = mid ? u1 : u1 + __uint_as_float(q1[1]);
      st8<SC1>(v2u{san(__float_as_uint(o0)), san(__float_as_uint(o1))}, ws, (old & 3u) == 3u ? (oSt == OOB ? OOB : oSt ^ xm) : OOB, so);
    }
    LAS char* sK = sK0 + (s & 1) * 16384;
#pragma unroll
    for (int lp = 0; lp < 4; ++lp) {
      const v4u a = __builtin_bit_cast(v4u, acc[2 * lp]), bq = ror8(__builtin_bit_cast(v4u, acc[2 * lp + 1]));
      *(LAS v4u*)(sK + (w * 4 + (lp ^ (2 * odd))) * 1024 + l * 16) = n < 8 ? a : bq;
    }
    drain();
    bar();
    if (t == 0) st4<SC1>((unsigned)s, ws, O_FLG + (F_PA + idx) * 256, 0);
    const v4f k0 = *(LAS v4f*)(sK + w * 1024 + l * 16), k1 = *(LAS v4f*)(sK + (4 + w) * 1024 + l * 16), k2 = *(LAS v4f*)(sK + (8 + w) * 1024 + l * 16), k3 = *(LAS v4f*)(sK + (12 + w) * 1024 + l * 16);
    const v4f kk = (k0 + k1) + (k2 + k3);
    st8<SC1>(v2u{pk(kk.x, kk.y), pk(kk.z, kk.w)}, ws, oKM, s * 8 * 14 * 1024);
  }
  (void)mrg;
  drain(); bar();
  if (t == 0) st4<SC1>((unsigned)NSEG, ws, O_FLG + (F_PA + idx) * 256, 0);
}

DEV void role_X(const Params& p, LAS char* sm, int rep, int h) {
  const int t = threadIdx.x, l = t & 63, w = rfl(t >> 6), g = l >> 4, n = l & 15, r = n & 7, odd = w & 1;
  const rsrc_t ws = mkrs(p.ws, WS_USED);
  const int tab = (int)ld4<0>(ws, O_TAB + l * 4, 0);
  const int oMa = m_frag(rep, lk2ks(w, n >> 3), g, r), oMb = m_frag(rep, lk2ks(w, 2 + (n >> 3)), g, r);
  const int oPr = l < 4 ? O_FLG + (F_KS + (28 * h) / 8 + l) * 256 : OOB;
  const int rr = l >> 3, rg = (l >> 1) & 3, rk = l & 1;
  const int rdE = (rk * 64 + rg * 16 + rr) * 16, rdO = ((rk ^ 1) * 64 + rg * 16 + rr) * 16;
  const int oE = e_piece(rr >> 2, h) + (rr & 3) * 64 + rg * 16 + rk * 8;
  const int oT = (l & 7) == 0 ? e_piece(rr >> 2, h) + 256 + (rr & 3) * 8 : OOB;
  LAS char* sS = sm; LAS unsigned* cnt = (LAS unsigned*)(sm + 8192);
  if (w == 0) cnt[l] = 0;
  bar();
  int have = 0;
  v4u F[7];
  int fo[7];
#pragma unroll
  for (int i = 0; i < 7; ++i) fo[i] = (2 * lk2ks(w, i >> 1) + ((i & 1) ^ odd)) * 1024;
  if (!wait_prog(ws, oPr, 1, have)) return;
#pragma unroll
  for (int i = 0; i < 7; ++i) F[i] = ld16<SC1>(ws, l * 16, O_KX + h * 28672 + fo[i]);
  for (int s = 0; s < NSEG - 1; ++s) {
    const int xm = __builtin_amdgcn_readlane(tab, s), so = O_CRIT + s * 262144;
    v4u Ma, Mb; unsigned sp = 0;
    for (;;) {
      fence_c();
      Ma = ld16<SC1>(ws, oMa ^ xm, so); Mb = ld16<SC1>(ws, oMb ^ xm, so);
      if (__all(max(mx4(Ma), mx4(Mb)) != SENT)) break;
      if (spin_fail(sp, ws)) return;
    }
    const v4f z = {0, 0, 0, 0};
    v4f e0 = mfma32(F[0], Ma, z), e1 = mfma32(F[1], Ma, z), o0 = mfma32(F[2], Ma, z), o1 = mfma32(F[3], Ma, z);
    e0 = mfma32(F[4], Mb, e0); e1 = mfma32(F[5], Mb, e1); o0 = mfma32(F[6], Mb, o0);
    *(LAS v4f*)(sS + (w * 2) * 1024 + l * 16) = e0 + __builtin_bit_cast(v4f, ror8(__builtin_bit_cast(v4u, o0)));
    *(LAS v4f*)(sS + (w * 2 + 1) * 1024 + l * 16) = e1 + __builtin_bit_cast(v4f, ror8(__builtin_bit_cast(v4u, o1)));
    fence_c();
    const unsigned old = __hip_atomic_fetch_add(cnt + l, 1u, __ATOMIC_RELAXED, __HIP_MEMORY_SCOPE_WORKGROUP);
    fence_c();
    const v4f a = (*(LAS v4f*)(sS + rdE) + *(LAS v4f*)(sS + 2048 + rdO)) + (*(LAS v4f*)(sS + 4096 + rdE) + *(LAS v4f*)(sS + 6144 + rdO));
    float m = fmaxf(fmaxf(a.x, a.y), fmaxf(a.z, a.w));
    m = fmaxf(m, dppf<XOR1>(m)); m = fmaxf(m, dppf<XOR2>(m)); m = fmaxf(m, dppf<HMIR>(m));
    const float p0 = ex2(a.x - m), p1 = ex2(a.y - m), p2 = ex2(a.z - m), p3 = ex2(a.w - m);
    const bool last = (old & 3u) == 3u;
    st8<SC1>(v2u{san(pk(p0, p1)), san(pk(p2, p3))}, ws, last ? oE ^ xm : OOB, so);
    float sum = (p0 + p1) + (p2 + p3);
    sum += dppf<XOR1>(sum); sum += dppf<XOR2>(sum); sum += dppf<HMIR>(sum);
    st8<SC1>(v2u{san(__float_as_uint(m)), san(__float_as_uint(sum))}, ws, (last && oT != OOB) ? oT ^ xm : OOB, so);
    if (s + 1 < NSEG - 1) {
      if (!wait_prog(ws, oPr, s + 2, have)) return;
#pragma unroll
      for (int i = 0; i < 7; ++i) F[i] = ld16<SC1>(ws, l * 16, O_KX + ((s + 1) * 16 + h) * 28672 + fo[i]);
    }
  }
}

DEV void role_C(const Params& p, LAS char* sm, int hf, int ct) {
  const int t = threadIdx.x, l = t & 63, w = rfl(t >> 6), g = l >> 4, n = l & 15, rep = 2 + hf;
  const rsrc_t ws = mkrs(p.ws, WS_USED);
  v4u W[4][14];
#pragma unroll
  for (int hh = 0; hh < 4; ++hh)
#pragma unroll
    for (int ks = 0; ks < 14; ++ks) W[hh][ks] = ld16<0>(ws, l * 16, O_BFR + (((4 * w + hh) * 28 + ct) * 14 + ks) * 1024);
  const int tab = (int)ld4<0>(ws, O_TAB + l * 4, 0);
  const int p0 = 16 * w + (l >> 3), p1 = p0 + 8, c8 = l & 7;
  const int oM0 = m_piece(rep, p0) + c8 * 16, oM1 = p1 < 56 ? m_piece(rep, p1) + c8 * 16 : OOB;
  auto mlds = [&](int pc) { const int c = pc >> 1; return (((c >> 1) * 4 + (c & 1) * 2 + (c8 & 1)) * 8 + 4 * (pc & 1) + (c8 >> 1)) * 16; };
  const int wM0 = mlds(p0), wM1 = mlds(p1);
  const int rl = n >> 2, sub = n & 3;
  const int oS = s_piece(hf, (4 * w + g) * 4 + sub) + rl * 32;
  const int oE = e_piece(hf, 4 * w + (n >> 2)) + (n & 3) * 64 + g * 16;
  const int oT = e_piece(hf, 4 * w + g) + 256 + rl * 8;
  const unsigned mk0 = rl == 0 ? 0xFFFFu : rl == 1 ? 0xFFFF0000u : 0u, mk1 = rl == 2 ? 0xFFFFu : rl == 3 ? 0xFFFF0000u : 0u;
  const int oPr = l < 4 ? O_FLG + (F_VS + ((4 * w + l) * 28 + ct) / 8) * 256 : OOB;
  const int oO0 = m_piece(l >> 5, 2 * ct + hf) + (l & 31) * 4, oO1 = m_piece(2 + (l >> 5), 2 * ct + hf) + (l & 31) * 4;
  LAS char* sR = sm + 16384; LAS unsigned* cnt = (LAS unsigned*)(sm + 16384 + 1024);
  if (w == 0) cnt[l] = 0;
  bar();
  int have = 0;
  v4u V[4];
  if (!wait_prog(ws, oPr, 1, have)) return;
#pragma unroll
  for (int hh = 0; hh < 4; ++hh) V[hh] = ld16<SC1>(ws, l * 16 + hh * 1024, O_VX + (ct * 16 + 4 * w) * 1024);
  for (int s = 0; s < NSEG; ++s) {
    const int xm = __builtin_amdgcn_readlane(tab, s), so = O_CRIT + s * 262144;
    v4u M0, M1; unsigned sp = 0;
    for (;;) {
      fence_c();
      M0 = ld16<SC1>(ws, oM0 ^ xm, so); M1 = ld16<SC1>(ws, oM1 == OOB ? OOB : oM1 ^ xm, so);
      if (__all(max(mx4(M0), mx4(M1)) != SENT)) break;
      if (spin_fail(sp, ws)) return;
    }
    LAS char* sM = sm + (s & 1) * 8192;
    *(LAS v4u*)(sM + wM0) = M0; *(LAS v4u*)(sM + wM1) = M1;
    drain();
    bar();
    if (t == 0) st4<SC1>((unsigned)s, ws, O_FLG + (F_PC + hf * 28 + ct) * 256, 0);
    v4f u[4] = {{0, 0, 0, 0}, {0, 0, 0, 0}, {0, 0, 0, 0}, {0, 0, 0, 0}};
#pragma unroll
    for (int ks = 0; ks < 14; ++ks) {
      const v4u mf = *(LAS v4u*)(sM + ((ks * 4 + g) * 8 + (n & 7)) * 16);
#pragma unroll
      for (int hh = 0; hh < 4; ++hh) u[hh] = mfma32(mf, W[hh][ks], u[hh]);
    }
    const v4f ux = g < 2 ? u[0] : u[2], uy = g < 2 ? u[1] : u[3];
    const v4u up = pk8(ux, uy);
    if ((w >> 1) == hf) st16<SC1>(up, ws, l * 16, O_U + ((s * 28 + ct) * 4 + w) * 1024);
    if (s == NSEG - 1) break;
    auto q0 = __builtin_amdgcn_permlane16_swap(up.x, up.z, false, false);
    auto q1 = __builtin_amdgcn_permlane16_swap(up.y, up.w, false, false);
    const v4u Uop = {q0[0], q1[0], q0[1], q1[1]};
    v4u E, X0, Y0; v2u T;
    for (;;) {
      fence_c();
      E = ld16<SC1>(ws, oE ^ xm, so); T = ld8<SC1>(ws, oT ^ xm, so);
      X0 = ld16<SC1>(ws, oS ^ xm, so); Y0 = ld16<SC1>(ws, (oS ^ xm) + 16, so);
      if (__all(max(max(mx4(E), max(T.x, T.y)), max(mx4(X0), mx4(Y0))) != SENT)) break;
      if (spin_fail(sp, ws)) return;
    }
    const v4f z = {0, 0, 0, 0};
    const v4f a0 = mfma32(E, V[0], z), a1 = mfma32(E, V[1], z), a2 = mfma32(E, V[2], z), a3 = mfma32(E, V[3], z);
    const float x0 = __uint_as_float(X0.x) + dppf<XOR2>(__uint_as_float(Y0.x)), x1 = __uint_as_float(X0.y) + dppf<XOR2>(__uint_as_float(Y0.y));
    const float x2 = __uint_as_float(X0.z) + dppf<XOR2>(__uint_as_float(Y0.z)), x3 = __uint_as_float(X0.w) + dppf<XOR2>(__uint_as_float(Y0.w));
    const float s0 = x0 + dppf<XOR1>(x2), s1 = x1 + dppf<XOR1>(x3), mx = __uint_as_float(T.x), sx = __uint_as_float(T.y);
    float m = fmaxf(fmaxf(s0, s1), mx);
    m = fmaxf(m, dppf<XOR1>(m)); m = fmaxf(m, dppf<XOR2>(m));
    const float e0 = ex2(s0 - m), e1 = ex2(s1 - m), ex = ex2(mx - m);
    float tt = e0 + e1; tt += dppf<XOR1>(tt); tt += dppf<XOR2>(tt);
    const float inv = __builtin_amdgcn_rcpf(fmaf(ex, sx, tt));
    const unsigned pp = pk(e0 * inv, e1 * inv), ww = pk(ex * inv, ex * inv);
    const v4u Pop = {dppu<0x00>(pp), dppu<0x55>(pp), dppu<0xAA>(pp), dppu<0xFF>(pp)};
    const v4f ox = g < 2 ? (g == 0 ? a0 : a1) : (g == 2 ? a2 : a3);
    v4f d = mfma32(Pop, Uop, z);
    d = mfma16(v2u{ww & mk0, ww & mk1}, v2u{pk(ox.x, ox.y), pk(ox.z, ox.w)}, d);
    *(LAS float*)(sR + ((g * 8 + (n >> 1)) * 4 + w) * 8 + (n & 1) * 4) = d.x;
    fence_c();
    const unsigned old = __hip_atomic_fetch_add(cnt + l, 1u, __ATOMIC_RELAXED, __HIP_MEMORY_SCOPE_WORKGROUP);
    fence_c();
    const v4f r0 = *(LAS v4f*)(sR + (l & 31) * 32), r1 = *(LAS v4f*)(sR + (l & 31) * 32 + 16);
    const unsigned mo = san(pk((r0.x + r0.z) + (r1.x + r1.z), (r0.y + r0.w) + (r1.y + r1.w)));
    const int xn = __builtin_amdgcn_readlane(tab, s + 1);
    const bool last = (old & 3u) == 3u;
    st4<SC1>(mo, ws, last ? oO0 ^ xn : OOB, so + 262144);
    st4<SC1>(mo, ws, last ? oO1 ^ xn : OOB, so + 262144);
    if (s + 1 < NSEG - 1) {
      if (!wait_prog(ws, oPr, s + 2, have)) return;
#pragma unroll
      for (int hh = 0; hh < 4; ++hh) V[hh] = ld16<SC1>(ws, l * 16 + hh * 1024, O_VX + (((s + 1) * 28 + ct) * 16 + 4 * w) * 1024);
    }
  }
  drain(); bar();
  if (t == 0) st4<SC1>((unsigned)NSEG, ws, O_FLG + (F_PC + hf * 28 + ct) * 256, 0);
}

template <int ISV>
DEV void role_server(const Params& p, LAS char* sm, int idx) {
  const int t = threadIdx.x, l = t & 63, w = rfl(t >> 6);
  const rsrc_t ws = mkrs(p.ws, WS_USED);
  const int T = 8 * idx + 2 * w, h = T / 28, it = T % 28;
  v4u W[2][14];
#pragma unroll
  for (int a = 0; a < 2; ++a)
#pragma unroll
    for (int ks = 0; ks < 14; ++ks) W[a][ks] = ld16<0>(ws, l * 16, (ISV ? O_BFR : O_AFR) + ((T + a) * 14 + ks) * 1024);
  v4u st[7];
#pragma unroll
  for (int q = 0; q < 7; ++q) st[q] = ld16<0>(ws, l * 16, O_XFR + (7 * w + q) * 1024);
#pragma unroll
  for (int q = 0; q < 7; ++q) *(LAS v4u*)(sm + (7 * w + q) * 1024 + l * 16) = st[q];
  bar();
  for (int s = 0; s < NSEG; ++s) {
    LAS char* cur = sm + (s & 1) * 28672; LAS char* nxt = sm + ((s & 1) ^ 1) * 28672;
    const int sn = s + 1 < NSEG ? s + 1 : s;
#pragma unroll
    for (int q = 0; q < 7; ++q) st[q] = ld16<0>(ws, l * 16, O_XFR + (sn * 28 + 7 * w + q) * 1024);
    v4f acc[2][2] = {{{0, 0, 0, 0}, {0, 0, 0, 0}}, {{0, 0, 0, 0}, {0, 0, 0, 0}}};
#pragma unroll
    for (int ks = 0; ks < 14; ++ks)
#pragma unroll
      for (int tt = 0; tt < 2; ++tt) {
        const v4u xf = *(LAS v4u*)(cur + (tt * 14 + ks) * 1024 + l * 16);
#pragma unroll
        for (int a = 0; a < 2; ++a) acc[a][tt] = ISV ? mfma32(xf, W[a][ks], acc[a][tt]) : mfma32(W[a][ks], xf, acc[a][tt]);
      }
    if (ISV) {
#pragma unroll
      for (int a = 0; a < 2; ++a) st16<SC1>(pk8(acc[a][0], acc[a][1]), ws, l * 16, O_VX + ((s * 28 + it + a) * 16 + h) * 1024);
    } else {
#pragma unroll
      for (int tt = 0; tt < 2; ++tt) st16<SC1>(pk8(acc[0][tt], acc[1][tt]), ws, l * 16, O_KX + ((s * 16 + h) * 28 + it + tt) * 1024);
    }
    if (s < 2) drain(); else asm volatile("s_waitcnt vmcnt(2)" ::: "memory");
#pragma unroll
    for (int q = 0; q < 7; ++q) *(LAS v4u*)(nxt + (7 * w + q) * 1024 + l * 16) = st[q];
    bar();
    if (t == 0) st4<SC1>((unsigned)(s < 2 ? s + 1 : s), ws, O_FLG + ((ISV ? F_VS : F_KS) + idx) * 256, 0);
  }
  drain(); bar();
  if (t == 0) st4<SC1>((unsigned)NSEG, ws, O_FLG + ((ISV ? F_VS : F_KS) + idx) * 256, 0);
}

DEV float phi(float v) { return (v > 0.0f ? v + 1.0f : __expf(v)) + 1e-6f; }
DEV v4f phi4(v4f a) { return v4f{phi(a.x), phi(a.y), phi(a.z), phi(a.w)}; }

DEV bool role_L(const Params& p, LAS char* sm, int li) {
  const int t = threadIdx.x, l = t & 63, w = rfl(t >> 6), g = l >> 4, n = l & 15;
  const rsrc_t ws = mkrs(p.ws, WS_USED);
  const v4f z = {0, 0, 0, 0};
  for (int q = 0; q < 28; q += 4) {
    v4u v[4];
#pragma unroll
    for (int i = 0; i < 4; ++i) v[i] = ld16<0>(ws, l * 16, O_XFR + (li * 112 + w * 28 + q + i) * 1024);
#pragma unroll
    for (int i = 0; i < 4; ++i) *(LAS v4u*)(sm + (w * 28 + q + i) * 1024 + l * 16) = v[i];
  }
  bar();
#pragma unroll 1
  for (int hh = 0; hh < 4; ++hh) {
    const int h = 4 * w + hh;
    v4f acc[4][8];
#pragma unroll
    for (int a = 0; a < 4; ++a)
#pragma unroll
      for (int tt = 0; tt < 8; ++tt) acc[a][tt] = z;
    v4u wn[4];
#pragma unroll
    for (int a = 0; a < 4; ++a) wn[a] = ld16<0>(ws, l * 16, O_LW + ((((1 + (a >> 1)) * 16 + h) * 2 + (a & 1)) * 14) * 1024);
#pragma unroll 1
    for (int ks = 0; ks < 14; ++ks) {
      v4u wf[4];
      const int kn = ks < 13 ? ks + 1 : ks;
#pragma unroll
      for (int a = 0; a < 4; ++a) { wf[a] = wn[a]; wn[a] = ld16<0>(ws, l * 16, O_LW + ((((1 + (a >> 1)) * 16 + h) * 2 + (a & 1)) * 14 + kn) * 1024); }
#pragma unroll
      for (int tt = 0; tt < 8; ++tt) {
        const v4u xf = *(LAS v4u*)(sm + (tt * 14 + ks) * 1024 + l * 16);
#pragma unroll
        for (int a = 0; a < 4; ++a) acc[a][tt] = mfma32(xf, wf[a], acc[a][tt]);
      }
    }
    v4f kv[2][2] = {{z, z}, {z, z}};
    const v4f one = {1.0f, 1.0f, 1.0f, 1.0f};
#pragma unroll
    for (int q = 0; q < 4; ++q) {
      const v4u k0 = pk8(phi4(acc[0][2 * q]), phi4(acc[0][2 * q + 1])), k1 = pk8(phi4(acc[1][2 * q]), phi4(acc[1][2 * q + 1]));
      const v4u v0 = pk8(acc[2][2 * q], acc[2][2 * q + 1]), v1 = pk8(n == 12 ? one : acc[3][2 * q], n == 12 ? one : acc[3][2 * q + 1]);
      kv[0][0] = mfma32(k0, v0, kv[0][0]); kv[0][1] = mfma32(k0, v1, kv[0][1]);
      kv[1][0] = mfma32(k1, v0, kv[1][0]); kv[1][1] = mfma32(k1, v1, kv[1][1]);
    }
#pragma unroll
    for (int a = 0; a < 4; ++a) st16<SC1>(__builtin_bit_cast(v4u, kv[a >> 1][a & 1]), ws, l * 16, O_KVP + ((h * 16 + li) * 4 + a) * 1024);
  }
  drain(); bar();
  if (t == 0) st4<SC1>(1u, ws, O_FLG + (F_L1 + li) * 256, 0);
  v4u pq[4][8];
#pragma unroll
  for (int hp = 0; hp < 2; ++hp) {
    v4f acc[4][8];
#pragma unroll
    for (int a = 0; a < 4; ++a)
#pragma unroll
      for (int tt = 0; tt < 8; ++tt) acc[a][tt] = z;
    v4u wn[4];
#pragma unroll
    for (int a = 0; a < 4; ++a) wn[a] = ld16<0>(ws, l * 16, O_LW + (((4 * w + 2 * hp) * 2 + a) * 14) * 1024);
#pragma unroll 1
    for (int ks = 0; ks < 14; ++ks) {
      v4u wf[4];
      const int kn = ks < 13 ? ks + 1 : ks;
#pragma unroll
      for (int a = 0; a < 4; ++a) { wf[a] = wn[a]; wn[a] = ld16<0>(ws, l * 16, O_LW + (((4 * w + 2 * hp) * 2 + a) * 14 + kn) * 1024); }
#pragma unroll
      for (int tt = 0; tt < 8; ++tt) {
        const v4u xf = *(LAS v4u*)(sm + (tt * 14 + ks) * 1024 + l * 16);
#pragma unroll
        for (int a = 0; a < 4; ++a) acc[a][tt] = mfma32(wf[a], xf, acc[a][tt]);
      }
    }
#pragma unroll
    for (int tt = 0; tt < 8; ++tt) { pq[2 * hp][tt] = pk8(phi4(acc[0][tt]), phi4(acc[1][tt])); pq[2 * hp + 1][tt] = pk8(phi4(acc[2][tt]), phi4(acc[3][tt]));
      asm volatile("" : "+v"(pq[2 * hp][tt]), "+v"(pq[2 * hp + 1][tt])); }
  }
  int have = 0;
  if (!wait_prog(ws, l < 16 ? O_FLG + (F_L1 + l) * 256 : OOB, 1, have)) return false;
  if (w < 2) {
    v4f k0 = z, k1 = z;
#pragma unroll 4
    for (int q = 0; q < 16; ++q) {
      k0 += __builtin_bit_cast(v4f, ld16<SC1>(ws, l * 16, O_KVP + ((li * 16 + q) * 4 + w) * 1024));
      k1 += __builtin_bit_cast(v4f, ld16<SC1>(ws, l * 16, O_KVP + ((li * 16 + q) * 4 + 2 + w) * 1024));
    }
    if (g == 3) k1 = z;
    const v4u f = pk8(k0, k1);
    st16<SC1>(f, ws, l * 16, O_KVF + (li * 3 + w) * 1024);
    const int src = (l & 48) | 12;
    const v4u zf = {(unsigned)__shfl((int)f.x, src), (unsigned)__shfl((int)f.y, src), (unsigned)__shfl((int)f.z, src), (unsigned)__shfl((int)f.w, src)};
    if (w == 1) st16<SC1>(zf, ws, l * 16, O_KVF + (li * 3 + 2) * 1024);
  }
  drain(); bar();
  if (t == 0) st4<SC1>(1u, ws, O_FLG + (F_L2 + li) * 256, 0);
  have = 0;
  if (!wait_prog(ws, l < 16 ? O_FLG + (F_L2 + l) * 256 : OOB, 1, have)) return false;
#pragma unroll
  for (int hh = 0; hh < 4; ++hh) {
    const int h = 4 * w + hh;
    const v4u f0 = ld16<SC1>(ws, l * 16, O_KVF + (h * 3) * 1024), f1 = ld16<SC1>(ws, l * 16, O_KVF + (h * 3 + 1) * 1024), fz = ld16<SC1>(ws, l * 16, O_KVF + (h * 3 + 2) * 1024);
#pragma unroll
    for (int tt = 0; tt < 8; ++tt) {
      const v4f n0 = mfma32(f0, pq[hh][tt], z), n1 = mfma32(f1, pq[hh][tt], z), dn = mfma32(fz, pq[hh][tt], z);
      const float inv = 1.0f / (dn.x + 1e-6f);
      *(LAS v4u*)(sm + (h * 8 + tt) * 1024 + l * 16) = pk8(n0 * inv, n1 * inv);
    }
  }
  bar();
#pragma unroll
  for (int ps = 0; ps < 2; ++ps) {
    const int c0 = 7 * w + (ps ? 3 : 0);
    v4f acc[4][8];
#pragma unroll
    for (int a = 0; a < 4; ++a)
#pragma unroll
      for (int tt = 0; tt < 8; ++tt) acc[a][tt] = z;
    v4u wn[4];
#pragma unroll
    for (int a = 0; a < 4; ++a) wn[a] = ld16<0>(ws, l * 16, O_LWO + ((c0 + a) * 16) * 1024);
#pragma unroll 1
    for (int h = 0; h < 16; ++h) {
      v4u wf[4];
      const int hn = h < 15 ? h + 1 : h;
#pragma unroll
      for (int a = 0; a < 4; ++a) { wf[a] = wn[a]; wn[a] = ld16<0>(ws, l * 16, O_LWO + ((c0 + a) * 16 + hn) * 1024); }
#pragma unroll
      for (int tt = 0; tt < 8; ++tt) {
        const v4u of = *(LAS v4u*)(sm + (h * 8 + tt) * 1024 + l * 16);
#pragma unroll
        for (int a = 0; a < 4; ++a) acc[a][tt] = mfma32(wf[a], of, acc[a][tt]);
      }
    }
#pragma unroll
    for (int a = 0; a < 4; ++a)
#pragma unroll
      for (int tt = 0; tt < 8; ++tt) st16<SC1>(__builtin_bit_cast(v4u, acc[a][tt]), ws, l * 16, O_AOUT + ((li * 8 + tt) * 28 + c0 + a) * 1024);
  }
  drain(); bar();
  if (t == 0) st4<SC1>(1u, ws, O_FLG + (F_L3 + li) * 256, 0);
  return true;
}

DEV void role_F(const Params& p, LAS char* sm, int s, int hf) {
  const int t = threadIdx.x, l = t & 63, w = rfl(t >> 6), g = l >> 4, n = l & 15;
  const rsrc_t ws = mkrs(p.ws, WS_USED);
  const v4f z = {0, 0, 0, 0};
  v4u X[2][14];
#pragma unroll
  for (int tt = 0; tt < 2; ++tt)
#pragma unroll
    for (int ks = 0; ks < 14; ++ks) X[tt][ks] = ld16<0>(ws, l * 16, O_XFR + ((s * 2 + tt) * 14 + ks) * 1024);
  float gate[2];
  {
    const rsrc_t rg = mkrs(p.gw, 448 * 2);
    v4f g0 = z, g1 = z;
#pragma unroll
    for (int ks = 0; ks < 14; ++ks) { const v4u gf = ld16<0>(rg, g * 16 + ks * 64, 0); g0 = mfma32(gf, X[0][ks], g0); g1 = mfma32(gf, X[1][ks], g1); }
    gate[0] = 1.0f / (1.0f + __expf(-g0.x)); gate[1] = 1.0f / (1.0f + __expf(-g1.x));
  }
  int have = 0;
  if (!wait_prog(ws, l < 56 ? O_FLG + (F_KS + l) * 256 : OOB, s + 1, have)) return;
  v4f sx[4][3];
#pragma unroll
  for (int hh = 0; hh < 4; ++hh) {
    v4u kf[28];
#pragma unroll
    for (int f = 0; f < 28; ++f) kf[f] = ld16<SC1>(ws, l * 16, O_KX + ((s * 16 + 4 * w + hh) * 28 + f) * 1024);
    sx[hh][0] = z; sx[hh][1] = z; sx[hh][2] = z;
#pragma unroll
    for (int ks = 0; ks < 14; ++ks) {
      sx[hh][0] = mfma32(kf[2 * ks], X[0][ks], sx[hh][0]);
      sx[hh][1] = mfma32(kf[2 * ks], X[1][ks], sx[hh][1]);
      sx[hh][2] = mfma32(kf[2 * ks + 1], X[1][ks], sx[hh][2]);
    }
    fence_c();
  }
  have = 0;
  if (!wait_prog(ws, l < 56 ? O_FLG + (F_PA + l) * 256 : OOB, s + 1, have)) return;
  v4f sq[2][2];
  {
    v4u kf[28];
#pragma unroll
    for (int f = 0; f < 28; ++f) kf[f] = ld16<SC1>(ws, l * 16, O_KM + ((s * 8 + 2 * w) * 14 + f) * 1024);
#pragma unroll
    for (int pr = 0; pr < 2; ++pr) {
      sq[pr][0] = z; sq[pr][1] = z;
#pragma unroll
      for (int ks = 0; ks < 14; ++ks) { sq[pr][0] = mfma32(kf[pr * 14 + ks], X[0][ks], sq[pr][0]); sq[pr][1] = mfma32(kf[pr * 14 + ks], X[1][ks], sq[pr][1]); }
    }
  }
  const float NINF = -__builtin_inff();
#pragma unroll
  for (int tt = 0; tt < 2; ++tt) {
    v4f pm[2] = {z, z};
#pragma unroll
    for (int hh = 0; hh < 4; ++hh) {
      v4f d = tt ? sx[hh][2] : sx[hh][0];
      d.x = 4 * g + 0 > n ? MASKV : d.x; d.y = 4 * g + 1 > n ? MASKV : d.y; d.z = 4 * g + 2 > n ? MASKV : d.z; d.w = 4 * g + 3 > n ? MASKV : d.w;
      const v4f t0 = tt ? sx[hh][1] : d, t1 = tt ? d : v4f{MASKV, MASKV, MASKV, MASKV};
      const bool mine = (g >> 1) == (hh >> 1);
      const v4f mm = mine ? sq[hh & 1][tt] : v4f{NINF, NINF, NINF, NINF};
      float m = fmaxf(fmaxf(fmaxf(t0.x, t0.y), fmaxf(t0.z, t0.w)), fmaxf(fmaxf(t1.x, t1.y), fmaxf(t1.z, t1.w)));
      m = xmax(fmaxf(m, fmaxf(fmaxf(mm.x, mm.y), fmaxf(mm.z, mm.w))));
      const v4f e0 = {ex2(t0.x - m), ex2(t0.y - m), ex2(t0.z - m), ex2(t0.w - m)};
      const v4f e1 = {ex2(t1.x - m), ex2(t1.y - m), ex2(t1.z - m), ex2(t1.w - m)};
      const v4f em = {ex2(mm.x - m), ex2(mm.y - m), ex2(mm.z - m), ex2(mm.w - m)};
      const v4f es = (e0 + e1) + em;
      const float inv = 1.0f / xsum((es.x + es.y) + (es.z + es.w));
      *(LAS v4u*)(sm + ((4 * w + hh) * 2 + tt) * 1024 + l * 16) = pk8(e0 * inv, e1 * inv);
      pm[hh & 1] = mine ? em * inv : pm[hh & 1];
    }
    *(LAS v4u*)(sm + (32 + w * 2 + tt) * 1024 + l * 16) = pk8(pm[0], pm[1]);
  }
  bar();
  have = 0;
  if (!wait_prog(ws, l < 56 ? O_FLG + (F_VS + l) * 256 : OOB, s + 1, have)) return;
  have = 0;
  if (!wait_prog(ws, l < 56 ? O_FLG + (F_PC + l) * 256 : OOB, s + 1, have)) return;
  have = 0;
  if (!wait_prog(ws, l == 0 ? O_FLG + (F_L3 + (s >> 2)) * 256 : OOB, 1, have)) return;
  v4u Pf[20][2];
#pragma unroll
  for (int q = 0; q < 20; ++q)
#pragma unroll
    for (int tt = 0; tt < 2; ++tt) Pf[q][tt] = *(LAS v4u*)(sm + (q * 2 + tt) * 1024 + l * 16);
  const rsrc_t ro = mkrs(p.out, 2048 * 448 * 2);
  const int c0 = hf * 14 + (w < 2 ? 4 * w : 3 * w + 2), nc = w < 2 ? 4 : 3;
#pragma unroll 1
  for (int ci = 0; ci < nc; ++ci) {
    const int ct = c0 + ci;
    v4u zf[20];
#pragma unroll
    for (int q = 0; q < 16; ++q) zf[q] = ld16<SC1>(ws, l * 16, O_VX + ((s * 28 + ct) * 16 + q) * 1024);
#pragma unroll
    for (int q = 0; q < 4; ++q) zf[16 + q] = ld16<SC1>(ws, l * 16, O_U + ((s * 28 + ct) * 4 + q) * 1024);
#pragma unroll
    for (int tt = 0; tt < 2; ++tt) {
      const v4f a = __builtin_bit_cast(v4f, ld16<SC1>(ws, l * 16, O_AOUT + ((s * 2 + tt) * 28 + ct) * 1024));
      v4f acc = z;
#pragma unroll
      for (int q = 0; q < 20; ++q) acc = mfma32(zf[q], Pf[q][tt], acc);
      const v4f o = a * gate[tt] + acc * (1.0f - gate[tt]);
      st8<0>(v2u{pk(o.x, o.y), pk(o.z, o.w)}, ro, ((s * 32 + tt * 16 + n) * 448 + ct * 16 + g * 4) * 2, 0);
    }
  }
}

constexpr int LDS_BYTES = 128 * 1024;
__global__ void __launch_bounds__(256, 1) k_main(Params p) {
  extern __shared__ __attribute__((aligned(16))) char smem[];
  LAS char* sm = (LAS char*)smem;
  const int b = blockIdx.x, x = b & 7, j = b >> 3;
  if (x < 4) {
    if (!(p.mask & 2u)) return;
    if (j >= 28) role_X(p, sm, x, x * 4 + (j - 28));
    else if (x < 2) role_A(p, sm, x, x * 28 + j);
    else role_C(p, sm, x - 2, j);
  } else {
    if (p.mask & 1u) {
      if (j >= 28) { if (!role_L(p, sm, (x - 4) * 4 + (j - 28))) return; }
      else if (x < 6) role_server<0>(p, sm, (x - 4) * 28 + j);
      else role_server<1>(p, sm, (x - 6) * 28 + j);
    }
    if (p.mask & 4u) role_F(p, sm, (j >> 1) * 4 + (x - 4), j & 1);
  }
}

#define SPLIT 0
extern "C" void kernel_launch(void* const* d_in, const int* in_sizes, int n_in, void* d_out, int out_size, void* d_ws, size_t ws_size,
                              hipStream_t stream) {
  (void)in_sizes; (void)n_in; (void)out_size; (void)ws_size;
  Params p;
  p.x = (const unsigned short*)d_in[0]; p.wqkv = (const unsigned short*)d_in[1]; p.awo = (const unsigned short*)d_in[2];
  p.mem0 = (const unsigned short*)d_in[4]; p.wq = (const unsigned short*)d_in[5]; p.wk = (const unsigned short*)d_in[6];
  p.wv = (const unsigned short*)d_in[7]; p.wo = (const unsigned short*)d_in[8]; p.gw = (const unsigned short*)d_in[10];
  p.out = (unsigned short*)d_out;
  p.ws = (char*)(((size_t)d_ws + (2u << 20) - 1) & ~(size_t)((2u << 20) - 1));
  p.mask = 7u; p.pad = 0u;
  (void)hipFuncSetAttribute((const void*)k_main, hipFuncAttributeMaxDynamicSharedMemorySize, LDS_BYTES);
  k_prep<<<256, 256, 0, stream>>>(p);
  k_main<<<256, 256, LDS_BYTES, stream>>>(p);
}
```

```cpp
#include <hip/hip_runtime.h>
#include <hip/hip_bf16.h>

typedef unsigned v4u __attribute__((ext_vector_type(4)));
typedef unsigned v2u __attribute__((ext_vector_type(2)));
typedef float v4f __attribute__((ext_vector_type(4)));
typedef float v2f __attribute__((ext_vector_type(2)));
typedef short v4s __attribute__((ext_vector_type(4)));
typedef __bf16 v8bf __attribute__((ext_vector_type(8)));
typedef __bf16 v2bf __attribute__((ext_vector_type(2)));
typedef __amdgpu_buffer_rsrc_t rsrc_t;
#define LAS __attribute__((address_space(3)))
#define DEV __device__ __forceinline__

#define ROT 2
#define CODEPF 1
#define XPF 8
#define NSAMP 3
#define WSOFF 0
constexpr int NSEG = 64;
constexpr int SC1 = 16;
constexpr int OOB = 0x7ff00000;
constexpr unsigned SENT = 0xFFFFFFFFu;
constexpr int MB = 1 << 20;
constexpr int O_CRIT = 0;
constexpr int O_AFR = 16 * MB + 512 * 1024, O_BFR = 23 * MB, O_LW = 32 * MB, O_LWO = 34 * MB;
constexpr int O_KVP = 35 * MB, O_KVF = 36 * MB, O_AOUT = 37 * MB, O_FLG = 41 * MB;
constexpr int O_KX = 48 * MB, O_KM = 112 * MB, O_U = 120 * MB, O_VX = 128 * MB, WS_USED = 196 * MB;
constexpr int F_KS = 0, F_VS = 64, F_PA = 128, F_PC = 192, F_L1 = 256, F_L2 = 272, F_L3 = 288, F_ABORT = 304, F_COUNT = 320;
static_assert(O_AOUT == 37 * MB && F_L3 == 288 && O_FLG + F_ABORT * 256 == 41 * MB + 304 * 256, "the generated code holds this offset");
constexpr int O_TAB = O_FLG + F_COUNT * 256;
constexpr int O_DBG = 44 * MB;
constexpr unsigned SPIN_CAP = 1u << 18;
constexpr float CA = 1.4426950408889634f / 16.0f;
constexpr float MASKV = -1.0e9f * 1.4426950408889634f;

struct Params {
  const unsigned short* x; const unsigned short* wqkv; const unsigned short* awo; const unsigned short* mem0;
  const unsigned short* wq; const unsigned short* wk; const unsigned short* wv; const unsigned short* wo; const unsigned short* gw;
  unsigned short* out; char* ws; unsigned mask; unsigned pad;
};

DEV rsrc_t mkrs(const void* p, unsigned bytes) { return __builtin_amdgcn_make_buffer_rsrc((void*)p, 0, bytes, 0x00020000); }
template <int AUX> DEV v4u ld16(rsrc_t r, int v, int s) { return __builtin_amdgcn_raw_buffer_load_b128(r, v, s, AUX); }
template <int AUX> DEV v2u ld8(rsrc_t r, int v, int s) { return __builtin_amdgcn_raw_buffer_load_b64(r, v, s, AUX); }
template <int AUX> DEV unsigned ld4(rsrc_t r, int v, int s) { return __builtin_amdgcn_raw_buffer_load_b32(r, v, s, AUX); }
template <int AUX> DEV void st16(v4u d, rsrc_t r, int v, int s) { __builtin_amdgcn_raw_buffer_store_b128(d, r, v, s, AUX); }
template <int AUX> DEV void st8(v2u d, rsrc_t r, int v, int s) { __builtin_amdgcn_raw_buffer_store_b64(d, r, v, s, AUX); }
template <int AUX> DEV void st4(unsigned d, rsrc_t r, int v, int s) { __builtin_amdgcn_raw_buffer_store_b32(d, r, v, s, AUX); }
DEV v4f mfma32(v4u a, v4u b, v4f c) { return __builtin_amdgcn_mfma_f32_16x16x32_bf16(__builtin_bit_cast(v8bf, a), __builtin_bit_cast(v8bf, b), c, 0, 0, 0); }
DEV v4f mfma16(v2u a, v2u b, v4f c) { return __builtin_amdgcn_mfma_f32_16x16x16bf16_1k(__builtin_bit_cast(v4s, a), __builtin_bit_cast(v4s, b), c, 0, 0, 0); }
DEV unsigned pk(float lo, float hi) { v2f f = {lo, hi}; return __builtin_bit_cast(unsigned, __builtin_convertvector(f, v2bf)); }
DEV v4u pk8(v4f a, v4f b) { return v4u{pk(a.x, a.y), pk(a.z, a.w), pk(b.x, b.y), pk(b.z, b.w)}; }
template <int CTRL> DEV unsigned dppu(unsigned x) { return (unsigned)__builtin_amdgcn_mov_dpp((int)x, CTRL, 0xf, 0xf, true); }
template <int CTRL> DEV float dppf(float x) { return __uint_as_float(dppu<CTRL>(__float_as_uint(x))); }
constexpr int XOR1 = 0xB1, XOR2 = 0x4E, ROR8 = 0x128, HMIR = 0x141;
DEV v4u ror8(v4u a) { return v4u{dppu<ROR8>(a.x), dppu<ROR8>(a.y), dppu<ROR8>(a.z), dppu<ROR8>(a.w)}; }
DEV unsigned mx4(v4u a) { return max(max(a.x, a.y), max(a.z, a.w)); }
DEV unsigned san(unsigned x) { return min(x, 0xFFFFFFFEu); }
DEV float ex2(float x) { return __builtin_amdgcn_exp2f(x); }
DEV void fence_c() { asm volatile("" ::: "memory"); }
DEV void drain() { asm volatile("s_waitcnt vmcnt(0)" ::: "memory"); }
DEV void bar() { asm volatile("s_waitcnt lgkmcnt(0)\n\ts_barrier" ::: "memory"); }
DEV int rfl(int x) { return __builtin_amdgcn_readfirstlane(x); }
DEV float xmax(float x) {
  auto s = __builtin_amdgcn_permlane16_swap(__float_as_uint(x), __float_as_uint(x), false, false);
  x = fmaxf(__uint_as_float(s[0]), __uint_as_float(s[1]));
  auto t = __builtin_amdgcn_permlane32_swap(__float_as_uint(x), __float_as_uint(x), false, false);
  return fmaxf(__uint_as_float(t[0]), __uint_as_float(t[1]));
}
DEV float xsum(float x) {
  auto s = __builtin_amdgcn_permlane16_swap(__float_as_uint(x), __float_as_uint(x), false, false);
  x = __uint_as_float(s[0]) + __uint_as_float(s[1]);
  auto t = __builtin_amdgcn_permlane32_swap(__float_as_uint(x), __float_as_uint(x), false, false);
  return __uint_as_float(t[0]) + __uint_as_float(t[1]);
}
DEV bool spin_fail(unsigned& n, rsrc_t r) {
  ++n;
  if (n > SPIN_CAP) { st4<SC1>(1u, r, O_FLG + F_ABORT * 256, 0); return true; }
  return (n & 255u) == 0u && rfl((int)ld4<SC1>(r, O_FLG + F_ABORT * 256, 0)) != 0;
}
DEV bool wait_prog(rsrc_t r, int voff, int need, int& have) {
  unsigned n = 0;
  while (have < need) {
    fence_c();
    int v = voff == OOB ? 0x7fffffff : 0;
    v |= (int)ld4<SC1>(r, voff, 0);
#pragma unroll
    for (int m = 32; m >= 1; m >>= 1) v = min(v, __shfl_xor(v, m));
    have = rfl(v);
    if (have >= need) break;
    __builtin_amdgcn_s_sleep(4);
    if (spin_fail(n, r)) return false;
  }
  fence_c();
  return true;
}
#define TS(k) do {} while (0)
DEV int m_piece(int rep, int p) { return (2 * (rep >> 1) + (p >= 28)) * 32768 + ((rep & 1) << 14) + (p >= 28 ? p - 28 : p) * 256; }
DEV int m_blk(int rep, int Q, int l) { return Q < 7 ? (m_piece(rep, 8 * Q + (l >> 3)) + (l & 7) * 16) & ~(1 << 14) : OOB; }
DEV int s_piece(int y, int p) { return (4 + (p >> 5)) * 32768 + (y << 14) + (p & 31) * 256; }
DEV int e_piece(int y, int h) { return (6 + (h >> 3)) * 32768 + (y << 14) + (h & 7) * 1024; }

#define ASM_DBG 0
#define ASM_SM 0
#define ASM_WR 3
#define ASM_AH 0
#define ASM_XC 2
#define ASM_NM 4
#define ASM_MIX 0
#define ASM_A \
  "s_nop 4\n" \
  "buffer_load_dwordx4 v[32:35], %[oMa], %[rs], 0 offen sc1\n" \
  "buffer_load_dwordx4 v[36:39], %[oMb], %[rs], 0 offen sc1\n" \
  "buffer_load_dwordx4 v[208:211], %[oCal], %[rs], 0 offen\n" \
  "buffer_load_dwordx4 v[212:215], %[oCal], %[rs], 0 offen offset:128\n" \
  "buffer_load_dwordx4 v[216:219], %[oCal], %[rs], 0 offen offset:256\n" \
  "buffer_load_dwordx4 v[220:223], %[oCal], %[rs], 0 offen offset:384\n" \
  "s_add_u32 s46, %[sW], 0\n" \
  "buffer_load_dwordx4 a[0:3], %[l16], %[rs], s46 offen\n" \
  "buffer_load_dwordx4 a[4:7], %[l16], %[rs], s46 offen offset:1024\n" \
  "buffer_load_dwordx4 a[8:11], %[l16b], %[rs], s46 offen offset:2048\n" \
  "buffer_load_dwordx4 a[12:15], %[l16b], %[rs], s46 offen offset:3072\n" \
  "s_add_u32 s46, %[sW], 14336\n" \
  "buffer_load_dwordx4 a[16:19], %[l16], %[rs], s46 offen\n" \
  "buffer_load_dwordx4 a[20:23], %[l16], %[rs], s46 offen offset:1024\n" \
  "buffer_load_dwordx4 a[24:27], %[l16b], %[rs], s46 offen offset:2048\n" \
  "buffer_load_dwordx4 a[28:31], %[l16b], %[rs], s46 offen offset:3072\n" \
  "s_add_u32 s46, %[sW], 28672\n" \
  "buffer_load_dwordx4 a[32:35], %[l16], %[rs], s46 offen\n" \
  "buffer_load_dwordx4 a[36:39], %[l16], %[rs], s46 offen offset:1024\n" \
  "buffer_load_dwordx4 a[40:43], %[l16b], %[rs], s46 offen offset:2048\n" \
  "buffer_load_dwordx4 a[44:47], %[l16b], %[rs], s46 offen offset:3072\n" \
  "s_add_u32 s46, %[sW], 43008\n" \
  "buffer_load_dwordx4 a[48:51], %[l16], %[rs], s46 offen\n" \
  "buffer_load_dwordx4 a[52:55], %[l16], %[rs], s46 offen offset:1024\n" \
  "buffer_load_dwordx4 a[56:59], %[l16b], %[rs], s46 offen offset:2048\n" \
  "buffer_load_dwordx4 a[60:63], %[l16b], %[rs], s46 offen offset:3072\n" \
  "s_add_u32 s46, %[sW], 57344\n" \
  "buffer_load_dwordx4 a[64:67], %[l16], %[rs], s46 offen\n" \
  "buffer_load_dwordx4 a[68:71], %[l16], %[rs], s46 offen offset:1024\n" \
  "buffer_load_dwordx4 a[72:75], %[l16b], %[rs], s46 offen offset:2048\n" \
  "buffer_load_dwordx4 a[76:79], %[l16b], %[rs], s46 offen offset:3072\n" \
  "s_add_u32 s46, %[sW], 71680\n" \
  "buffer_load_dwordx4 a[80:83], %[l16], %[rs], s46 offen\n" \
  "buffer_load_dwordx4 a[84:87], %[l16], %[rs], s46 offen offset:1024\n" \
  "buffer_load_dwordx4 a[88:91], %[l16b], %[rs], s46 offen offset:2048\n" \
  "buffer_load_dwordx4 a[92:95], %[l16b], %[rs], s46 offen offset:3072\n" \
  "s_add_u32 s46, %[sW], 86016\n" \
  "buffer_load_dwordx4 a[96:99], %[l16], %[rs], s46 offen\n" \
  "buffer_load_dwordx4 a[100:103], %[l16], %[rs], s46 offen offset:1024\n" \
  "buffer_load_dwordx4 a[104:107], %[l16b], %[rs], s46 offen offset:2048\n" \
  "buffer_load_dwordx4 a[108:111], %[l16b], %[rs], s46 offen offset:3072\n" \
  "s_add_u32 s46, %[sW], 100352\n" \
  "buffer_load_dwordx4 a[112:115], %[l16], %[rs], s46 offen\n" \
  "buffer_load_dwordx4 a[116:119], %[l16], %[rs], s46 offen offset:1024\n" \
  "buffer_load_dwordx4 a[120:123], %[l16b], %[rs], s46 offen offset:2048\n" \
  "buffer_load_dwordx4 a[124:127], %[l16b], %[rs], s46 offen offset:3072\n" \
  "s_waitcnt vmcnt(32)\n" \
  "v_add3_u32 v208, v208, v212, v216\n" \
  "v_add_u32 v208, v208, v220\n" \
  "v_lshl_or_b32 v208, v208, 2, 0\n" \
  "v_add3_u32 v209, v209, v213, v217\n" \
  "v_add_u32 v209, v209, v221\n" \
  "v_lshl_or_b32 v209, v209, 2, 1\n" \
  "v_add3_u32 v210, v210, v214, v218\n" \
  "v_add_u32 v210, v210, v222\n" \
  "v_lshl_or_b32 v210, v210, 2, 2\n" \
  "v_add3_u32 v211, v211, v215, v219\n" \
  "v_add_u32 v211, v211, v223\n" \
  "v_lshl_or_b32 v211, v211, 2, 3\n" \
  "v_min3_u32 v208, v208, v209, v210\n" \
  "v_min_u32 v208, v208, v211\n" \
  "v_and_b32 v208, 3, v208\n" \
  "v_lshlrev_b32 v208, 13, v208\n" \
  "v_lshlrev_b32 v209, 9, %[l16]\n" \
  "v_and_b32 v209, 0x4000, v209\n" \
  "v_xor_b32 v30, v208, v209\n" \
  "v_mov_b32 v152, -1\n" \
  "v_mov_b32 v153, -1\n" \
  "s_cmp_eq_u32 %[w], 3\n" \
  "s_cselect_b32 s46, 0x7ff00000, 0\n" \
  "v_or_b32 v151, s46, %[oSt]\n" \
  "s_cmp_eq_u32 %[w], 0\n" \
  "s_cselect_b64 vcc, -1, 0\n" \
  "s_cselect_b32 s92, 0, -1\n" \
  "v_cndmask_b32 v151, v151, %[oPr], vcc\n" \
  "v_mov_b32 v152, s92\n" \
  "s_cmp_lt_u32 %[w], 2\n" \
  "s_cselect_b64 s[54:55], -1, 0\n" \
  "s_mov_b32 s97, 0\n" \
  "s_mov_b32 s96, 0\n" \
  "v_readlane_b32 s86, v30, 0\n" \
  "s_nop 0\n" \
  "s_xor_b32 s86, s86, %[side]\n" \
  "v_mov_b32 v147, 0x4444\n" \
  "v_mov_b32 v140, 0xeeee\n" \
  "s_nop 1\n" \
  "v_mov_b32_dpp v147, v140 quad_perm:[0,1,2,3] row_mask:0xf bank_mask:0xa\n" \
  "v_mov_b32 v156, 0\n" \
  "v_mov_b32 v157, 0\n" \
  "v_mov_b32 v158, 0\n" \
  "v_mov_b32 v159, 0\n" \
  "v_mov_b32_dpp v156, %[sel0] quad_perm:[0,1,2,3] row_mask:0xf bank_mask:0x3\n" \
  "v_mov_b32_dpp v157, %[sel1] quad_perm:[0,1,2,3] row_mask:0xf bank_mask:0x3\n" \
  "v_mov_b32_dpp v158, %[sel0] quad_perm:[0,1,2,3] row_mask:0xf bank_mask:0xc\n" \
  "v_mov_b32_dpp v159, %[sel1] quad_perm:[0,1,2,3] row_mask:0xf bank_mask:0xc\n" \
  "v_mov_b32 v143, 1\n" \
  "v_mov_b32 v144, %[wrK]\n" \
  "v_mov_b32 v145, %[rdK]\n" \
  "v_mov_b32 v146, 0x7ff00000\n" \
  "s_mov_b32 s40, 0\n" \
  "s_mov_b32 s41, 0\n" \
  "s_mov_b32 s52, 7225344\n" \
  "v_mov_b32 v164, -1\n" \
  "v_mov_b32 v165, -1\n" \
  ".LA_top%=:\n" \
  "v_readlane_b32 s42, v30, s40\n" \
  "s_mov_b32 s44, 262144\n" \
  "s_xor_b32 s43, s42, %[side]\n" \
  "s_add_u32 s43, s43, s41\n" \
  "s_add_u32 s82, s41, s42\n" \
  "s_xor_b32 s83, s82, 16384\n" \
  "s_cmp_eq_u32 s40, 63\n" \
  "s_cselect_b32 s82, 0x7ff00000, s82\n" \
  "s_cselect_b32 s83, 0x7ff00000, s83\n" \
  "v_mov_b32 v56, 0\n" \
  "v_mov_b32 v57, 0\n" \
  "v_mov_b32 v58, 0\n" \
  "v_mov_b32 v59, 0\n" \
  "v_mov_b32 v60, 0\n" \
  "v_mov_b32 v61, 0\n" \
  "v_mov_b32 v62, 0\n" \
  "v_mov_b32 v63, 0\n" \
  "v_mov_b32 v64, 0\n" \
  "v_mov_b32 v65, 0\n" \
  "v_mov_b32 v66, 0\n" \
  "v_mov_b32 v67, 0\n" \
  "v_mov_b32 v68, 0\n" \
  "v_mov_b32 v69, 0\n" \
  "v_mov_b32 v70, 0\n" \
  "v_mov_b32 v71, 0\n" \
  "v_mov_b32 v72, 0\n" \
  "v_mov_b32 v73, 0\n" \
  "v_mov_b32 v74, 0\n" \
  "v_mov_b32 v75, 0\n" \
  "v_mov_b32 v76, 0\n" \
  "v_mov_b32 v77, 0\n" \
  "v_mov_b32 v78, 0\n" \
  "v_mov_b32 v79, 0\n" \
  "v_mov_b32 v80, 0\n" \
  "v_mov_b32 v81, 0\n" \
  "v_mov_b32 v82, 0\n" \
  "v_mov_b32 v83, 0\n" \
  "v_mov_b32 v84, 0\n" \
  "v_mov_b32 v85, 0\n" \
  "v_mov_b32 v86, 0\n" \
  "v_mov_b32 v87, 0\n" \
  "s_add_u32 s76, s40, 2\n" \
  "s_and_b32 s77, s76, 63\n" \
  "v_readlane_b32 s77, v30, s77\n" \
  "s_min_u32 s76, s76, 64\n" \
  "s_lshl_b32 s76, s76, 18\n" \
  "s_add_u32 s76, s76, s77\n" \
  "s_xor_b32 s77, s76, 16384\n" \
  "s_cmp_eq_u32 %[w], 2\n" \
  "s_cselect_b32 s76, s77, s76\n" \
  "s_cmp_eq_u32 %[w], 0\n" \
  "s_cselect_b32 s76, 0, s76\n" \
  "s_sub_u32 s97, s97, 1\n" \
  "s_max_i32 s97, s97, 0\n" \
  "s_lshr_b32 s96, s97, 4\n" \
  "s_cmp_eq_u32 s40, 1\n" \
  "s_cselect_b32 s96, 8, s96\n" \
  "s_cmp_eq_u32 s40, 0\n" \
  "s_cbranch_scc1 .LA_j0%=\n" \
  "buffer_load_dword v242, %[cIn], %[rs], s86 offen sc1\n" \
  "s_nop 15\n" \
  "buffer_load_dword v243, %[cIn], %[rs], s86 offen sc1\n" \
  "s_nop 15\n" \
  "s_waitcnt vmcnt(2)\n" \
  "s_cmp_lg_u32 %[w], 0\n" \
  "s_cbranch_scc1 .LA_cbar%=\n" \
  "s_mov_b64 exec, 3\n" \
  ".LA_cw%=:\n" \
  "buffer_load_dword v244, %[cIn], %[rs], s86 offen sc1\n" \
  "s_sub_u32 s44, s44, 1\n" \
  "s_cbranch_scc1 .LA_abort%=\n" \
  "s_waitcnt vmcnt(2)\n" \
  "v_cmp_gt_i32 vcc, s40, v242\n" \
  "s_cbranch_vccz .LA_cok%=\n" \
  "buffer_load_dword v242, %[cIn], %[rs], s86 offen sc1\n" \
  "s_waitcnt vmcnt(2)\n" \
  "v_cmp_gt_i32 vcc, s40, v243\n" \
  "s_cbranch_vccz .LA_cok%=\n" \
  "buffer_load_dword v243, %[cIn], %[rs], s86 offen sc1\n" \
  "s_waitcnt vmcnt(2)\n" \
  "v_cmp_gt_i32 vcc, s40, v244\n" \
  "s_cbranch_vccnz .LA_cw%=\n" \
  ".LA_cok%=:\n" \
  "s_cmp_gt_i32 s96, 0\n" \
  "s_cbranch_scc1 .LA_cdly%=\n" \
  ".LA_cbar%=:\n" \
  "s_barrier\n" \
  "s_mov_b64 exec, -1\n" \
  "s_nop 11\n" \
  "buffer_load_dwordx4 v[32:35], %[oMa], %[rs], s43 offen sc1\n" \
  "s_nop 7\n" \
  "buffer_load_dwordx4 v[36:39], %[oMb], %[rs], s43 offen sc1\n" \
  ".LA_j0%=:\n" \
  "s_mov_b32 s87, 1\n" \
  "s_mov_b32 s94, 0\n" \
  "s_mov_b32 s95, 0x7ff00000\n" \
  "s_nop 11\n" \
  "buffer_store_dwordx2 v[164:165], %[oKM], %[rs], s52 offen sc1\n" \
  "buffer_store_dword v152, v151, %[rs], s76 offen sc1\n" \
  "s_waitcnt vmcnt(3)\n" \
  ".LA_critA%=:\n" \
  "v_smfmac_f32_16x16x64_bf16 v[56:59], v[32:35], a[0:7], v147\n" \
  "ds_write_b128 %[wrM], v[32:35]\n" \
  "v_smfmac_f32_16x16x64_bf16 v[72:75], v[32:35], a[64:71], v147\n" \
  "v_max3_u32 v140, v32, v33, v34\n" \
  "v_smfmac_f32_16x16x64_bf16 v[60:63], v[32:35], a[16:23], v147\n" \
  "v_smfmac_f32_16x16x64_bf16 v[76:79], v[32:35], a[80:87], v147\n" \
  "s_waitcnt vmcnt(2)\n" \
  "v_smfmac_f32_16x16x64_bf16 v[56:59], v[36:39], a[8:15], v147\n" \
  "ds_write_b128 %[wrM], v[36:39] offset:1024\n" \
  "v_smfmac_f32_16x16x64_bf16 v[72:75], v[36:39], a[72:79], v147\n" \
  "v_max3_u32 v141, v35, v36, v37\n" \
  "v_smfmac_f32_16x16x64_bf16 v[60:63], v[36:39], a[24:31], v147\n" \
  "v_max3_u32 v140, v140, v38, v39\n" \
  "v_max_u32 v140, v140, v141\n" \
  "v_smfmac_f32_16x16x64_bf16 v[76:79], v[36:39], a[88:95], v147\n" \
  "v_cmp_eq_u32 vcc, -1, v140\n" \
  "v_smfmac_f32_16x16x64_bf16 v[64:67], v[32:35], a[32:39], v147\n" \
  "v_smfmac_f32_16x16x64_bf16 v[80:83], v[32:35], a[96:103], v147\n" \
  "s_cbranch_vccnz .LA_failA%=\n" \
  "v_smfmac_f32_16x16x64_bf16 v[68:71], v[32:35], a[48:55], v147\n" \
  "v_cvt_pk_bf16_f32 v88, v56, v57\n" \
  "s_waitcnt lgkmcnt(0)\n" \
  "s_barrier\n" \
  "v_smfmac_f32_16x16x64_bf16 v[84:87], v[32:35], a[112:119], v147\n" \
  "v_cvt_pk_bf16_f32 v89, v58, v59\n" \
  "ds_read_b64 v[48:49], %[rdL0]\n" \
  "v_smfmac_f32_16x16x64_bf16 v[64:67], v[36:39], a[40:47], v147\n" \
  "v_cvt_pk_bf16_f32 v90, v72, v73\n" \
  "ds_read_b64 v[50:51], %[rdL0] offset:64\n" \
  "v_smfmac_f32_16x16x64_bf16 v[80:83], v[36:39], a[104:111], v147\n" \
  "v_cvt_pk_bf16_f32 v91, v74, v75\n" \
  "ds_read_b64 v[52:53], %[rdL1]\n" \
  "v_smfmac_f32_16x16x64_bf16 v[68:71], v[36:39], a[56:63], v147\n" \
  "v_cvt_pk_bf16_f32 v92, v60, v61\n" \
  "v_cvt_pk_bf16_f32 v93, v62, v63\n" \
  "ds_read_b64 v[54:55], %[rdL1] offset:64\n" \
  "v_smfmac_f32_16x16x64_bf16 v[84:87], v[36:39], a[120:127], v147\n" \
  "v_cvt_pk_bf16_f32 v94, v76, v77\n" \
  "v_cvt_pk_bf16_f32 v95, v78, v79\n" \
  "v_mfma_f32_16x16x32_bf16 v[104:107], v[88:91], v[156:159], 0\n" \
  "v_cvt_pk_bf16_f32 v96, v64, v65\n" \
  "v_cvt_pk_bf16_f32 v97, v66, v67\n" \
  "v_mfma_f32_16x16x32_bf16 v[108:111], v[92:95], v[156:159], 0\n" \
  "v_cvt_pk_bf16_f32 v98, v80, v81\n" \
  "v_cvt_pk_bf16_f32 v99, v82, v83\n" \
  "v_cvt_pk_bf16_f32 v100, v68, v69\n" \
  "v_cvt_pk_bf16_f32 v101, v70, v71\n" \
  "v_mfma_f32_16x16x32_bf16 v[112:115], v[96:99], v[156:159], 0\n" \
  "v_cvt_pk_bf16_f32 v102, v84, v85\n" \
  "v_cvt_pk_bf16_f32 v103, v86, v87\n" \
  "v_cvt_pk_bf16_f32 v120, v104, v105\n" \
  "v_cvt_pk_bf16_f32 v122, v106, v107\n" \
  "v_mfma_f32_16x16x32_bf16 v[116:119], v[100:103], v[156:159], 0\n" \
  "v_cvt_pk_bf16_f32 v121, v108, v109\n" \
  "v_cvt_pk_bf16_f32 v123, v110, v111\n" \
  "s_waitcnt lgkmcnt(0)\n" \
  "s_nop 0\n" \
  "v_mfma_f32_16x16x32_bf16 v[128:131], v[48:51], v[120:123], 0\n" \
  "v_cvt_pk_bf16_f32 v124, v112, v113\n" \
  "v_cvt_pk_bf16_f32 v126, v114, v115\n" \
  "s_nop 0\n" \
  "v_cvt_pk_bf16_f32 v125, v116, v117\n" \
  "v_cvt_pk_bf16_f32 v127, v118, v119\n" \
  "s_nop 1\n" \
  "v_mfma_f32_16x16x32_bf16 v[128:131], v[52:55], v[124:127], v[128:131]\n" \
  "s_nop 7\n" \
  "v_add_f32_dpp v128, v130, v128 row_ror:8 row_mask:0xf bank_mask:0xf bound_ctrl:1\n" \
  "v_add_f32_dpp v129, v131, v129 row_ror:8 row_mask:0xf bank_mask:0xf bound_ctrl:1\n" \
  "ds_write2_b32 %[wrS], v128, v129 offset0:0 offset1:32\n" \
  "ds_add_rtn_u32 v142, %[cnt], v143\n" \
  "ds_read_b128 v[132:135], %[rdS]\n" \
  "s_waitcnt lgkmcnt(1)\n" \
  "v_and_b32 v142, 3, v142\n" \
  "v_cmpx_eq_u32 vcc, 3, v142\n" \
  "s_waitcnt lgkmcnt(0)\n" \
  "v_add_f32 v132, v132, v134\n" \
  "v_add_f32 v133, v133, v135\n" \
  "v_add_f32 v132, v132, v133\n" \
  "v_min_u32 v132, -2, v132\n" \
  "s_cbranch_execz .LA_skipa%=\n" \
  "buffer_store_dword v132, %[oSt], %[rs], s82 offen sc1\n" \
  "buffer_store_dword v132, %[oSt], %[rs], s83 offen sc1\n" \
  ".LA_skipa%=:\n" \
  "s_mov_b64 exec, -1\n" \
  "s_branch .LA_off%=\n" \
  ".LA_pollA%=:\n" \
  "buffer_load_dwordx4 v[40:43], %[oMa], %[rs], s43 offen sc1\n" \
  "buffer_load_dwordx4 v[44:47], %[oMb], %[rs], s43 offen sc1\n" \
  "v_mov_b32 v56, 0\n" \
  "v_mov_b32 v57, 0\n" \
  "v_mov_b32 v58, 0\n" \
  "v_mov_b32 v59, 0\n" \
  "v_mov_b32 v72, 0\n" \
  "v_mov_b32 v73, 0\n" \
  "v_mov_b32 v74, 0\n" \
  "v_mov_b32 v75, 0\n" \
  "v_mov_b32 v60, 0\n" \
  "v_mov_b32 v61, 0\n" \
  "v_mov_b32 v62, 0\n" \
  "v_mov_b32 v63, 0\n" \
  "v_mov_b32 v76, 0\n" \
  "v_mov_b32 v77, 0\n" \
  "v_mov_b32 v78, 0\n" \
  "v_mov_b32 v79, 0\n" \
  "v_mov_b32 v64, 0\n" \
  "v_mov_b32 v65, 0\n" \
  "v_mov_b32 v66, 0\n" \
  "v_mov_b32 v67, 0\n" \
  "v_mov_b32 v80, 0\n" \
  "v_mov_b32 v81, 0\n" \
  "v_mov_b32 v82, 0\n" \
  "v_mov_b32 v83, 0\n" \
  "s_waitcnt vmcnt(2)\n" \
  "s_branch .LA_critA%=\n" \
  ".LA_failA%=:\n" \
  "s_sub_u32 s44, s44, 1\n" \
  "s_cbranch_scc1 .LA_abort%=\n" \
  "s_cmp_eq_u32 s87, 0\n" \
  "s_cbranch_scc1 .LA_fail2%=\n" \
  "s_mov_b32 s95, 0\n" \
  "s_mov_b32 s94, 0x7ff00000\n" \
  "s_add_u32 s97, s97, 16\n" \
  "s_min_u32 s97, s97, 96\n" \
  "s_mov_b32 s87, 0\n" \
  "buffer_load_dwordx4 v[40:43], %[oMa], %[rs], s43 offen sc1\n" \
  "buffer_load_dwordx4 v[44:47], %[oMb], %[rs], s43 offen sc1\n" \
  "s_nop 15\n" \
  "s_nop 15\n" \
  ".LA_fail2%=:\n" \
  "buffer_load_dwordx4 v[32:35], %[oMa], %[rs], s43 offen sc1\n" \
  "buffer_load_dwordx4 v[36:39], %[oMb], %[rs], s43 offen sc1\n" \
  "v_mov_b32 v56, 0\n" \
  "v_mov_b32 v57, 0\n" \
  "v_mov_b32 v58, 0\n" \
  "v_mov_b32 v59, 0\n" \
  "v_mov_b32 v72, 0\n" \
  "v_mov_b32 v73, 0\n" \
  "v_mov_b32 v74, 0\n" \
  "v_mov_b32 v75, 0\n" \
  "v_mov_b32 v60, 0\n" \
  "v_mov_b32 v61, 0\n" \
  "v_mov_b32 v62, 0\n" \
  "v_mov_b32 v63, 0\n" \
  "v_mov_b32 v76, 0\n" \
  "v_mov_b32 v77, 0\n" \
  "v_mov_b32 v78, 0\n" \
  "v_mov_b32 v79, 0\n" \
  "v_mov_b32 v64, 0\n" \
  "v_mov_b32 v65, 0\n" \
  "v_mov_b32 v66, 0\n" \
  "v_mov_b32 v67, 0\n" \
  "v_mov_b32 v80, 0\n" \
  "v_mov_b32 v81, 0\n" \
  "v_mov_b32 v82, 0\n" \
  "v_mov_b32 v83, 0\n" \
  "s_waitcnt vmcnt(2)\n" \
  "v_smfmac_f32_16x16x64_bf16 v[56:59], v[40:43], a[0:7], v147\n" \
  "ds_write_b128 %[wrM], v[40:43]\n" \
  "v_smfmac_f32_16x16x64_bf16 v[72:75], v[40:43], a[64:71], v147\n" \
  "v_max3_u32 v140, v40, v41, v42\n" \
  "v_smfmac_f32_16x16x64_bf16 v[60:63], v[40:43], a[16:23], v147\n" \
  "v_smfmac_f32_16x16x64_bf16 v[76:79], v[40:43], a[80:87], v147\n" \
  "s_waitcnt vmcnt(2)\n" \
  "v_smfmac_f32_16x16x64_bf16 v[56:59], v[44:47], a[8:15], v147\n" \
  "ds_write_b128 %[wrM], v[44:47] offset:1024\n" \
  "v_smfmac_f32_16x16x64_bf16 v[72:75], v[44:47], a[72:79], v147\n" \
  "v_max3_u32 v141, v43, v44, v45\n" \
  "v_smfmac_f32_16x16x64_bf16 v[60:63], v[44:47], a[24:31], v147\n" \
  "v_max3_u32 v140, v140, v46, v47\n" \
  "v_max_u32 v140, v140, v141\n" \
  "v_smfmac_f32_16x16x64_bf16 v[76:79], v[44:47], a[88:95], v147\n" \
  "v_cmp_eq_u32 vcc, -1, v140\n" \
  "v_smfmac_f32_16x16x64_bf16 v[64:67], v[40:43], a[32:39], v147\n" \
  "v_smfmac_f32_16x16x64_bf16 v[80:83], v[40:43], a[96:103], v147\n" \
  "s_cbranch_vccnz .LA_pollA%=\n" \
  "v_smfmac_f32_16x16x64_bf16 v[68:71], v[40:43], a[48:55], v147\n" \
  "v_cvt_pk_bf16_f32 v88, v56, v57\n" \
  "s_waitcnt lgkmcnt(0)\n" \
  "s_barrier\n" \
  "v_smfmac_f32_16x16x64_bf16 v[84:87], v[40:43], a[112:119], v147\n" \
  "v_cvt_pk_bf16_f32 v89, v58, v59\n" \
  "ds_read_b64 v[48:49], %[rdL0]\n" \
  "v_smfmac_f32_16x16x64_bf16 v[64:67], v[44:47], a[40:47], v147\n" \
  "v_cvt_pk_bf16_f32 v90, v72, v73\n" \
  "ds_read_b64 v[50:51], %[rdL0] offset:64\n" \
  "v_smfmac_f32_16x16x64_bf16 v[80:83], v[44:47], a[104:111], v147\n" \
  "v_cvt_pk_bf16_f32 v91, v74, v75\n" \
  "ds_read_b64 v[52:53], %[rdL1]\n" \
  "v_smfmac_f32_16x16x64_bf16 v[68:71], v[44:47], a[56:63], v147\n" \
  "v_cvt_pk_bf16_f32 v92, v60, v61\n" \
  "v_cvt_pk_bf16_f32 v93, v62, v63\n" \
  "ds_read_b64 v[54:55], %[rdL1] offset:64\n" \
  "v_smfmac_f32_16x16x64_bf16 v[84:87], v[44:47], a[120:127], v147\n" \
  "v_cvt_pk_bf16_f32 v94, v76, v77\n" \
  "v_cvt_pk_bf16_f32 v95, v78, v79\n" \
  "v_mfma_f32_16x16x32_bf16 v[104:107], v[88:91], v[156:159], 0\n" \
  "v_cvt_pk_bf16_f32 v96, v64, v65\n" \
  "v_cvt_pk_bf16_f32 v97, v66, v67\n" \
  "v_mfma_f32_16x16x32_bf16 v[108:111], v[92:95], v[156:159], 0\n" \
  "v_cvt_pk_bf16_f32 v98, v80, v81\n" \
  "v_cvt_pk_bf16_f32 v99, v82, v83\n" \
  "v_cvt_pk_bf16_f32 v100, v68, v69\n" \
  "v_cvt_pk_bf16_f32 v101, v70, v71\n" \
  "v_mfma_f32_16x16x32_bf16 v[112:115], v[96:99], v[156:159], 0\n" \
  "v_cvt_pk_bf16_f32 v102, v84, v85\n" \
  "v_cvt_pk_bf16_f32 v103, v86, v87\n" \
  "v_cvt_pk_bf16_f32 v120, v104, v105\n" \
  "v_cvt_pk_bf16_f32 v122, v106, v107\n" \
  "v_mfma_f32_16x16x32_bf16 v[116:119], v[100:103], v[156:159], 0\n" \
  "v_cvt_pk_bf16_f32 v121, v108, v109\n" \
  "v_cvt_pk_bf16_f32 v123, v110, v111\n" \
  "s_waitcnt lgkmcnt(0)\n" \
  "s_nop 0\n" \
  "v_mfma_f32_16x16x32_bf16 v[128:131], v[48:51], v[120:123], 0\n" \
  "v_cvt_pk_bf16_f32 v124, v112, v113\n" \
  "v_cvt_pk_bf16_f32 v126, v114, v115\n" \
  "s_nop 0\n" \
  "v_cvt_pk_bf16_f32 v125, v116, v117\n" \
  "v_cvt_pk_bf16_f32 v127, v118, v119\n" \
  "s_nop 1\n" \
  "v_mfma_f32_16x16x32_bf16 v[128:131], v[52:55], v[124:127], v[128:131]\n" \
  "s_nop 7\n" \
  "v_add_f32_dpp v128, v130, v128 row_ror:8 row_mask:0xf bank_mask:0xf bound_ctrl:1\n" \
  "v_add_f32_dpp v129, v131, v129 row_ror:8 row_mask:0xf bank_mask:0xf bound_ctrl:1\n" \
  "ds_write2_b32 %[wrS], v128, v129 offset0:0 offset1:32\n" \
  "ds_add_rtn_u32 v142, %[cnt], v143\n" \
  "ds_read_b128 v[132:135], %[rdS]\n" \
  "s_waitcnt lgkmcnt(1)\n" \
  "v_and_b32 v142, 3, v142\n" \
  "v_cmpx_eq_u32 vcc, 3, v142\n" \
  "s_waitcnt lgkmcnt(0)\n" \
  "v_add_f32 v132, v132, v134\n" \
  "v_add_f32 v133, v133, v135\n" \
  "v_add_f32 v132, v132, v133\n" \
  "v_min_u32 v132, -2, v132\n" \
  "s_cbranch_execz .LA_skipb%=\n" \
  "buffer_store_dword v132, %[oSt], %[rs], s82 offen sc1\n" \
  "buffer_store_dword v132, %[oSt], %[rs], s83 offen sc1\n" \
  ".LA_skipb%=:\n" \
  "s_mov_b64 exec, -1\n" \
  ".LA_off%=:\n" \
  "ds_write_b128 v144, v[104:107]\n" \
  "ds_write_b128 v144, v[108:111] offset:1024\n" \
  "ds_write_b128 v144, v[112:115] offset:2048\n" \
  "ds_write_b128 v144, v[116:119] offset:3072\n" \
  "s_or_b32 s46, s40, s92\n" \
  "v_mov_b32 v152, s46\n" \
  "s_mul_i32 s52, s40, 114688\n" \
  "s_waitcnt lgkmcnt(0)\n" \
  "s_barrier\n" \
  "ds_read_b128 v[160:163], v145\n" \
  "ds_read_b128 v[164:167], v145 offset:4096\n" \
  "ds_read_b128 v[168:171], v145 offset:8192\n" \
  "ds_read_b128 v[172:175], v145 offset:12288\n" \
  "s_waitcnt lgkmcnt(0)\n" \
  "v_add_f32 v160, v160, v164\n" \
  "v_add_f32 v161, v161, v165\n" \
  "v_add_f32 v168, v168, v172\n" \
  "v_add_f32 v169, v169, v173\n" \
  "v_add_f32 v162, v162, v166\n" \
  "v_add_f32 v163, v163, v167\n" \
  "v_add_f32 v170, v170, v174\n" \
  "v_add_f32 v171, v171, v175\n" \
  "v_add_f32 v160, v160, v168\n" \
  "v_add_f32 v161, v161, v169\n" \
  "v_add_f32 v162, v162, v170\n" \
  "v_add_f32 v163, v163, v171\n" \
  "v_cvt_pk_bf16_f32 v164, v160, v161\n" \
  "v_cvt_pk_bf16_f32 v165, v162, v163\n" \
  "s_cmp_eq_u32 s40, 63\n" \
  "s_cbranch_scc1 .LA_fin%=\n" \
  "s_add_u32 s40, s40, 1\n" \
  "s_add_u32 s41, s41, 262144\n" \
  "s_branch .LA_top%=\n" \
  ".LA_fin%=:\n" \
  "v_min_u32 v164, -2, v164\n" \
  "v_min_u32 v165, -2, v165\n" \
  "buffer_store_dwordx2 v[164:165], %[oKM], %[rs], s52 offen sc1\n" \
  "v_mov_b32 v140, 63\n" \
  "v_mov_b32 v141, 64\n" \
  "s_waitcnt vmcnt(1)\n" \
  "s_barrier\n" \
  "buffer_store_dword v140, %[oPr], %[rs], 0 offen sc1\n" \
  "s_waitcnt vmcnt(0)\n" \
  "s_barrier\n" \
  "buffer_store_dword v141, %[oPr], %[rs], 0 offen sc1\n" \
  "s_branch .LA_end%=\n" \
  ".LA_cdly%=:\n" \
  "s_sub_u32 s96, s96, 1\n" \
  "s_cmp_gt_i32 s96, 0\n" \
  "s_cbranch_scc1 .LA_cdly%=\n" \
  "s_branch .LA_cbar%=\n" \
  ".LA_abort%=:\n" \
  "s_waitcnt vmcnt(0) lgkmcnt(0)\n" \
  "s_mov_b64 exec, -1\n" \
  "v_mov_b32 v140, 1\n" \
  "v_mov_b32 v141, 0x2913000\n" \
  "buffer_store_dword v140, v141, %[rs], 0 offen sc1\n" \
  ".LA_end%=:\n" \
  "s_waitcnt vmcnt(0) lgkmcnt(0)\n" \
  ".LA_zend%=:\n" \
  ""
#define ASM_A_CLOB "memory", "scc", "vcc", "a0", "a1", "a2", "a3", "a4", "a5", "a6", "a7", "a8", "a9", "a10", "a11", "a12", "a13", "a14", "a15", "a16", "a17", "a18", "a19", "a20", "a21", "a22", "a23", "a24", "a25", "a26", "a27", "a28", "a29", "a30", "a31", "a32", "a33", "a34", "a35", "a36", "a37", "a38", "a39", "a40", "a41", "a42", "a43", "a44", "a45", "a46", "a47", "a48", "a49", "a50", "a51", "a52", "a53", "a54", "a55", "a56", "a57", "a58", "a59", "a60", "a61", "a62", "a63", "a64", "a65", "a66", "a67", "a68", "a69", "a70", "a71", "a72", "a73", "a74", "a75", "a76", "a77", "a78", "a79", "a80", "a81", "a82", "a83", "a84", "a85", "a86", "a87", "a88", "a89", "a90", "a91", "a92", "a93", "a94", "a95", "a96", "a97", "a98", "a99", "a100", "a101", "a102", "a103", "a104", "a105", "a106", "a107", "a108", "a109", "a110", "a111", "a112", "a113", "a114", "a115", "a116", "a117", "a118", "a119", "a120", "a121", "a122", "a123", "a124", "a125", "a126", "a127", "s40", "s41", "s42", "s43", "s44", "s46", "s52", "s54", "s55", "s76", "s77", "s82", "s83", "s86", "s87", "s92", "s94", "s95", "s96", "s97", "v30", "v32", "v33", "v34", "v35", "v36", "v37", "v38", "v39", "v40", "v41", "v42", "v43", "v44", "v45", "v46", "v47", "v48", "v49", "v50", "v51", "v52", "v53", "v54", "v55", "v56", "v57", "v58", "v59", "v60", "v61", "v62", "v63", "v64", "v65", "v66", "v67", "v68", "v69", "v70", "v71", "v72", "v73", "v74", "v75", "v76", "v77", "v78", "v79", "v80", "v81", "v82", "v83", "v84", "v85", "v86", "v87", "v88", "v89", "v90", "v91", "v92", "v93", "v94", "v95", "v96", "v97", "v98", "v99", "v100", "v101", "v102", "v103", "v104", "v105", "v106", "v107", "v108", "v109", "v110", "v111", "v112", "v113", "v114", "v115", "v116", "v117", "v118", "v119", "v120", "v121", "v122", "v123", "v124", "v125", "v126", "v127", "v128", "v129", "v130", "v131", "v132", "v133", "v134", "v135", "v140", "v141", "v142", "v143", "v144", "v145", "v146", "v147", "v151", "v152", "v153", "v156", "v157", "v158", "v159", "v160", "v161", "v162", "v163", "v164", "v165", "v166", "v167", "v168", "v169", "v170", "v171", "v172", "v173", "v174", "v175", "v208", "v209", "v210", "v211", "v212", "v213", "v214", "v215", "v216", "v217", "v218", "v219", "v220", "v221", "v222", "v223", "v242", "v243", "v244"
#define ASM_M \
  "s_nop 4\n" \
  "buffer_load_dwordx4 v[32:35], %[oMa], %[rs], 0 offen sc1\n" \
  "buffer_load_dwordx4 v[36:39], %[oMb], %[rs], 0 offen sc1\n" \
  "buffer_load_dwordx4 v[208:211], %[oCal], %[rs], 0 offen\n" \
  "buffer_load_dwordx4 v[212:215], %[oCal], %[rs], 0 offen offset:128\n" \
  "buffer_load_dwordx4 v[216:219], %[oCal], %[rs], 0 offen offset:256\n" \
  "buffer_load_dwordx4 v[220:223], %[oCal], %[rs], 0 offen offset:384\n" \
  "s_add_u32 s46, %[sW], 0\n" \
  "buffer_load_dwordx4 a[0:3], %[l16], %[rs], s46 offen\n" \
  "buffer_load_dwordx4 a[4:7], %[l16], %[rs], s46 offen offset:1024\n" \
  "buffer_load_dwordx4 a[8:11], %[l16b], %[rs], s46 offen offset:2048\n" \
  "buffer_load_dwordx4 a[12:15], %[l16b], %[rs], s46 offen offset:3072\n" \
  "s_add_u32 s46, %[sW], 14336\n" \
  "buffer_load_dwordx4 a[16:19], %[l16], %[rs], s46 offen\n" \
  "buffer_load_dwordx4 a[20:23], %[l16], %[rs], s46 offen offset:1024\n" \
  "buffer_load_dwordx4 a[24:27], %[l16b], %[rs], s46 offen offset:2048\n" \
  "buffer_load_dwordx4 a[28:31], %[l16b], %[rs], s46 offen offset:3072\n" \
  "s_add_u32 s46, %[sW], 28672\n" \
  "buffer_load_dwordx4 a[32:35], %[l16], %[rs], s46 offen\n" \
  "buffer_load_dwordx4 a[36:39], %[l16], %[rs], s46 offen offset:1024\n" \
  "buffer_load_dwordx4 a[40:43], %[l16b], %[rs], s46 offen offset:2048\n" \
  "buffer_load_dwordx4 a[44:47], %[l16b], %[rs], s46 offen offset:3072\n" \
  "s_add_u32 s46, %[sW], 43008\n" \
  "buffer_load_dwordx4 a[48:51], %[l16], %[rs], s46 offen\n" \
  "buffer_load_dwordx4 a[52:55], %[l16], %[rs], s46 offen offset:1024\n" \
  "buffer_load_dwordx4 a[56:59], %[l16b], %[rs], s46 offen offset:2048\n" \
  "buffer_load_dwordx4 a[60:63], %[l16b], %[rs], s46 offen offset:3072\n" \
  "s_add_u32 s46, %[sW], 57344\n" \
  "buffer_load_dwordx4 a[64:67], %[l16], %[rs], s46 offen\n" \
  "buffer_load_dwordx4 a[68:71], %[l16], %[rs], s46 offen offset:1024\n" \
  "buffer_load_dwordx4 a[72:75], %[l16b], %[rs], s46 offen offset:2048\n" \
  "buffer_load_dwordx4 a[76:79], %[l16b], %[rs], s46 offen offset:3072\n" \
  "s_add_u32 s46, %[sW], 71680\n" \
  "buffer_load_dwordx4 a[80:83], %[l16], %[rs], s46 offen\n" \
  "buffer_load_dwordx4 a[84:87], %[l16], %[rs], s46 offen offset:1024\n" \
  "buffer_load_dwordx4 a[88:91], %[l16b], %[rs], s46 offen offset:2048\n" \
  "buffer_load_dwordx4 a[92:95], %[l16b], %[rs], s46 offen offset:3072\n" \
  "s_add_u32 s46, %[sW], 86016\n" \
  "buffer_load_dwordx4 a[96:99], %[l16], %[rs], s46 offen\n" \
  "buffer_load_dwordx4 a[100:103], %[l16], %[rs], s46 offen offset:1024\n" \
  "buffer_load_dwordx4 a[104:107], %[l16b], %[rs], s46 offen offset:2048\n" \
  "buffer_load_dwordx4 a[108:111], %[l16b], %[rs], s46 offen offset:3072\n" \
  "s_add_u32 s46, %[sW], 100352\n" \
  "buffer_load_dwordx4 a[112:115], %[l16], %[rs], s46 offen\n" \
  "buffer_load_dwordx4 a[116:119], %[l16], %[rs], s46 offen offset:1024\n" \
  "buffer_load_dwordx4 a[120:123], %[l16b], %[rs], s46 offen offset:2048\n" \
  "buffer_load_dwordx4 a[124:127], %[l16b], %[rs], s46 offen offset:3072\n" \
  "s_waitcnt vmcnt(32)\n" \
  "v_add3_u32 v208, v208, v212, v216\n" \
  "v_add_u32 v208, v208, v220\n" \
  "v_lshl_or_b32 v208, v208, 2, 0\n" \
  "v_add3_u32 v209, v209, v213, v217\n" \
  "v_add_u32 v209, v209, v221\n" \
  "v_lshl_or_b32 v209, v209, 2, 1\n" \
  "v_add3_u32 v210, v210, v214, v218\n" \
  "v_add_u32 v210, v210, v222\n" \
  "v_lshl_or_b32 v210, v210, 2, 2\n" \
  "v_add3_u32 v211, v211, v215, v219\n" \
  "v_add_u32 v211, v211, v223\n" \
  "v_lshl_or_b32 v211, v211, 2, 3\n" \
  "v_min3_u32 v208, v208, v209, v210\n" \
  "v_min_u32 v208, v208, v211\n" \
  "v_and_b32 v208, 3, v208\n" \
  "v_lshlrev_b32 v208, 13, v208\n" \
  "v_lshlrev_b32 v209, 9, %[l16]\n" \
  "v_and_b32 v209, 0x4000, v209\n" \
  "v_xor_b32 v30, v208, v209\n" \
  "v_mov_b32 v152, -1\n" \
  "v_mov_b32 v153, -1\n" \
  "s_cmp_eq_u32 %[w], 3\n" \
  "s_cselect_b32 s46, 0x7ff00000, 0\n" \
  "v_or_b32 v151, s46, %[oSt]\n" \
  "s_cmp_eq_u32 %[w], 0\n" \
  "s_cselect_b64 vcc, -1, 0\n" \
  "s_cselect_b32 s92, 0, -1\n" \
  "v_cndmask_b32 v151, v151, %[oPr], vcc\n" \
  "v_mov_b32 v152, s92\n" \
  "s_cmp_lt_u32 %[w], 2\n" \
  "s_cselect_b64 s[54:55], -1, 0\n" \
  "s_mov_b32 s97, 0\n" \
  "s_mov_b32 s96, 0\n" \
  "v_readlane_b32 s86, v30, 0\n" \
  "s_nop 0\n" \
  "s_xor_b32 s86, s86, %[side]\n" \
  "v_mov_b32 v147, 0x4444\n" \
  "v_mov_b32 v140, 0xeeee\n" \
  "s_nop 1\n" \
  "v_mov_b32_dpp v147, v140 quad_perm:[0,1,2,3] row_mask:0xf bank_mask:0xa\n" \
  "v_mov_b32 v156, 0\n" \
  "v_mov_b32 v157, 0\n" \
  "v_mov_b32 v158, 0\n" \
  "v_mov_b32 v159, 0\n" \
  "v_mov_b32_dpp v156, %[sel0] quad_perm:[0,1,2,3] row_mask:0xf bank_mask:0x3\n" \
  "v_mov_b32_dpp v157, %[sel1] quad_perm:[0,1,2,3] row_mask:0xf bank_mask:0x3\n" \
  "v_mov_b32_dpp v158, %[sel0] quad_perm:[0,1,2,3] row_mask:0xf bank_mask:0xc\n" \
  "v_mov_b32_dpp v159, %[sel1] quad_perm:[0,1,2,3] row_mask:0xf bank_mask:0xc\n" \
  "v_mov_b32 v143, 1\n" \
  "v_mov_b32 v144, %[wrK]\n" \
  "v_mov_b32 v145, %[rdK]\n" \
  "v_mov_b32 v146, 0x7ff00000\n" \
  "s_mov_b32 s40, 0\n" \
  "s_mov_b32 s41, 0\n" \
  "s_mov_b32 s52, 7225344\n" \
  "v_mov_b32 v164, -1\n" \
  "v_mov_b32 v165, -1\n" \
  ".LM_top%=:\n" \
  "v_readlane_b32 s42, v30, s40\n" \
  "s_mov_b32 s44, 262144\n" \
  "s_xor_b32 s43, s42, %[side]\n" \
  "s_add_u32 s43, s43, s41\n" \
  "s_add_u32 s82, s41, s42\n" \
  "s_xor_b32 s83, s82, 16384\n" \
  "s_cmp_eq_u32 s40, 63\n" \
  "s_cselect_b32 s82, 0x7ff00000, s82\n" \
  "s_cselect_b32 s83, 0x7ff00000, s83\n" \
  "v_mov_b32 v56, 0\n" \
  "v_mov_b32 v57, 0\n" \
  "v_mov_b32 v58, 0\n" \
  "v_mov_b32 v59, 0\n" \
  "v_mov_b32 v60, 0\n" \
  "v_mov_b32 v61, 0\n" \
  "v_mov_b32 v62, 0\n" \
  "v_mov_b32 v63, 0\n" \
  "v_mov_b32 v64, 0\n" \
  "v_mov_b32 v65, 0\n" \
  "v_mov_b32 v66, 0\n" \
  "v_mov_b32 v67, 0\n" \
  "v_mov_b32 v68, 0\n" \
  "v_mov_b32 v69, 0\n" \
  "v_mov_b32 v70, 0\n" \
  "v_mov_b32 v71, 0\n" \
  "v_mov_b32 v72, 0\n" \
  "v_mov_b32 v73, 0\n" \
  "v_mov_b32 v74, 0\n" \
  "v_mov_b32 v75, 0\n" \
  "v_mov_b32 v76, 0\n" \
  "v_mov_b32 v77, 0\n" \
  "v_mov_b32 v78, 0\n" \
  "v_mov_b32 v79, 0\n" \
  "v_mov_b32 v80, 0\n" \
  "v_mov_b32 v81, 0\n" \
  "v_mov_b32 v82, 0\n" \
  "v_mov_b32 v83, 0\n" \
  "v_mov_b32 v84, 0\n" \
  "v_mov_b32 v85, 0\n" \
  "v_mov_b32 v86, 0\n" \
  "v_mov_b32 v87, 0\n" \
  "s_add_u32 s76, s40, 2\n" \
  "s_and_b32 s77, s76, 63\n" \
  "v_readlane_b32 s77, v30, s77\n" \
  "s_min_u32 s76, s76, 64\n" \
  "s_lshl_b32 s76, s76, 18\n" \
  "s_add_u32 s76, s76, s77\n" \
  "s_xor_b32 s77, s76, 16384\n" \
  "s_cmp_eq_u32 %[w], 2\n" \
  "s_cselect_b32 s76, s77, s76\n" \
  "s_cmp_eq_u32 %[w], 0\n" \
  "s_cselect_b32 s76, 0, s76\n" \
  "s_sub_u32 s97, s97, 1\n" \
  "s_max_i32 s97, s97, 0\n" \
  "s_lshr_b32 s96, s97, 4\n" \
  "s_cmp_eq_u32 s40, 1\n" \
  "s_cselect_b32 s96, 8, s96\n" \
  "s_cmp_eq_u32 s40, 0\n" \
  "s_cbranch_scc1 .LM_j0%=\n" \
  "buffer_load_dword v242, %[cIn], %[rs], s86 offen sc1\n" \
  "s_nop 15\n" \
  "buffer_load_dword v243, %[cIn], %[rs], s86 offen sc1\n" \
  "s_nop 15\n" \
  "s_waitcnt vmcnt(2)\n" \
  "s_cmp_lg_u32 %[w], 0\n" \
  "s_cbranch_scc1 .LM_cbar%=\n" \
  "s_mov_b64 exec, 3\n" \
  ".LM_cw%=:\n" \
  "buffer_load_dword v244, %[cIn], %[rs], s86 offen sc1\n" \
  "s_sub_u32 s44, s44, 1\n" \
  "s_cbranch_scc1 .LM_abort%=\n" \
  "s_waitcnt vmcnt(2)\n" \
  "v_cmp_gt_i32 vcc, s40, v242\n" \
  "s_cbranch_vccz .LM_cok%=\n" \
  "buffer_load_dword v242, %[cIn], %[rs], s86 offen sc1\n" \
  "s_waitcnt vmcnt(2)\n" \
  "v_cmp_gt_i32 vcc, s40, v243\n" \
  "s_cbranch_vccz .LM_cok%=\n" \
  "buffer_load_dword v243, %[cIn], %[rs], s86 offen sc1\n" \
  "s_waitcnt vmcnt(2)\n" \
  "v_cmp_gt_i32 vcc, s40, v244\n" \
  "s_cbranch_vccnz .LM_cw%=\n" \
  ".LM_cok%=:\n" \
  "s_cmp_gt_i32 s96, 0\n" \
  "s_cbranch_scc1 .LM_cdly%=\n" \
  ".LM_cbar%=:\n" \
  "s_barrier\n" \
  "s_mov_b64 exec, -1\n" \
  "s_nop 11\n" \
  "buffer_load_dwordx4 v[32:35], %[oMa], %[rs], s43 offen sc1\n" \
  "s_nop 7\n" \
  "buffer_load_dwordx4 v[36:39], %[oMb], %[rs], s43 offen sc1\n" \
  ".LM_j0%=:\n" \
  "s_mov_b32 s87, 1\n" \
  "s_mov_b32 s94, 0\n" \
  "s_mov_b32 s95, 0x7ff00000\n" \
  "s_nop 11\n" \
  "buffer_store_dwordx2 v[164:165], %[oKM], %[rs], s52 offen sc1\n" \
  "buffer_store_dwordx2 v[152:153], v151, %[rs], s76 offen sc1\n" \
  "s_waitcnt vmcnt(3)\n" \
  ".LM_critA%=:\n" \
  "v_smfmac_f32_16x16x64_bf16 v[56:59], v[32:35], a[0:7], v147\n" \
  "ds_write_b128 %[wrM], v[32:35]\n" \
  "v_smfmac_f32_16x16x64_bf16 v[72:75], v[32:35], a[64:71], v147\n" \
  "v_max3_u32 v140, v32, v33, v34\n" \
  "v_smfmac_f32_16x16x64_bf16 v[60:63], v[32:35], a[16:23], v147\n" \
  "v_smfmac_f32_16x16x64_bf16 v[76:79], v[32:35], a[80:87], v147\n" \
  "s_waitcnt vmcnt(2)\n" \
  "v_smfmac_f32_16x16x64_bf16 v[56:59], v[36:39], a[8:15], v147\n" \
  "ds_write_b128 %[wrM], v[36:39] offset:1024\n" \
  "v_smfmac_f32_16x16x64_bf16 v[72:75], v[36:39], a[72:79], v147\n" \
  "v_max3_u32 v141, v35, v36, v37\n" \
  "v_smfmac_f32_16x16x64_bf16 v[60:63], v[36:39], a[24:31], v147\n" \
  "v_max3_u32 v140, v140, v38, v39\n" \
  "v_max_u32 v140, v140, v141\n" \
  "v_smfmac_f32_16x16x64_bf16 v[76:79], v[36:39], a[88:95], v147\n" \
  "v_cmp_eq_u32 vcc, -1, v140\n" \
  "v_smfmac_f32_16x16x64_bf16 v[64:67], v[32:35], a[32:39], v147\n" \
  "v_smfmac_f32_16x16x64_bf16 v[80:83], v[32:35], a[96:103], v147\n" \
  "s_cbranch_vccnz .LM_failA%=\n" \
  "v_smfmac_f32_16x16x64_bf16 v[68:71], v[32:35], a[48:55], v147\n" \
  "v_cvt_pk_bf16_f32 v88, v56, v57\n" \
  "s_waitcnt lgkmcnt(0)\n" \
  "s_barrier\n" \
  "v_smfmac_f32_16x16x64_bf16 v[84:87], v[32:35], a[112:119], v147\n" \
  "v_cvt_pk_bf16_f32 v89, v58, v59\n" \
  "ds_read_b64 v[48:49], %[rdL0]\n" \
  "v_smfmac_f32_16x16x64_bf16 v[64:67], v[36:39], a[40:47], v147\n" \
  "v_cvt_pk_bf16_f32 v90, v72, v73\n" \
  "ds_read_b64 v[50:51], %[rdL0] offset:64\n" \
  "v_smfmac_f32_16x16x64_bf16 v[80:83], v[36:39], a[104:111], v147\n" \
  "v_cvt_pk_bf16_f32 v91, v74, v75\n" \
  "ds_read_b64 v[52:53], %[rdL1]\n" \
  "v_smfmac_f32_16x16x64_bf16 v[68:71], v[36:39], a[56:63], v147\n" \
  "v_cvt_pk_bf16_f32 v92, v60, v61\n" \
  "v_cvt_pk_bf16_f32 v93, v62, v63\n" \
  "ds_read_b64 v[54:55], %[rdL1] offset:64\n" \
  "v_smfmac_f32_16x16x64_bf16 v[84:87], v[36:39], a[120:127], v147\n" \
  "v_cvt_pk_bf16_f32 v94, v76, v77\n" \
  "v_cvt_pk_bf16_f32 v95, v78, v79\n" \
  "v_mfma_f32_16x16x32_bf16 v[104:107], v[88:91], v[156:159], 0\n" \
  "v_cvt_pk_bf16_f32 v96, v64, v65\n" \
  "v_cvt_pk_bf16_f32 v97, v66, v67\n" \
  "v_mfma_f32_16x16x32_bf16 v[108:111], v[92:95], v[156:159], 0\n" \
  "v_cvt_pk_bf16_f32 v98, v80, v81\n" \
  "v_cvt_pk_bf16_f32 v99, v82, v83\n" \
  "v_cvt_pk_bf16_f32 v100, v68, v69\n" \
  "v_cvt_pk_bf16_f32 v101, v70, v71\n" \
  "v_mfma_f32_16x16x32_bf16 v[112:115], v[96:99], v[156:159], 0\n" \
  "v_cvt_pk_bf16_f32 v102, v84, v85\n" \
  "v_cvt_pk_bf16_f32 v103, v86, v87\n" \
  "v_cvt_pk_bf16_f32 v120, v104, v105\n" \
  "v_cvt_pk_bf16_f32 v122, v106, v107\n" \
  "v_mfma_f32_16x16x32_bf16 v[116:119], v[100:103], v[156:159], 0\n" \
  "v_cvt_pk_bf16_f32 v121, v108, v109\n" \
  "v_cvt_pk_bf16_f32 v123, v110, v111\n" \
  "s_waitcnt lgkmcnt(0)\n" \
  "s_nop 0\n" \
  "v_mfma_f32_16x16x32_bf16 v[128:131], v[48:51], v[120:123], 0\n" \
  "v_cvt_pk_bf16_f32 v124, v112, v113\n" \
  "v_cvt_pk_bf16_f32 v126, v114, v115\n" \
  "s_nop 0\n" \
  "v_cvt_pk_bf16_f32 v125, v116, v117\n" \
  "v_cvt_pk_bf16_f32 v127, v118, v119\n" \
  "s_nop 1\n" \
  "v_mfma_f32_16x16x32_bf16 v[128:131], v[52:55], v[124:127], v[128:131]\n" \
  "s_nop 7\n" \
  "v_mov_b32_dpp v128, v130 quad_perm:[0,1,2,3] row_mask:0xf bank_mask:0xc\n" \
  "v_mov_b32_dpp v129, v131 quad_perm:[0,1,2,3] row_mask:0xf bank_mask:0xc\n" \
  "ds_write2_b32 %[wrS], v128, v129 offset0:0 offset1:32\n" \
  "ds_add_rtn_u32 v142, %[cnt], v143\n" \
  "ds_read_b128 v[132:135], %[rdS]\n" \
  "ds_read_b128 v[136:139], %[rdS] offset:16\n" \
  "s_waitcnt lgkmcnt(2)\n" \
  "v_and_b32 v142, 3, v142\n" \
  "v_cmpx_eq_u32 vcc, 3, v142\n" \
  "s_waitcnt lgkmcnt(1)\n" \
  "v_add_f32 v132, v132, v134\n" \
  "v_add_f32 v133, v133, v135\n" \
  "v_add_f32 v132, v132, v133\n" \
  "s_waitcnt lgkmcnt(0)\n" \
  "v_add_f32 v136, v136, v138\n" \
  "v_add_f32 v137, v137, v139\n" \
  "v_min_u32 v132, -2, v132\n" \
  "v_add_f32 v133, v136, v137\n" \
  "v_min_u32 v133, -2, v133\n" \
  "s_cbranch_execz .LM_skipa%=\n" \
  "buffer_store_dwordx2 v[132:133], %[oSt], %[rs], s82 offen sc1\n" \
  "buffer_store_dwordx2 v[132:133], %[oSt], %[rs], s83 offen sc1\n" \
  ".LM_skipa%=:\n" \
  "s_mov_b64 exec, -1\n" \
  "s_branch .LM_off%=\n" \
  ".LM_pollA%=:\n" \
  "buffer_load_dwordx4 v[40:43], %[oMa], %[rs], s43 offen sc1\n" \
  "buffer_load_dwordx4 v[44:47], %[oMb], %[rs], s43 offen sc1\n" \
  "v_mov_b32 v56, 0\n" \
  "v_mov_b32 v57, 0\n" \
  "v_mov_b32 v58, 0\n" \
  "v_mov_b32 v59, 0\n" \
  "v_mov_b32 v72, 0\n" \
  "v_mov_b32 v73, 0\n" \
  "v_mov_b32 v74, 0\n" \
  "v_mov_b32 v75, 0\n" \
  "v_mov_b32 v60, 0\n" \
  "v_mov_b32 v61, 0\n" \
  "v_mov_b32 v62, 0\n" \
  "v_mov_b32 v63, 0\n" \
  "v_mov_b32 v76, 0\n" \
  "v_mov_b32 v77, 0\n" \
  "v_mov_b32 v78, 0\n" \
  "v_mov_b32 v79, 0\n" \
  "v_mov_b32 v64, 0\n" \
  "v_mov_b32 v65, 0\n" \
  "v_mov_b32 v66, 0\n" \
  "v_mov_b32 v67, 0\n" \
  "v_mov_b32 v80, 0\n" \
  "v_mov_b32 v81, 0\n" \
  "v_mov_b32 v82, 0\n" \
  "v_mov_b32 v83, 0\n" \
  "s_waitcnt vmcnt(2)\n" \
  "s_branch .LM_critA%=\n" \
  ".LM_failA%=:\n" \
  "s_sub_u32 s44, s44, 1\n" \
  "s_cbranch_scc1 .LM_abort%=\n" \
  "s_cmp_eq_u32 s87, 0\n" \
  "s_cbranch_scc1 .LM_fail2%=\n" \
  "s_mov_b32 s95, 0\n" \
  "s_mov_b32 s94, 0x7ff00000\n" \
  "s_add_u32 s97, s97, 16\n" \
  "s_min_u32 s97, s97, 96\n" \
  "s_mov_b32 s87, 0\n" \
  "buffer_load_dwordx4 v[40:43], %[oMa], %[rs], s43 offen sc1\n" \
  "buffer_load_dwordx4 v[44:47], %[oMb], %[rs], s43 offen sc1\n" \
  "s_nop 15\n" \
  "s_nop 15\n" \
  ".LM_fail2%=:\n" \
  "buffer_load_dwordx4 v[32:35], %[oMa], %[rs], s43 offen sc1\n" \
  "buffer_load_dwordx4 v[36:39], %[oMb], %[rs], s43 offen sc1\n" \
  "v_mov_b32 v56, 0\n" \
  "v_mov_b32 v57, 0\n" \
  "v_mov_b32 v58, 0\n" \
  "v_mov_b32 v59, 0\n" \
  "v_mov_b32 v72, 0\n" \
  "v_mov_b32 v73, 0\n" \
  "v_mov_b32 v74, 0\n" \
  "v_mov_b32 v75, 0\n" \
  "v_mov_b32 v60, 0\n" \
  "v_mov_b32 v61, 0\n" \
  "v_mov_b32 v62, 0\n" \
  "v_mov_b32 v63, 0\n" \
  "v_mov_b32 v76, 0\n" \
  "v_mov_b32 v77, 0\n" \
  "v_mov_b32 v78, 0\n" \
  "v_mov_b32 v79, 0\n" \
  "v_mov_b32 v64, 0\n" \
  "v_mov_b32 v65, 0\n" \
  "v_mov_b32 v66, 0\n" \
  "v_mov_b32 v67, 0\n" \
  "v_mov_b32 v80, 0\n" \
  "v_mov_b32 v81, 0\n" \
  "v_mov_b32 v82, 0\n" \
  "v_mov_b32 v83, 0\n" \
  "s_waitcnt vmcnt(2)\n" \
  "v_smfmac_f32_16x16x64_bf16 v[56:59], v[40:43], a[0:7], v147\n" \
  "ds_write_b128 %[wrM], v[40:43]\n" \
  "v_smfmac_f32_16x16x64_bf16 v[72:75], v[40:43], a[64:71], v147\n" \
  "v_max3_u32 v140, v40, v41, v42\n" \
  "v_smfmac_f32_16x16x64_bf16 v[60:63], v[40:43], a[16:23], v147\n" \
  "v_smfmac_f32_16x16x64_bf16 v[76:79], v[40:43], a[80:87], v147\n" \
  "s_waitcnt vmcnt(2)\n" \
  "v_smfmac_f32_16x16x64_bf16 v[56:59], v[44:47], a[8:15], v147\n" \
  "ds_write_b128 %[wrM], v[44:47] offset:1024\n" \
  "v_smfmac_f32_16x16x64_bf16 v[72:75], v[44:47], a[72:79], v147\n" \
  "v_max3_u32 v141, v43, v44, v45\n" \
  "v_smfmac_f32_16x16x64_bf16 v[60:63], v[44:47], a[24:31], v147\n" \
  "v_max3_u32 v140, v140, v46, v47\n" \
  "v_max_u32 v140, v140, v141\n" \
  "v_smfmac_f32_16x16x64_bf16 v[76:79], v[44:47], a[88:95], v147\n" \
  "v_cmp_eq_u32 vcc, -1, v140\n" \
  "v_smfmac_f32_16x16x64_bf16 v[64:67], v[40:43], a[32:39], v147\n" \
  "v_smfmac_f32_16x16x64_bf16 v[80:83], v[40:43], a[96:103], v147\n" \
  "s_cbranch_vccnz .LM_pollA%=\n" \
  "v_smfmac_f32_16x16x64_bf16 v[68:71], v[40:43], a[48:55], v147\n" \
  "v_cvt_pk_bf16_f32 v88, v56, v57\n" \
  "s_waitcnt lgkmcnt(0)\n" \
  "s_barrier\n" \
  "v_smfmac_f32_16x16x64_bf16 v[84:87], v[40:43], a[112:119], v147\n" \
  "v_cvt_pk_bf16_f32 v89, v58, v59\n" \
  "ds_read_b64 v[48:49], %[rdL0]\n" \
  "v_smfmac_f32_16x16x64_bf16 v[64:67], v[44:47], a[40:47], v147\n" \
  "v_cvt_pk_bf16_f32 v90, v72, v73\n" \
  "ds_read_b64 v[50:51], %[rdL0] offset:64\n" \
  "v_smfmac_f32_16x16x64_bf16 v[80:83], v[44:47], a[104:111], v147\n" \
  "v_cvt_pk_bf16_f32 v91, v74, v75\n" \
  "ds_read_b64 v[52:53], %[rdL1]\n" \
  "v_smfmac_f32_16x16x64_bf16 v[68:71], v[44:47], a[56:63], v147\n" \
  "v_cvt_pk_bf16_f32 v92, v60, v61\n" \
  "v_cvt_pk_bf16_f32 v93, v62, v63\n" \
  "ds_read_b64 v[54:55], %[rdL1] offset:64\n" \
  "v_smfmac_f32_16x16x64_bf16 v[84:87], v[44:47], a[120:127], v147\n" \
  "v_cvt_pk_bf16_f32 v94, v76, v77\n" \
  "v_cvt_pk_bf16_f32 v95, v78, v79\n" \
  "v_mfma_f32_16x16x32_bf16 v[104:107], v[88:91], v[156:159], 0\n" \
  "v_cvt_pk_bf16_f32 v96, v64, v65\n" \
  "v_cvt_pk_bf16_f32 v97, v66, v67\n" \
  "v_mfma_f32_16x16x32_bf16 v[108:111], v[92:95], v[156:159], 0\n" \
  "v_cvt_pk_bf16_f32 v98, v80, v81\n" \
  "v_cvt_pk_bf16_f32 v99, v82, v83\n" \
  "v_cvt_pk_bf16_f32 v100, v68, v69\n" \
  "v_cvt_pk_bf16_f32 v101, v70, v71\n" \
  "v_mfma_f32_16x16x32_bf16 v[112:115], v[96:99], v[156:159], 0\n" \
  "v_cvt_pk_bf16_f32 v102, v84, v85\n" \
  "v_cvt_pk_bf16_f32 v103, v86, v87\n" \
  "v_cvt_pk_bf16_f32 v120, v104, v105\n" \
  "v_cvt_pk_bf16_f32 v122, v106, v107\n" \
  "v_mfma_f32_16x16x32_bf16 v[116:119], v[100:103], v[156:159], 0\n" \
  "v_cvt_pk_bf16_f32 v121, v108, v109\n" \
  "v_cvt_pk_bf16_f32 v123, v110, v111\n" \
  "s_waitcnt lgkmcnt(0)\n" \
  "s_nop 0\n" \
  "v_mfma_f32_16x16x32_bf16 v[128:131], v[48:51], v[120:123], 0\n" \
  "v_cvt_pk_bf16_f32 v124, v112, v113\n" \
  "v_cvt_pk_bf16_f32 v126, v114, v115\n" \
  "s_nop 0\n" \
  "v_cvt_pk_bf16_f32 v125, v116, v117\n" \
  "v_cvt_pk_bf16_f32 v127, v118, v119\n" \
  "s_nop 1\n" \
  "v_mfma_f32_16x16x32_bf16 v[128:131], v[52:55], v[124:127], v[128:131]\n" \
  "s_nop 7\n" \
  "v_mov_b32_dpp v128, v130 quad_perm:[0,1,2,3] row_mask:0xf bank_mask:0xc\n" \
  "v_mov_b32_dpp v129, v131 quad_perm:[0,1,2,3] row_mask:0xf bank_mask:0xc\n" \
  "ds_write2_b32 %[wrS], v128, v129 offset0:0 offset1:32\n" \
  "ds_add_rtn_u32 v142, %[cnt], v143\n" \
  "ds_read_b128 v[132:135], %[rdS]\n" \
  "ds_read_b128 v[136:139], %[rdS] offset:16\n" \
  "s_waitcnt lgkmcnt(2)\n" \
  "v_and_b32 v142, 3, v142\n" \
  "v_cmpx_eq_u32 vcc, 3, v142\n" \
  "s_waitcnt lgkmcnt(1)\n" \
  "v_add_f32 v132, v132, v134\n" \
  "v_add_f32 v133, v133, v135\n" \
  "v_add_f32 v132, v132, v133\n" \
  "s_waitcnt lgkmcnt(0)\n" \
  "v_add_f32 v136, v136, v138\n" \
  "v_add_f32 v137, v137, v139\n" \
  "v_min_u32 v132, -2, v132\n" \
  "v_add_f32 v133, v136, v137\n" \
  "v_min_u32 v133, -2, v133\n" \
  "s_cbranch_execz .LM_skipb%=\n" \
  "buffer_store_dwordx2 v[132:133], %[oSt], %[rs], s82 offen sc1\n" \
  "buffer_store_dwordx2 v[132:133], %[oSt], %[rs], s83 offen sc1\n" \
  ".LM_skipb%=:\n" \
  "s_mov_b64 exec, -1\n" \
  ".LM_off%=:\n" \
  "ds_write_b128 v144, v[104:107]\n" \
  "ds_write_b128 v144, v[108:111] offset:1024\n" \
  "ds_write_b128 v144, v[112:115] offset:2048\n" \
  "ds_write_b128 v144, v[116:119] offset:3072\n" \
  "s_or_b32 s46, s40, s92\n" \
  "v_mov_b32 v152, s46\n" \
  "s_mul_i32 s52, s40, 114688\n" \
  "s_waitcnt lgkmcnt(0)\n" \
  "s_barrier\n" \
  "ds_read_b128 v[160:163], v145\n" \
  "ds_read_b128 v[164:167], v145 offset:4096\n" \
  "ds_read_b128 v[168:171], v145 offset:8192\n" \
  "ds_read_b128 v[172:175], v145 offset:12288\n" \
  "s_waitcnt lgkmcnt(0)\n" \
  "v_add_f32 v160, v160, v164\n" \
  "v_add_f32 v161, v161, v165\n" \
  "v_add_f32 v168, v168, v172\n" \
  "v_add_f32 v169, v169, v173\n" \
  "v_add_f32 v162, v162, v166\n" \
  "v_add_f32 v163, v163, v167\n" \
  "v_add_f32 v170, v170, v174\n" \
  "v_add_f32 v171, v171, v175\n" \
  "v_add_f32 v160, v160, v168\n" \
  "v_add_f32 v161, v161, v169\n" \
  "v_add_f32 v162, v162, v170\n" \
  "v_add_f32 v163, v163, v171\n" \
  "v_cvt_pk_bf16_f32 v164, v160, v161\n" \
  "v_cvt_pk_bf16_f32 v165, v162, v163\n" \
  "s_cmp_eq_u32 s40, 63\n" \
  "s_cbranch_scc1 .LM_fin%=\n" \
  "s_add_u32 s40, s40, 1\n" \
  "s_add_u32 s41, s41, 262144\n" \
  "s_branch .LM_top%=\n" \
  ".LM_fin%=:\n" \
  "v_min_u32 v164, -2, v164\n" \
  "v_min_u32 v165, -2, v165\n" \
  "buffer_store_dwordx2 v[164:165], %[oKM], %[rs], s52 offen sc1\n" \
  "v_mov_b32 v140, 63\n" \
  "v_mov_b32 v141, 64\n" \
  "s_waitcnt vmcnt(1)\n" \
  "s_barrier\n" \
  "buffer_store_dword v140, %[oPr], %[rs], 0 offen sc1\n" \
  "s_waitcnt vmcnt(0)\n" \
  "s_barrier\n" \
  "buffer_store_dword v141, %[oPr], %[rs], 0 offen sc1\n" \
  "s_branch .LM_end%=\n" \
  ".LM_cdly%=:\n" \
  "s_sub_u32 s96, s96, 1\n" \
  "s_cmp_gt_i32 s96, 0\n" \
  "s_cbranch_scc1 .LM_cdly%=\n" \
  "s_branch .LM_cbar%=\n" \
  ".LM_abort%=:\n" \
  "s_waitcnt vmcnt(0) lgkmcnt(0)\n" \
  "s_mov_b64 exec, -1\n" \
  "v_mov_b32 v140, 1\n" \
  "v_mov_b32 v141, 0x2913000\n" \
  "buffer_store_dword v140, v141, %[rs], 0 offen sc1\n" \
  ".LM_end%=:\n" \
  "s_waitcnt vmcnt(0) lgkmcnt(0)\n" \
  ".LM_zend%=:\n" \
  ""
#define ASM_M_CLOB "memory", "scc", "vcc", "a0", "a1", "a2", "a3", "a4", "a5", "a6", "a7", "a8", "a9", "a10", "a11", "a12", "a13", "a14", "a15", "a16", "a17", "a18", "a19", "a20", "a21", "a22", "a23", "a24", "a25", "a26", "a27", "a28", "a29", "a30", "a31", "a32", "a33", "a34", "a35", "a36", "a37", "a38", "a39", "a40", "a41", "a42", "a43", "a44", "a45", "a46", "a47", "a48", "a49", "a50", "a51", "a52", "a53", "a54", "a55", "a56", "a57", "a58", "a59", "a60", "a61", "a62", "a63", "a64", "a65", "a66", "a67", "a68", "a69", "a70", "a71", "a72", "a73", "a74", "a75", "a76", "a77", "a78", "a79", "a80", "a81", "a82", "a83", "a84", "a85", "a86", "a87", "a88", "a89", "a90", "a91", "a92", "a93", "a94", "a95", "a96", "a97", "a98", "a99", "a100", "a101", "a102", "a103", "a104", "a105", "a106", "a107", "a108", "a109", "a110", "a111", "a112", "a113", "a114", "a115", "a116", "a117", "a118", "a119", "a120", "a121", "a122", "a123", "a124", "a125", "a126", "a127", "s40", "s41", "s42", "s43", "s44", "s46", "s52", "s54", "s55", "s76", "s77", "s82", "s83", "s86", "s87", "s92", "s94", "s95", "s96", "s97", "v30", "v32", "v33", "v34", "v35", "v36", "v37", "v38", "v39", "v40", "v41", "v42", "v43", "v44", "v45", "v46", "v47", "v48", "v49", "v50", "v51", "v52", "v53", "v54", "v55", "v56", "v57", "v58", "v59", "v60", "v61", "v62", "v63", "v64", "v65", "v66", "v67", "v68", "v69", "v70", "v71", "v72", "v73", "v74", "v75", "v76", "v77", "v78", "v79", "v80", "v81", "v82", "v83", "v84", "v85", "v86", "v87", "v88", "v89", "v90", "v91", "v92", "v93", "v94", "v95", "v96", "v97", "v98", "v99", "v100", "v101", "v102", "v103", "v104", "v105", "v106", "v107", "v108", "v109", "v110", "v111", "v112", "v113", "v114", "v115", "v116", "v117", "v118", "v119", "v120", "v121", "v122", "v123", "v124", "v125", "v126", "v127", "v128", "v129", "v130", "v131", "v132", "v133", "v134", "v135", "v136", "v137", "v138", "v139", "v140", "v141", "v142", "v143", "v144", "v145", "v146", "v147", "v151", "v152", "v153", "v156", "v157", "v158", "v159", "v160", "v161", "v162", "v163", "v164", "v165", "v166", "v167", "v168", "v169", "v170", "v171", "v172", "v173", "v174", "v175", "v208", "v209", "v210", "v211", "v212", "v213", "v214", "v215", "v216", "v217", "v218", "v219", "v220", "v221", "v222", "v223", "v242", "v243", "v244"
#define ASM_X \
  "s_nop 4\n" \
  "buffer_load_dwordx4 v[32:35], %[oMa], %[rs], 0 offen sc1\n" \
  "buffer_load_dwordx4 v[36:39], %[oMb], %[rs], 0 offen sc1\n" \
  "buffer_load_dwordx4 v[208:211], %[oCal], %[rs], 0 offen\n" \
  "buffer_load_dwordx4 v[212:215], %[oCal], %[rs], 0 offen offset:128\n" \
  "buffer_load_dwordx4 v[216:219], %[oCal], %[rs], 0 offen offset:256\n" \
  "buffer_load_dwordx4 v[220:223], %[oCal], %[rs], 0 offen offset:384\n" \
  "s_waitcnt vmcnt(0)\n" \
  "v_add3_u32 v208, v208, v212, v216\n" \
  "v_add_u32 v208, v208, v220\n" \
  "v_lshl_or_b32 v208, v208, 2, 0\n" \
  "v_add3_u32 v209, v209, v213, v217\n" \
  "v_add_u32 v209, v209, v221\n" \
  "v_lshl_or_b32 v209, v209, 2, 1\n" \
  "v_add3_u32 v210, v210, v214, v218\n" \
  "v_add_u32 v210, v210, v222\n" \
  "v_lshl_or_b32 v210, v210, 2, 2\n" \
  "v_add3_u32 v211, v211, v215, v219\n" \
  "v_add_u32 v211, v211, v223\n" \
  "v_lshl_or_b32 v211, v211, 2, 3\n" \
  "v_min3_u32 v208, v208, v209, v210\n" \
  "v_min_u32 v208, v208, v211\n" \
  "v_and_b32 v208, 3, v208\n" \
  "v_lshlrev_b32 v208, 13, v208\n" \
  "v_lshlrev_b32 v209, 9, %[l16]\n" \
  "v_and_b32 v209, 0x4000, v209\n" \
  "v_xor_b32 v30, v208, v209\n" \
  "v_mov_b32 v88, 0x4444\n" \
  "v_mov_b32 v108, 0xeeee\n" \
  "s_nop 1\n" \
  "v_mov_b32_dpp v88, v108 quad_perm:[0,1,2,3] row_mask:0xf bank_mask:0xa\n" \
  "v_mov_b32 v156, -1\n" \
  "v_mov_b32 v157, -1\n" \
  "v_mov_b32 v158, -1\n" \
  "v_mov_b32 v159, -1\n" \
  "s_mov_b32 s97, 0\n" \
  "s_mov_b32 s96, 0\n" \
  "v_readlane_b32 s86, v30, 0\n" \
  "s_nop 1\n" \
  "v_xor_b32 v123, s86, %[cOut]\n" \
  "s_xor_b32 s86, s86, %[side]\n" \
  "v_readlane_b32 s88, v30, 0\n" \
  "s_nop 0\n" \
  "s_and_b32 s88, s88, 8192\n" \
  "s_xor_b32 s88, s88, 8192\n" \
  "s_waitcnt vmcnt(0)\n" \
  "v_mov_b32 v113, 1\n" \
  "v_mov_b32 v114, 0x7ff00000\n" \
  "s_mov_b32 s40, 0\n" \
  "s_mov_b32 s41, 0\n" \
  "s_mov_b32 s45, 2\n" \
  "s_lshl_b32 s49, s40, 12\n" \
  "s_and_b32 s49, s49, 8192\n" \
  "s_xor_b32 s49, s49, s88\n" \
  "s_cmp_lt_u32 s40, 2\n" \
  "s_cselect_b32 s49, 0, s49\n" \
  "s_lshl_b32 s52, s40, 20\n" \
  "s_add_u32 s52, s52, %[sK]\n" \
  "s_xor_b32 s52, s52, s49\n" \
  "s_add_u32 s53, s52, 4096\n" \
  "buffer_load_dwordx4 v[48:51], %[l16], %[rs], s52 offen sc1\n" \
  "buffer_load_dwordx4 v[56:59], %[l16], %[rs], s52 offen offset:1024 sc1\n" \
  "buffer_load_dwordx4 v[52:55], %[l16], %[rs], s52 offen offset:2048 sc1\n" \
  "buffer_load_dwordx4 v[60:63], %[l16], %[rs], s52 offen offset:3072 sc1\n" \
  "buffer_load_dwordx4 v[64:67], %[l16b], %[rs], s53 offen sc1\n" \
  "buffer_load_dwordx4 v[72:75], %[l16b], %[rs], s53 offen offset:1024 sc1\n" \
  "buffer_load_dwordx4 v[68:71], %[l16b], %[rs], s53 offen offset:2048 sc1\n" \
  "buffer_load_dwordx4 v[76:79], %[l16b], %[rs], s53 offen offset:3072 sc1\n" \
  "v_mov_b32 v164, 2\n" \
  ".LX_top%=:\n" \
  "s_add_u32 s54, s40, 1\n" \
  "s_add_u32 s55, s40, 2\n" \
  "s_lshl_b32 s49, s54, 12\n" \
  "s_and_b32 s49, s49, 8192\n" \
  "s_xor_b32 s49, s49, s88\n" \
  "s_cmp_lt_u32 s54, 2\n" \
  "s_cselect_b32 s49, 0, s49\n" \
  "s_lshl_b32 s52, s54, 20\n" \
  "s_add_u32 s52, s52, %[sK]\n" \
  "s_xor_b32 s52, s52, s49\n" \
  "s_add_u32 s53, s52, 4096\n" \
  "v_readlane_b32 s42, v30, s40\n" \
  "s_mov_b32 s44, 262144\n" \
  "s_xor_b32 s43, s42, %[side]\n" \
  "s_add_u32 s43, s43, s41\n" \
  "s_add_u32 s82, s41, s42\n" \
  "s_xor_b32 s83, s82, 16384\n" \
  "v_xor_b32 v111, s42, %[oT]\n" \
  "v_mov_b32 v124, s54\n" \
  "s_cmp_ge_i32 s45, s55\n" \
  "s_cselect_b32 s57, 0, 0x7ff00000\n" \
  "v_or_b32 v165, s57, %[l16]\n" \
  "v_or_b32 v166, s57, %[l16b]\n" \
  "v_mov_b32 v80, 0\n" \
  "v_mov_b32 v81, 0\n" \
  "v_mov_b32 v82, 0\n" \
  "v_mov_b32 v83, 0\n" \
  "v_mov_b32 v84, 0\n" \
  "v_mov_b32 v85, 0\n" \
  "v_mov_b32 v86, 0\n" \
  "v_mov_b32 v87, 0\n" \
  "s_add_u32 s76, s40, 2\n" \
  "s_and_b32 s77, s76, 63\n" \
  "v_readlane_b32 s77, v30, s77\n" \
  "s_min_u32 s76, s76, 64\n" \
  "s_lshl_b32 s76, s76, 18\n" \
  "s_add_u32 s76, s76, s77\n" \
  "s_lshl_b32 s77, %[w], 14\n" \
  "s_and_b32 s77, s77, 16384\n" \
  "s_xor_b32 s76, s76, s77\n" \
  "s_sub_u32 s97, s97, 1\n" \
  "s_max_i32 s97, s97, 0\n" \
  "s_lshr_b32 s96, s97, 4\n" \
  "s_cmp_eq_u32 s40, 1\n" \
  "s_cselect_b32 s96, 8, s96\n" \
  "s_cmp_eq_u32 s40, 0\n" \
  "s_cbranch_scc1 .LX_j0%=\n" \
  "buffer_load_dword v242, %[cIn], %[rs], s86 offen sc1\n" \
  "s_nop 15\n" \
  "buffer_load_dword v243, %[cIn], %[rs], s86 offen sc1\n" \
  "s_nop 15\n" \
  "s_waitcnt vmcnt(2)\n" \
  "s_cmp_lg_u32 %[w], 0\n" \
  "s_cbranch_scc1 .LX_cbar%=\n" \
  "s_mov_b64 exec, 3\n" \
  ".LX_cw%=:\n" \
  "buffer_load_dword v244, %[cIn], %[rs], s86 offen sc1\n" \
  "s_sub_u32 s44, s44, 1\n" \
  "s_cbranch_scc1 .LX_abort%=\n" \
  "s_waitcnt vmcnt(2)\n" \
  "v_cmp_gt_i32 vcc, s40, v242\n" \
  "s_cbranch_vccz .LX_cok%=\n" \
  "buffer_load_dword v242, %[cIn], %[rs], s86 offen sc1\n" \
  "s_waitcnt vmcnt(2)\n" \
  "v_cmp_gt_i32 vcc, s40, v243\n" \
  "s_cbranch_vccz .LX_cok%=\n" \
  "buffer_load_dword v243, %[cIn], %[rs], s86 offen sc1\n" \
  "s_waitcnt vmcnt(2)\n" \
  "v_cmp_gt_i32 vcc, s40, v244\n" \
  "s_cbranch_vccnz .LX_cw%=\n" \
  ".LX_cok%=:\n" \
  "s_cmp_gt_i32 s96, 0\n" \
  "s_cbranch_scc1 .LX_cdly%=\n" \
  ".LX_cbar%=:\n" \
  "s_barrier\n" \
  "s_mov_b64 exec, -1\n" \
  "s_nop 11\n" \
  "buffer_load_dwordx4 v[32:35], %[oMa], %[rs], s43 offen sc1\n" \
  "buffer_load_dwordx4 v[36:39], %[oMb], %[rs], s43 offen sc1\n" \
  ".LX_j0%=:\n" \
  "s_mov_b32 s87, 1\n" \
  "s_mov_b32 s94, 0\n" \
  "s_mov_b32 s95, 0x7ff00000\n" \
  "s_nop 3\n" \
  "buffer_store_dwordx4 v[156:159], %[oFz], %[rs], s76 offen sc1\n" \
  "buffer_load_dwordx4 v[168:171], v165, %[rs], s52 offen sc1\n" \
  "buffer_load_dwordx4 v[176:179], v165, %[rs], s52 offen offset:1024 sc1\n" \
  "s_add_u32 s46, s55, 3\n" \
  "s_min_u32 s46, s46, 64\n" \
  "s_cmp_ge_i32 s45, s46\n" \
  "s_cselect_b64 exec, 0, 15\n" \
  "buffer_load_dword v164, %[oPrV], %[rs], 0 offen sc1\n" \
  "s_mov_b64 exec, -1\n" \
  "s_waitcnt vmcnt(4)\n" \
  ".LX_critA%=:\n" \
  "v_smfmac_f32_16x16x64_bf16 v[80:83], v[32:35], v[48:55], v88\n" \
  "v_max3_u32 v108, v32, v33, v34\n" \
  "v_max3_u32 v109, v35, v36, v37\n" \
  "v_smfmac_f32_16x16x64_bf16 v[84:87], v[32:35], v[56:63], v88\n" \
  "v_max3_u32 v108, v108, v38, v39\n" \
  "v_max_u32 v108, v108, v109\n" \
  "v_smfmac_f32_16x16x64_bf16 v[80:83], v[36:39], v[64:71], v88\n" \
  "v_cmp_eq_u32 vcc, -1, v108\n" \
  "v_smfmac_f32_16x16x64_bf16 v[84:87], v[36:39], v[72:79], v88\n" \
  "s_cbranch_vccnz .LX_failA%=\n" \
  "buffer_store_dword v124, v123, %[rs], s94 offen sc1\n" \
  "s_nop 7\n" \
  "v_permlane16_swap_b32 v80, v84\n" \
  "v_permlane16_swap_b32 v81, v85\n" \
  "v_permlane16_swap_b32 v82, v86\n" \
  "v_permlane16_swap_b32 v83, v87\n" \
  "v_add_f32 v80, v80, v84\n" \
  "v_add_f32 v81, v81, v85\n" \
  "v_add_f32 v82, v82, v86\n" \
  "v_add_f32 v83, v83, v87\n" \
  "ds_write2_b32 %[wrS], v80, v81 offset0:0 offset1:16\n" \
  "ds_write2_b32 %[wrS], v82, v83 offset0:32 offset1:48\n" \
  "ds_add_rtn_u32 v112, %[cnt], v113\n" \
  "ds_read_b128 v[92:95], %[rdE]\n" \
  "ds_read_b128 v[96:99], %[rdE] offset:1024\n" \
  "ds_read_b128 v[100:103], %[rdE] offset:2048\n" \
  "ds_read_b128 v[104:107], %[rdE] offset:3072\n" \
  "s_waitcnt lgkmcnt(4)\n" \
  "v_and_b32 v112, 3, v112\n" \
  "v_cmp_eq_u32 vcc, 3, v112\n" \
  "s_waitcnt lgkmcnt(2)\n" \
  "v_add_f32 v92, v92, v96\n" \
  "v_add_f32 v93, v93, v97\n" \
  "v_add_f32 v94, v94, v98\n" \
  "v_add_f32 v95, v95, v99\n" \
  "s_waitcnt lgkmcnt(0)\n" \
  "v_add_f32 v100, v100, v104\n" \
  "v_add_f32 v101, v101, v105\n" \
  "v_add_f32 v102, v102, v106\n" \
  "v_add_f32 v103, v103, v107\n" \
  "v_add_f32 v92, v92, v100\n" \
  "v_add_f32 v93, v93, v101\n" \
  "v_add_f32 v94, v94, v102\n" \
  "v_add_f32 v95, v95, v103\n" \
  "v_max3_f32 v116, v92, v93, v94\n" \
  "v_max_f32 v116, v116, v95\n" \
  "s_cbranch_vccz .LX_skipfailA%=\n" \
  "s_nop 0\n" \
  "v_max_f32_dpp v116, v116, v116 quad_perm:[1,0,3,2] row_mask:0xf bank_mask:0xf bound_ctrl:1\n" \
  "s_nop 1\n" \
  "v_max_f32_dpp v116, v116, v116 row_ror:8 row_mask:0xf bank_mask:0xf bound_ctrl:1\n" \
  "v_mov_b32 v117, v116\n" \
  "s_nop 1\n" \
  "v_permlane16_swap_b32 v117, v116\n" \
  "v_max_f32 v116, v116, v117\n" \
  "s_mov_b64 exec, vcc\n" \
  "v_sub_f32 v92, v92, v116\n" \
  "v_sub_f32 v93, v93, v116\n" \
  "v_sub_f32 v94, v94, v116\n" \
  "v_sub_f32 v95, v95, v116\n" \
  "v_exp_f32 v92, v92\n" \
  "v_exp_f32 v93, v93\n" \
  "v_exp_f32 v94, v94\n" \
  "v_exp_f32 v95, v95\n" \
  "v_cvt_pk_bf16_f32 v118, v92, v93\n" \
  "v_cvt_pk_bf16_f32 v119, v94, v95\n" \
  "v_min_u32 v118, -2, v118\n" \
  "v_min_u32 v119, -2, v119\n" \
  "buffer_store_dwordx2 v[118:119], %[oE], %[rs], s82 offen sc1\n" \
  "buffer_store_dwordx2 v[118:119], %[oE], %[rs], s83 offen sc1\n" \
  "v_min_u32 v116, -2, v116\n" \
  "buffer_store_dword v116, v111, %[rs], s41 offen sc1\n" \
  "s_mov_b64 exec, -1\n" \
  ".LX_skipfailA%=:\n" \
  "s_branch .LX_off%=\n" \
  ".LX_pollA%=:\n" \
  "buffer_load_dwordx4 v[40:43], %[oMa], %[rs], s43 offen sc1\n" \
  "buffer_load_dwordx4 v[44:47], %[oMb], %[rs], s43 offen sc1\n" \
  "s_nop 4\n" \
  "v_mov_b32 v80, 0\n" \
  "v_mov_b32 v81, 0\n" \
  "v_mov_b32 v82, 0\n" \
  "v_mov_b32 v83, 0\n" \
  "v_mov_b32 v84, 0\n" \
  "v_mov_b32 v85, 0\n" \
  "v_mov_b32 v86, 0\n" \
  "v_mov_b32 v87, 0\n" \
  "s_waitcnt vmcnt(2)\n" \
  "s_branch .LX_critA%=\n" \
  ".LX_failA%=:\n" \
  "s_sub_u32 s44, s44, 1\n" \
  "s_cbranch_scc1 .LX_abort%=\n" \
  "s_cmp_eq_u32 s87, 0\n" \
  "s_cbranch_scc1 .LX_fail2%=\n" \
  "s_mov_b32 s95, 0\n" \
  "s_mov_b32 s94, 0x7ff00000\n" \
  "s_add_u32 s97, s97, 16\n" \
  "s_min_u32 s97, s97, 96\n" \
  "s_mov_b32 s87, 0\n" \
  "buffer_load_dwordx4 v[40:43], %[oMa], %[rs], s43 offen sc1\n" \
  "buffer_load_dwordx4 v[44:47], %[oMb], %[rs], s43 offen sc1\n" \
  "s_nop 15\n" \
  "s_nop 15\n" \
  ".LX_fail2%=:\n" \
  "buffer_load_dwordx4 v[32:35], %[oMa], %[rs], s43 offen sc1\n" \
  "buffer_load_dwordx4 v[36:39], %[oMb], %[rs], s43 offen sc1\n" \
  "s_nop 0\n" \
  "v_mov_b32 v80, 0\n" \
  "v_mov_b32 v81, 0\n" \
  "v_mov_b32 v82, 0\n" \
  "v_mov_b32 v83, 0\n" \
  "v_mov_b32 v84, 0\n" \
  "v_mov_b32 v85, 0\n" \
  "v_mov_b32 v86, 0\n" \
  "v_mov_b32 v87, 0\n" \
  "s_waitcnt vmcnt(2)\n" \
  "v_smfmac_f32_16x16x64_bf16 v[80:83], v[40:43], v[48:55], v88\n" \
  "v_max3_u32 v108, v40, v41, v42\n" \
  "v_max3_u32 v109, v43, v44, v45\n" \
  "v_smfmac_f32_16x16x64_bf16 v[84:87], v[40:43], v[56:63], v88\n" \
  "v_max3_u32 v108, v108, v46, v47\n" \
  "v_max_u32 v108, v108, v109\n" \
  "v_smfmac_f32_16x16x64_bf16 v[80:83], v[44:47], v[64:71], v88\n" \
  "v_cmp_eq_u32 vcc, -1, v108\n" \
  "v_smfmac_f32_16x16x64_bf16 v[84:87], v[44:47], v[72:79], v88\n" \
  "s_cbranch_vccnz .LX_pollA%=\n" \
  "buffer_store_dword v124, v123, %[rs], s94 offen sc1\n" \
  "s_nop 7\n" \
  "v_permlane16_swap_b32 v80, v84\n" \
  "v_permlane16_swap_b32 v81, v85\n" \
  "v_permlane16_swap_b32 v82, v86\n" \
  "v_permlane16_swap_b32 v83, v87\n" \
  "v_add_f32 v80, v80, v84\n" \
  "v_add_f32 v81, v81, v85\n" \
  "v_add_f32 v82, v82, v86\n" \
  "v_add_f32 v83, v83, v87\n" \
  "ds_write2_b32 %[wrS], v80, v81 offset0:0 offset1:16\n" \
  "ds_write2_b32 %[wrS], v82, v83 offset0:32 offset1:48\n" \
  "ds_add_rtn_u32 v112, %[cnt], v113\n" \
  "ds_read_b128 v[92:95], %[rdE]\n" \
  "ds_read_b128 v[96:99], %[rdE] offset:1024\n" \
  "ds_read_b128 v[100:103], %[rdE] offset:2048\n" \
  "ds_read_b128 v[104:107], %[rdE] offset:3072\n" \
  "s_waitcnt lgkmcnt(4)\n" \
  "v_and_b32 v112, 3, v112\n" \
  "v_cmp_eq_u32 vcc, 3, v112\n" \
  "s_waitcnt lgkmcnt(2)\n" \
  "v_add_f32 v92, v92, v96\n" \
  "v_add_f32 v93, v93, v97\n" \
  "v_add_f32 v94, v94, v98\n" \
  "v_add_f32 v95, v95, v99\n" \
  "s_waitcnt lgkmcnt(0)\n" \
  "v_add_f32 v100, v100, v104\n" \
  "v_add_f32 v101, v101, v105\n" \
  "v_add_f32 v102, v102, v106\n" \
  "v_add_f32 v103, v103, v107\n" \
  "v_add_f32 v92, v92, v100\n" \
  "v_add_f32 v93, v93, v101\n" \
  "v_add_f32 v94, v94, v102\n" \
  "v_add_f32 v95, v95, v103\n" \
  "v_max3_f32 v116, v92, v93, v94\n" \
  "v_max_f32 v116, v116, v95\n" \
  "s_cbranch_vccz .LX_skippollA%=\n" \
  "s_nop 0\n" \
  "v_max_f32_dpp v116, v116, v116 quad_perm:[1,0,3,2] row_mask:0xf bank_mask:0xf bound_ctrl:1\n" \
  "s_nop 1\n" \
  "v_max_f32_dpp v116, v116, v116 row_ror:8 row_mask:0xf bank_mask:0xf bound_ctrl:1\n" \
  "v_mov_b32 v117, v116\n" \
  "s_nop 1\n" \
  "v_permlane16_swap_b32 v117, v116\n" \
  "v_max_f32 v116, v116, v117\n" \
  "s_mov_b64 exec, vcc\n" \
  "v_sub_f32 v92, v92, v116\n" \
  "v_sub_f32 v93, v93, v116\n" \
  "v_sub_f32 v94, v94, v116\n" \
  "v_sub_f32 v95, v95, v116\n" \
  "v_exp_f32 v92, v92\n" \
  "v_exp_f32 v93, v93\n" \
  "v_exp_f32 v94, v94\n" \
  "v_exp_f32 v95, v95\n" \
  "v_cvt_pk_bf16_f32 v118, v92, v93\n" \
  "v_cvt_pk_bf16_f32 v119, v94, v95\n" \
  "v_min_u32 v118, -2, v118\n" \
  "v_min_u32 v119, -2, v119\n" \
  "buffer_store_dwordx2 v[118:119], %[oE], %[rs], s82 offen sc1\n" \
  "buffer_store_dwordx2 v[118:119], %[oE], %[rs], s83 offen sc1\n" \
  "v_min_u32 v116, -2, v116\n" \
  "buffer_store_dword v116, v111, %[rs], s41 offen sc1\n" \
  "s_mov_b64 exec, -1\n" \
  ".LX_skippollA%=:\n" \
  ".LX_off%=:\n" \
  "s_barrier\n" \
  "buffer_store_dword v124, v123, %[rs], s95 offen sc1\n" \
  "s_cmp_eq_u32 s57, 0\n" \
  "s_cbranch_scc1 .LX_rdy%=\n" \
  "s_waitcnt vmcnt(2)\n" \
  "v_readlane_b32 s45, v164, 0\n" \
  "v_readlane_b32 s46, v164, 1\n" \
  "v_readlane_b32 s47, v164, 2\n" \
  "v_readlane_b32 s48, v164, 3\n" \
  "s_min_i32 s45, s45, s46\n" \
  "s_min_i32 s47, s47, s48\n" \
  "s_min_i32 s45, s45, s47\n" \
  "s_cmp_ge_i32 s45, s55\n" \
  "s_cbranch_scc1 .LX_wp_ok%=\n" \
  "s_mov_b32 s49, 262144\n" \
  ".LX_wp%=:\n" \
  "buffer_load_dword v164, %[oPrV], %[rs], 0 offen sc1\n" \
  "s_sub_u32 s49, s49, 1\n" \
  "s_cbranch_scc1 .LX_abort%=\n" \
  "s_waitcnt vmcnt(0)\n" \
  "v_readlane_b32 s45, v164, 0\n" \
  "v_readlane_b32 s46, v164, 1\n" \
  "v_readlane_b32 s47, v164, 2\n" \
  "v_readlane_b32 s48, v164, 3\n" \
  "s_min_i32 s45, s45, s46\n" \
  "s_min_i32 s47, s47, s48\n" \
  "s_min_i32 s45, s45, s47\n" \
  "s_cmp_lt_i32 s45, s55\n" \
  "s_cbranch_scc1 .LX_wp%=\n" \
  ".LX_wp_ok%=:\n" \
  "buffer_load_dwordx4 v[48:51], %[l16], %[rs], s52 offen sc1\n" \
  "buffer_load_dwordx4 v[56:59], %[l16], %[rs], s52 offen offset:1024 sc1\n" \
  "buffer_load_dwordx4 v[52:55], %[l16], %[rs], s52 offen offset:2048 sc1\n" \
  "buffer_load_dwordx4 v[60:63], %[l16], %[rs], s52 offen offset:3072 sc1\n" \
  "buffer_load_dwordx4 v[64:67], %[l16b], %[rs], s53 offen sc1\n" \
  "buffer_load_dwordx4 v[72:75], %[l16b], %[rs], s53 offen offset:1024 sc1\n" \
  "buffer_load_dwordx4 v[68:71], %[l16b], %[rs], s53 offen offset:2048 sc1\n" \
  "buffer_load_dwordx4 v[76:79], %[l16b], %[rs], s53 offen offset:3072 sc1\n" \
  "s_branch .LX_rd2%=\n" \
  ".LX_rdy%=:\n" \
  "buffer_load_dwordx4 v[52:55], %[l16], %[rs], s52 offen offset:2048 sc1\n" \
  "buffer_load_dwordx4 v[60:63], %[l16], %[rs], s52 offen offset:3072 sc1\n" \
  "buffer_load_dwordx4 v[64:67], %[l16b], %[rs], s53 offen sc1\n" \
  "buffer_load_dwordx4 v[72:75], %[l16b], %[rs], s53 offen offset:1024 sc1\n" \
  "buffer_load_dwordx4 v[68:71], %[l16b], %[rs], s53 offen offset:2048 sc1\n" \
  "buffer_load_dwordx4 v[76:79], %[l16b], %[rs], s53 offen offset:3072 sc1\n" \
  "s_waitcnt vmcnt(8)\n" \
  "v_readlane_b32 s45, v164, 0\n" \
  "v_readlane_b32 s46, v164, 1\n" \
  "v_readlane_b32 s47, v164, 2\n" \
  "v_readlane_b32 s48, v164, 3\n" \
  "s_min_i32 s45, s45, s46\n" \
  "s_min_i32 s47, s47, s48\n" \
  "s_min_i32 s45, s45, s47\n" \
  "v_mov_b32 v48, v168\n" \
  "v_mov_b32 v49, v169\n" \
  "v_mov_b32 v50, v170\n" \
  "v_mov_b32 v51, v171\n" \
  "v_mov_b32 v56, v176\n" \
  "v_mov_b32 v57, v177\n" \
  "v_mov_b32 v58, v178\n" \
  "v_mov_b32 v59, v179\n" \
  ".LX_rd2%=:\n" \
  "s_add_u32 s40, s40, 1\n" \
  "s_add_u32 s41, s41, 262144\n" \
  "s_cmp_lt_u32 s40, 63\n" \
  "s_cbranch_scc1 .LX_top%=\n" \
  "s_branch .LX_end%=\n" \
  ".LX_cdly%=:\n" \
  "s_sub_u32 s96, s96, 1\n" \
  "s_cmp_gt_i32 s96, 0\n" \
  "s_cbranch_scc1 .LX_cdly%=\n" \
  "s_branch .LX_cbar%=\n" \
  ".LX_abort%=:\n" \
  "s_waitcnt vmcnt(0) lgkmcnt(0)\n" \
  "s_mov_b64 exec, -1\n" \
  "v_mov_b32 v108, 1\n" \
  "v_mov_b32 v109, 0x2913000\n" \
  "buffer_store_dword v108, v109, %[rs], 0 offen sc1\n" \
  ".LX_end%=:\n" \
  "s_waitcnt vmcnt(0) lgkmcnt(0)\n" \
  ".LX_zend%=:\n" \
  ""
#define ASM_X_CLOB "memory", "scc", "vcc", "s40", "s41", "s42", "s43", "s44", "s45", "s46", "s47", "s48", "s49", "s52", "s53", "s54", "s55", "s57", "s76", "s77", "s82", "s83", "s86", "s87", "s88", "s94", "s95", "s96", "s97", "v30", "v32", "v33", "v34", "v35", "v36", "v37", "v38", "v39", "v40", "v41", "v42", "v43", "v44", "v45", "v46", "v47", "v48", "v49", "v50", "v51", "v52", "v53", "v54", "v55", "v56", "v57", "v58", "v59", "v60", "v61", "v62", "v63", "v64", "v65", "v66", "v67", "v68", "v69", "v70", "v71", "v72", "v73", "v74", "v75", "v76", "v77", "v78", "v79", "v80", "v81", "v82", "v83", "v84", "v85", "v86", "v87", "v88", "v92", "v93", "v94", "v95", "v96", "v97", "v98", "v99", "v100", "v101", "v102", "v103", "v104", "v105", "v106", "v107", "v108", "v109", "v111", "v112", "v113", "v114", "v116", "v117", "v118", "v119", "v123", "v124", "v156", "v157", "v158", "v159", "v164", "v165", "v166", "v168", "v169", "v170", "v171", "v176", "v177", "v178", "v179", "v208", "v209", "v210", "v211", "v212", "v213", "v214", "v215", "v216", "v217", "v218", "v219", "v220", "v221", "v222", "v223", "v242", "v243", "v244"
#define ASM_C \
  "s_nop 4\n" \
  "buffer_load_dwordx4 v[32:35], %[oM0], %[rs], 0 offen sc1\n" \
  "buffer_load_dwordx4 v[36:39], %[oM1], %[rs], 0 offen sc1\n" \
  "buffer_load_dwordx4 v[128:131], %[l16], %[rs], %[sV] offen sc1\n" \
  "buffer_load_dwordx4 v[132:135], %[l16], %[rs], %[sV] offen offset:1024 sc1\n" \
  "buffer_load_dwordx4 v[136:139], %[l16], %[rs], %[sV] offen offset:2048 sc1\n" \
  "buffer_load_dwordx4 v[140:143], %[l16], %[rs], %[sV] offen offset:3072 sc1\n" \
  "buffer_load_dwordx4 v[208:211], %[oCal], %[rs], 0 offen\n" \
  "buffer_load_dwordx4 v[212:215], %[oCal], %[rs], 0 offen offset:128\n" \
  "buffer_load_dwordx4 v[216:219], %[oCal], %[rs], 0 offen offset:256\n" \
  "buffer_load_dwordx4 v[220:223], %[oCal], %[rs], 0 offen offset:384\n" \
  "s_add_u32 s46, %[sW], 0\n" \
  "buffer_load_dwordx4 a[0:3], %[l16], %[rs], s46 offen\n" \
  "buffer_load_dwordx4 a[4:7], %[l16], %[rs], s46 offen offset:1024\n" \
  "buffer_load_dwordx4 a[8:11], %[l16], %[rs], s46 offen offset:2048\n" \
  "buffer_load_dwordx4 a[12:15], %[l16], %[rs], s46 offen offset:3072\n" \
  "s_add_u32 s46, %[sW], 4096\n" \
  "buffer_load_dwordx4 a[16:19], %[l16], %[rs], s46 offen\n" \
  "buffer_load_dwordx4 a[20:23], %[l16], %[rs], s46 offen offset:1024\n" \
  "buffer_load_dwordx4 a[24:27], %[l16], %[rs], s46 offen offset:2048\n" \
  "buffer_load_dwordx4 a[28:31], %[l16], %[rs], s46 offen offset:3072\n" \
  "s_add_u32 s46, %[sW], 8192\n" \
  "buffer_load_dwordx4 a[32:35], %[l16], %[rs], s46 offen\n" \
  "buffer_load_dwordx4 a[36:39], %[l16], %[rs], s46 offen offset:1024\n" \
  "buffer_load_dwordx4 a[40:43], %[l16], %[rs], s46 offen offset:2048\n" \
  "buffer_load_dwordx4 a[44:47], %[l16], %[rs], s46 offen offset:3072\n" \
  "s_add_u32 s46, %[sW], 12288\n" \
  "buffer_load_dwordx4 a[48:51], %[l16], %[rs], s46 offen\n" \
  "buffer_load_dwordx4 a[52:55], %[l16], %[rs], s46 offen offset:1024\n" \
  "s_add_u32 s46, %[sW], 401408\n" \
  "buffer_load_dwordx4 a[56:59], %[l16], %[rs], s46 offen\n" \
  "buffer_load_dwordx4 a[60:63], %[l16], %[rs], s46 offen offset:1024\n" \
  "buffer_load_dwordx4 a[64:67], %[l16], %[rs], s46 offen offset:2048\n" \
  "buffer_load_dwordx4 a[68:71], %[l16], %[rs], s46 offen offset:3072\n" \
  "s_add_u32 s46, %[sW], 405504\n" \
  "buffer_load_dwordx4 a[72:75], %[l16], %[rs], s46 offen\n" \
  "buffer_load_dwordx4 a[76:79], %[l16], %[rs], s46 offen offset:1024\n" \
  "buffer_load_dwordx4 a[80:83], %[l16], %[rs], s46 offen offset:2048\n" \
  "buffer_load_dwordx4 a[84:87], %[l16], %[rs], s46 offen offset:3072\n" \
  "s_add_u32 s46, %[sW], 409600\n" \
  "buffer_load_dwordx4 a[88:91], %[l16], %[rs], s46 offen\n" \
  "buffer_load_dwordx4 a[92:95], %[l16], %[rs], s46 offen offset:1024\n" \
  "buffer_load_dwordx4 a[96:99], %[l16], %[rs], s46 offen offset:2048\n" \
  "buffer_load_dwordx4 a[100:103], %[l16], %[rs], s46 offen offset:3072\n" \
  "s_add_u32 s46, %[sW], 413696\n" \
  "buffer_load_dwordx4 a[104:107], %[l16], %[rs], s46 offen\n" \
  "buffer_load_dwordx4 a[108:111], %[l16], %[rs], s46 offen offset:1024\n" \
  "s_add_u32 s46, %[sW], 802816\n" \
  "buffer_load_dwordx4 a[112:115], %[l16], %[rs], s46 offen\n" \
  "buffer_load_dwordx4 a[116:119], %[l16], %[rs], s46 offen offset:1024\n" \
  "buffer_load_dwordx4 a[120:123], %[l16], %[rs], s46 offen offset:2048\n" \
  "buffer_load_dwordx4 a[124:127], %[l16], %[rs], s46 offen offset:3072\n" \
  "s_add_u32 s46, %[sW], 806912\n" \
  "buffer_load_dwordx4 a[128:131], %[l16], %[rs], s46 offen\n" \
  "buffer_load_dwordx4 a[132:135], %[l16], %[rs], s46 offen offset:1024\n" \
  "buffer_load_dwordx4 a[136:139], %[l16], %[rs], s46 offen offset:2048\n" \
  "buffer_load_dwordx4 a[140:143], %[l16], %[rs], s46 offen offset:3072\n" \
  "s_add_u32 s46, %[sW], 811008\n" \
  "buffer_load_dwordx4 a[144:147], %[l16], %[rs], s46 offen\n" \
  "buffer_load_dwordx4 a[148:151], %[l16], %[rs], s46 offen offset:1024\n" \
  "buffer_load_dwordx4 a[152:155], %[l16], %[rs], s46 offen offset:2048\n" \
  "buffer_load_dwordx4 a[156:159], %[l16], %[rs], s46 offen offset:3072\n" \
  "s_add_u32 s46, %[sW], 815104\n" \
  "buffer_load_dwordx4 a[160:163], %[l16], %[rs], s46 offen\n" \
  "buffer_load_dwordx4 a[164:167], %[l16], %[rs], s46 offen offset:1024\n" \
  "s_add_u32 s46, %[sW], 1204224\n" \
  "buffer_load_dwordx4 a[168:171], %[l16], %[rs], s46 offen\n" \
  "buffer_load_dwordx4 a[172:175], %[l16], %[rs], s46 offen offset:1024\n" \
  "buffer_load_dwordx4 a[176:179], %[l16], %[rs], s46 offen offset:2048\n" \
  "buffer_load_dwordx4 a[180:183], %[l16], %[rs], s46 offen offset:3072\n" \
  "s_add_u32 s46, %[sW], 1208320\n" \
  "buffer_load_dwordx4 a[184:187], %[l16], %[rs], s46 offen\n" \
  "buffer_load_dwordx4 a[188:191], %[l16], %[rs], s46 offen offset:1024\n" \
  "buffer_load_dwordx4 a[192:195], %[l16], %[rs], s46 offen offset:2048\n" \
  "buffer_load_dwordx4 a[196:199], %[l16], %[rs], s46 offen offset:3072\n" \
  "s_add_u32 s46, %[sW], 1212416\n" \
  "buffer_load_dwordx4 a[200:203], %[l16], %[rs], s46 offen\n" \
  "buffer_load_dwordx4 a[204:207], %[l16], %[rs], s46 offen offset:1024\n" \
  "buffer_load_dwordx4 a[208:211], %[l16], %[rs], s46 offen offset:2048\n" \
  "buffer_load_dwordx4 a[212:215], %[l16], %[rs], s46 offen offset:3072\n" \
  "s_add_u32 s46, %[sW], 1216512\n" \
  "buffer_load_dwordx4 a[216:219], %[l16], %[rs], s46 offen\n" \
  "buffer_load_dwordx4 a[220:223], %[l16], %[rs], s46 offen offset:1024\n" \
  ".LC_w1%=:\n" \
  "s_waitcnt vmcnt(56)\n" \
  "v_add3_u32 v208, v208, v212, v216\n" \
  "v_add_u32 v208, v208, v220\n" \
  "v_lshl_or_b32 v208, v208, 2, 0\n" \
  "v_add3_u32 v209, v209, v213, v217\n" \
  "v_add_u32 v209, v209, v221\n" \
  "v_lshl_or_b32 v209, v209, 2, 1\n" \
  "v_add3_u32 v210, v210, v214, v218\n" \
  "v_add_u32 v210, v210, v222\n" \
  "v_lshl_or_b32 v210, v210, 2, 2\n" \
  "v_add3_u32 v211, v211, v215, v219\n" \
  "v_add_u32 v211, v211, v223\n" \
  "v_lshl_or_b32 v211, v211, 2, 3\n" \
  "v_min3_u32 v208, v208, v209, v210\n" \
  "v_min_u32 v208, v208, v211\n" \
  "v_and_b32 v208, 3, v208\n" \
  "v_lshlrev_b32 v208, 13, v208\n" \
  "v_lshlrev_b32 v209, 9, %[l16]\n" \
  "v_and_b32 v209, 0x4000, v209\n" \
  "v_xor_b32 v30, v208, v209\n" \
  "v_mov_b32 v240, -1\n" \
  "s_cmp_eq_u32 %[w], 2\n" \
  "s_cselect_b64 vcc, -1, 0\n" \
  "v_cndmask_b32 v238, %[oO0], %[oO1], vcc\n" \
  "s_cmp_eq_u32 %[w], 3\n" \
  "s_cselect_b32 s46, 0x7ff00000, 0\n" \
  "v_or_b32 v238, s46, v238\n" \
  "s_cmp_eq_u32 %[w], 0\n" \
  "s_cselect_b32 s92, -1, 0\n" \
  "v_mov_b32 v189, 1\n" \
  "v_mov_b32 v190, 0x7ff00000\n" \
  "v_mov_b32 v96, 0x3f803f80\n" \
  "v_mov_b32 v97, 0x3f803f80\n" \
  "v_mov_b32 v98, 0x3f803f80\n" \
  "v_mov_b32 v99, 0x3f803f80\n" \
  "v_mov_b32 v226, 0\n" \
  "v_mov_b32 v227, 0\n" \
  "v_mov_b32 v230, 0\n" \
  "v_mov_b32 v231, 0\n" \
  "s_mov_b32 s56, 0xffff\n" \
  "s_mov_b32 s57, 0\n" \
  "s_mov_b32 s58, 0xffff0000\n" \
  "s_mov_b32 s59, 0\n" \
  "s_mov_b32 s60, 0\n" \
  "s_mov_b32 s61, 0xffff\n" \
  "s_mov_b32 s62, 0\n" \
  "s_mov_b32 s63, 0xffff0000\n" \
  "s_mov_b32 s40, 0\n" \
  "s_mov_b32 s41, 0\n" \
  "s_mov_b32 s52, %[sU]\n" \
  "s_mov_b32 s45, 2\n" \
  "s_mov_b32 s97, 0\n" \
  "s_mov_b32 s96, 0\n" \
  "v_readlane_b32 s86, v30, 0\n" \
  "s_nop 1\n" \
  "v_xor_b32 v235, s86, %[cOut]\n" \
  "s_xor_b32 s86, s86, %[side]\n" \
  "v_readlane_b32 s88, v30, 0\n" \
  "s_nop 0\n" \
  "s_and_b32 s88, s88, 8192\n" \
  "s_xor_b32 s88, s88, 8192\n" \
  "s_xor_b32 s88, s88, 8192\n" \
  "v_min_u32 v235, v235, %[oPr]\n" \
  "s_cmp_eq_u32 %[w], 0\n" \
  "s_cselect_b64 vcc, -1, 0\n" \
  "v_cndmask_b32 v238, v238, v235, vcc\n" \
  "v_cmp_gt_u32 vcc, 64, %[l16]\n" \
  "s_nop 1\n" \
  "v_cndmask_b32 v235, 0, 1, vcc\n" \
  "v_mov_b32 v237, 0x4444\n" \
  "v_mov_b32 v186, 0xeeee\n" \
  "s_nop 1\n" \
  "v_mov_b32_dpp v237, v186 quad_perm:[0,1,2,3] row_mask:0xf bank_mask:0xa\n" \
  "v_mov_b32 v241, 2\n" \
  ".LC_top%=:\n" \
  "s_add_u32 s84, s40, 1\n" \
  "s_min_u32 s54, s84, 63\n" \
  "v_readlane_b32 s45, v241, 0\n" \
  "v_readlane_b32 s46, v241, 1\n" \
  "v_readlane_b32 s47, v241, 2\n" \
  "v_readlane_b32 s48, v241, 3\n" \
  "s_min_i32 s45, s45, s46\n" \
  "s_min_i32 s47, s47, s48\n" \
  "s_min_i32 s45, s45, s47\n" \
  "s_cmp_ge_i32 s45, s54\n" \
  "s_cbranch_scc1 .LC_wp_ok%=\n" \
  "s_mov_b32 s49, 262144\n" \
  ".LC_wp%=:\n" \
  "buffer_load_dword v241, %[oPrV], %[rs], 0 offen sc1\n" \
  "s_sub_u32 s49, s49, 1\n" \
  "s_cbranch_scc1 .LC_abort%=\n" \
  "s_waitcnt vmcnt(0)\n" \
  "v_readlane_b32 s45, v241, 0\n" \
  "v_readlane_b32 s46, v241, 1\n" \
  "v_readlane_b32 s47, v241, 2\n" \
  "v_readlane_b32 s48, v241, 3\n" \
  "s_min_i32 s45, s45, s46\n" \
  "s_min_i32 s47, s47, s48\n" \
  "s_min_i32 s45, s45, s47\n" \
  "s_cmp_lt_i32 s45, s54\n" \
  "s_cbranch_scc1 .LC_wp%=\n" \
  ".LC_wp_ok%=:\n" \
  "v_readlane_b32 s50, v30, s54\n" \
  "v_readlane_b32 s42, v30, s40\n" \
  "s_mov_b32 s44, 262144\n" \
  "s_xor_b32 s43, s42, %[side]\n" \
  "s_add_u32 s43, s43, s41\n" \
  "s_lshl_b32 s49, s40, 12\n" \
  "s_and_b32 s49, s49, 8192\n" \
  "s_xor_b32 s49, s49, s88\n" \
  "s_cmp_lt_u32 s40, 2\n" \
  "s_cselect_b32 s49, 0, s49\n" \
  "s_lshl_b32 s53, s40, 20\n" \
  "s_add_u32 s53, s53, %[sV]\n" \
  "s_xor_b32 s53, s53, s49\n" \
  "s_add_u32 s55, s41, 262144\n" \
  "v_xor_b32 v194, s50, %[oO0]\n" \
  "v_xor_b32 v195, s50, %[oO1]\n" \
  "s_barrier\n" \
  "s_add_u32 s76, s40, 3\n" \
  "s_and_b32 s77, s76, 63\n" \
  "v_readlane_b32 s77, v30, s77\n" \
  "s_min_u32 s76, s76, 64\n" \
  "s_lshl_b32 s76, s76, 18\n" \
  "s_andn2_b32 s76, s76, s92\n" \
  "s_andn2_b32 s77, s77, s92\n" \
  "s_not_b32 s46, s92\n" \
  "v_xor_b32 v239, s77, v238\n" \
  "v_add_u32 v236, s40, v235\n" \
  "v_or_b32 v236, s46, v236\n" \
  "s_sub_u32 s97, s97, 1\n" \
  "s_max_i32 s97, s97, 0\n" \
  "s_lshr_b32 s96, s97, 4\n" \
  "v_mov_b32 v104, 0\n" \
  "v_mov_b32 v105, 0\n" \
  "v_mov_b32 v106, 0\n" \
  "v_mov_b32 v107, 0\n" \
  "v_mov_b32 v108, 0\n" \
  "v_mov_b32 v109, 0\n" \
  "v_mov_b32 v110, 0\n" \
  "v_mov_b32 v111, 0\n" \
  "v_mov_b32 v112, 0\n" \
  "v_mov_b32 v113, 0\n" \
  "v_mov_b32 v114, 0\n" \
  "v_mov_b32 v115, 0\n" \
  "v_mov_b32 v116, 0\n" \
  "v_mov_b32 v117, 0\n" \
  "v_mov_b32 v118, 0\n" \
  "v_mov_b32 v119, 0\n" \
  "s_cmp_eq_u32 s40, 0\n" \
  "s_cbranch_scc1 .LC_first%=\n" \
  "s_nop 15\n" \
  "s_nop 2\n" \
  "buffer_load_dwordx4 v[32:35], %[oM0], %[rs], s43 offen sc1\n" \
  "buffer_load_dwordx4 v[36:39], %[oM1], %[rs], s43 offen sc1\n" \
  "s_nop 15\n" \
  "buffer_load_dwordx4 v[128:131], %[l16], %[rs], s53 offen sc1\n" \
  "buffer_load_dwordx4 v[132:135], %[l16], %[rs], s53 offen offset:1024 sc1\n" \
  "buffer_load_dwordx4 v[136:139], %[l16], %[rs], s53 offen offset:2048 sc1\n" \
  "buffer_load_dwordx4 v[140:143], %[l16], %[rs], s53 offen offset:3072 sc1\n" \
  "s_add_u32 s46, s54, 3\n" \
  "s_min_u32 s46, s46, 64\n" \
  "s_cmp_ge_i32 s45, s46\n" \
  "s_cselect_b64 exec, 0, 15\n" \
  "buffer_load_dword v241, %[oPrV], %[rs], 0 offen sc1\n" \
  "s_mov_b64 exec, -1\n" \
  "s_waitcnt vmcnt(5)\n" \
  ".LC_mA%=:\n" \
  "v_max3_u32 v186, v32, v33, v34\n" \
  "v_max3_u32 v187, v35, v36, v37\n" \
  "v_max3_u32 v186, v186, v38, v39\n" \
  "v_max_u32 v186, v186, v187\n" \
  "v_cmp_eq_u32 vcc, -1, v186\n" \
  "s_cbranch_vccnz .LC_mfA%=\n" \
  "ds_write_b128 %[wrM], v[32:35]\n" \
  "ds_write_b128 %[wrM], v[36:39] offset:1024\n" \
  "s_waitcnt lgkmcnt(0)\n" \
  "s_barrier\n" \
  "ds_read_b128 v[48:51], %[rdM]\n" \
  "ds_read_b128 v[52:55], %[rdM] offset:1024\n" \
  "ds_read_b128 v[56:59], %[rdM] offset:2048\n" \
  "ds_read_b128 v[60:63], %[rdM] offset:3072\n" \
  "ds_read_b128 v[64:67], %[rdM] offset:4096\n" \
  "ds_read_b128 v[68:71], %[rdM] offset:5120\n" \
  "ds_read_b128 v[72:75], %[rdM] offset:6144\n" \
  ".LC_wok%=:\n" \
  "s_waitcnt lgkmcnt(6)\n" \
  "v_smfmac_f32_16x16x64_bf16 v[104:107], v[48:51], a[0:7], v237\n" \
  "v_smfmac_f32_16x16x64_bf16 v[108:111], v[48:51], a[56:63], v237\n" \
  "v_smfmac_f32_16x16x64_bf16 v[112:115], v[48:51], a[112:119], v237\n" \
  "v_smfmac_f32_16x16x64_bf16 v[116:119], v[48:51], a[168:175], v237\n" \
  "s_waitcnt lgkmcnt(5)\n" \
  "v_smfmac_f32_16x16x64_bf16 v[104:107], v[52:55], a[8:15], v237\n" \
  "v_smfmac_f32_16x16x64_bf16 v[108:111], v[52:55], a[64:71], v237\n" \
  "v_smfmac_f32_16x16x64_bf16 v[112:115], v[52:55], a[120:127], v237\n" \
  "v_smfmac_f32_16x16x64_bf16 v[116:119], v[52:55], a[176:183], v237\n" \
  "s_waitcnt lgkmcnt(4)\n" \
  "v_smfmac_f32_16x16x64_bf16 v[104:107], v[56:59], a[16:23], v237\n" \
  "v_smfmac_f32_16x16x64_bf16 v[108:111], v[56:59], a[72:79], v237\n" \
  "buffer_load_dword v242, %[cIn], %[rs], s86 offen sc1\n" \
  "v_smfmac_f32_16x16x64_bf16 v[112:115], v[56:59], a[128:135], v237\n" \
  "v_smfmac_f32_16x16x64_bf16 v[116:119], v[56:59], a[184:191], v237\n" \
  "s_waitcnt lgkmcnt(3)\n" \
  "v_smfmac_f32_16x16x64_bf16 v[104:107], v[60:63], a[24:31], v237\n" \
  "v_smfmac_f32_16x16x64_bf16 v[108:111], v[60:63], a[80:87], v237\n" \
  "v_smfmac_f32_16x16x64_bf16 v[112:115], v[60:63], a[136:143], v237\n" \
  "buffer_load_dword v243, %[cIn], %[rs], s86 offen sc1\n" \
  "v_smfmac_f32_16x16x64_bf16 v[116:119], v[60:63], a[192:199], v237\n" \
  "s_waitcnt lgkmcnt(2)\n" \
  "v_smfmac_f32_16x16x64_bf16 v[104:107], v[64:67], a[32:39], v237\n" \
  "v_smfmac_f32_16x16x64_bf16 v[108:111], v[64:67], a[88:95], v237\n" \
  "v_smfmac_f32_16x16x64_bf16 v[112:115], v[64:67], a[144:151], v237\n" \
  "v_smfmac_f32_16x16x64_bf16 v[116:119], v[64:67], a[200:207], v237\n" \
  "s_cmp_lg_u32 %[w], 0\n" \
  "s_cbranch_scc1 .LC_cbar%=\n" \
  "s_mov_b64 exec, 15\n" \
  ".LC_cw%=:\n" \
  "buffer_load_dword v244, %[cIn], %[rs], s86 offen sc1\n" \
  "s_sub_u32 s44, s44, 1\n" \
  "s_cbranch_scc1 .LC_abort%=\n" \
  "s_waitcnt vmcnt(2)\n" \
  "v_cmp_gt_i32 vcc, s54, v242\n" \
  "s_cbranch_vccz .LC_cok%=\n" \
  "buffer_load_dword v242, %[cIn], %[rs], s86 offen sc1\n" \
  "s_waitcnt vmcnt(2)\n" \
  "v_cmp_gt_i32 vcc, s54, v243\n" \
  "s_cbranch_vccz .LC_cok%=\n" \
  "buffer_load_dword v243, %[cIn], %[rs], s86 offen sc1\n" \
  "s_waitcnt vmcnt(2)\n" \
  "v_cmp_gt_i32 vcc, s54, v244\n" \
  "s_cbranch_vccnz .LC_cw%=\n" \
  ".LC_cok%=:\n" \
  "s_cmp_gt_i32 s96, 0\n" \
  "s_cbranch_scc1 .LC_cdly%=\n" \
  ".LC_cbar%=:\n" \
  "s_barrier\n" \
  "s_mov_b64 exec, -1\n" \
  "s_waitcnt lgkmcnt(1)\n" \
  "v_smfmac_f32_16x16x64_bf16 v[104:107], v[68:71], a[40:47], v237\n" \
  "buffer_load_dwordx4 v[144:147], %[oE], %[rs], s43 offen sc1\n" \
  "buffer_load_dword v148, %[oT], %[rs], s43 offen sc1\n" \
  "v_smfmac_f32_16x16x64_bf16 v[108:111], v[68:71], a[96:103], v237\n" \
  "v_smfmac_f32_16x16x64_bf16 v[112:115], v[68:71], a[152:159], v237\n" \
  "v_smfmac_f32_16x16x64_bf16 v[116:119], v[68:71], a[208:215], v237\n" \
  "s_waitcnt lgkmcnt(0)\n" \
  "v_smfmac_f32_16x16x64_bf16 v[104:107], v[72:75], a[48:55], v237\n" \
  "v_smfmac_f32_16x16x64_bf16 v[108:111], v[72:75], a[104:111], v237\n" \
  "v_smfmac_f32_16x16x64_bf16 v[112:115], v[72:75], a[160:167], v237\n" \
  "v_smfmac_f32_16x16x64_bf16 v[116:119], v[72:75], a[216:223], v237\n" \
  "buffer_load_dwordx4 v[152:155], %[oS], %[rs], s43 offen sc1\n" \
  "buffer_load_dwordx4 v[156:159], %[oS], %[rs], s43 offen offset:512 sc1\n" \
  "s_nop 3\n" \
  "s_nop 3\n" \
  "v_permlane16_swap_b32 v104, v108\n" \
  "v_permlane16_swap_b32 v105, v109\n" \
  "v_permlane16_swap_b32 v106, v110\n" \
  "v_permlane16_swap_b32 v107, v111\n" \
  "v_permlane16_swap_b32 v112, v116\n" \
  "v_permlane16_swap_b32 v113, v117\n" \
  "v_permlane16_swap_b32 v114, v118\n" \
  "v_permlane16_swap_b32 v115, v119\n" \
  "v_add_f32 v104, v104, v108\n" \
  "v_add_f32 v105, v105, v109\n" \
  "v_add_f32 v106, v106, v110\n" \
  "v_add_f32 v107, v107, v111\n" \
  "v_add_f32 v112, v112, v116\n" \
  "v_add_f32 v113, v113, v117\n" \
  "v_add_f32 v114, v114, v118\n" \
  "v_add_f32 v115, v115, v119\n" \
  "v_cvt_pk_bf16_f32 v124, v104, v105\n" \
  "v_cvt_pk_bf16_f32 v125, v106, v107\n" \
  "v_cvt_pk_bf16_f32 v126, v112, v113\n" \
  "v_cvt_pk_bf16_f32 v127, v114, v115\n" \
  "s_nop 0\n" \
  "v_permlane32_swap_b32 v124, v126\n" \
  "v_permlane32_swap_b32 v125, v127\n" \
  "v_mov_b32 v120, v124\n" \
  "v_mov_b32 v121, v125\n" \
  "v_mov_b32 v122, v126\n" \
  "v_mov_b32 v123, v127\n" \
  "s_nop 0\n" \
  "v_permlane16_swap_b32 v120, v122\n" \
  "v_permlane16_swap_b32 v121, v123\n" \
  "s_cmp_eq_u32 s40, 63\n" \
  "s_cbranch_scc1 .LC_fin%=\n" \
  "buffer_store_dwordx4 v[120:123], %[oU], %[rs], s52 offen sc1\n" \
  "s_cmp_eq_u32 s40, 0\n" \
  "s_cbranch_scc1 .LC_usent%=\n" \
  ".LC_usb%=:\n" \
  "s_waitcnt vmcnt(4)\n" \
  ".LC_swait%=:\n" \
  "v_mfma_f32_16x16x32_bf16 v[168:171], v[144:147], v[128:131], 0\n" \
  "v_mfma_f32_16x16x32_bf16 v[172:175], v[144:147], v[132:135], 0\n" \
  "v_mfma_f32_16x16x32_bf16 v[176:179], v[144:147], v[136:139], 0\n" \
  "v_mfma_f32_16x16x32_bf16 v[180:183], v[144:147], v[140:143], 0\n" \
  "v_mfma_f32_16x16x32_bf16 v[92:95], v[144:147], v[96:99], 0\n" \
  "buffer_store_dword v236, v239, %[rs], s76 offen sc1\n" \
  "s_nop 2\n" \
  "v_cvt_pk_bf16_f32 v224, v168, v169\n" \
  "v_cvt_pk_bf16_f32 v225, v170, v171\n" \
  "s_mov_b64 exec, s[58:59]\n" \
  "v_cvt_pk_bf16_f32 v224, v172, v173\n" \
  "v_cvt_pk_bf16_f32 v225, v174, v175\n" \
  "s_mov_b64 exec, s[60:61]\n" \
  "v_cvt_pk_bf16_f32 v224, v176, v177\n" \
  "v_cvt_pk_bf16_f32 v225, v178, v179\n" \
  "s_mov_b64 exec, s[62:63]\n" \
  "v_cvt_pk_bf16_f32 v224, v180, v181\n" \
  "v_cvt_pk_bf16_f32 v225, v182, v183\n" \
  "s_mov_b64 exec, -1\n" \
  "v_mov_b32_dpp v92, v93 quad_perm:[0,1,2,3] row_mask:0xf bank_mask:0x2\n" \
  "s_nop 1\n" \
  "v_mov_b32_dpp v92, v94 quad_perm:[0,1,2,3] row_mask:0xf bank_mask:0x4\n" \
  "s_nop 1\n" \
  "v_mov_b32_dpp v92, v95 quad_perm:[0,1,2,3] row_mask:0xf bank_mask:0x8\n" \
  "s_waitcnt vmcnt(2)\n" \
  "v_add_f32 v198, v158, v154\n" \
  "v_add_f32 v199, v159, v155\n" \
  "v_add_f32 v196, v156, v152\n" \
  "v_add_f32 v197, v157, v153\n" \
  "s_waitcnt vmcnt(2)\n" \
  "v_add_f32_dpp v196, v198, v196 row_ror:8 row_mask:0xf bank_mask:0xf bound_ctrl:1\n" \
  "v_add_f32_dpp v197, v199, v197 row_ror:8 row_mask:0xf bank_mask:0xf bound_ctrl:1\n" \
  "v_max3_f32 v200, v196, v197, v148\n" \
  "s_nop 1\n" \
  "v_max_f32_dpp v200, v200, v200 quad_perm:[1,0,3,2] row_mask:0xf bank_mask:0xf bound_ctrl:1\n" \
  "s_nop 1\n" \
  "v_max_f32_dpp v200, v200, v200 quad_perm:[2,3,0,1] row_mask:0xf bank_mask:0xf bound_ctrl:1\n" \
  "v_sub_f32 v196, v196, v200\n" \
  "v_sub_f32 v197, v197, v200\n" \
  "v_sub_f32 v201, v148, v200\n" \
  "v_exp_f32 v196, v196\n" \
  "v_exp_f32 v197, v197\n" \
  "v_exp_f32 v201, v201\n" \
  "v_add_f32 v204, v196, v197\n" \
  "s_nop 1\n" \
  "v_add_f32_dpp v204, v204, v204 quad_perm:[1,0,3,2] row_mask:0xf bank_mask:0xf bound_ctrl:1\n" \
  "s_nop 1\n" \
  "v_add_f32_dpp v204, v204, v204 quad_perm:[2,3,0,1] row_mask:0xf bank_mask:0xf bound_ctrl:1\n" \
  "v_fmac_f32 v204, v201, v92\n" \
  "v_rcp_f32 v200, v204\n" \
  "v_cmp_u_f32 vcc, v204, v204\n" \
  "v_mul_f32 v196, v196, v200\n" \
  "v_mul_f32 v197, v197, v200\n" \
  "v_mul_f32 v201, v201, v200\n" \
  "v_cvt_pk_bf16_f32 v205, v196, v197\n" \
  "v_cvt_pk_bf16_f32 v201, v201, v201\n" \
  "v_and_b32 v228, v201, %[mk0]\n" \
  "v_and_b32 v229, v201, %[mk1]\n" \
  "v_mov_b32_dpp v208, v205 quad_perm:[0,0,0,0] row_mask:0xf bank_mask:0xf bound_ctrl:1\n" \
  "v_mov_b32_dpp v209, v205 quad_perm:[1,1,1,1] row_mask:0xf bank_mask:0xf bound_ctrl:1\n" \
  "v_mfma_f32_16x16x32_bf16 v[212:215], v[228:231], v[224:227], 0\n" \
  "v_mov_b32_dpp v210, v205 quad_perm:[2,2,2,2] row_mask:0xf bank_mask:0xf bound_ctrl:1\n" \
  "v_mov_b32_dpp v211, v205 quad_perm:[3,3,3,3] row_mask:0xf bank_mask:0xf bound_ctrl:1\n" \
  "s_nop 1\n" \
  "v_mfma_f32_16x16x32_bf16 v[212:215], v[208:211], v[124:127], v[212:215]\n" \
  "s_cbranch_vccnz .LC_failS%=\n" \
  ".LC_sgo%=:\n" \
  "s_nop 6\n" \
  "ds_write_b32 %[wrR], v212\n" \
  "ds_add_rtn_u32 v188, %[cnt], v189\n" \
  "ds_read_b128 v[216:219], %[rdR]\n" \
  "ds_read_b128 v[220:223], %[rdR] offset:16\n" \
  "s_waitcnt lgkmcnt(2)\n" \
  "v_and_b32 v188, 3, v188\n" \
  "v_cmpx_eq_u32 vcc, 3, v188\n" \
  "s_waitcnt lgkmcnt(1)\n" \
  "v_add_f32 v216, v216, v218\n" \
  "v_add_f32 v217, v217, v219\n" \
  "v_add_f32 v216, v216, v217\n" \
  "s_waitcnt lgkmcnt(0)\n" \
  "v_add_f32 v220, v220, v222\n" \
  "v_add_f32 v221, v221, v223\n" \
  "v_add_f32 v220, v220, v221\n" \
  "v_cvt_pk_bf16_f32 v216, v216, v220\n" \
  "v_min_u32 v216, -2, v216\n" \
  "s_cbranch_execz .LC_skipfailS%=\n" \
  "buffer_store_dword v216, v194, %[rs], s55 offen sc1\n" \
  "buffer_store_dword v216, v195, %[rs], s55 offen sc1\n" \
  ".LC_skipfailS%=:\n" \
  "s_mov_b64 exec, -1\n" \
  "s_add_u32 s40, s40, 1\n" \
  "s_mov_b32 s41, s55\n" \
  "s_add_u32 s52, s52, 114688\n" \
  "s_branch .LC_top%=\n" \
  ".LC_usent%=:\n" \
  "v_mov_b32 v216, -1\n" \
  "v_mov_b32 v217, -1\n" \
  "v_mov_b32 v218, -1\n" \
  "v_mov_b32 v219, -1\n" \
  "s_add_u32 s46, %[sU], 7225344\n" \
  "s_waitcnt vmcnt(1)\n" \
  "buffer_store_dwordx4 v[216:219], %[oU], %[rs], s46 offen sc1\n" \
  "s_branch .LC_usb%=\n" \
  ".LC_first%=:\n" \
  "ds_write_b128 %[wrM], v[32:35]\n" \
  "ds_write_b128 %[wrM], v[36:39] offset:1024\n" \
  "s_waitcnt lgkmcnt(0)\n" \
  "s_barrier\n" \
  "ds_read_b128 v[48:51], %[rdM]\n" \
  "ds_read_b128 v[52:55], %[rdM] offset:1024\n" \
  "ds_read_b128 v[56:59], %[rdM] offset:2048\n" \
  "ds_read_b128 v[60:63], %[rdM] offset:3072\n" \
  "ds_read_b128 v[64:67], %[rdM] offset:4096\n" \
  "ds_read_b128 v[68:71], %[rdM] offset:5120\n" \
  "ds_read_b128 v[72:75], %[rdM] offset:6144\n" \
  "s_waitcnt vmcnt(0)\n" \
  "s_branch .LC_wok%=\n" \
  ".LC_mfA%=:\n" \
  "buffer_load_dwordx4 v[32:35], %[oM0], %[rs], s43 offen sc1\n" \
  "buffer_load_dwordx4 v[36:39], %[oM1], %[rs], s43 offen sc1\n" \
  "s_sub_u32 s44, s44, 1\n" \
  "s_waitcnt vmcnt(0)\n" \
  "s_cbranch_scc0 .LC_mA%=\n" \
  "s_branch .LC_abort%=\n" \
  ".LC_failS%=:\n" \
  "v_max_u32 v186, v148, v152\n" \
  "v_max3_u32 v186, v186, v153, v154\n" \
  "v_max3_u32 v186, v186, v155, v156\n" \
  "v_max3_u32 v186, v186, v157, v158\n" \
  "v_max3_u32 v186, v186, v159, v144\n" \
  "v_max3_u32 v186, v186, v145, v146\n" \
  "v_max3_u32 v186, v186, v147, v144\n" \
  "v_cmp_eq_u32 vcc, -1, v186\n" \
  "s_cbranch_vccz .LC_sgo%=\n" \
  "buffer_load_dwordx4 v[144:147], %[oE], %[rs], s43 offen sc1\n" \
  "buffer_load_dword v148, %[oT], %[rs], s43 offen sc1\n" \
  "buffer_load_dwordx4 v[152:155], %[oS], %[rs], s43 offen sc1\n" \
  "buffer_load_dwordx4 v[156:159], %[oS], %[rs], s43 offen offset:512 sc1\n" \
  "s_add_u32 s97, s97, 16\n" \
  "s_min_u32 s97, s97, 96\n" \
  "s_sub_u32 s44, s44, 1\n" \
  "s_waitcnt vmcnt(0)\n" \
  "s_cbranch_scc0 .LC_swait%=\n" \
  "s_branch .LC_abort%=\n" \
  ".LC_fin%=:\n" \
  "v_min_u32 v120, -2, v120\n" \
  "v_min_u32 v121, -2, v121\n" \
  "v_min_u32 v122, -2, v122\n" \
  "v_min_u32 v123, -2, v123\n" \
  "buffer_store_dwordx4 v[120:123], %[oU], %[rs], s52 offen sc1\n" \
  "v_mov_b32 v186, 63\n" \
  "v_mov_b32 v187, 64\n" \
  "s_barrier\n" \
  "buffer_store_dword v186, %[oPr], %[rs], 0 offen sc1\n" \
  "s_waitcnt vmcnt(0)\n" \
  "s_barrier\n" \
  "buffer_store_dword v187, %[oPr], %[rs], 0 offen sc1\n" \
  "s_branch .LC_end%=\n" \
  ".LC_cdly%=:\n" \
  "s_sub_u32 s96, s96, 1\n" \
  "s_cmp_gt_i32 s96, 0\n" \
  "s_cbranch_scc1 .LC_cdly%=\n" \
  "s_branch .LC_cbar%=\n" \
  ".LC_abort%=:\n" \
  "s_waitcnt vmcnt(0) lgkmcnt(0)\n" \
  "s_mov_b64 exec, -1\n" \
  "v_mov_b32 v186, 1\n" \
  "v_mov_b32 v187, 0x2913000\n" \
  "buffer_store_dword v186, v187, %[rs], 0 offen sc1\n" \
  ".LC_end%=:\n" \
  "s_waitcnt vmcnt(0) lgkmcnt(0)\n" \
  ".LC_zend%=:\n" \
  ""
#define ASM_C_CLOB "memory", "scc", "vcc", "a0", "a1", "a2", "a3", "a4", "a5", "a6", "a7", "a8", "a9", "a10", "a11", "a12", "a13", "a14", "a15", "a16", "a17", "a18", "a19", "a20", "a21", "a22", "a23", "a24", "a25", "a26", "a27", "a28", "a29", "a30", "a31", "a32", "a33", "a34", "a35", "a36", "a37", "a38", "a39", "a40", "a41", "a42", "a43", "a44", "a45", "a46", "a47", "a48", "a49", "a50", "a51", "a52", "a53", "a54", "a55", "a56", "a57", "a58", "a59", "a60", "a61", "a62", "a63", "a64", "a65", "a66", "a67", "a68", "a69", "a70", "a71", "a72", "a73", "a74", "a75", "a76", "a77", "a78", "a79", "a80", "a81", "a82", "a83", "a84", "a85", "a86", "a87", "a88", "a89", "a90", "a91", "a92", "a93", "a94", "a95", "a96", "a97", "a98", "a99", "a100", "a101", "a102", "a103", "a104", "a105", "a106", "a107", "a108", "a109", "a110", "a111", "a112", "a113", "a114", "a115", "a116", "a117", "a118", "a119", "a120", "a121", "a122", "a123", "a124", "a125", "a126", "a127", "a128", "a129", "a130", "a131", "a132", "a133", "a134", "a135", "a136", "a137", "a138", "a139", "a140", "a141", "a142", "a143", "a144", "a145", "a146", "a147", "a148", "a149", "a150", "a151", "a152", "a153", "a154", "a155", "a156", "a157", "a158", "a159", "a160", "a161", "a162", "a163", "a164", "a165", "a166", "a167", "a168", "a169", "a170", "a171", "a172", "a173", "a174", "a175", "a176", "a177", "a178", "a179", "a180", "a181", "a182", "a183", "a184", "a185", "a186", "a187", "a188", "a189", "a190", "a191", "a192", "a193", "a194", "a195", "a196", "a197", "a198", "a199", "a200", "a201", "a202", "a203", "a204", "a205", "a206", "a207", "a208", "a209", "a210", "a211", "a212", "a213", "a214", "a215", "a216", "a217", "a218", "a219", "a220", "a221", "a222", "a223", "s40", "s41", "s42", "s43", "s44", "s45", "s46", "s47", "s48", "s49", "s50", "s52", "s53", "s54", "s55", "s56", "s57", "s58", "s59", "s60", "s61", "s62", "s63", "s76", "s77", "s84", "s86", "s88", "s92", "s96", "s97", "v30", "v32", "v33", "v34", "v35", "v36", "v37", "v38", "v39", "v48", "v49", "v50", "v51", "v52", "v53", "v54", "v55", "v56", "v57", "v58", "v59", "v60", "v61", "v62", "v63", "v64", "v65", "v66", "v67", "v68", "v69", "v70", "v71", "v72", "v73", "v74", "v75", "v92", "v93", "v94", "v95", "v96", "v97", "v98", "v99", "v104", "v105", "v106", "v107", "v108", "v109", "v110", "v111", "v112", "v113", "v114", "v115", "v116", "v117", "v118", "v119", "v120", "v121", "v122", "v123", "v124", "v125", "v126", "v127", "v128", "v129", "v130", "v131", "v132", "v133", "v134", "v135", "v136", "v137", "v138", "v139", "v140", "v141", "v142", "v143", "v144", "v145", "v146", "v147", "v148", "v152", "v153", "v154", "v155", "v156", "v157", "v158", "v159", "v168", "v169", "v170", "v171", "v172", "v173", "v174", "v175", "v176", "v177", "v178", "v179", "v180", "v181", "v182", "v183", "v186", "v187", "v188", "v189", "v190", "v194", "v195", "v196", "v197", "v198", "v199", "v200", "v201", "v204", "v205", "v208", "v209", "v210", "v211", "v212", "v213", "v214", "v215", "v216", "v217", "v218", "v219", "v220", "v221", "v222", "v223", "v224", "v225", "v226", "v227", "v228", "v229", "v230", "v231", "v235", "v236", "v237", "v238", "v239", "v240", "v241", "v242", "v243", "v244"
#define ASM_KS \
  "s_nop 4\n" \
  "s_mov_b32 s52, 0\n" \
  "s_mov_b32 s53, %[sX]\n" \
  "s_mov_b32 s54, 0\n" \
  "s_mov_b32 s56, 0\n" \
  "s_mov_b32 s40, 2\n" \
  "s_mov_b32 s55, 28672\n" \
  "buffer_load_dwordx4 v[208:211], %[oCal], %[rs], 0 offen\n" \
  "buffer_load_dwordx4 v[212:215], %[oCal], %[rs], 0 offen offset:128\n" \
  "buffer_load_dwordx4 v[216:219], %[oCal], %[rs], 0 offen offset:256\n" \
  "buffer_load_dwordx4 v[220:223], %[oCal], %[rs], 0 offen offset:384\n" \
  "s_add_u32 s46, %[sW], 0\n" \
  "buffer_load_dwordx4 a[0:3], %[l16], %[rs], s46 offen\n" \
  "buffer_load_dwordx4 a[4:7], %[l16], %[rs], s46 offen offset:1024\n" \
  "buffer_load_dwordx4 a[8:11], %[l16], %[rs], s46 offen offset:2048\n" \
  "buffer_load_dwordx4 a[12:15], %[l16], %[rs], s46 offen offset:3072\n" \
  "s_add_u32 s46, %[sW], 4096\n" \
  "buffer_load_dwordx4 a[16:19], %[l16], %[rs], s46 offen\n" \
  "buffer_load_dwordx4 a[20:23], %[l16], %[rs], s46 offen offset:1024\n" \
  "buffer_load_dwordx4 a[24:27], %[l16], %[rs], s46 offen offset:2048\n" \
  "buffer_load_dwordx4 a[28:31], %[l16], %[rs], s46 offen offset:3072\n" \
  "v_add_u32 v103, 0, %[l16]\n" \
  "v_lshrrev_b32 v103, 8, v103\n" \
  "v_mul_u32_u24 v103, 9363, v103\n" \
  "v_lshrrev_b32 v103, 12, v103\n" \
  "v_and_b32 v103, 0x70, v103\n" \
  "v_xor_b32 v96, %[l16], v103\n" \
  "v_xor_b32 v96, %[odd], v96\n" \
  "v_add_u32 v103, 1024, %[l16]\n" \
  "v_lshrrev_b32 v103, 8, v103\n" \
  "v_mul_u32_u24 v103, 9363, v103\n" \
  "v_lshrrev_b32 v103, 12, v103\n" \
  "v_and_b32 v103, 0x70, v103\n" \
  "v_xor_b32 v97, %[l16], v103\n" \
  "v_xor_b32 v97, %[odd], v97\n" \
  "v_add_u32 v103, 2048, %[l16]\n" \
  "v_lshrrev_b32 v103, 8, v103\n" \
  "v_mul_u32_u24 v103, 9363, v103\n" \
  "v_lshrrev_b32 v103, 12, v103\n" \
  "v_and_b32 v103, 0x70, v103\n" \
  "v_xor_b32 v98, %[l16], v103\n" \
  "v_xor_b32 v98, %[odd], v98\n" \
  "v_add_u32 v103, 3072, %[l16]\n" \
  "v_lshrrev_b32 v103, 8, v103\n" \
  "v_mul_u32_u24 v103, 9363, v103\n" \
  "v_lshrrev_b32 v103, 12, v103\n" \
  "v_and_b32 v103, 0x70, v103\n" \
  "v_xor_b32 v99, %[l16], v103\n" \
  "v_xor_b32 v99, %[odd], v99\n" \
  "v_add_u32 v103, 4096, %[l16]\n" \
  "v_lshrrev_b32 v103, 8, v103\n" \
  "v_mul_u32_u24 v103, 9363, v103\n" \
  "v_lshrrev_b32 v103, 12, v103\n" \
  "v_and_b32 v103, 0x70, v103\n" \
  "v_xor_b32 v100, %[l16], v103\n" \
  "v_xor_b32 v100, %[odd], v100\n" \
  "v_add_u32 v103, 5120, %[l16]\n" \
  "v_lshrrev_b32 v103, 8, v103\n" \
  "v_mul_u32_u24 v103, 9363, v103\n" \
  "v_lshrrev_b32 v103, 12, v103\n" \
  "v_and_b32 v103, 0x70, v103\n" \
  "v_xor_b32 v101, %[l16], v103\n" \
  "v_xor_b32 v101, %[odd], v101\n" \
  "v_add_u32 v103, 6144, %[l16]\n" \
  "v_lshrrev_b32 v103, 8, v103\n" \
  "v_mul_u32_u24 v103, 9363, v103\n" \
  "v_lshrrev_b32 v103, 12, v103\n" \
  "v_and_b32 v103, 0x70, v103\n" \
  "v_xor_b32 v102, %[l16], v103\n" \
  "v_xor_b32 v102, %[odd], v102\n" \
  "s_add_u32 m0, %[mb], s52\n" \
  "s_nop 0\n" \
  "buffer_load_dwordx4 v96, %[rx], s53 offen lds\n" \
  "buffer_load_dwordx4 v97, %[rx], s53 offen offset:1024 lds\n" \
  "buffer_load_dwordx4 v98, %[rx], s53 offen offset:2048 lds\n" \
  "buffer_load_dwordx4 v99, %[rx], s53 offen offset:3072 lds\n" \
  "s_add_u32 m0, m0, 4096\n" \
  "s_add_u32 s53, s53, 4096\n" \
  "buffer_load_dwordx4 v100, %[rx], s53 offen lds\n" \
  "buffer_load_dwordx4 v101, %[rx], s53 offen offset:1024 lds\n" \
  "buffer_load_dwordx4 v102, %[rx], s53 offen offset:2048 lds\n" \
  "s_add_u32 s53, s53, 24576\n" \
  "s_add_u32 s52, s52, 28672\n" \
  "s_cmp_eq_u32 s52, 114688\n" \
  "s_cselect_b32 s52, 0, s52\n" \
  "s_add_u32 s46, %[sW], 8192\n" \
  "buffer_load_dwordx4 a[32:35], %[l16], %[rs], s46 offen\n" \
  "buffer_load_dwordx4 a[36:39], %[l16], %[rs], s46 offen offset:1024\n" \
  "buffer_load_dwordx4 a[40:43], %[l16], %[rs], s46 offen offset:2048\n" \
  "buffer_load_dwordx4 a[44:47], %[l16], %[rs], s46 offen offset:3072\n" \
  "s_add_u32 s46, %[sW], 12288\n" \
  "buffer_load_dwordx4 a[48:51], %[l16], %[rs], s46 offen\n" \
  "buffer_load_dwordx4 a[52:55], %[l16], %[rs], s46 offen offset:1024\n" \
  "buffer_load_dwordx4 a[56:59], %[l16], %[rs], s46 offen offset:2048\n" \
  "buffer_load_dwordx4 a[60:63], %[l16], %[rs], s46 offen offset:3072\n" \
  "s_add_u32 s46, %[sW], 16384\n" \
  "buffer_load_dwordx4 a[64:67], %[l16], %[rs], s46 offen\n" \
  "buffer_load_dwordx4 a[68:71], %[l16], %[rs], s46 offen offset:1024\n" \
  "buffer_load_dwordx4 a[72:75], %[l16], %[rs], s46 offen offset:2048\n" \
  "buffer_load_dwordx4 a[76:79], %[l16], %[rs], s46 offen offset:3072\n" \
  "s_add_u32 s46, %[sW], 20480\n" \
  "buffer_load_dwordx4 a[80:83], %[l16], %[rs], s46 offen\n" \
  "buffer_load_dwordx4 a[84:87], %[l16], %[rs], s46 offen offset:1024\n" \
  "buffer_load_dwordx4 a[88:91], %[l16], %[rs], s46 offen offset:2048\n" \
  "buffer_load_dwordx4 a[92:95], %[l16], %[rs], s46 offen offset:3072\n" \
  "s_add_u32 s46, %[sW], 24576\n" \
  "buffer_load_dwordx4 a[96:99], %[l16], %[rs], s46 offen\n" \
  "buffer_load_dwordx4 a[100:103], %[l16], %[rs], s46 offen offset:1024\n" \
  "buffer_load_dwordx4 a[104:107], %[l16], %[rs], s46 offen offset:2048\n" \
  "buffer_load_dwordx4 a[108:111], %[l16], %[rs], s46 offen offset:3072\n" \
  "s_add_u32 m0, %[mb], s52\n" \
  "s_nop 0\n" \
  "buffer_load_dwordx4 v96, %[rx], s53 offen lds\n" \
  "buffer_load_dwordx4 v97, %[rx], s53 offen offset:1024 lds\n" \
  "buffer_load_dwordx4 v98, %[rx], s53 offen offset:2048 lds\n" \
  "buffer_load_dwordx4 v99, %[rx], s53 offen offset:3072 lds\n" \
  "s_add_u32 m0, m0, 4096\n" \
  "s_add_u32 s53, s53, 4096\n" \
  "buffer_load_dwordx4 v100, %[rx], s53 offen lds\n" \
  "buffer_load_dwordx4 v101, %[rx], s53 offen offset:1024 lds\n" \
  "buffer_load_dwordx4 v102, %[rx], s53 offen offset:2048 lds\n" \
  "s_add_u32 s53, s53, 24576\n" \
  "s_add_u32 s52, s52, 28672\n" \
  "s_cmp_eq_u32 s52, 114688\n" \
  "s_cselect_b32 s52, 0, s52\n" \
  "s_add_u32 m0, %[mb], s52\n" \
  "s_nop 0\n" \
  "buffer_load_dwordx4 v96, %[rx], s53 offen lds\n" \
  "buffer_load_dwordx4 v97, %[rx], s53 offen offset:1024 lds\n" \
  "buffer_load_dwordx4 v98, %[rx], s53 offen offset:2048 lds\n" \
  "buffer_load_dwordx4 v99, %[rx], s53 offen offset:3072 lds\n" \
  "s_add_u32 m0, m0, 4096\n" \
  "s_add_u32 s53, s53, 4096\n" \
  "buffer_load_dwordx4 v100, %[rx], s53 offen lds\n" \
  "buffer_load_dwordx4 v101, %[rx], s53 offen offset:1024 lds\n" \
  "buffer_load_dwordx4 v102, %[rx], s53 offen offset:2048 lds\n" \
  "s_add_u32 s53, s53, 24576\n" \
  "s_add_u32 s52, s52, 28672\n" \
  "s_cmp_eq_u32 s52, 114688\n" \
  "s_cselect_b32 s52, 0, s52\n" \
  "s_waitcnt vmcnt(49)\n" \
  "v_add3_u32 v208, v208, v212, v216\n" \
  "v_add_u32 v208, v208, v220\n" \
  "v_lshl_or_b32 v208, v208, 2, 0\n" \
  "v_add3_u32 v209, v209, v213, v217\n" \
  "v_add_u32 v209, v209, v221\n" \
  "v_lshl_or_b32 v209, v209, 2, 1\n" \
  "v_add3_u32 v210, v210, v214, v218\n" \
  "v_add_u32 v210, v210, v222\n" \
  "v_lshl_or_b32 v210, v210, 2, 2\n" \
  "v_add3_u32 v211, v211, v215, v219\n" \
  "v_add_u32 v211, v211, v223\n" \
  "v_lshl_or_b32 v211, v211, 2, 3\n" \
  "v_min3_u32 v208, v208, v209, v210\n" \
  "v_min_u32 v208, v208, v211\n" \
  "v_and_b32 v208, 3, v208\n" \
  "v_lshlrev_b32 v208, 13, v208\n" \
  "v_lshlrev_b32 v209, 9, %[l16]\n" \
  "v_and_b32 v209, 0x4000, v209\n" \
  "v_xor_b32 v30, v208, v209\n" \
  "s_nop 0\n" \
  "v_readlane_b32 s88, v30, 0\n" \
  "s_nop 0\n" \
  "s_and_b32 s88, s88, 8192\n" \
  "s_xor_b32 s88, s88, 8192\n" \
  "v_mov_b32 v84, %[rX]\n" \
  "v_xor_b32 v85, 64, %[rX]\n" \
  "v_add_u32 v86, 28672, v84\n" \
  "v_add_u32 v87, 28672, v85\n" \
  "v_mov_b32 v89, 0x7ff00000\n" \
  "s_waitcnt vmcnt(14)\n" \
  "s_barrier\n" \
  "ds_read_b128 v[32:35], v84\n" \
  "ds_read_b128 v[36:39], v85\n" \
  "ds_read_b128 v[40:43], v84 offset:128\n" \
  "ds_read_b128 v[44:47], v85 offset:128\n" \
  "ds_read_b128 v[48:51], v84 offset:256\n" \
  "ds_read_b128 v[52:55], v85 offset:256\n" \
  ".LK_top%=:\n" \
  "ds_read_b128 v[56:59], v84 offset:384\n" \
  "s_waitcnt lgkmcnt(6)\n" \
  "v_mfma_f32_16x16x32_bf16 v[60:63], a[0:3], v[32:35], 0\n" \
  "v_mfma_f32_16x16x32_bf16 v[64:67], a[56:59], v[32:35], 0\n" \
  "ds_read_b128 v[32:35], v85 offset:384\n" \
  "s_waitcnt lgkmcnt(6)\n" \
  "v_mfma_f32_16x16x32_bf16 v[60:63], a[4:7], v[36:39], v[60:63]\n" \
  "v_mfma_f32_16x16x32_bf16 v[64:67], a[60:63], v[36:39], v[64:67]\n" \
  "ds_read_b128 v[36:39], v84 offset:512\n" \
  "s_waitcnt lgkmcnt(6)\n" \
  "v_mfma_f32_16x16x32_bf16 v[60:63], a[8:11], v[40:43], v[60:63]\n" \
  "v_mfma_f32_16x16x32_bf16 v[64:67], a[64:67], v[40:43], v[64:67]\n" \
  "v_cvt_pk_bf16_f32 v80, v68, v69\n" \
  "v_cvt_pk_bf16_f32 v81, v70, v71\n" \
  "ds_read_b128 v[40:43], v85 offset:512\n" \
  "s_waitcnt lgkmcnt(6)\n" \
  "v_mfma_f32_16x16x32_bf16 v[60:63], a[12:15], v[44:47], v[60:63]\n" \
  "v_mfma_f32_16x16x32_bf16 v[64:67], a[68:71], v[44:47], v[64:67]\n" \
  "v_cvt_pk_bf16_f32 v82, v72, v73\n" \
  "v_cvt_pk_bf16_f32 v83, v74, v75\n" \
  "ds_read_b128 v[44:47], v84 offset:640\n" \
  "s_waitcnt lgkmcnt(6)\n" \
  "v_mfma_f32_16x16x32_bf16 v[60:63], a[16:19], v[48:51], v[60:63]\n" \
  "v_mfma_f32_16x16x32_bf16 v[64:67], a[72:75], v[48:51], v[64:67]\n" \
  "buffer_store_dwordx4 v[80:83], v89, %[rs], s56 offen sc1\n" \
  "ds_read_b128 v[48:51], v85 offset:640\n" \
  "s_waitcnt lgkmcnt(6)\n" \
  "v_mfma_f32_16x16x32_bf16 v[60:63], a[20:23], v[52:55], v[60:63]\n" \
  "v_mfma_f32_16x16x32_bf16 v[64:67], a[76:79], v[52:55], v[64:67]\n" \
  "v_mov_b32 v89, %[l16]\n" \
  "s_sub_u32 s46, s40, 1\n" \
  "s_cmp_lt_u32 s40, 6\n" \
  "s_cselect_b32 s46, s40, s46\n" \
  "v_mov_b32 v88, s46\n" \
  "ds_read_b128 v[52:55], v84 offset:768\n" \
  "s_waitcnt lgkmcnt(6)\n" \
  "v_mfma_f32_16x16x32_bf16 v[60:63], a[24:27], v[56:59], v[60:63]\n" \
  "v_mfma_f32_16x16x32_bf16 v[64:67], a[80:83], v[56:59], v[64:67]\n" \
  "s_lshl_b32 s49, s40, 12\n" \
  "s_and_b32 s49, s49, 8192\n" \
  "s_xor_b32 s49, s49, s88\n" \
  "s_cmp_lt_u32 s40, 2\n" \
  "s_cselect_b32 s49, 0, s49\n" \
  "s_lshl_b32 s54, s40, 20\n" \
  "s_add_u32 s54, s54, %[sO]\n" \
  "s_xor_b32 s54, s54, s49\n" \
  "ds_read_b128 v[56:59], v85 offset:768\n" \
  "s_waitcnt lgkmcnt(6)\n" \
  "v_mfma_f32_16x16x32_bf16 v[60:63], a[28:31], v[32:35], v[60:63]\n" \
  "v_mfma_f32_16x16x32_bf16 v[64:67], a[84:87], v[32:35], v[64:67]\n" \
  "s_lshl_b32 s49, s40, 12\n" \
  "s_and_b32 s49, s49, 8192\n" \
  "s_xor_b32 s49, s49, s88\n" \
  "s_cmp_lt_u32 s40, 2\n" \
  "s_cselect_b32 s49, 0, s49\n" \
  "s_lshl_b32 s56, s40, 20\n" \
  "s_add_u32 s56, s56, %[sO2]\n" \
  "s_xor_b32 s56, s56, s49\n" \
  "ds_read_b128 v[32:35], v84 offset:14336\n" \
  "s_waitcnt lgkmcnt(6)\n" \
  "v_mfma_f32_16x16x32_bf16 v[60:63], a[32:35], v[36:39], v[60:63]\n" \
  "v_mfma_f32_16x16x32_bf16 v[64:67], a[88:91], v[36:39], v[64:67]\n" \
  "ds_read_b128 v[36:39], v85 offset:14336\n" \
  "s_waitcnt lgkmcnt(6)\n" \
  "v_mfma_f32_16x16x32_bf16 v[60:63], a[36:39], v[40:43], v[60:63]\n" \
  "v_mfma_f32_16x16x32_bf16 v[64:67], a[92:95], v[40:43], v[64:67]\n" \
  "ds_read_b128 v[40:43], v84 offset:14464\n" \
  "s_waitcnt lgkmcnt(6)\n" \
  "v_mfma_f32_16x16x32_bf16 v[60:63], a[40:43], v[44:47], v[60:63]\n" \
  "v_mfma_f32_16x16x32_bf16 v[64:67], a[96:99], v[44:47], v[64:67]\n" \
  "ds_read_b128 v[44:47], v85 offset:14464\n" \
  "s_waitcnt lgkmcnt(6)\n" \
  "v_mfma_f32_16x16x32_bf16 v[60:63], a[44:47], v[48:51], v[60:63]\n" \
  "v_mfma_f32_16x16x32_bf16 v[64:67], a[100:103], v[48:51], v[64:67]\n" \
  "ds_read_b128 v[48:51], v84 offset:14592\n" \
  "s_waitcnt lgkmcnt(6)\n" \
  "v_mfma_f32_16x16x32_bf16 v[60:63], a[48:51], v[52:55], v[60:63]\n" \
  "v_mfma_f32_16x16x32_bf16 v[64:67], a[104:107], v[52:55], v[64:67]\n" \
  "ds_read_b128 v[52:55], v85 offset:14592\n" \
  "s_waitcnt lgkmcnt(6)\n" \
  "v_mfma_f32_16x16x32_bf16 v[60:63], a[52:55], v[56:59], v[60:63]\n" \
  "v_mfma_f32_16x16x32_bf16 v[64:67], a[108:111], v[56:59], v[64:67]\n" \
  "ds_read_b128 v[56:59], v84 offset:14720\n" \
  "s_waitcnt lgkmcnt(6)\n" \
  "v_mfma_f32_16x16x32_bf16 v[68:71], a[0:3], v[32:35], 0\n" \
  "v_mfma_f32_16x16x32_bf16 v[72:75], a[56:59], v[32:35], 0\n" \
  "ds_read_b128 v[32:35], v85 offset:14720\n" \
  "s_waitcnt lgkmcnt(6)\n" \
  "v_mfma_f32_16x16x32_bf16 v[68:71], a[4:7], v[36:39], v[68:71]\n" \
  "v_mfma_f32_16x16x32_bf16 v[72:75], a[60:63], v[36:39], v[72:75]\n" \
  "ds_read_b128 v[36:39], v84 offset:14848\n" \
  "s_waitcnt lgkmcnt(6)\n" \
  "v_mfma_f32_16x16x32_bf16 v[68:71], a[8:11], v[40:43], v[68:71]\n" \
  "v_mfma_f32_16x16x32_bf16 v[72:75], a[64:67], v[40:43], v[72:75]\n" \
  "v_cvt_pk_bf16_f32 v76, v60, v61\n" \
  "v_cvt_pk_bf16_f32 v77, v62, v63\n" \
  "ds_read_b128 v[40:43], v85 offset:14848\n" \
  "s_waitcnt lgkmcnt(6)\n" \
  "v_mfma_f32_16x16x32_bf16 v[68:71], a[12:15], v[44:47], v[68:71]\n" \
  "v_mfma_f32_16x16x32_bf16 v[72:75], a[68:71], v[44:47], v[72:75]\n" \
  "v_cvt_pk_bf16_f32 v78, v64, v65\n" \
  "v_cvt_pk_bf16_f32 v79, v66, v67\n" \
  "ds_read_b128 v[44:47], v84 offset:14976\n" \
  "s_waitcnt lgkmcnt(6)\n" \
  "v_mfma_f32_16x16x32_bf16 v[68:71], a[16:19], v[48:51], v[68:71]\n" \
  "v_mfma_f32_16x16x32_bf16 v[72:75], a[72:75], v[48:51], v[72:75]\n" \
  "s_cmp_lt_u32 s40, 6\n" \
  "s_cbranch_scc0 .LK_w%=\n" \
  "s_waitcnt vmcnt(0)\n" \
  ".LK_w%=:\n" \
  "s_waitcnt vmcnt(10)\n" \
  "s_barrier\n" \
  "buffer_store_dword v88, %[oPr], %[rs], 0 offen sc1\n" \
  "buffer_store_dwordx4 v[76:79], v89, %[rs], s54 offen sc1\n" \
  "ds_read_b128 v[48:51], v85 offset:14976\n" \
  "s_waitcnt lgkmcnt(6)\n" \
  "v_mfma_f32_16x16x32_bf16 v[68:71], a[20:23], v[52:55], v[68:71]\n" \
  "v_mfma_f32_16x16x32_bf16 v[72:75], a[76:79], v[52:55], v[72:75]\n" \
  "s_add_u32 m0, %[mb], s52\n" \
  "s_nop 0\n" \
  "buffer_load_dwordx4 v96, %[rx], s53 offen lds\n" \
  "ds_read_b128 v[52:55], v84 offset:15104\n" \
  "s_waitcnt lgkmcnt(6)\n" \
  "v_mfma_f32_16x16x32_bf16 v[68:71], a[24:27], v[56:59], v[68:71]\n" \
  "v_mfma_f32_16x16x32_bf16 v[72:75], a[80:83], v[56:59], v[72:75]\n" \
  "buffer_load_dwordx4 v97, %[rx], s53 offen offset:1024 lds\n" \
  "ds_read_b128 v[56:59], v85 offset:15104\n" \
  "s_waitcnt lgkmcnt(6)\n" \
  "v_mfma_f32_16x16x32_bf16 v[68:71], a[28:31], v[32:35], v[68:71]\n" \
  "v_mfma_f32_16x16x32_bf16 v[72:75], a[84:87], v[32:35], v[72:75]\n" \
  "buffer_load_dwordx4 v98, %[rx], s53 offen offset:2048 lds\n" \
  "ds_read_b128 v[32:35], v86\n" \
  "s_waitcnt lgkmcnt(6)\n" \
  "v_mfma_f32_16x16x32_bf16 v[68:71], a[32:35], v[36:39], v[68:71]\n" \
  "v_mfma_f32_16x16x32_bf16 v[72:75], a[88:91], v[36:39], v[72:75]\n" \
  "buffer_load_dwordx4 v99, %[rx], s53 offen offset:3072 lds\n" \
  "ds_read_b128 v[36:39], v87\n" \
  "s_waitcnt lgkmcnt(6)\n" \
  "v_mfma_f32_16x16x32_bf16 v[68:71], a[36:39], v[40:43], v[68:71]\n" \
  "v_mfma_f32_16x16x32_bf16 v[72:75], a[92:95], v[40:43], v[72:75]\n" \
  "s_add_u32 m0, m0, 4096\n" \
  "s_add_u32 s53, s53, 4096\n" \
  "buffer_load_dwordx4 v100, %[rx], s53 offen lds\n" \
  "ds_read_b128 v[40:43], v86 offset:128\n" \
  "s_waitcnt lgkmcnt(6)\n" \
  "v_mfma_f32_16x16x32_bf16 v[68:71], a[40:43], v[44:47], v[68:71]\n" \
  "v_mfma_f32_16x16x32_bf16 v[72:75], a[96:99], v[44:47], v[72:75]\n" \
  "buffer_load_dwordx4 v101, %[rx], s53 offen offset:1024 lds\n" \
  "ds_read_b128 v[44:47], v87 offset:128\n" \
  "s_waitcnt lgkmcnt(6)\n" \
  "v_mfma_f32_16x16x32_bf16 v[68:71], a[44:47], v[48:51], v[68:71]\n" \
  "v_mfma_f32_16x16x32_bf16 v[72:75], a[100:103], v[48:51], v[72:75]\n" \
  "buffer_load_dwordx4 v102, %[rx], s53 offen offset:2048 lds\n" \
  "s_add_u32 s53, s53, 24576\n" \
  "s_add_u32 s52, s52, 28672\n" \
  "s_cmp_eq_u32 s52, 114688\n" \
  "s_cselect_b32 s52, 0, s52\n" \
  "ds_read_b128 v[48:51], v86 offset:256\n" \
  "s_waitcnt lgkmcnt(6)\n" \
  "v_mfma_f32_16x16x32_bf16 v[68:71], a[48:51], v[52:55], v[68:71]\n" \
  "v_mfma_f32_16x16x32_bf16 v[72:75], a[104:107], v[52:55], v[72:75]\n" \
  "ds_read_b128 v[52:55], v87 offset:256\n" \
  "s_waitcnt lgkmcnt(6)\n" \
  "v_mfma_f32_16x16x32_bf16 v[68:71], a[52:55], v[56:59], v[68:71]\n" \
  "v_mfma_f32_16x16x32_bf16 v[72:75], a[108:111], v[56:59], v[72:75]\n" \
  "s_cmp_lg_u32 s40, 2\n" \
  "s_cbranch_scc1 .LK_n0%=\n" \
  "s_nop 4\n" \
  "v_cvt_pk_bf16_f32 v80, v68, v69\n" \
  "v_cvt_pk_bf16_f32 v81, v70, v71\n" \
  "v_cvt_pk_bf16_f32 v82, v72, v73\n" \
  "v_cvt_pk_bf16_f32 v83, v74, v75\n" \
  "buffer_store_dwordx4 v[80:83], v89, %[rs], s56 offen sc1\n" \
  "v_mov_b32 v88, 3\n" \
  "s_waitcnt vmcnt(0)\n" \
  "s_barrier\n" \
  "buffer_store_dword v88, %[oPr], %[rs], 0 offen sc1\n" \
  "v_mov_b32 v89, 0x7ff00000\n" \
  ".LK_n0%=:\n" \
  "s_add_u32 s55, s55, 28672\n" \
  "s_cmp_eq_u32 s55, 114688\n" \
  "s_cselect_b32 s55, 0, s55\n" \
  "v_mov_b32 v84, v86\n" \
  "v_mov_b32 v85, v87\n" \
  "v_add_u32 v86, s55, %[rX]\n" \
  "v_xor_b32 v87, 64, v86\n" \
  "s_add_u32 s40, s40, 1\n" \
  "s_cmp_lt_u32 s40, 64\n" \
  "s_cbranch_scc1 .LK_top%=\n" \
  "v_cvt_pk_bf16_f32 v80, v68, v69\n" \
  "v_cvt_pk_bf16_f32 v81, v70, v71\n" \
  "v_cvt_pk_bf16_f32 v82, v72, v73\n" \
  "v_cvt_pk_bf16_f32 v83, v74, v75\n" \
  "buffer_store_dwordx4 v[80:83], v89, %[rs], s56 offen sc1\n" \
  "v_mov_b32 v88, 64\n" \
  "s_waitcnt vmcnt(0) lgkmcnt(0)\n" \
  "s_barrier\n" \
  "buffer_store_dword v88, %[oPr], %[rs], 0 offen sc1\n" \
  ".LK_zend%=:\n" \
  ""
#define ASM_KS_CLOB "memory", "scc", "vcc", "a0", "a1", "a2", "a3", "a4", "a5", "a6", "a7", "a8", "a9", "a10", "a11", "a12", "a13", "a14", "a15", "a16", "a17", "a18", "a19", "a20", "a21", "a22", "a23", "a24", "a25", "a26", "a27", "a28", "a29", "a30", "a31", "a32", "a33", "a34", "a35", "a36", "a37", "a38", "a39", "a40", "a41", "a42", "a43", "a44", "a45", "a46", "a47", "a48", "a49", "a50", "a51", "a52", "a53", "a54", "a55", "a56", "a57", "a58", "a59", "a60", "a61", "a62", "a63", "a64", "a65", "a66", "a67", "a68", "a69", "a70", "a71", "a72", "a73", "a74", "a75", "a76", "a77", "a78", "a79", "a80", "a81", "a82", "a83", "a84", "a85", "a86", "a87", "a88", "a89", "a90", "a91", "a92", "a93", "a94", "a95", "a96", "a97", "a98", "a99", "a100", "a101", "a102", "a103", "a104", "a105", "a106", "a107", "a108", "a109", "a110", "a111", "s40", "s46", "s49", "s52", "s53", "s54", "s55", "s56", "s88", "v30", "v32", "v33", "v34", "v35", "v36", "v37", "v38", "v39", "v40", "v41", "v42", "v43", "v44", "v45", "v46", "v47", "v48", "v49", "v50", "v51", "v52", "v53", "v54", "v55", "v56", "v57", "v58", "v59", "v60", "v61", "v62", "v63", "v64", "v65", "v66", "v67", "v68", "v69", "v70", "v71", "v72", "v73", "v74", "v75", "v76", "v77", "v78", "v79", "v80", "v81", "v82", "v83", "v84", "v85", "v86", "v87", "v88", "v89", "v96", "v97", "v98", "v99", "v100", "v101", "v102", "v103", "v208", "v209", "v210", "v211", "v212", "v213", "v214", "v215", "v216", "v217", "v218", "v219", "v220", "v221", "v222", "v223"
#define ASM_VS \
  "s_nop 4\n" \
  "s_mov_b32 s52, 0\n" \
  "s_mov_b32 s53, %[sX]\n" \
  "s_mov_b32 s54, 0\n" \
  "s_mov_b32 s56, 0\n" \
  "s_mov_b32 s40, 2\n" \
  "s_mov_b32 s55, 28672\n" \
  "buffer_load_dwordx4 v[208:211], %[oCal], %[rs], 0 offen\n" \
  "buffer_load_dwordx4 v[212:215], %[oCal], %[rs], 0 offen offset:128\n" \
  "buffer_load_dwordx4 v[216:219], %[oCal], %[rs], 0 offen offset:256\n" \
  "buffer_load_dwordx4 v[220:223], %[oCal], %[rs], 0 offen offset:384\n" \
  "s_add_u32 s46, %[sW], 0\n" \
  "buffer_load_dwordx4 a[0:3], %[l16], %[rs], s46 offen\n" \
  "buffer_load_dwordx4 a[4:7], %[l16], %[rs], s46 offen offset:1024\n" \
  "buffer_load_dwordx4 a[8:11], %[l16], %[rs], s46 offen offset:2048\n" \
  "buffer_load_dwordx4 a[12:15], %[l16], %[rs], s46 offen offset:3072\n" \
  "s_add_u32 s46, %[sW], 4096\n" \
  "buffer_load_dwordx4 a[16:19], %[l16], %[rs], s46 offen\n" \
  "buffer_load_dwordx4 a[20:23], %[l16], %[rs], s46 offen offset:1024\n" \
  "buffer_load_dwordx4 a[24:27], %[l16], %[rs], s46 offen offset:2048\n" \
  "buffer_load_dwordx4 a[28:31], %[l16], %[rs], s46 offen offset:3072\n" \
  "v_add_u32 v103, 0, %[l16]\n" \
  "v_lshrrev_b32 v103, 8, v103\n" \
  "v_mul_u32_u24 v103, 9363, v103\n" \
  "v_lshrrev_b32 v103, 12, v103\n" \
  "v_and_b32 v103, 0x70, v103\n" \
  "v_xor_b32 v96, %[l16], v103\n" \
  "v_xor_b32 v96, %[odd], v96\n" \
  "v_add_u32 v103, 1024, %[l16]\n" \
  "v_lshrrev_b32 v103, 8, v103\n" \
  "v_mul_u32_u24 v103, 9363, v103\n" \
  "v_lshrrev_b32 v103, 12, v103\n" \
  "v_and_b32 v103, 0x70, v103\n" \
  "v_xor_b32 v97, %[l16], v103\n" \
  "v_xor_b32 v97, %[odd], v97\n" \
  "v_add_u32 v103, 2048, %[l16]\n" \
  "v_lshrrev_b32 v103, 8, v103\n" \
  "v_mul_u32_u24 v103, 9363, v103\n" \
  "v_lshrrev_b32 v103, 12, v103\n" \
  "v_and_b32 v103, 0x70, v103\n" \
  "v_xor_b32 v98, %[l16], v103\n" \
  "v_xor_b32 v98, %[odd], v98\n" \
  "v_add_u32 v103, 3072, %[l16]\n" \
  "v_lshrrev_b32 v103, 8, v103\n" \
  "v_mul_u32_u24 v103, 9363, v103\n" \
  "v_lshrrev_b32 v103, 12, v103\n" \
  "v_and_b32 v103, 0x70, v103\n" \
  "v_xor_b32 v99, %[l16], v103\n" \
  "v_xor_b32 v99, %[odd], v99\n" \
  "v_add_u32 v103, 4096, %[l16]\n" \
  "v_lshrrev_b32 v103, 8, v103\n" \
  "v_mul_u32_u24 v103, 9363, v103\n" \
  "v_lshrrev_b32 v103, 12, v103\n" \
  "v_and_b32 v103, 0x70, v103\n" \
  "v_xor_b32 v100, %[l16], v103\n" \
  "v_xor_b32 v100, %[odd], v100\n" \
  "v_add_u32 v103, 5120, %[l16]\n" \
  "v_lshrrev_b32 v103, 8, v103\n" \
  "v_mul_u32_u24 v103, 9363, v103\n" \
  "v_lshrrev_b32 v103, 12, v103\n" \
  "v_and_b32 v103, 0x70, v103\n" \
  "v_xor_b32 v101, %[l16], v103\n" \
  "v_xor_b32 v101, %[odd], v101\n" \
  "v_add_u32 v103, 6144, %[l16]\n" \
  "v_lshrrev_b32 v103, 8, v103\n" \
  "v_mul_u32_u24 v103, 9363, v103\n" \
  "v_lshrrev_b32 v103, 12, v103\n" \
  "v_and_b32 v103, 0x70, v103\n" \
  "v_xor_b32 v102, %[l16], v103\n" \
  "v_xor_b32 v102, %[odd], v102\n" \
  "s_add_u32 m0, %[mb], s52\n" \
  "s_nop 0\n" \
  "buffer_load_dwordx4 v96, %[rx], s53 offen lds\n" \
  "buffer_load_dwordx4 v97, %[rx], s53 offen offset:1024 lds\n" \
  "buffer_load_dwordx4 v98, %[rx], s53 offen offset:2048 lds\n" \
  "buffer_load_dwordx4 v99, %[rx], s53 offen offset:3072 lds\n" \
  "s_add_u32 m0, m0, 4096\n" \
  "s_add_u32 s53, s53, 4096\n" \
  "buffer_load_dwordx4 v100, %[rx], s53 offen lds\n" \
  "buffer_load_dwordx4 v101, %[rx], s53 offen offset:1024 lds\n" \
  "buffer_load_dwordx4 v102, %[rx], s53 offen offset:2048 lds\n" \
  "s_add_u32 s53, s53, 24576\n" \
  "s_add_u32 s52, s52, 28672\n" \
  "s_cmp_eq_u32 s52, 114688\n" \
  "s_cselect_b32 s52, 0, s52\n" \
  "s_add_u32 s46, %[sW], 8192\n" \
  "buffer_load_dwordx4 a[32:35], %[l16], %[rs], s46 offen\n" \
  "buffer_load_dwordx4 a[36:39], %[l16], %[rs], s46 offen offset:1024\n" \
  "buffer_load_dwordx4 a[40:43], %[l16], %[rs], s46 offen offset:2048\n" \
  "buffer_load_dwordx4 a[44:47], %[l16], %[rs], s46 offen offset:3072\n" \
  "s_add_u32 s46, %[sW], 12288\n" \
  "buffer_load_dwordx4 a[48:51], %[l16], %[rs], s46 offen\n" \
  "buffer_load_dwordx4 a[52:55], %[l16], %[rs], s46 offen offset:1024\n" \
  "buffer_load_dwordx4 a[56:59], %[l16], %[rs], s46 offen offset:2048\n" \
  "buffer_load_dwordx4 a[60:63], %[l16], %[rs], s46 offen offset:3072\n" \
  "s_add_u32 s46, %[sW], 16384\n" \
  "buffer_load_dwordx4 a[64:67], %[l16], %[rs], s46 offen\n" \
  "buffer_load_dwordx4 a[68:71], %[l16], %[rs], s46 offen offset:1024\n" \
  "buffer_load_dwordx4 a[72:75], %[l16], %[rs], s46 offen offset:2048\n" \
  "buffer_load_dwordx4 a[76:79], %[l16], %[rs], s46 offen offset:3072\n" \
  "s_add_u32 s46, %[sW], 20480\n" \
  "buffer_load_dwordx4 a[80:83], %[l16], %[rs], s46 offen\n" \
  "buffer_load_dwordx4 a[84:87], %[l16], %[rs], s46 offen offset:1024\n" \
  "buffer_load_dwordx4 a[88:91], %[l16], %[rs], s46 offen offset:2048\n" \
  "buffer_load_dwordx4 a[92:95], %[l16], %[rs], s46 offen offset:3072\n" \
  "s_add_u32 s46, %[sW], 24576\n" \
  "buffer_load_dwordx4 a[96:99], %[l16], %[rs], s46 offen\n" \
  "buffer_load_dwordx4 a[100:103], %[l16], %[rs], s46 offen offset:1024\n" \
  "buffer_load_dwordx4 a[104:107], %[l16], %[rs], s46 offen offset:2048\n" \
  "buffer_load_dwordx4 a[108:111], %[l16], %[rs], s46 offen offset:3072\n" \
  "s_add_u32 m0, %[mb], s52\n" \
  "s_nop 0\n" \
  "buffer_load_dwordx4 v96, %[rx], s53 offen lds\n" \
  "buffer_load_dwordx4 v97, %[rx], s53 offen offset:1024 lds\n" \
  "buffer_load_dwordx4 v98, %[rx], s53 offen offset:2048 lds\n" \
  "buffer_load_dwordx4 v99, %[rx], s53 offen offset:3072 lds\n" \
  "s_add_u32 m0, m0, 4096\n" \
  "s_add_u32 s53, s53, 4096\n" \
  "buffer_load_dwordx4 v100, %[rx], s53 offen lds\n" \
  "buffer_load_dwordx4 v101, %[rx], s53 offen offset:1024 lds\n" \
  "buffer_load_dwordx4 v102, %[rx], s53 offen offset:2048 lds\n" \
  "s_add_u32 s53, s53, 24576\n" \
  "s_add_u32 s52, s52, 28672\n" \
  "s_cmp_eq_u32 s52, 114688\n" \
  "s_cselect_b32 s52, 0, s52\n" \
  "s_add_u32 m0, %[mb], s52\n" \
  "s_nop 0\n" \
  "buffer_load_dwordx4 v96, %[rx], s53 offen lds\n" \
  "buffer_load_dwordx4 v97, %[rx], s53 offen offset:1024 lds\n" \
  "buffer_load_dwordx4 v98, %[rx], s53 offen offset:2048 lds\n" \
  "buffer_load_dwordx4 v99, %[rx], s53 offen offset:3072 lds\n" \
  "s_add_u32 m0, m0, 4096\n" \
  "s_add_u32 s53, s53, 4096\n" \
  "buffer_load_dwordx4 v100, %[rx], s53 offen lds\n" \
  "buffer_load_dwordx4 v101, %[rx], s53 offen offset:1024 lds\n" \
  "buffer_load_dwordx4 v102, %[rx], s53 offen offset:2048 lds\n" \
  "s_add_u32 s53, s53, 24576\n" \
  "s_add_u32 s52, s52, 28672\n" \
  "s_cmp_eq_u32 s52, 114688\n" \
  "s_cselect_b32 s52, 0, s52\n" \
  "s_waitcnt vmcnt(49)\n" \
  "v_add3_u32 v208, v208, v212, v216\n" \
  "v_add_u32 v208, v208, v220\n" \
  "v_lshl_or_b32 v208, v208, 2, 0\n" \
  "v_add3_u32 v209, v209, v213, v217\n" \
  "v_add_u32 v209, v209, v221\n" \
  "v_lshl_or_b32 v209, v209, 2, 1\n" \
  "v_add3_u32 v210, v210, v214, v218\n" \
  "v_add_u32 v210, v210, v222\n" \
  "v_lshl_or_b32 v210, v210, 2, 2\n" \
  "v_add3_u32 v211, v211, v215, v219\n" \
  "v_add_u32 v211, v211, v223\n" \
  "v_lshl_or_b32 v211, v211, 2, 3\n" \
  "v_min3_u32 v208, v208, v209, v210\n" \
  "v_min_u32 v208, v208, v211\n" \
  "v_and_b32 v208, 3, v208\n" \
  "v_lshlrev_b32 v208, 13, v208\n" \
  "v_lshlrev_b32 v209, 9, %[l16]\n" \
  "v_and_b32 v209, 0x4000, v209\n" \
  "v_xor_b32 v30, v208, v209\n" \
  "s_nop 0\n" \
  "v_readlane_b32 s88, v30, 0\n" \
  "s_nop 0\n" \
  "s_and_b32 s88, s88, 8192\n" \
  "s_xor_b32 s88, s88, 8192\n" \
  "v_mov_b32 v84, %[rX]\n" \
  "v_xor_b32 v85, 64, %[rX]\n" \
  "v_add_u32 v86, 28672, v84\n" \
  "v_add_u32 v87, 28672, v85\n" \
  "v_mov_b32 v89, 0x7ff00000\n" \
  "s_waitcnt vmcnt(14)\n" \
  "s_barrier\n" \
  "ds_read_b128 v[32:35], v84\n" \
  "ds_read_b128 v[36:39], v85\n" \
  "ds_read_b128 v[40:43], v84 offset:128\n" \
  "ds_read_b128 v[44:47], v85 offset:128\n" \
  "ds_read_b128 v[48:51], v84 offset:256\n" \
  "ds_read_b128 v[52:55], v85 offset:256\n" \
  ".LV_top%=:\n" \
  "ds_read_b128 v[56:59], v84 offset:384\n" \
  "s_waitcnt lgkmcnt(6)\n" \
  "v_mfma_f32_16x16x32_bf16 v[60:63], v[32:35], a[0:3], 0\n" \
  "v_mfma_f32_16x16x32_bf16 v[64:67], v[32:35], a[56:59], 0\n" \
  "ds_read_b128 v[32:35], v85 offset:384\n" \
  "s_waitcnt lgkmcnt(6)\n" \
  "v_mfma_f32_16x16x32_bf16 v[60:63], v[36:39], a[4:7], v[60:63]\n" \
  "v_mfma_f32_16x16x32_bf16 v[64:67], v[36:39], a[60:63], v[64:67]\n" \
  "ds_read_b128 v[36:39], v84 offset:512\n" \
  "s_waitcnt lgkmcnt(6)\n" \
  "v_mfma_f32_16x16x32_bf16 v[60:63], v[40:43], a[8:11], v[60:63]\n" \
  "v_mfma_f32_16x16x32_bf16 v[64:67], v[40:43], a[64:67], v[64:67]\n" \
  "v_cvt_pk_bf16_f32 v78, v68, v69\n" \
  "v_cvt_pk_bf16_f32 v79, v70, v71\n" \
  "ds_read_b128 v[40:43], v85 offset:512\n" \
  "s_waitcnt lgkmcnt(6)\n" \
  "v_mfma_f32_16x16x32_bf16 v[60:63], v[44:47], a[12:15], v[60:63]\n" \
  "v_mfma_f32_16x16x32_bf16 v[64:67], v[44:47], a[68:71], v[64:67]\n" \
  "v_cvt_pk_bf16_f32 v82, v72, v73\n" \
  "v_cvt_pk_bf16_f32 v83, v74, v75\n" \
  "ds_read_b128 v[44:47], v84 offset:640\n" \
  "s_waitcnt lgkmcnt(6)\n" \
  "v_mfma_f32_16x16x32_bf16 v[60:63], v[48:51], a[16:19], v[60:63]\n" \
  "v_mfma_f32_16x16x32_bf16 v[64:67], v[48:51], a[72:75], v[64:67]\n" \
  "s_xor_b32 s47, s54, 8192\n" \
  "buffer_store_dwordx4 v[76:79], v89, %[rs], s47 offen sc1\n" \
  "buffer_store_dwordx4 v[76:79], v89, %[rs], s54 offen sc1\n" \
  "s_xor_b32 s47, s56, 8192\n" \
  "buffer_store_dwordx4 v[80:83], v89, %[rs], s47 offen sc1\n" \
  "buffer_store_dwordx4 v[80:83], v89, %[rs], s56 offen sc1\n" \
  "ds_read_b128 v[48:51], v85 offset:640\n" \
  "s_waitcnt lgkmcnt(6)\n" \
  "v_mfma_f32_16x16x32_bf16 v[60:63], v[52:55], a[20:23], v[60:63]\n" \
  "v_mfma_f32_16x16x32_bf16 v[64:67], v[52:55], a[76:79], v[64:67]\n" \
  "v_mov_b32 v89, %[l16]\n" \
  "s_sub_u32 s46, s40, 1\n" \
  "s_cmp_lt_u32 s40, 6\n" \
  "s_cselect_b32 s46, s40, s46\n" \
  "v_mov_b32 v88, s46\n" \
  "ds_read_b128 v[52:55], v84 offset:768\n" \
  "s_waitcnt lgkmcnt(6)\n" \
  "v_mfma_f32_16x16x32_bf16 v[60:63], v[56:59], a[24:27], v[60:63]\n" \
  "v_mfma_f32_16x16x32_bf16 v[64:67], v[56:59], a[80:83], v[64:67]\n" \
  "s_lshl_b32 s49, s40, 12\n" \
  "s_and_b32 s49, s49, 8192\n" \
  "s_xor_b32 s49, s49, s88\n" \
  "s_cmp_lt_u32 s40, 2\n" \
  "s_cselect_b32 s49, 0, s49\n" \
  "s_lshl_b32 s54, s40, 20\n" \
  "s_add_u32 s54, s54, %[sO]\n" \
  "s_xor_b32 s54, s54, s49\n" \
  "ds_read_b128 v[56:59], v85 offset:768\n" \
  "s_waitcnt lgkmcnt(6)\n" \
  "v_mfma_f32_16x16x32_bf16 v[60:63], v[32:35], a[28:31], v[60:63]\n" \
  "v_mfma_f32_16x16x32_bf16 v[64:67], v[32:35], a[84:87], v[64:67]\n" \
  "s_lshl_b32 s49, s40, 12\n" \
  "s_and_b32 s49, s49, 8192\n" \
  "s_xor_b32 s49, s49, s88\n" \
  "s_cmp_lt_u32 s40, 2\n" \
  "s_cselect_b32 s49, 0, s49\n" \
  "s_lshl_b32 s56, s40, 20\n" \
  "s_add_u32 s56, s56, %[sO2]\n" \
  "s_xor_b32 s56, s56, s49\n" \
  "ds_read_b128 v[32:35], v84 offset:14336\n" \
  "s_waitcnt lgkmcnt(6)\n" \
  "v_mfma_f32_16x16x32_bf16 v[60:63], v[36:39], a[32:35], v[60:63]\n" \
  "v_mfma_f32_16x16x32_bf16 v[64:67], v[36:39], a[88:91], v[64:67]\n" \
  "ds_read_b128 v[36:39], v85 offset:14336\n" \
  "s_waitcnt lgkmcnt(6)\n" \
  "v_mfma_f32_16x16x32_bf16 v[60:63], v[40:43], a[36:39], v[60:63]\n" \
  "v_mfma_f32_16x16x32_bf16 v[64:67], v[40:43], a[92:95], v[64:67]\n" \
  "ds_read_b128 v[40:43], v84 offset:14464\n" \
  "s_waitcnt lgkmcnt(6)\n" \
  "v_mfma_f32_16x16x32_bf16 v[60:63], v[44:47], a[40:43], v[60:63]\n" \
  "v_mfma_f32_16x16x32_bf16 v[64:67], v[44:47], a[96:99], v[64:67]\n" \
  "ds_read_b128 v[44:47], v85 offset:14464\n" \
  "s_waitcnt lgkmcnt(6)\n" \
  "v_mfma_f32_16x16x32_bf16 v[60:63], v[48:51], a[44:47], v[60:63]\n" \
  "v_mfma_f32_16x16x32_bf16 v[64:67], v[48:51], a[100:103], v[64:67]\n" \
  "ds_read_b128 v[48:51], v84 offset:14592\n" \
  "s_waitcnt lgkmcnt(6)\n" \
  "v_mfma_f32_16x16x32_bf16 v[60:63], v[52:55], a[48:51], v[60:63]\n" \
  "v_mfma_f32_16x16x32_bf16 v[64:67], v[52:55], a[104:107], v[64:67]\n" \
  "ds_read_b128 v[52:55], v85 offset:14592\n" \
  "s_waitcnt lgkmcnt(6)\n" \
  "v_mfma_f32_16x16x32_bf16 v[60:63], v[56:59], a[52:55], v[60:63]\n" \
  "v_mfma_f32_16x16x32_bf16 v[64:67], v[56:59], a[108:111], v[64:67]\n" \
  "ds_read_b128 v[56:59], v84 offset:14720\n" \
  "s_waitcnt lgkmcnt(6)\n" \
  "v_mfma_f32_16x16x32_bf16 v[68:71], v[32:35], a[0:3], 0\n" \
  "v_mfma_f32_16x16x32_bf16 v[72:75], v[32:35], a[56:59], 0\n" \
  "ds_read_b128 v[32:35], v85 offset:14720\n" \
  "s_waitcnt lgkmcnt(6)\n" \
  "v_mfma_f32_16x16x32_bf16 v[68:71], v[36:39], a[4:7], v[68:71]\n" \
  "v_mfma_f32_16x16x32_bf16 v[72:75], v[36:39], a[60:63], v[72:75]\n" \
  "ds_read_b128 v[36:39], v84 offset:14848\n" \
  "s_waitcnt lgkmcnt(6)\n" \
  "v_mfma_f32_16x16x32_bf16 v[68:71], v[40:43], a[8:11], v[68:71]\n" \
  "v_mfma_f32_16x16x32_bf16 v[72:75], v[40:43], a[64:67], v[72:75]\n" \
  "v_cvt_pk_bf16_f32 v76, v60, v61\n" \
  "v_cvt_pk_bf16_f32 v77, v62, v63\n" \
  "ds_read_b128 v[40:43], v85 offset:14848\n" \
  "s_waitcnt lgkmcnt(6)\n" \
  "v_mfma_f32_16x16x32_bf16 v[68:71], v[44:47], a[12:15], v[68:71]\n" \
  "v_mfma_f32_16x16x32_bf16 v[72:75], v[44:47], a[68:71], v[72:75]\n" \
  "v_cvt_pk_bf16_f32 v80, v64, v65\n" \
  "v_cvt_pk_bf16_f32 v81, v66, v67\n" \
  "ds_read_b128 v[44:47], v84 offset:14976\n" \
  "s_waitcnt lgkmcnt(6)\n" \
  "v_mfma_f32_16x16x32_bf16 v[68:71], v[48:51], a[16:19], v[68:71]\n" \
  "v_mfma_f32_16x16x32_bf16 v[72:75], v[48:51], a[72:75], v[72:75]\n" \
  "s_cmp_lt_u32 s40, 6\n" \
  "s_cbranch_scc0 .LV_w%=\n" \
  "s_waitcnt vmcnt(0)\n" \
  ".LV_w%=:\n" \
  "s_waitcnt vmcnt(12)\n" \
  "s_barrier\n" \
  "buffer_store_dword v88, %[oPr], %[rs], 0 offen sc1\n" \
  "ds_read_b128 v[48:51], v85 offset:14976\n" \
  "s_waitcnt lgkmcnt(6)\n" \
  "v_mfma_f32_16x16x32_bf16 v[68:71], v[52:55], a[20:23], v[68:71]\n" \
  "v_mfma_f32_16x16x32_bf16 v[72:75], v[52:55], a[76:79], v[72:75]\n" \
  "s_add_u32 m0, %[mb], s52\n" \
  "s_nop 0\n" \
  "buffer_load_dwordx4 v96, %[rx], s53 offen lds\n" \
  "ds_read_b128 v[52:55], v84 offset:15104\n" \
  "s_waitcnt lgkmcnt(6)\n" \
  "v_mfma_f32_16x16x32_bf16 v[68:71], v[56:59], a[24:27], v[68:71]\n" \
  "v_mfma_f32_16x16x32_bf16 v[72:75], v[56:59], a[80:83], v[72:75]\n" \
  "buffer_load_dwordx4 v97, %[rx], s53 offen offset:1024 lds\n" \
  "ds_read_b128 v[56:59], v85 offset:15104\n" \
  "s_waitcnt lgkmcnt(6)\n" \
  "v_mfma_f32_16x16x32_bf16 v[68:71], v[32:35], a[28:31], v[68:71]\n" \
  "v_mfma_f32_16x16x32_bf16 v[72:75], v[32:35], a[84:87], v[72:75]\n" \
  "buffer_load_dwordx4 v98, %[rx], s53 offen offset:2048 lds\n" \
  "ds_read_b128 v[32:35], v86\n" \
  "s_waitcnt lgkmcnt(6)\n" \
  "v_mfma_f32_16x16x32_bf16 v[68:71], v[36:39], a[32:35], v[68:71]\n" \
  "v_mfma_f32_16x16x32_bf16 v[72:75], v[36:39], a[88:91], v[72:75]\n" \
  "buffer_load_dwordx4 v99, %[rx], s53 offen offset:3072 lds\n" \
  "ds_read_b128 v[36:39], v87\n" \
  "s_waitcnt lgkmcnt(6)\n" \
  "v_mfma_f32_16x16x32_bf16 v[68:71], v[40:43], a[36:39], v[68:71]\n" \
  "v_mfma_f32_16x16x32_bf16 v[72:75], v[40:43], a[92:95], v[72:75]\n" \
  "s_add_u32 m0, m0, 4096\n" \
  "s_add_u32 s53, s53, 4096\n" \
  "buffer_load_dwordx4 v100, %[rx], s53 offen lds\n" \
  "ds_read_b128 v[40:43], v86 offset:128\n" \
  "s_waitcnt lgkmcnt(6)\n" \
  "v_mfma_f32_16x16x32_bf16 v[68:71], v[44:47], a[40:43], v[68:71]\n" \
  "v_mfma_f32_16x16x32_bf16 v[72:75], v[44:47], a[96:99], v[72:75]\n" \
  "buffer_load_dwordx4 v101, %[rx], s53 offen offset:1024 lds\n" \
  "ds_read_b128 v[44:47], v87 offset:128\n" \
  "s_waitcnt lgkmcnt(6)\n" \
  "v_mfma_f32_16x16x32_bf16 v[68:71], v[48:51], a[44:47], v[68:71]\n" \
  "v_mfma_f32_16x16x32_bf16 v[72:75], v[48:51], a[100:103], v[72:75]\n" \
  "buffer_load_dwordx4 v102, %[rx], s53 offen offset:2048 lds\n" \
  "s_add_u32 s53, s53, 24576\n" \
  "s_add_u32 s52, s52, 28672\n" \
  "s_cmp_eq_u32 s52, 114688\n" \
  "s_cselect_b32 s52, 0, s52\n" \
  "ds_read_b128 v[48:51], v86 offset:256\n" \
  "s_waitcnt lgkmcnt(6)\n" \
  "v_mfma_f32_16x16x32_bf16 v[68:71], v[52:55], a[48:51], v[68:71]\n" \
  "v_mfma_f32_16x16x32_bf16 v[72:75], v[52:55], a[104:107], v[72:75]\n" \
  "ds_read_b128 v[52:55], v87 offset:256\n" \
  "s_waitcnt lgkmcnt(6)\n" \
  "v_mfma_f32_16x16x32_bf16 v[68:71], v[56:59], a[52:55], v[68:71]\n" \
  "v_mfma_f32_16x16x32_bf16 v[72:75], v[56:59], a[108:111], v[72:75]\n" \
  "s_cmp_lg_u32 s40, 2\n" \
  "s_cbranch_scc1 .LV_n0%=\n" \
  "s_nop 4\n" \
  "v_cvt_pk_bf16_f32 v78, v68, v69\n" \
  "v_cvt_pk_bf16_f32 v79, v70, v71\n" \
  "v_cvt_pk_bf16_f32 v82, v72, v73\n" \
  "v_cvt_pk_bf16_f32 v83, v74, v75\n" \
  "s_xor_b32 s47, s54, 8192\n" \
  "buffer_store_dwordx4 v[76:79], v89, %[rs], s47 offen sc1\n" \
  "buffer_store_dwordx4 v[76:79], v89, %[rs], s54 offen sc1\n" \
  "s_xor_b32 s47, s56, 8192\n" \
  "buffer_store_dwordx4 v[80:83], v89, %[rs], s47 offen sc1\n" \
  "buffer_store_dwordx4 v[80:83], v89, %[rs], s56 offen sc1\n" \
  "v_mov_b32 v88, 3\n" \
  "s_waitcnt vmcnt(0)\n" \
  "s_barrier\n" \
  "buffer_store_dword v88, %[oPr], %[rs], 0 offen sc1\n" \
  "v_mov_b32 v89, 0x7ff00000\n" \
  ".LV_n0%=:\n" \
  "s_add_u32 s55, s55, 28672\n" \
  "s_cmp_eq_u32 s55, 114688\n" \
  "s_cselect_b32 s55, 0, s55\n" \
  "v_mov_b32 v84, v86\n" \
  "v_mov_b32 v85, v87\n" \
  "v_add_u32 v86, s55, %[rX]\n" \
  "v_xor_b32 v87, 64, v86\n" \
  "s_add_u32 s40, s40, 1\n" \
  "s_cmp_lt_u32 s40, 64\n" \
  "s_cbranch_scc1 .LV_top%=\n" \
  "v_cvt_pk_bf16_f32 v78, v68, v69\n" \
  "v_cvt_pk_bf16_f32 v79, v70, v71\n" \
  "v_cvt_pk_bf16_f32 v82, v72, v73\n" \
  "v_cvt_pk_bf16_f32 v83, v74, v75\n" \
  "s_xor_b32 s47, s54, 8192\n" \
  "buffer_store_dwordx4 v[76:79], v89, %[rs], s47 offen sc1\n" \
  "buffer_store_dwordx4 v[76:79], v89, %[rs], s54 offen sc1\n" \
  "s_xor_b32 s47, s56, 8192\n" \
  "buffer_store_dwordx4 v[80:83], v89, %[rs], s47 offen sc1\n" \
  "buffer_store_dwordx4 v[80:83], v89, %[rs], s56 offen sc1\n" \
  "v_mov_b32 v88, 64\n" \
  "s_waitcnt vmcnt(0) lgkmcnt(0)\n" \
  "s_barrier\n" \
  "buffer_store_dword v88, %[oPr], %[rs], 0 offen sc1\n" \
  ".LV_zend%=:\n" \
  ""
#define ASM_VS_CLOB "memory", "scc", "vcc", "a0", "a1", "a2", "a3", "a4", "a5", "a6", "a7", "a8", "a9", "a10", "a11", "a12", "a13", "a14", "a15", "a16", "a17", "a18", "a19", "a20", "a21", "a22", "a23", "a24", "a25", "a26", "a27", "a28", "a29", "a30", "a31", "a32", "a33", "a34", "a35", "a36", "a37", "a38", "a39", "a40", "a41", "a42", "a43", "a44", "a45", "a46", "a47", "a48", "a49", "a50", "a51", "a52", "a53", "a54", "a55", "a56", "a57", "a58", "a59", "a60", "a61", "a62", "a63", "a64", "a65", "a66", "a67", "a68", "a69", "a70", "a71", "a72", "a73", "a74", "a75", "a76", "a77", "a78", "a79", "a80", "a81", "a82", "a83", "a84", "a85", "a86", "a87", "a88", "a89", "a90", "a91", "a92", "a93", "a94", "a95", "a96", "a97", "a98", "a99", "a100", "a101", "a102", "a103", "a104", "a105", "a106", "a107", "a108", "a109", "a110", "a111", "s40", "s46", "s47", "s49", "s52", "s53", "s54", "s55", "s56", "s88", "v30", "v32", "v33", "v34", "v35", "v36", "v37", "v38", "v39", "v40", "v41", "v42", "v43", "v44", "v45", "v46", "v47", "v48", "v49", "v50", "v51", "v52", "v53", "v54", "v55", "v56", "v57", "v58", "v59", "v60", "v61", "v62", "v63", "v64", "v65", "v66", "v67", "v68", "v69", "v70", "v71", "v72", "v73", "v74", "v75", "v76", "v77", "v78", "v79", "v80", "v81", "v82", "v83", "v84", "v85", "v86", "v87", "v88", "v89", "v96", "v97", "v98", "v99", "v100", "v101", "v102", "v103", "v208", "v209", "v210", "v211", "v212", "v213", "v214", "v215", "v216", "v217", "v218", "v219", "v220", "v221", "v222", "v223"
#define ASM_L \
  "s_nop 4\n" \
  "s_mov_b32 s58, 0x3fb8aa3b\n" \
  "s_mov_b32 s59, 0x358637bd\n" \
  "s_mov_b32 s62, 0x10001000\n" \
  "s_mov_b32 s63, 0x10001000\n" \
  "v_mov_b32 v126, 0x3f803f80\n" \
  "v_mov_b32 v127, 1\n" \
  "s_sleep 36\n" \
  "s_sleep 36\n" \
  "s_sleep 36\n" \
  "s_mul_i32 s47, %[w], 7\n" \
  "s_lshl_b32 m0, s47, 10\n" \
  "s_add_u32 m0, m0, 0\n" \
  "s_mul_i32 s46, s47, 43008\n" \
  "s_add_u32 s46, s46, 0\n" \
  "buffer_load_dwordx4 %[oWq], %[rq], s46 offen lds\n" \
  "s_add_u32 m0, m0, 1024\n" \
  "s_add_u32 s46, s46, 43008\n" \
  "buffer_load_dwordx4 %[oWq], %[rq], s46 offen lds\n" \
  "s_add_u32 m0, m0, 1024\n" \
  "s_add_u32 s46, s46, 43008\n" \
  "buffer_load_dwordx4 %[oWq], %[rq], s46 offen lds\n" \
  "s_add_u32 m0, m0, 1024\n" \
  "s_add_u32 s46, s46, 43008\n" \
  "buffer_load_dwordx4 %[oWq], %[rq], s46 offen lds\n" \
  "s_add_u32 m0, m0, 1024\n" \
  "s_add_u32 s46, s46, 43008\n" \
  "buffer_load_dwordx4 %[oWq], %[rq], s46 offen lds\n" \
  "s_add_u32 m0, m0, 1024\n" \
  "s_add_u32 s46, s46, 43008\n" \
  "buffer_load_dwordx4 %[oWq], %[rq], s46 offen lds\n" \
  "s_add_u32 m0, m0, 1024\n" \
  "s_add_u32 s46, s46, 43008\n" \
  "buffer_load_dwordx4 %[oWq], %[rq], s46 offen lds\n" \
  "s_add_u32 m0, m0, 1024\n" \
  "s_add_u32 s46, s46, 43008\n" \
  "s_lshl_b32 m0, s47, 10\n" \
  "s_add_u32 m0, m0, 28672\n" \
  "s_mul_i32 s46, s47, 43008\n" \
  "s_add_u32 s46, s46, 896\n" \
  "buffer_load_dwordx4 %[oWq], %[rq], s46 offen lds\n" \
  "s_add_u32 m0, m0, 1024\n" \
  "s_add_u32 s46, s46, 43008\n" \
  "buffer_load_dwordx4 %[oWq], %[rq], s46 offen lds\n" \
  "s_add_u32 m0, m0, 1024\n" \
  "s_add_u32 s46, s46, 43008\n" \
  "buffer_load_dwordx4 %[oWq], %[rq], s46 offen lds\n" \
  "s_add_u32 m0, m0, 1024\n" \
  "s_add_u32 s46, s46, 43008\n" \
  "buffer_load_dwordx4 %[oWq], %[rq], s46 offen lds\n" \
  "s_add_u32 m0, m0, 1024\n" \
  "s_add_u32 s46, s46, 43008\n" \
  "buffer_load_dwordx4 %[oWq], %[rq], s46 offen lds\n" \
  "s_add_u32 m0, m0, 1024\n" \
  "s_add_u32 s46, s46, 43008\n" \
  "buffer_load_dwordx4 %[oWq], %[rq], s46 offen lds\n" \
  "s_add_u32 m0, m0, 1024\n" \
  "s_add_u32 s46, s46, 43008\n" \
  "buffer_load_dwordx4 %[oWq], %[rq], s46 offen lds\n" \
  "s_add_u32 m0, m0, 1024\n" \
  "s_add_u32 s46, s46, 43008\n" \
  "s_lshl_b32 m0, s47, 10\n" \
  "s_add_u32 m0, m0, 57344\n" \
  "s_mul_i32 s46, s47, 43008\n" \
  "s_add_u32 s46, s46, 1792\n" \
  "buffer_load_dwordx4 %[oWq], %[rq], s46 offen lds\n" \
  "s_add_u32 m0, m0, 1024\n" \
  "s_add_u32 s46, s46, 43008\n" \
  "buffer_load_dwordx4 %[oWq], %[rq], s46 offen lds\n" \
  "s_add_u32 m0, m0, 1024\n" \
  "s_add_u32 s46, s46, 43008\n" \
  "buffer_load_dwordx4 %[oWq], %[rq], s46 offen lds\n" \
  "s_add_u32 m0, m0, 1024\n" \
  "s_add_u32 s46, s46, 43008\n" \
  "buffer_load_dwordx4 %[oWq], %[rq], s46 offen lds\n" \
  "s_add_u32 m0, m0, 1024\n" \
  "s_add_u32 s46, s46, 43008\n" \
  "buffer_load_dwordx4 %[oWq], %[rq], s46 offen lds\n" \
  "s_add_u32 m0, m0, 1024\n" \
  "s_add_u32 s46, s46, 43008\n" \
  "buffer_load_dwordx4 %[oWq], %[rq], s46 offen lds\n" \
  "s_add_u32 m0, m0, 1024\n" \
  "s_add_u32 s46, s46, 43008\n" \
  "buffer_load_dwordx4 %[oWq], %[rq], s46 offen lds\n" \
  "s_add_u32 m0, m0, 1024\n" \
  "s_add_u32 s46, s46, 43008\n" \
  "s_lshl_b32 s46, s47, 10\n" \
  "s_add_u32 m0, s46, 86016\n" \
  "s_add_u32 s46, s46, %[sWo]\n" \
  "buffer_load_dwordx4 %[l16], %[ro], s46 offen lds\n" \
  "s_add_u32 m0, m0, 1024\n" \
  "s_add_u32 s46, s46, 1024\n" \
  "buffer_load_dwordx4 %[l16], %[ro], s46 offen lds\n" \
  "s_add_u32 m0, m0, 1024\n" \
  "s_add_u32 s46, s46, 1024\n" \
  "buffer_load_dwordx4 %[l16], %[ro], s46 offen lds\n" \
  "s_add_u32 m0, m0, 1024\n" \
  "s_add_u32 s46, s46, 1024\n" \
  "buffer_load_dwordx4 %[l16], %[ro], s46 offen lds\n" \
  "s_add_u32 m0, m0, 1024\n" \
  "s_add_u32 s46, s46, 1024\n" \
  "buffer_load_dwordx4 %[l16], %[ro], s46 offen lds\n" \
  "s_add_u32 m0, m0, 1024\n" \
  "s_add_u32 s46, s46, 1024\n" \
  "buffer_load_dwordx4 %[l16], %[ro], s46 offen lds\n" \
  "s_add_u32 m0, m0, 1024\n" \
  "s_add_u32 s46, s46, 1024\n" \
  "buffer_load_dwordx4 %[l16], %[ro], s46 offen lds\n" \
  "s_add_u32 m0, m0, 1024\n" \
  "s_add_u32 s46, s46, 1024\n" \
  "s_waitcnt vmcnt(0)\n" \
  "s_barrier\n" \
  "ds_read_b64_tr_b16 v[128:129], %[rWq]\n" \
  "ds_read_b64_tr_b16 v[130:131], %[rWq] offset:256\n" \
  "ds_read_b64_tr_b16 v[132:133], %[rWq] offset:2048\n" \
  "ds_read_b64_tr_b16 v[134:135], %[rWq] offset:2304\n" \
  "ds_read_b64_tr_b16 v[136:137], %[rWq] offset:4096\n" \
  "ds_read_b64_tr_b16 v[138:139], %[rWq] offset:4352\n" \
  "ds_read_b64_tr_b16 v[140:141], %[rWq] offset:6144\n" \
  "ds_read_b64_tr_b16 v[142:143], %[rWq] offset:6400\n" \
  "ds_read_b64_tr_b16 v[144:145], %[rWq] offset:8192\n" \
  "ds_read_b64_tr_b16 v[146:147], %[rWq] offset:8448\n" \
  "ds_read_b64_tr_b16 v[148:149], %[rWq] offset:10240\n" \
  "ds_read_b64_tr_b16 v[150:151], %[rWq] offset:10496\n" \
  "ds_read_b64_tr_b16 v[152:153], %[rWq] offset:12288\n" \
  "ds_read_b64_tr_b16 v[154:155], %[rWq] offset:12544\n" \
  "ds_read_b64_tr_b16 v[156:157], %[rWq] offset:28672\n" \
  "ds_read_b64_tr_b16 v[158:159], %[rWq] offset:28928\n" \
  "ds_read_b64_tr_b16 v[160:161], %[rWq] offset:30720\n" \
  "ds_read_b64_tr_b16 v[162:163], %[rWq] offset:30976\n" \
  "ds_read_b64_tr_b16 v[164:165], %[rWq] offset:32768\n" \
  "ds_read_b64_tr_b16 v[166:167], %[rWq] offset:33024\n" \
  "ds_read_b64_tr_b16 v[168:169], %[rWq] offset:34816\n" \
  "ds_read_b64_tr_b16 v[170:171], %[rWq] offset:35072\n" \
  "ds_read_b64_tr_b16 v[172:173], %[rWq] offset:36864\n" \
  "ds_read_b64_tr_b16 v[174:175], %[rWq] offset:37120\n" \
  "ds_read_b64_tr_b16 v[176:177], %[rWq] offset:38912\n" \
  "ds_read_b64_tr_b16 v[178:179], %[rWq] offset:39168\n" \
  "ds_read_b64_tr_b16 v[180:181], %[rWq] offset:40960\n" \
  "ds_read_b64_tr_b16 v[182:183], %[rWq] offset:41216\n" \
  "v_add_u32 v96, 57344, %[rWq]\n" \
  "ds_read_b64_tr_b16 v[184:185], v96\n" \
  "ds_read_b64_tr_b16 v[186:187], v96 offset:256\n" \
  "ds_read_b64_tr_b16 v[188:189], v96 offset:2048\n" \
  "ds_read_b64_tr_b16 v[190:191], v96 offset:2304\n" \
  "ds_read_b64_tr_b16 v[192:193], v96 offset:4096\n" \
  "ds_read_b64_tr_b16 v[194:195], v96 offset:4352\n" \
  "ds_read_b64_tr_b16 v[196:197], v96 offset:6144\n" \
  "ds_read_b64_tr_b16 v[198:199], v96 offset:6400\n" \
  "ds_read_b64_tr_b16 v[200:201], v96 offset:8192\n" \
  "ds_read_b64_tr_b16 v[202:203], v96 offset:8448\n" \
  "ds_read_b64_tr_b16 v[204:205], v96 offset:10240\n" \
  "ds_read_b64_tr_b16 v[206:207], v96 offset:10496\n" \
  "ds_read_b64_tr_b16 v[208:209], v96 offset:12288\n" \
  "ds_read_b64_tr_b16 v[210:211], v96 offset:12544\n" \
  "ds_read_b64_tr_b16 v[212:213], %[rWo]\n" \
  "ds_read_b64_tr_b16 v[214:215], %[rWo] offset:14336\n" \
  "ds_read_b64_tr_b16 v[216:217], %[rWo] offset:32\n" \
  "ds_read_b64_tr_b16 v[218:219], %[rWo] offset:14368\n" \
  "ds_read_b64_tr_b16 v[220:221], %[rWo] offset:64\n" \
  "ds_read_b64_tr_b16 v[222:223], %[rWo] offset:14400\n" \
  "ds_read_b64_tr_b16 v[224:225], %[rWo] offset:96\n" \
  "ds_read_b64_tr_b16 v[226:227], %[rWo] offset:14432\n" \
  "ds_read_b64_tr_b16 v[228:229], %[rWo] offset:128\n" \
  "ds_read_b64_tr_b16 v[230:231], %[rWo] offset:14464\n" \
  "ds_read_b64_tr_b16 v[232:233], %[rWo] offset:160\n" \
  "ds_read_b64_tr_b16 v[234:235], %[rWo] offset:14496\n" \
  "ds_read_b64_tr_b16 v[236:237], %[rWo] offset:192\n" \
  "ds_read_b64_tr_b16 v[238:239], %[rWo] offset:14528\n" \
  "s_add_u32 s46, %[sLW], 0\n" \
  "s_waitcnt lgkmcnt(15)\n" \
  "buffer_store_dwordx4 v[128:131], %[l16], %[rs], s46 offen sc1\n" \
  "buffer_store_dwordx4 v[132:135], %[l16], %[rs], s46 offen offset:1024 sc1\n" \
  "buffer_store_dwordx4 v[136:139], %[l16], %[rs], s46 offen offset:2048 sc1\n" \
  "buffer_store_dwordx4 v[140:143], %[l16], %[rs], s46 offen offset:3072 sc1\n" \
  "s_add_u32 s46, %[sLW], 4096\n" \
  "buffer_store_dwordx4 v[144:147], %[l16], %[rs], s46 offen sc1\n" \
  "buffer_store_dwordx4 v[148:151], %[l16], %[rs], s46 offen offset:1024 sc1\n" \
  "buffer_store_dwordx4 v[152:155], %[l16], %[rs], s46 offen offset:2048 sc1\n" \
  "s_add_u32 s46, %[sLW], 458752\n" \
  "buffer_store_dwordx4 v[156:159], %[l16], %[rs], s46 offen sc1\n" \
  "buffer_store_dwordx4 v[160:163], %[l16], %[rs], s46 offen offset:1024 sc1\n" \
  "buffer_store_dwordx4 v[164:167], %[l16], %[rs], s46 offen offset:2048 sc1\n" \
  "buffer_store_dwordx4 v[168:171], %[l16], %[rs], s46 offen offset:3072 sc1\n" \
  "s_add_u32 s46, %[sLW], 462848\n" \
  "buffer_store_dwordx4 v[172:175], %[l16], %[rs], s46 offen sc1\n" \
  "buffer_store_dwordx4 v[176:179], %[l16], %[rs], s46 offen offset:1024 sc1\n" \
  "buffer_store_dwordx4 v[180:183], %[l16], %[rs], s46 offen offset:2048 sc1\n" \
  "s_add_u32 s46, %[sLW], 917504\n" \
  "buffer_store_dwordx4 v[184:187], %[l16], %[rs], s46 offen sc1\n" \
  "buffer_store_dwordx4 v[188:191], %[l16], %[rs], s46 offen offset:1024 sc1\n" \
  "buffer_store_dwordx4 v[192:195], %[l16], %[rs], s46 offen offset:2048 sc1\n" \
  "buffer_store_dwordx4 v[196:199], %[l16], %[rs], s46 offen offset:3072 sc1\n" \
  "s_add_u32 s46, %[sLW], 921600\n" \
  "buffer_store_dwordx4 v[200:203], %[l16], %[rs], s46 offen sc1\n" \
  "buffer_store_dwordx4 v[204:207], %[l16], %[rs], s46 offen offset:1024 sc1\n" \
  "s_waitcnt lgkmcnt(14)\n" \
  "buffer_store_dwordx4 v[208:211], %[l16], %[rs], s46 offen offset:2048 sc1\n" \
  "s_waitcnt lgkmcnt(0)\n" \
  "s_mov_b32 exec_lo, 0\n" \
  "s_mov_b32 exec_hi, 0xffff0000\n" \
  "v_mov_b32 v214, 0\n" \
  "v_mov_b32 v215, 0\n" \
  "v_mov_b32 v218, 0\n" \
  "v_mov_b32 v219, 0\n" \
  "v_mov_b32 v222, 0\n" \
  "v_mov_b32 v223, 0\n" \
  "v_mov_b32 v226, 0\n" \
  "v_mov_b32 v227, 0\n" \
  "v_mov_b32 v230, 0\n" \
  "v_mov_b32 v231, 0\n" \
  "v_mov_b32 v234, 0\n" \
  "v_mov_b32 v235, 0\n" \
  "v_mov_b32 v238, 0\n" \
  "v_mov_b32 v239, 0\n" \
  "s_mov_b64 exec, -1\n" \
  "s_add_u32 s46, %[sLO], 0\n" \
  "buffer_store_dwordx4 v[212:215], %[l16], %[rs], s46 offen sc1\n" \
  "s_add_u32 s46, %[sLO], 16384\n" \
  "buffer_store_dwordx4 v[216:219], %[l16], %[rs], s46 offen sc1\n" \
  "s_add_u32 s46, %[sLO], 32768\n" \
  "buffer_store_dwordx4 v[220:223], %[l16], %[rs], s46 offen sc1\n" \
  "s_add_u32 s46, %[sLO], 49152\n" \
  "buffer_store_dwordx4 v[224:227], %[l16], %[rs], s46 offen sc1\n" \
  "s_add_u32 s46, %[sLO], 65536\n" \
  "buffer_store_dwordx4 v[228:231], %[l16], %[rs], s46 offen sc1\n" \
  "s_add_u32 s46, %[sLO], 81920\n" \
  "buffer_store_dwordx4 v[232:235], %[l16], %[rs], s46 offen sc1\n" \
  "s_add_u32 s46, %[sLO], 98304\n" \
  "buffer_store_dwordx4 v[236:239], %[l16], %[rs], s46 offen sc1\n" \
  "s_waitcnt vmcnt(0) lgkmcnt(0)\n" \
  "s_barrier\n" \
  "buffer_store_dword v127, %[oL0], %[rs], 0 offen sc1\n" \
  "v_add_u32 v39, 0, %[l16]\n" \
  "v_lshrrev_b32 v39, 8, v39\n" \
  "v_mul_u32_u24 v39, 9363, v39\n" \
  "v_lshrrev_b32 v39, 12, v39\n" \
  "v_and_b32 v39, 0x70, v39\n" \
  "v_xor_b32 v32, %[l16], v39\n" \
  "v_add_u32 v39, 1024, %[l16]\n" \
  "v_lshrrev_b32 v39, 8, v39\n" \
  "v_mul_u32_u24 v39, 9363, v39\n" \
  "v_lshrrev_b32 v39, 12, v39\n" \
  "v_and_b32 v39, 0x70, v39\n" \
  "v_xor_b32 v33, %[l16], v39\n" \
  "v_add_u32 v39, 2048, %[l16]\n" \
  "v_lshrrev_b32 v39, 8, v39\n" \
  "v_mul_u32_u24 v39, 9363, v39\n" \
  "v_lshrrev_b32 v39, 12, v39\n" \
  "v_and_b32 v39, 0x70, v39\n" \
  "v_xor_b32 v34, %[l16], v39\n" \
  "v_add_u32 v39, 3072, %[l16]\n" \
  "v_lshrrev_b32 v39, 8, v39\n" \
  "v_mul_u32_u24 v39, 9363, v39\n" \
  "v_lshrrev_b32 v39, 12, v39\n" \
  "v_and_b32 v39, 0x70, v39\n" \
  "v_xor_b32 v35, %[l16], v39\n" \
  "v_add_u32 v39, 4096, %[l16]\n" \
  "v_lshrrev_b32 v39, 8, v39\n" \
  "v_mul_u32_u24 v39, 9363, v39\n" \
  "v_lshrrev_b32 v39, 12, v39\n" \
  "v_and_b32 v39, 0x70, v39\n" \
  "v_xor_b32 v36, %[l16], v39\n" \
  "v_add_u32 v39, 5120, %[l16]\n" \
  "v_lshrrev_b32 v39, 8, v39\n" \
  "v_mul_u32_u24 v39, 9363, v39\n" \
  "v_lshrrev_b32 v39, 12, v39\n" \
  "v_and_b32 v39, 0x70, v39\n" \
  "v_xor_b32 v37, %[l16], v39\n" \
  "v_add_u32 v39, 6144, %[l16]\n" \
  "v_lshrrev_b32 v39, 8, v39\n" \
  "v_mul_u32_u24 v39, 9363, v39\n" \
  "v_lshrrev_b32 v39, 12, v39\n" \
  "v_and_b32 v39, 0x70, v39\n" \
  "v_xor_b32 v38, %[l16], v39\n" \
  "s_mov_b32 m0, %[mb]\n" \
  "s_mov_b32 s46, %[sX]\n" \
  "buffer_load_dwordx4 v32, %[rx], s46 offen lds\n" \
  "v_xor_b32 v32, 64, v32\n" \
  "s_add_u32 m0, m0, 1024\n" \
  "s_add_u32 s46, s46, 1024\n" \
  "buffer_load_dwordx4 v33, %[rx], s46 offen lds\n" \
  "v_xor_b32 v33, 64, v33\n" \
  "s_add_u32 m0, m0, 1024\n" \
  "s_add_u32 s46, s46, 1024\n" \
  "buffer_load_dwordx4 v34, %[rx], s46 offen lds\n" \
  "v_xor_b32 v34, 64, v34\n" \
  "s_add_u32 m0, m0, 1024\n" \
  "s_add_u32 s46, s46, 1024\n" \
  "buffer_load_dwordx4 v35, %[rx], s46 offen lds\n" \
  "v_xor_b32 v35, 64, v35\n" \
  "s_add_u32 m0, m0, 1024\n" \
  "s_add_u32 s46, s46, 1024\n" \
  "buffer_load_dwordx4 v36, %[rx], s46 offen lds\n" \
  "v_xor_b32 v36, 64, v36\n" \
  "s_add_u32 m0, m0, 1024\n" \
  "s_add_u32 s46, s46, 1024\n" \
  "buffer_load_dwordx4 v37, %[rx], s46 offen lds\n" \
  "v_xor_b32 v37, 64, v37\n" \
  "s_add_u32 m0, m0, 1024\n" \
  "s_add_u32 s46, s46, 1024\n" \
  "buffer_load_dwordx4 v38, %[rx], s46 offen lds\n" \
  "v_xor_b32 v38, 64, v38\n" \
  "s_add_u32 m0, m0, 1024\n" \
  "s_add_u32 s46, s46, 1024\n" \
  "buffer_load_dwordx4 v32, %[rx], s46 offen lds\n" \
  "v_xor_b32 v32, 64, v32\n" \
  "s_add_u32 m0, m0, 1024\n" \
  "s_add_u32 s46, s46, 1024\n" \
  "buffer_load_dwordx4 v33, %[rx], s46 offen lds\n" \
  "v_xor_b32 v33, 64, v33\n" \
  "s_add_u32 m0, m0, 1024\n" \
  "s_add_u32 s46, s46, 1024\n" \
  "buffer_load_dwordx4 v34, %[rx], s46 offen lds\n" \
  "v_xor_b32 v34, 64, v34\n" \
  "s_add_u32 m0, m0, 1024\n" \
  "s_add_u32 s46, s46, 1024\n" \
  "buffer_load_dwordx4 v35, %[rx], s46 offen lds\n" \
  "v_xor_b32 v35, 64, v35\n" \
  "s_add_u32 m0, m0, 1024\n" \
  "s_add_u32 s46, s46, 1024\n" \
  "buffer_load_dwordx4 v36, %[rx], s46 offen lds\n" \
  "v_xor_b32 v36, 64, v36\n" \
  "s_add_u32 m0, m0, 1024\n" \
  "s_add_u32 s46, s46, 1024\n" \
  "buffer_load_dwordx4 v37, %[rx], s46 offen lds\n" \
  "v_xor_b32 v37, 64, v37\n" \
  "s_add_u32 m0, m0, 1024\n" \
  "s_add_u32 s46, s46, 1024\n" \
  "buffer_load_dwordx4 v38, %[rx], s46 offen lds\n" \
  "v_xor_b32 v38, 64, v38\n" \
  "s_add_u32 m0, m0, 1024\n" \
  "s_add_u32 s46, s46, 1024\n" \
  "buffer_load_dwordx4 v32, %[rx], s46 offen lds\n" \
  "v_xor_b32 v32, 64, v32\n" \
  "s_add_u32 m0, m0, 1024\n" \
  "s_add_u32 s46, s46, 1024\n" \
  "buffer_load_dwordx4 v33, %[rx], s46 offen lds\n" \
  "v_xor_b32 v33, 64, v33\n" \
  "s_add_u32 m0, m0, 1024\n" \
  "s_add_u32 s46, s46, 1024\n" \
  "buffer_load_dwordx4 v34, %[rx], s46 offen lds\n" \
  "v_xor_b32 v34, 64, v34\n" \
  "s_add_u32 m0, m0, 1024\n" \
  "s_add_u32 s46, s46, 1024\n" \
  "buffer_load_dwordx4 v35, %[rx], s46 offen lds\n" \
  "v_xor_b32 v35, 64, v35\n" \
  "s_add_u32 m0, m0, 1024\n" \
  "s_add_u32 s46, s46, 1024\n" \
  "buffer_load_dwordx4 v36, %[rx], s46 offen lds\n" \
  "v_xor_b32 v36, 64, v36\n" \
  "s_add_u32 m0, m0, 1024\n" \
  "s_add_u32 s46, s46, 1024\n" \
  "buffer_load_dwordx4 v37, %[rx], s46 offen lds\n" \
  "v_xor_b32 v37, 64, v37\n" \
  "s_add_u32 m0, m0, 1024\n" \
  "s_add_u32 s46, s46, 1024\n" \
  "buffer_load_dwordx4 v38, %[rx], s46 offen lds\n" \
  "v_xor_b32 v38, 64, v38\n" \
  "s_add_u32 m0, m0, 1024\n" \
  "s_add_u32 s46, s46, 1024\n" \
  "buffer_load_dwordx4 v32, %[rx], s46 offen lds\n" \
  "v_xor_b32 v32, 64, v32\n" \
  "s_add_u32 m0, m0, 1024\n" \
  "s_add_u32 s46, s46, 1024\n" \
  "buffer_load_dwordx4 v33, %[rx], s46 offen lds\n" \
  "v_xor_b32 v33, 64, v33\n" \
  "s_add_u32 m0, m0, 1024\n" \
  "s_add_u32 s46, s46, 1024\n" \
  "buffer_load_dwordx4 v34, %[rx], s46 offen lds\n" \
  "v_xor_b32 v34, 64, v34\n" \
  "s_add_u32 m0, m0, 1024\n" \
  "s_add_u32 s46, s46, 1024\n" \
  "buffer_load_dwordx4 v35, %[rx], s46 offen lds\n" \
  "v_xor_b32 v35, 64, v35\n" \
  "s_add_u32 m0, m0, 1024\n" \
  "s_add_u32 s46, s46, 1024\n" \
  "buffer_load_dwordx4 v36, %[rx], s46 offen lds\n" \
  "v_xor_b32 v36, 64, v36\n" \
  "s_add_u32 m0, m0, 1024\n" \
  "s_add_u32 s46, s46, 1024\n" \
  "buffer_load_dwordx4 v37, %[rx], s46 offen lds\n" \
  "v_xor_b32 v37, 64, v37\n" \
  "s_add_u32 m0, m0, 1024\n" \
  "s_add_u32 s46, s46, 1024\n" \
  "buffer_load_dwordx4 v38, %[rx], s46 offen lds\n" \
  "v_xor_b32 v38, 64, v38\n" \
  "s_add_u32 m0, m0, 1024\n" \
  "s_add_u32 s46, s46, 1024\n" \
  "s_mov_b32 s44, 262144\n" \
  "s_mov_b32 exec_hi, 0\n" \
  "s_mov_b32 exec_lo, 0xf\n" \
  "s_branch .LL_w0_in%=\n" \
  ".LL_w0%=:\n" \
  "s_sleep 2\n" \
  "s_sub_u32 s44, s44, 1\n" \
  "s_cbranch_scc1 .LL_abort%=\n" \
  ".LL_w0_in%=:\n" \
  "buffer_load_dword v96, %[pL0], %[rs], 0 offen sc1\n" \
  "s_waitcnt vmcnt(0)\n" \
  "v_cmp_gt_i32 vcc, 1, v96\n" \
  "s_cbranch_vccnz .LL_w0%=\n" \
  "s_mov_b64 exec, -1\n" \
  "s_barrier\n" \
  "s_mov_b32 s57, 0\n" \
  "s_mov_b32 s60, %[sKVP]\n" \
  "s_mul_i32 s46, s57, 28672\n" \
  "s_add_u32 s46, s46, %[sWq]\n" \
  "s_add_u32 s52, s46, 458752\n" \
  "s_add_u32 s53, s46, 473088\n" \
  "s_add_u32 s54, s46, 917504\n" \
  "s_add_u32 s55, s46, 931840\n" \
  "buffer_load_dwordx4 a[128:131], %[l16], %[rs], s52 offen sc1\n" \
  "buffer_load_dwordx4 a[132:135], %[l16], %[rs], s53 offen sc1\n" \
  "buffer_load_dwordx4 a[136:139], %[l16], %[rs], s54 offen sc1\n" \
  "buffer_load_dwordx4 a[140:143], %[l16], %[rs], s55 offen sc1\n" \
  "buffer_load_dwordx4 a[144:147], %[l16], %[rs], s52 offen offset:1024 sc1\n" \
  "buffer_load_dwordx4 a[148:151], %[l16], %[rs], s53 offen offset:1024 sc1\n" \
  "buffer_load_dwordx4 a[152:155], %[l16], %[rs], s54 offen offset:1024 sc1\n" \
  "buffer_load_dwordx4 a[156:159], %[l16], %[rs], s55 offen offset:1024 sc1\n" \
  "v_mov_b32 v120, %[rX]\n" \
  "v_add_u32 v121, 57344, %[rX]\n" \
  "v_xor_b32 v122, 64, v120\n" \
  "v_xor_b32 v123, 64, v121\n" \
  "ds_read_b128 v[32:35], v120\n" \
  "ds_read_b128 v[36:39], v120 offset:14336\n" \
  "ds_read_b128 v[40:43], v120 offset:28672\n" \
  "ds_read_b128 v[44:47], v120 offset:43008\n" \
  "ds_read_b128 v[48:51], v121\n" \
  "ds_read_b128 v[52:55], v121 offset:14336\n" \
  "ds_read_b128 v[56:59], v121 offset:28672\n" \
  "ds_read_b128 v[60:63], v121 offset:43008\n" \
  "v_add_u32 v120, 128, v120\n" \
  "v_add_u32 v121, 128, v121\n" \
  ".LL_kv%=:\n" \
  "s_waitcnt vmcnt(7) lgkmcnt(7)\n" \
  "v_mfma_f32_16x16x32_bf16 v[128:131], v[32:35], a[128:131], 0\n" \
  "s_waitcnt lgkmcnt(6)\n" \
  "v_mfma_f32_16x16x32_bf16 v[132:135], v[36:39], a[128:131], 0\n" \
  "s_waitcnt lgkmcnt(5)\n" \
  "v_mfma_f32_16x16x32_bf16 v[136:139], v[40:43], a[128:131], 0\n" \
  "s_waitcnt lgkmcnt(4)\n" \
  "v_mfma_f32_16x16x32_bf16 v[140:143], v[44:47], a[128:131], 0\n" \
  "s_waitcnt lgkmcnt(3)\n" \
  "v_mfma_f32_16x16x32_bf16 v[144:147], v[48:51], a[128:131], 0\n" \
  "s_waitcnt lgkmcnt(2)\n" \
  "v_mfma_f32_16x16x32_bf16 v[148:151], v[52:55], a[128:131], 0\n" \
  "s_waitcnt lgkmcnt(1)\n" \
  "v_mfma_f32_16x16x32_bf16 v[152:155], v[56:59], a[128:131], 0\n" \
  "s_waitcnt lgkmcnt(0)\n" \
  "v_mfma_f32_16x16x32_bf16 v[156:159], v[60:63], a[128:131], 0\n" \
  "buffer_load_dwordx4 a[128:131], %[l16], %[rs], s52 offen offset:2048 sc1\n" \
  "s_waitcnt vmcnt(7)\n" \
  "v_mfma_f32_16x16x32_bf16 v[160:163], v[32:35], a[132:135], 0\n" \
  "ds_read_b128 v[64:67], v122\n" \
  "v_mfma_f32_16x16x32_bf16 v[164:167], v[36:39], a[132:135], 0\n" \
  "ds_read_b128 v[68:71], v122 offset:14336\n" \
  "v_mfma_f32_16x16x32_bf16 v[168:171], v[40:43], a[132:135], 0\n" \
  "ds_read_b128 v[72:75], v122 offset:28672\n" \
  "v_mfma_f32_16x16x32_bf16 v[172:175], v[44:47], a[132:135], 0\n" \
  "ds_read_b128 v[76:79], v122 offset:43008\n" \
  "v_mfma_f32_16x16x32_bf16 v[176:179], v[48:51], a[132:135], 0\n" \
  "ds_read_b128 v[80:83], v123\n" \
  "v_mfma_f32_16x16x32_bf16 v[180:183], v[52:55], a[132:135], 0\n" \
  "ds_read_b128 v[84:87], v123 offset:14336\n" \
  "v_mfma_f32_16x16x32_bf16 v[184:187], v[56:59], a[132:135], 0\n" \
  "ds_read_b128 v[88:91], v123 offset:28672\n" \
  "v_mfma_f32_16x16x32_bf16 v[188:191], v[60:63], a[132:135], 0\n" \
  "ds_read_b128 v[92:95], v123 offset:43008\n" \
  "v_add_u32 v122, 128, v122\n" \
  "v_add_u32 v123, 128, v123\n" \
  "buffer_load_dwordx4 a[132:135], %[l16], %[rs], s53 offen offset:2048 sc1\n" \
  "s_waitcnt vmcnt(7)\n" \
  "v_mfma_f32_16x16x32_bf16 v[192:195], v[32:35], a[136:139], 0\n" \
  "v_mfma_f32_16x16x32_bf16 v[196:199], v[36:39], a[136:139], 0\n" \
  "v_mfma_f32_16x16x32_bf16 v[200:203], v[40:43], a[136:139], 0\n" \
  "v_mfma_f32_16x16x32_bf16 v[204:207], v[44:47], a[136:139], 0\n" \
  "v_mfma_f32_16x16x32_bf16 v[208:211], v[48:51], a[136:139], 0\n" \
  "v_mfma_f32_16x16x32_bf16 v[212:215], v[52:55], a[136:139], 0\n" \
  "v_mfma_f32_16x16x32_bf16 v[216:219], v[56:59], a[136:139], 0\n" \
  "v_mfma_f32_16x16x32_bf16 v[220:223], v[60:63], a[136:139], 0\n" \
  "buffer_load_dwordx4 a[136:139], %[l16], %[rs], s54 offen offset:2048 sc1\n" \
  "s_waitcnt vmcnt(7)\n" \
  "v_mfma_f32_16x16x32_bf16 v[224:227], v[32:35], a[140:143], 0\n" \
  "v_mfma_f32_16x16x32_bf16 v[228:231], v[36:39], a[140:143], 0\n" \
  "v_mfma_f32_16x16x32_bf16 v[232:235], v[40:43], a[140:143], 0\n" \
  "v_mfma_f32_16x16x32_bf16 v[236:239], v[44:47], a[140:143], 0\n" \
  "v_mfma_f32_16x16x32_bf16 v[240:243], v[48:51], a[140:143], 0\n" \
  "v_mfma_f32_16x16x32_bf16 v[244:247], v[52:55], a[140:143], 0\n" \
  "v_mfma_f32_16x16x32_bf16 v[248:251], v[56:59], a[140:143], 0\n" \
  "v_mfma_f32_16x16x32_bf16 v[252:255], v[60:63], a[140:143], 0\n" \
  "buffer_load_dwordx4 a[140:143], %[l16], %[rs], s55 offen offset:2048 sc1\n" \
  "s_waitcnt vmcnt(7) lgkmcnt(7)\n" \
  "v_mfma_f32_16x16x32_bf16 v[128:131], v[64:67], a[144:147], v[128:131]\n" \
  "s_waitcnt lgkmcnt(6)\n" \
  "v_mfma_f32_16x16x32_bf16 v[132:135], v[68:71], a[144:147], v[132:135]\n" \
  "s_waitcnt lgkmcnt(5)\n" \
  "v_mfma_f32_16x16x32_bf16 v[136:139], v[72:75], a[144:147], v[136:139]\n" \
  "s_waitcnt lgkmcnt(4)\n" \
  "v_mfma_f32_16x16x32_bf16 v[140:143], v[76:79], a[144:147], v[140:143]\n" \
  "s_waitcnt lgkmcnt(3)\n" \
  "v_mfma_f32_16x16x32_bf16 v[144:147], v[80:83], a[144:147], v[144:147]\n" \
  "s_waitcnt lgkmcnt(2)\n" \
  "v_mfma_f32_16x16x32_bf16 v[148:151], v[84:87], a[144:147], v[148:151]\n" \
  "s_waitcnt lgkmcnt(1)\n" \
  "v_mfma_f32_16x16x32_bf16 v[152:155], v[88:91], a[144:147], v[152:155]\n" \
  "s_waitcnt lgkmcnt(0)\n" \
  "v_mfma_f32_16x16x32_bf16 v[156:159], v[92:95], a[144:147], v[156:159]\n" \
  "buffer_load_dwordx4 a[144:147], %[l16], %[rs], s52 offen offset:3072 sc1\n" \
  "s_waitcnt vmcnt(7)\n" \
  "v_mfma_f32_16x16x32_bf16 v[160:163], v[64:67], a[148:151], v[160:163]\n" \
  "ds_read_b128 v[32:35], v120\n" \
  "v_mfma_f32_16x16x32_bf16 v[164:167], v[68:71], a[148:151], v[164:167]\n" \
  "ds_read_b128 v[36:39], v120 offset:14336\n" \
  "v_mfma_f32_16x16x32_bf16 v[168:171], v[72:75], a[148:151], v[168:171]\n" \
  "ds_read_b128 v[40:43], v120 offset:28672\n" \
  "v_mfma_f32_16x16x32_bf16 v[172:175], v[76:79], a[148:151], v[172:175]\n" \
  "ds_read_b128 v[44:47], v120 offset:43008\n" \
  "v_mfma_f32_16x16x32_bf16 v[176:179], v[80:83], a[148:151], v[176:179]\n" \
  "ds_read_b128 v[48:51], v121\n" \
  "v_mfma_f32_16x16x32_bf16 v[180:183], v[84:87], a[148:151], v[180:183]\n" \
  "ds_read_b128 v[52:55], v121 offset:14336\n" \
  "v_mfma_f32_16x16x32_bf16 v[184:187], v[88:91], a[148:151], v[184:187]\n" \
  "ds_read_b128 v[56:59], v121 offset:28672\n" \
  "v_mfma_f32_16x16x32_bf16 v[188:191], v[92:95], a[148:151], v[188:191]\n" \
  "ds_read_b128 v[60:63], v121 offset:43008\n" \
  "v_add_u32 v120, 128, v120\n" \
  "v_add_u32 v121, 128, v121\n" \
  "buffer_load_dwordx4 a[148:151], %[l16], %[rs], s53 offen offset:3072 sc1\n" \
  "s_waitcnt vmcnt(7)\n" \
  "v_mfma_f32_16x16x32_bf16 v[192:195], v[64:67], a[152:155], v[192:195]\n" \
  "v_mfma_f32_16x16x32_bf16 v[196:199], v[68:71], a[152:155], v[196:199]\n" \
  "v_mfma_f32_16x16x32_bf16 v[200:203], v[72:75], a[152:155], v[200:203]\n" \
  "v_mfma_f32_16x16x32_bf16 v[204:207], v[76:79], a[152:155], v[204:207]\n" \
  "v_mfma_f32_16x16x32_bf16 v[208:211], v[80:83], a[152:155], v[208:211]\n" \
  "v_mfma_f32_16x16x32_bf16 v[212:215], v[84:87], a[152:155], v[212:215]\n" \
  "v_mfma_f32_16x16x32_bf16 v[216:219], v[88:91], a[152:155], v[216:219]\n" \
  "v_mfma_f32_16x16x32_bf16 v[220:223], v[92:95], a[152:155], v[220:223]\n" \
  "buffer_load_dwordx4 a[152:155], %[l16], %[rs], s54 offen offset:3072 sc1\n" \
  "s_waitcnt vmcnt(7)\n" \
  "v_mfma_f32_16x16x32_bf16 v[224:227], v[64:67], a[156:159], v[224:227]\n" \
  "v_mfma_f32_16x16x32_bf16 v[228:231], v[68:71], a[156:159], v[228:231]\n" \
  "v_mfma_f32_16x16x32_bf16 v[232:235], v[72:75], a[156:159], v[232:235]\n" \
  "v_mfma_f32_16x16x32_bf16 v[236:239], v[76:79], a[156:159], v[236:239]\n" \
  "v_mfma_f32_16x16x32_bf16 v[240:243], v[80:83], a[156:159], v[240:243]\n" \
  "v_mfma_f32_16x16x32_bf16 v[244:247], v[84:87], a[156:159], v[244:247]\n" \
  "v_mfma_f32_16x16x32_bf16 v[248:251], v[88:91], a[156:159], v[248:251]\n" \
  "v_mfma_f32_16x16x32_bf16 v[252:255], v[92:95], a[156:159], v[252:255]\n" \
  "buffer_load_dwordx4 a[156:159], %[l16], %[rs], s55 offen offset:3072 sc1\n" \
  "s_add_u32 s52, s52, 2048\n" \
  "s_add_u32 s53, s53, 2048\n" \
  "s_add_u32 s54, s54, 2048\n" \
  "s_add_u32 s55, s55, 2048\n" \
  "s_mov_b32 s56, 6\n" \
  ".LL_k1%=:\n" \
  "s_waitcnt vmcnt(7) lgkmcnt(7)\n" \
  "v_mfma_f32_16x16x32_bf16 v[128:131], v[32:35], a[128:131], v[128:131]\n" \
  "s_waitcnt lgkmcnt(6)\n" \
  "v_mfma_f32_16x16x32_bf16 v[132:135], v[36:39], a[128:131], v[132:135]\n" \
  "s_waitcnt lgkmcnt(5)\n" \
  "v_mfma_f32_16x16x32_bf16 v[136:139], v[40:43], a[128:131], v[136:139]\n" \
  "s_waitcnt lgkmcnt(4)\n" \
  "v_mfma_f32_16x16x32_bf16 v[140:143], v[44:47], a[128:131], v[140:143]\n" \
  "s_waitcnt lgkmcnt(3)\n" \
  "v_mfma_f32_16x16x32_bf16 v[144:147], v[48:51], a[128:131], v[144:147]\n" \
  "s_waitcnt lgkmcnt(2)\n" \
  "v_mfma_f32_16x16x32_bf16 v[148:151], v[52:55], a[128:131], v[148:151]\n" \
  "s_waitcnt lgkmcnt(1)\n" \
  "v_mfma_f32_16x16x32_bf16 v[152:155], v[56:59], a[128:131], v[152:155]\n" \
  "s_waitcnt lgkmcnt(0)\n" \
  "v_mfma_f32_16x16x32_bf16 v[156:159], v[60:63], a[128:131], v[156:159]\n" \
  "buffer_load_dwordx4 a[128:131], %[l16], %[rs], s52 offen offset:2048 sc1\n" \
  "s_waitcnt vmcnt(7)\n" \
  "v_mfma_f32_16x16x32_bf16 v[160:163], v[32:35], a[132:135], v[160:163]\n" \
  "ds_read_b128 v[64:67], v122\n" \
  "v_mfma_f32_16x16x32_bf16 v[164:167], v[36:39], a[132:135], v[164:167]\n" \
  "ds_read_b128 v[68:71], v122 offset:14336\n" \
  "v_mfma_f32_16x16x32_bf16 v[168:171], v[40:43], a[132:135], v[168:171]\n" \
  "ds_read_b128 v[72:75], v122 offset:28672\n" \
  "v_mfma_f32_16x16x32_bf16 v[172:175], v[44:47], a[132:135], v[172:175]\n" \
  "ds_read_b128 v[76:79], v122 offset:43008\n" \
  "v_mfma_f32_16x16x32_bf16 v[176:179], v[48:51], a[132:135], v[176:179]\n" \
  "ds_read_b128 v[80:83], v123\n" \
  "v_mfma_f32_16x16x32_bf16 v[180:183], v[52:55], a[132:135], v[180:183]\n" \
  "ds_read_b128 v[84:87], v123 offset:14336\n" \
  "v_mfma_f32_16x16x32_bf16 v[184:187], v[56:59], a[132:135], v[184:187]\n" \
  "ds_read_b128 v[88:91], v123 offset:28672\n" \
  "v_mfma_f32_16x16x32_bf16 v[188:191], v[60:63], a[132:135], v[188:191]\n" \
  "ds_read_b128 v[92:95], v123 offset:43008\n" \
  "v_add_u32 v122, 128, v122\n" \
  "v_add_u32 v123, 128, v123\n" \
  "buffer_load_dwordx4 a[132:135], %[l16], %[rs], s53 offen offset:2048 sc1\n" \
  "s_waitcnt vmcnt(7)\n" \
  "v_mfma_f32_16x16x32_bf16 v[192:195], v[32:35], a[136:139], v[192:195]\n" \
  "v_mfma_f32_16x16x32_bf16 v[196:199], v[36:39], a[136:139], v[196:199]\n" \
  "v_mfma_f32_16x16x32_bf16 v[200:203], v[40:43], a[136:139], v[200:203]\n" \
  "v_mfma_f32_16x16x32_bf16 v[204:207], v[44:47], a[136:139], v[204:207]\n" \
  "v_mfma_f32_16x16x32_bf16 v[208:211], v[48:51], a[136:139], v[208:211]\n" \
  "v_mfma_f32_16x16x32_bf16 v[212:215], v[52:55], a[136:139], v[212:215]\n" \
  "v_mfma_f32_16x16x32_bf16 v[216:219], v[56:59], a[136:139], v[216:219]\n" \
  "v_mfma_f32_16x16x32_bf16 v[220:223], v[60:63], a[136:139], v[220:223]\n" \
  "buffer_load_dwordx4 a[136:139], %[l16], %[rs], s54 offen offset:2048 sc1\n" \
  "s_waitcnt vmcnt(7)\n" \
  "v_mfma_f32_16x16x32_bf16 v[224:227], v[32:35], a[140:143], v[224:227]\n" \
  "v_mfma_f32_16x16x32_bf16 v[228:231], v[36:39], a[140:143], v[228:231]\n" \
  "v_mfma_f32_16x16x32_bf16 v[232:235], v[40:43], a[140:143], v[232:235]\n" \
  "v_mfma_f32_16x16x32_bf16 v[236:239], v[44:47], a[140:143], v[236:239]\n" \
  "v_mfma_f32_16x16x32_bf16 v[240:243], v[48:51], a[140:143], v[240:243]\n" \
  "v_mfma_f32_16x16x32_bf16 v[244:247], v[52:55], a[140:143], v[244:247]\n" \
  "v_mfma_f32_16x16x32_bf16 v[248:251], v[56:59], a[140:143], v[248:251]\n" \
  "v_mfma_f32_16x16x32_bf16 v[252:255], v[60:63], a[140:143], v[252:255]\n" \
  "buffer_load_dwordx4 a[140:143], %[l16], %[rs], s55 offen offset:2048 sc1\n" \
  "s_waitcnt vmcnt(7) lgkmcnt(7)\n" \
  "v_mfma_f32_16x16x32_bf16 v[128:131], v[64:67], a[144:147], v[128:131]\n" \
  "s_waitcnt lgkmcnt(6)\n" \
  "v_mfma_f32_16x16x32_bf16 v[132:135], v[68:71], a[144:147], v[132:135]\n" \
  "s_waitcnt lgkmcnt(5)\n" \
  "v_mfma_f32_16x16x32_bf16 v[136:139], v[72:75], a[144:147], v[136:139]\n" \
  "s_waitcnt lgkmcnt(4)\n" \
  "v_mfma_f32_16x16x32_bf16 v[140:143], v[76:79], a[144:147], v[140:143]\n" \
  "s_waitcnt lgkmcnt(3)\n" \
  "v_mfma_f32_16x16x32_bf16 v[144:147], v[80:83], a[144:147], v[144:147]\n" \
  "s_waitcnt lgkmcnt(2)\n" \
  "v_mfma_f32_16x16x32_bf16 v[148:151], v[84:87], a[144:147], v[148:151]\n" \
  "s_waitcnt lgkmcnt(1)\n" \
  "v_mfma_f32_16x16x32_bf16 v[152:155], v[88:91], a[144:147], v[152:155]\n" \
  "s_waitcnt lgkmcnt(0)\n" \
  "v_mfma_f32_16x16x32_bf16 v[156:159], v[92:95], a[144:147], v[156:159]\n" \
  "buffer_load_dwordx4 a[144:147], %[l16], %[rs], s52 offen offset:3072 sc1\n" \
  "s_waitcnt vmcnt(7)\n" \
  "v_mfma_f32_16x16x32_bf16 v[160:163], v[64:67], a[148:151], v[160:163]\n" \
  "ds_read_b128 v[32:35], v120\n" \
  "v_mfma_f32_16x16x32_bf16 v[164:167], v[68:71], a[148:151], v[164:167]\n" \
  "ds_read_b128 v[36:39], v120 offset:14336\n" \
  "v_mfma_f32_16x16x32_bf16 v[168:171], v[72:75], a[148:151], v[168:171]\n" \
  "ds_read_b128 v[40:43], v120 offset:28672\n" \
  "v_mfma_f32_16x16x32_bf16 v[172:175], v[76:79], a[148:151], v[172:175]\n" \
  "ds_read_b128 v[44:47], v120 offset:43008\n" \
  "v_mfma_f32_16x16x32_bf16 v[176:179], v[80:83], a[148:151], v[176:179]\n" \
  "ds_read_b128 v[48:51], v121\n" \
  "v_mfma_f32_16x16x32_bf16 v[180:183], v[84:87], a[148:151], v[180:183]\n" \
  "ds_read_b128 v[52:55], v121 offset:14336\n" \
  "v_mfma_f32_16x16x32_bf16 v[184:187], v[88:91], a[148:151], v[184:187]\n" \
  "ds_read_b128 v[56:59], v121 offset:28672\n" \
  "v_mfma_f32_16x16x32_bf16 v[188:191], v[92:95], a[148:151], v[188:191]\n" \
  "ds_read_b128 v[60:63], v121 offset:43008\n" \
  "v_add_u32 v120, 128, v120\n" \
  "v_add_u32 v121, 128, v121\n" \
  "buffer_load_dwordx4 a[148:151], %[l16], %[rs], s53 offen offset:3072 sc1\n" \
  "s_waitcnt vmcnt(7)\n" \
  "v_mfma_f32_16x16x32_bf16 v[192:195], v[64:67], a[152:155], v[192:195]\n" \
  "v_mfma_f32_16x16x32_bf16 v[196:199], v[68:71], a[152:155], v[196:199]\n" \
  "v_mfma_f32_16x16x32_bf16 v[200:203], v[72:75], a[152:155], v[200:203]\n" \
  "v_mfma_f32_16x16x32_bf16 v[204:207], v[76:79], a[152:155], v[204:207]\n" \
  "v_mfma_f32_16x16x32_bf16 v[208:211], v[80:83], a[152:155], v[208:211]\n" \
  "v_mfma_f32_16x16x32_bf16 v[212:215], v[84:87], a[152:155], v[212:215]\n" \
  "v_mfma_f32_16x16x32_bf16 v[216:219], v[88:91], a[152:155], v[216:219]\n" \
  "v_mfma_f32_16x16x32_bf16 v[220:223], v[92:95], a[152:155], v[220:223]\n" \
  "buffer_load_dwordx4 a[152:155], %[l16], %[rs], s54 offen offset:3072 sc1\n" \
  "s_waitcnt vmcnt(7)\n" \
  "v_mfma_f32_16x16x32_bf16 v[224:227], v[64:67], a[156:159], v[224:227]\n" \
  "v_mfma_f32_16x16x32_bf16 v[228:231], v[68:71], a[156:159], v[228:231]\n" \
  "v_mfma_f32_16x16x32_bf16 v[232:235], v[72:75], a[156:159], v[232:235]\n" \
  "v_mfma_f32_16x16x32_bf16 v[236:239], v[76:79], a[156:159], v[236:239]\n" \
  "v_mfma_f32_16x16x32_bf16 v[240:243], v[80:83], a[156:159], v[240:243]\n" \
  "v_mfma_f32_16x16x32_bf16 v[244:247], v[84:87], a[156:159], v[244:247]\n" \
  "v_mfma_f32_16x16x32_bf16 v[248:251], v[88:91], a[156:159], v[248:251]\n" \
  "v_mfma_f32_16x16x32_bf16 v[252:255], v[92:95], a[156:159], v[252:255]\n" \
  "buffer_load_dwordx4 a[156:159], %[l16], %[rs], s55 offen offset:3072 sc1\n" \
  "s_add_u32 s52, s52, 2048\n" \
  "s_add_u32 s53, s53, 2048\n" \
  "s_add_u32 s54, s54, 2048\n" \
  "s_add_u32 s55, s55, 2048\n" \
  "s_sub_u32 s56, s56, 1\n" \
  "s_cmp_lg_u32 s56, 0\n" \
  "s_cbranch_scc1 .LL_k1%=\n" \
  "s_cmp_eq_u32 s57, 3\n" \
  "s_cbranch_scc1 .LL_kvq%=\n" \
  "s_mul_i32 s46, s57, 28672\n" \
  "s_add_u32 s46, s46, %[sWq]\n" \
  "s_add_u32 s52, s46, 487424\n" \
  "s_add_u32 s53, s46, 501760\n" \
  "s_add_u32 s54, s46, 946176\n" \
  "s_add_u32 s55, s46, 960512\n" \
  "s_branch .LL_kvp%=\n" \
  ".LL_kvq%=:\n" \
  "s_add_u32 s52, %[sWq], 0\n" \
  "s_add_u32 s53, %[sWq], 14336\n" \
  "s_add_u32 s54, %[sWq], 28672\n" \
  "s_add_u32 s55, %[sWq], 43008\n" \
  ".LL_kvp%=:\n" \
  "buffer_load_dwordx4 a[128:131], %[l16], %[rs], s52 offen sc1\n" \
  "buffer_load_dwordx4 a[132:135], %[l16], %[rs], s53 offen sc1\n" \
  "buffer_load_dwordx4 a[136:139], %[l16], %[rs], s54 offen sc1\n" \
  "buffer_load_dwordx4 a[140:143], %[l16], %[rs], s55 offen sc1\n" \
  "buffer_load_dwordx4 a[144:147], %[l16], %[rs], s52 offen offset:1024 sc1\n" \
  "buffer_load_dwordx4 a[148:151], %[l16], %[rs], s53 offen offset:1024 sc1\n" \
  "buffer_load_dwordx4 a[152:155], %[l16], %[rs], s54 offen offset:1024 sc1\n" \
  "buffer_load_dwordx4 a[156:159], %[l16], %[rs], s55 offen offset:1024 sc1\n" \
  "v_mov_b32 v120, %[rX]\n" \
  "v_add_u32 v121, 57344, %[rX]\n" \
  "v_xor_b32 v122, 64, v120\n" \
  "v_xor_b32 v123, 64, v121\n" \
  "ds_read_b128 v[32:35], v120\n" \
  "ds_read_b128 v[36:39], v120 offset:14336\n" \
  "ds_read_b128 v[40:43], v120 offset:28672\n" \
  "ds_read_b128 v[44:47], v120 offset:43008\n" \
  "ds_read_b128 v[48:51], v121\n" \
  "ds_read_b128 v[52:55], v121 offset:14336\n" \
  "ds_read_b128 v[56:59], v121 offset:28672\n" \
  "ds_read_b128 v[60:63], v121 offset:43008\n" \
  "v_add_u32 v120, 128, v120\n" \
  "v_add_u32 v121, 128, v121\n" \
  "v_mul_f32 v96, s58, v128\n" \
  "v_mul_f32 v97, s58, v129\n" \
  "v_mul_f32 v98, s58, v130\n" \
  "v_mul_f32 v99, s58, v131\n" \
  "v_exp_f32 v96, v96 clamp\n" \
  "v_exp_f32 v97, v97 clamp\n" \
  "v_exp_f32 v98, v98 clamp\n" \
  "v_exp_f32 v99, v99 clamp\n" \
  "v_add_f32 v128, s59, v128\n" \
  "v_add_f32 v129, s59, v129\n" \
  "v_add_f32 v130, s59, v130\n" \
  "v_add_f32 v131, s59, v131\n" \
  "v_max_f32 v128, s59, v128\n" \
  "v_max_f32 v129, s59, v129\n" \
  "v_max_f32 v130, s59, v130\n" \
  "v_max_f32 v131, s59, v131\n" \
  "v_add_f32 v128, v128, v96\n" \
  "v_add_f32 v129, v129, v97\n" \
  "v_add_f32 v130, v130, v98\n" \
  "v_add_f32 v131, v131, v99\n" \
  "v_mul_f32 v96, s58, v132\n" \
  "v_mul_f32 v97, s58, v133\n" \
  "v_mul_f32 v98, s58, v134\n" \
  "v_mul_f32 v99, s58, v135\n" \
  "v_exp_f32 v96, v96 clamp\n" \
  "v_exp_f32 v97, v97 clamp\n" \
  "v_exp_f32 v98, v98 clamp\n" \
  "v_exp_f32 v99, v99 clamp\n" \
  "v_add_f32 v132, s59, v132\n" \
  "v_add_f32 v133, s59, v133\n" \
  "v_add_f32 v134, s59, v134\n" \
  "v_add_f32 v135, s59, v135\n" \
  "v_max_f32 v132, s59, v132\n" \
  "v_max_f32 v133, s59, v133\n" \
  "v_max_f32 v134, s59, v134\n" \
  "v_max_f32 v135, s59, v135\n" \
  "v_add_f32 v132, v132, v96\n" \
  "v_add_f32 v133, v133, v97\n" \
  "v_add_f32 v134, v134, v98\n" \
  "v_add_f32 v135, v135, v99\n" \
  "v_mul_f32 v96, s58, v136\n" \
  "v_mul_f32 v97, s58, v137\n" \
  "v_mul_f32 v98, s58, v138\n" \
  "v_mul_f32 v99, s58, v139\n" \
  "v_exp_f32 v96, v96 clamp\n" \
  "v_exp_f32 v97, v97 clamp\n" \
  "v_exp_f32 v98, v98 clamp\n" \
  "v_exp_f32 v99, v99 clamp\n" \
  "v_add_f32 v136, s59, v136\n" \
  "v_add_f32 v137, s59, v137\n" \
  "v_add_f32 v138, s59, v138\n" \
  "v_add_f32 v139, s59, v139\n" \
  "v_max_f32 v136, s59, v136\n" \
  "v_max_f32 v137, s59, v137\n" \
  "v_max_f32 v138, s59, v138\n" \
  "v_max_f32 v139, s59, v139\n" \
  "v_add_f32 v136, v136, v96\n" \
  "v_add_f32 v137, v137, v97\n" \
  "v_add_f32 v138, v138, v98\n" \
  "v_add_f32 v139, v139, v99\n" \
  "v_mul_f32 v96, s58, v140\n" \
  "v_mul_f32 v97, s58, v141\n" \
  "v_mul_f32 v98, s58, v142\n" \
  "v_mul_f32 v99, s58, v143\n" \
  "v_exp_f32 v96, v96 clamp\n" \
  "v_exp_f32 v97, v97 clamp\n" \
  "v_exp_f32 v98, v98 clamp\n" \
  "v_exp_f32 v99, v99 clamp\n" \
  "v_add_f32 v140, s59, v140\n" \
  "v_add_f32 v141, s59, v141\n" \
  "v_add_f32 v142, s59, v142\n" \
  "v_add_f32 v143, s59, v143\n" \
  "v_max_f32 v140, s59, v140\n" \
  "v_max_f32 v141, s59, v141\n" \
  "v_max_f32 v142, s59, v142\n" \
  "v_max_f32 v143, s59, v143\n" \
  "v_add_f32 v140, v140, v96\n" \
  "v_add_f32 v141, v141, v97\n" \
  "v_add_f32 v142, v142, v98\n" \
  "v_add_f32 v143, v143, v99\n" \
  "v_mul_f32 v96, s58, v144\n" \
  "v_mul_f32 v97, s58, v145\n" \
  "v_mul_f32 v98, s58, v146\n" \
  "v_mul_f32 v99, s58, v147\n" \
  "v_exp_f32 v96, v96 clamp\n" \
  "v_exp_f32 v97, v97 clamp\n" \
  "v_exp_f32 v98, v98 clamp\n" \
  "v_exp_f32 v99, v99 clamp\n" \
  "v_add_f32 v144, s59, v144\n" \
  "v_add_f32 v145, s59, v145\n" \
  "v_add_f32 v146, s59, v146\n" \
  "v_add_f32 v147, s59, v147\n" \
  "v_max_f32 v144, s59, v144\n" \
  "v_max_f32 v145, s59, v145\n" \
  "v_max_f32 v146, s59, v146\n" \
  "v_max_f32 v147, s59, v147\n" \
  "v_add_f32 v144, v144, v96\n" \
  "v_add_f32 v145, v145, v97\n" \
  "v_add_f32 v146, v146, v98\n" \
  "v_add_f32 v147, v147, v99\n" \
  "v_mul_f32 v96, s58, v148\n" \
  "v_mul_f32 v97, s58, v149\n" \
  "v_mul_f32 v98, s58, v150\n" \
  "v_mul_f32 v99, s58, v151\n" \
  "v_exp_f32 v96, v96 clamp\n" \
  "v_exp_f32 v97, v97 clamp\n" \
  "v_exp_f32 v98, v98 clamp\n" \
  "v_exp_f32 v99, v99 clamp\n" \
  "v_add_f32 v148, s59, v148\n" \
  "v_add_f32 v149, s59, v149\n" \
  "v_add_f32 v150, s59, v150\n" \
  "v_add_f32 v151, s59, v151\n" \
  "v_max_f32 v148, s59, v148\n" \
  "v_max_f32 v149, s59, v149\n" \
  "v_max_f32 v150, s59, v150\n" \
  "v_max_f32 v151, s59, v151\n" \
  "v_add_f32 v148, v148, v96\n" \
  "v_add_f32 v149, v149, v97\n" \
  "v_add_f32 v150, v150, v98\n" \
  "v_add_f32 v151, v151, v99\n" \
  "v_mul_f32 v96, s58, v152\n" \
  "v_mul_f32 v97, s58, v153\n" \
  "v_mul_f32 v98, s58, v154\n" \
  "v_mul_f32 v99, s58, v155\n" \
  "v_exp_f32 v96, v96 clamp\n" \
  "v_exp_f32 v97, v97 clamp\n" \
  "v_exp_f32 v98, v98 clamp\n" \
  "v_exp_f32 v99, v99 clamp\n" \
  "v_add_f32 v152, s59, v152\n" \
  "v_add_f32 v153, s59, v153\n" \
  "v_add_f32 v154, s59, v154\n" \
  "v_add_f32 v155, s59, v155\n" \
  "v_max_f32 v152, s59, v152\n" \
  "v_max_f32 v153, s59, v153\n" \
  "v_max_f32 v154, s59, v154\n" \
  "v_max_f32 v155, s59, v155\n" \
  "v_add_f32 v152, v152, v96\n" \
  "v_add_f32 v153, v153, v97\n" \
  "v_add_f32 v154, v154, v98\n" \
  "v_add_f32 v155, v155, v99\n" \
  "v_mul_f32 v96, s58, v156\n" \
  "v_mul_f32 v97, s58, v157\n" \
  "v_mul_f32 v98, s58, v158\n" \
  "v_mul_f32 v99, s58, v159\n" \
  "v_exp_f32 v96, v96 clamp\n" \
  "v_exp_f32 v97, v97 clamp\n" \
  "v_exp_f32 v98, v98 clamp\n" \
  "v_exp_f32 v99, v99 clamp\n" \
  "v_add_f32 v156, s59, v156\n" \
  "v_add_f32 v157, s59, v157\n" \
  "v_add_f32 v158, s59, v158\n" \
  "v_add_f32 v159, s59, v159\n" \
  "v_max_f32 v156, s59, v156\n" \
  "v_max_f32 v157, s59, v157\n" \
  "v_max_f32 v158, s59, v158\n" \
  "v_max_f32 v159, s59, v159\n" \
  "v_add_f32 v156, v156, v96\n" \
  "v_add_f32 v157, v157, v97\n" \
  "v_add_f32 v158, v158, v98\n" \
  "v_add_f32 v159, v159, v99\n" \
  "v_mul_f32 v96, s58, v160\n" \
  "v_mul_f32 v97, s58, v161\n" \
  "v_mul_f32 v98, s58, v162\n" \
  "v_mul_f32 v99, s58, v163\n" \
  "v_exp_f32 v96, v96 clamp\n" \
  "v_exp_f32 v97, v97 clamp\n" \
  "v_exp_f32 v98, v98 clamp\n" \
  "v_exp_f32 v99, v99 clamp\n" \
  "v_add_f32 v160, s59, v160\n" \
  "v_add_f32 v161, s59, v161\n" \
  "v_add_f32 v162, s59, v162\n" \
  "v_add_f32 v163, s59, v163\n" \
  "v_max_f32 v160, s59, v160\n" \
  "v_max_f32 v161, s59, v161\n" \
  "v_max_f32 v162, s59, v162\n" \
  "v_max_f32 v163, s59, v163\n" \
  "v_add_f32 v160, v160, v96\n" \
  "v_add_f32 v161, v161, v97\n" \
  "v_add_f32 v162, v162, v98\n" \
  "v_add_f32 v163, v163, v99\n" \
  "v_mul_f32 v96, s58, v164\n" \
  "v_mul_f32 v97, s58, v165\n" \
  "v_mul_f32 v98, s58, v166\n" \
  "v_mul_f32 v99, s58, v167\n" \
  "v_exp_f32 v96, v96 clamp\n" \
  "v_exp_f32 v97, v97 clamp\n" \
  "v_exp_f32 v98, v98 clamp\n" \
  "v_exp_f32 v99, v99 clamp\n" \
  "v_add_f32 v164, s59, v164\n" \
  "v_add_f32 v165, s59, v165\n" \
  "v_add_f32 v166, s59, v166\n" \
  "v_add_f32 v167, s59, v167\n" \
  "v_max_f32 v164, s59, v164\n" \
  "v_max_f32 v165, s59, v165\n" \
  "v_max_f32 v166, s59, v166\n" \
  "v_max_f32 v167, s59, v167\n" \
  "v_add_f32 v164, v164, v96\n" \
  "v_add_f32 v165, v165, v97\n" \
  "v_add_f32 v166, v166, v98\n" \
  "v_add_f32 v167, v167, v99\n" \
  "v_mul_f32 v96, s58, v168\n" \
  "v_mul_f32 v97, s58, v169\n" \
  "v_mul_f32 v98, s58, v170\n" \
  "v_mul_f32 v99, s58, v171\n" \
  "v_exp_f32 v96, v96 clamp\n" \
  "v_exp_f32 v97, v97 clamp\n" \
  "v_exp_f32 v98, v98 clamp\n" \
  "v_exp_f32 v99, v99 clamp\n" \
  "v_add_f32 v168, s59, v168\n" \
  "v_add_f32 v169, s59, v169\n" \
  "v_add_f32 v170, s59, v170\n" \
  "v_add_f32 v171, s59, v171\n" \
  "v_max_f32 v168, s59, v168\n" \
  "v_max_f32 v169, s59, v169\n" \
  "v_max_f32 v170, s59, v170\n" \
  "v_max_f32 v171, s59, v171\n" \
  "v_add_f32 v168, v168, v96\n" \
  "v_add_f32 v169, v169, v97\n" \
  "v_add_f32 v170, v170, v98\n" \
  "v_add_f32 v171, v171, v99\n" \
  "v_mul_f32 v96, s58, v172\n" \
  "v_mul_f32 v97, s58, v173\n" \
  "v_mul_f32 v98, s58, v174\n" \
  "v_mul_f32 v99, s58, v175\n" \
  "v_exp_f32 v96, v96 clamp\n" \
  "v_exp_f32 v97, v97 clamp\n" \
  "v_exp_f32 v98, v98 clamp\n" \
  "v_exp_f32 v99, v99 clamp\n" \
  "v_add_f32 v172, s59, v172\n" \
  "v_add_f32 v173, s59, v173\n" \
  "v_add_f32 v174, s59, v174\n" \
  "v_add_f32 v175, s59, v175\n" \
  "v_max_f32 v172, s59, v172\n" \
  "v_max_f32 v173, s59, v173\n" \
  "v_max_f32 v174, s59, v174\n" \
  "v_max_f32 v175, s59, v175\n" \
  "v_add_f32 v172, v172, v96\n" \
  "v_add_f32 v173, v173, v97\n" \
  "v_add_f32 v174, v174, v98\n" \
  "v_add_f32 v175, v175, v99\n" \
  "v_mul_f32 v96, s58, v176\n" \
  "v_mul_f32 v97, s58, v177\n" \
  "v_mul_f32 v98, s58, v178\n" \
  "v_mul_f32 v99, s58, v179\n" \
  "v_exp_f32 v96, v96 clamp\n" \
  "v_exp_f32 v97, v97 clamp\n" \
  "v_exp_f32 v98, v98 clamp\n" \
  "v_exp_f32 v99, v99 clamp\n" \
  "v_add_f32 v176, s59, v176\n" \
  "v_add_f32 v177, s59, v177\n" \
  "v_add_f32 v178, s59, v178\n" \
  "v_add_f32 v179, s59, v179\n" \
  "v_max_f32 v176, s59, v176\n" \
  "v_max_f32 v177, s59, v177\n" \
  "v_max_f32 v178, s59, v178\n" \
  "v_max_f32 v179, s59, v179\n" \
  "v_add_f32 v176, v176, v96\n" \
  "v_add_f32 v177, v177, v97\n" \
  "v_add_f32 v178, v178, v98\n" \
  "v_add_f32 v179, v179, v99\n" \
  "v_mul_f32 v96, s58, v180\n" \
  "v_mul_f32 v97, s58, v181\n" \
  "v_mul_f32 v98, s58, v182\n" \
  "v_mul_f32 v99, s58, v183\n" \
  "v_exp_f32 v96, v96 clamp\n" \
  "v_exp_f32 v97, v97 clamp\n" \
  "v_exp_f32 v98, v98 clamp\n" \
  "v_exp_f32 v99, v99 clamp\n" \
  "v_add_f32 v180, s59, v180\n" \
  "v_add_f32 v181, s59, v181\n" \
  "v_add_f32 v182, s59, v182\n" \
  "v_add_f32 v183, s59, v183\n" \
  "v_max_f32 v180, s59, v180\n" \
  "v_max_f32 v181, s59, v181\n" \
  "v_max_f32 v182, s59, v182\n" \
  "v_max_f32 v183, s59, v183\n" \
  "v_add_f32 v180, v180, v96\n" \
  "v_add_f32 v181, v181, v97\n" \
  "v_add_f32 v182, v182, v98\n" \
  "v_add_f32 v183, v183, v99\n" \
  "v_mul_f32 v96, s58, v184\n" \
  "v_mul_f32 v97, s58, v185\n" \
  "v_mul_f32 v98, s58, v186\n" \
  "v_mul_f32 v99, s58, v187\n" \
  "v_exp_f32 v96, v96 clamp\n" \
  "v_exp_f32 v97, v97 clamp\n" \
  "v_exp_f32 v98, v98 clamp\n" \
  "v_exp_f32 v99, v99 clamp\n" \
  "v_add_f32 v184, s59, v184\n" \
  "v_add_f32 v185, s59, v185\n" \
  "v_add_f32 v186, s59, v186\n" \
  "v_add_f32 v187, s59, v187\n" \
  "v_max_f32 v184, s59, v184\n" \
  "v_max_f32 v185, s59, v185\n" \
  "v_max_f32 v186, s59, v186\n" \
  "v_max_f32 v187, s59, v187\n" \
  "v_add_f32 v184, v184, v96\n" \
  "v_add_f32 v185, v185, v97\n" \
  "v_add_f32 v186, v186, v98\n" \
  "v_add_f32 v187, v187, v99\n" \
  "v_mul_f32 v96, s58, v188\n" \
  "v_mul_f32 v97, s58, v189\n" \
  "v_mul_f32 v98, s58, v190\n" \
  "v_mul_f32 v99, s58, v191\n" \
  "v_exp_f32 v96, v96 clamp\n" \
  "v_exp_f32 v97, v97 clamp\n" \
  "v_exp_f32 v98, v98 clamp\n" \
  "v_exp_f32 v99, v99 clamp\n" \
  "v_add_f32 v188, s59, v188\n" \
  "v_add_f32 v189, s59, v189\n" \
  "v_add_f32 v190, s59, v190\n" \
  "v_add_f32 v191, s59, v191\n" \
  "v_max_f32 v188, s59, v188\n" \
  "v_max_f32 v189, s59, v189\n" \
  "v_max_f32 v190, s59, v190\n" \
  "v_max_f32 v191, s59, v191\n" \
  "v_add_f32 v188, v188, v96\n" \
  "v_add_f32 v189, v189, v97\n" \
  "v_add_f32 v190, v190, v98\n" \
  "v_add_f32 v191, v191, v99\n" \
  "v_cvt_pk_bf16_f32 v104, v128, v129\n" \
  "v_cvt_pk_bf16_f32 v105, v130, v131\n" \
  "v_cvt_pk_bf16_f32 v106, v132, v133\n" \
  "v_cvt_pk_bf16_f32 v107, v134, v135\n" \
  "v_cvt_pk_bf16_f32 v108, v160, v161\n" \
  "v_cvt_pk_bf16_f32 v109, v162, v163\n" \
  "v_cvt_pk_bf16_f32 v110, v164, v165\n" \
  "v_cvt_pk_bf16_f32 v111, v166, v167\n" \
  "v_cvt_pk_bf16_f32 v112, v192, v193\n" \
  "v_cvt_pk_bf16_f32 v113, v194, v195\n" \
  "v_cvt_pk_bf16_f32 v114, v196, v197\n" \
  "v_cvt_pk_bf16_f32 v115, v198, v199\n" \
  "v_cvt_pk_bf16_f32 v116, v224, v225\n" \
  "v_cvt_pk_bf16_f32 v117, v226, v227\n" \
  "v_cvt_pk_bf16_f32 v118, v228, v229\n" \
  "v_cvt_pk_bf16_f32 v119, v230, v231\n" \
  "v_cndmask_b32 v116, v116, v126, s[62:63]\n" \
  "v_cndmask_b32 v117, v117, v126, s[62:63]\n" \
  "v_cndmask_b32 v118, v118, v126, s[62:63]\n" \
  "v_cndmask_b32 v119, v119, v126, s[62:63]\n" \
  "v_mfma_f32_16x16x32_bf16 v[64:67], v[104:107], v[112:115], 0\n" \
  "s_nop 0\n" \
  "v_mfma_f32_16x16x32_bf16 v[68:71], v[104:107], v[116:119], 0\n" \
  "v_mfma_f32_16x16x32_bf16 v[72:75], v[108:111], v[112:115], 0\n" \
  "v_mfma_f32_16x16x32_bf16 v[76:79], v[108:111], v[116:119], 0\n" \
  "v_cvt_pk_bf16_f32 v104, v136, v137\n" \
  "v_cvt_pk_bf16_f32 v105, v138, v139\n" \
  "v_cvt_pk_bf16_f32 v106, v140, v141\n" \
  "v_cvt_pk_bf16_f32 v107, v142, v143\n" \
  "v_cvt_pk_bf16_f32 v108, v168, v169\n" \
  "v_cvt_pk_bf16_f32 v109, v170, v171\n" \
  "v_cvt_pk_bf16_f32 v110, v172, v173\n" \
  "v_cvt_pk_bf16_f32 v111, v174, v175\n" \
  "v_cvt_pk_bf16_f32 v112, v200, v201\n" \
  "v_cvt_pk_bf16_f32 v113, v202, v203\n" \
  "v_cvt_pk_bf16_f32 v114, v204, v205\n" \
  "v_cvt_pk_bf16_f32 v115, v206, v207\n" \
  "v_cvt_pk_bf16_f32 v116, v232, v233\n" \
  "v_cvt_pk_bf16_f32 v117, v234, v235\n" \
  "v_cvt_pk_bf16_f32 v118, v236, v237\n" \
  "v_cvt_pk_bf16_f32 v119, v238, v239\n" \
  "v_cndmask_b32 v116, v116, v126, s[62:63]\n" \
  "v_cndmask_b32 v117, v117, v126, s[62:63]\n" \
  "v_cndmask_b32 v118, v118, v126, s[62:63]\n" \
  "v_cndmask_b32 v119, v119, v126, s[62:63]\n" \
  "v_mfma_f32_16x16x32_bf16 v[64:67], v[104:107], v[112:115], v[64:67]\n" \
  "s_nop 0\n" \
  "v_mfma_f32_16x16x32_bf16 v[68:71], v[104:107], v[116:119], v[68:71]\n" \
  "v_mfma_f32_16x16x32_bf16 v[72:75], v[108:111], v[112:115], v[72:75]\n" \
  "v_mfma_f32_16x16x32_bf16 v[76:79], v[108:111], v[116:119], v[76:79]\n" \
  "v_cvt_pk_bf16_f32 v104, v144, v145\n" \
  "v_cvt_pk_bf16_f32 v105, v146, v147\n" \
  "v_cvt_pk_bf16_f32 v106, v148, v149\n" \
  "v_cvt_pk_bf16_f32 v107, v150, v151\n" \
  "v_cvt_pk_bf16_f32 v108, v176, v177\n" \
  "v_cvt_pk_bf16_f32 v109, v178, v179\n" \
  "v_cvt_pk_bf16_f32 v110, v180, v181\n" \
  "v_cvt_pk_bf16_f32 v111, v182, v183\n" \
  "v_cvt_pk_bf16_f32 v112, v208, v209\n" \
  "v_cvt_pk_bf16_f32 v113, v210, v211\n" \
  "v_cvt_pk_bf16_f32 v114, v212, v213\n" \
  "v_cvt_pk_bf16_f32 v115, v214, v215\n" \
  "v_cvt_pk_bf16_f32 v116, v240, v241\n" \
  "v_cvt_pk_bf16_f32 v117, v242, v243\n" \
  "v_cvt_pk_bf16_f32 v118, v244, v245\n" \
  "v_cvt_pk_bf16_f32 v119, v246, v247\n" \
  "v_cndmask_b32 v116, v116, v126, s[62:63]\n" \
  "v_cndmask_b32 v117, v117, v126, s[62:63]\n" \
  "v_cndmask_b32 v118, v118, v126, s[62:63]\n" \
  "v_cndmask_b32 v119, v119, v126, s[62:63]\n" \
  "v_mfma_f32_16x16x32_bf16 v[64:67], v[104:107], v[112:115], v[64:67]\n" \
  "s_nop 0\n" \
  "v_mfma_f32_16x16x32_bf16 v[68:71], v[104:107], v[116:119], v[68:71]\n" \
  "v_mfma_f32_16x16x32_bf16 v[72:75], v[108:111], v[112:115], v[72:75]\n" \
  "v_mfma_f32_16x16x32_bf16 v[76:79], v[108:111], v[116:119], v[76:79]\n" \
  "v_cvt_pk_bf16_f32 v104, v152, v153\n" \
  "v_cvt_pk_bf16_f32 v105, v154, v155\n" \
  "v_cvt_pk_bf16_f32 v106, v156, v157\n" \
  "v_cvt_pk_bf16_f32 v107, v158, v159\n" \
  "v_cvt_pk_bf16_f32 v108, v184, v185\n" \
  "v_cvt_pk_bf16_f32 v109, v186, v187\n" \
  "v_cvt_pk_bf16_f32 v110, v188, v189\n" \
  "v_cvt_pk_bf16_f32 v111, v190, v191\n" \
  "v_cvt_pk_bf16_f32 v112, v216, v217\n" \
  "v_cvt_pk_bf16_f32 v113, v218, v219\n" \
  "v_cvt_pk_bf16_f32 v114, v220, v221\n" \
  "v_cvt_pk_bf16_f32 v115, v222, v223\n" \
  "v_cvt_pk_bf16_f32 v116, v248, v249\n" \
  "v_cvt_pk_bf16_f32 v117, v250, v251\n" \
  "v_cvt_pk_bf16_f32 v118, v252, v253\n" \
  "v_cvt_pk_bf16_f32 v119, v254, v255\n" \
  "v_cndmask_b32 v116, v116, v126, s[62:63]\n" \
  "v_cndmask_b32 v117, v117, v126, s[62:63]\n" \
  "v_cndmask_b32 v118, v118, v126, s[62:63]\n" \
  "v_cndmask_b32 v119, v119, v126, s[62:63]\n" \
  "v_mfma_f32_16x16x32_bf16 v[64:67], v[104:107], v[112:115], v[64:67]\n" \
  "s_nop 0\n" \
  "v_mfma_f32_16x16x32_bf16 v[68:71], v[104:107], v[116:119], v[68:71]\n" \
  "v_mfma_f32_16x16x32_bf16 v[72:75], v[108:111], v[112:115], v[72:75]\n" \
  "v_mfma_f32_16x16x32_bf16 v[76:79], v[108:111], v[116:119], v[76:79]\n" \
  "s_nop 3\n" \
  "buffer_store_dwordx4 v[64:67], %[l16], %[rs], s60 offen sc1\n" \
  "s_nop 0\n" \
  "buffer_store_dwordx4 v[68:71], %[l16], %[rs], s60 offen offset:1024 sc1\n" \
  "buffer_store_dwordx4 v[72:75], %[l16], %[rs], s60 offen offset:2048 sc1\n" \
  "buffer_store_dwordx4 v[76:79], %[l16], %[rs], s60 offen offset:3072 sc1\n" \
  "s_add_u32 s60, s60, 65536\n" \
  "s_add_u32 s57, s57, 1\n" \
  "s_cmp_lt_u32 s57, 4\n" \
  "s_cbranch_scc1 .LL_kv%=\n" \
  "s_waitcnt vmcnt(0) lgkmcnt(0)\n" \
  "s_barrier\n" \
  "buffer_store_dword v127, %[oL1], %[rs], 0 offen sc1\n" \
  "s_mov_b32 s57, 0\n" \
  ".LL_q%=:\n" \
  "s_waitcnt vmcnt(7) lgkmcnt(7)\n" \
  "v_mfma_f32_16x16x32_bf16 v[128:131], a[128:131], v[32:35], 0\n" \
  "s_waitcnt lgkmcnt(6)\n" \
  "v_mfma_f32_16x16x32_bf16 v[132:135], a[128:131], v[36:39], 0\n" \
  "s_waitcnt lgkmcnt(5)\n" \
  "v_mfma_f32_16x16x32_bf16 v[136:139], a[128:131], v[40:43], 0\n" \
  "s_waitcnt lgkmcnt(4)\n" \
  "v_mfma_f32_16x16x32_bf16 v[140:143], a[128:131], v[44:47], 0\n" \
  "s_waitcnt lgkmcnt(3)\n" \
  "v_mfma_f32_16x16x32_bf16 v[144:147], a[128:131], v[48:51], 0\n" \
  "s_waitcnt lgkmcnt(2)\n" \
  "v_mfma_f32_16x16x32_bf16 v[148:151], a[128:131], v[52:55], 0\n" \
  "s_waitcnt lgkmcnt(1)\n" \
  "v_mfma_f32_16x16x32_bf16 v[152:155], a[128:131], v[56:59], 0\n" \
  "s_waitcnt lgkmcnt(0)\n" \
  "v_mfma_f32_16x16x32_bf16 v[156:159], a[128:131], v[60:63], 0\n" \
  "buffer_load_dwordx4 a[128:131], %[l16], %[rs], s52 offen offset:2048 sc1\n" \
  "s_waitcnt vmcnt(7)\n" \
  "v_mfma_f32_16x16x32_bf16 v[160:163], a[132:135], v[32:35], 0\n" \
  "ds_read_b128 v[64:67], v122\n" \
  "v_mfma_f32_16x16x32_bf16 v[164:167], a[132:135], v[36:39], 0\n" \
  "ds_read_b128 v[68:71], v122 offset:14336\n" \
  "v_mfma_f32_16x16x32_bf16 v[168:171], a[132:135], v[40:43], 0\n" \
  "ds_read_b128 v[72:75], v122 offset:28672\n" \
  "v_mfma_f32_16x16x32_bf16 v[172:175], a[132:135], v[44:47], 0\n" \
  "ds_read_b128 v[76:79], v122 offset:43008\n" \
  "v_mfma_f32_16x16x32_bf16 v[176:179], a[132:135], v[48:51], 0\n" \
  "ds_read_b128 v[80:83], v123\n" \
  "v_mfma_f32_16x16x32_bf16 v[180:183], a[132:135], v[52:55], 0\n" \
  "ds_read_b128 v[84:87], v123 offset:14336\n" \
  "v_mfma_f32_16x16x32_bf16 v[184:187], a[132:135], v[56:59], 0\n" \
  "ds_read_b128 v[88:91], v123 offset:28672\n" \
  "v_mfma_f32_16x16x32_bf16 v[188:191], a[132:135], v[60:63], 0\n" \
  "ds_read_b128 v[92:95], v123 offset:43008\n" \
  "v_add_u32 v122, 128, v122\n" \
  "v_add_u32 v123, 128, v123\n" \
  "buffer_load_dwordx4 a[132:135], %[l16], %[rs], s53 offen offset:2048 sc1\n" \
  "s_waitcnt vmcnt(7)\n" \
  "v_mfma_f32_16x16x32_bf16 v[192:195], a[136:139], v[32:35], 0\n" \
  "v_mfma_f32_16x16x32_bf16 v[196:199], a[136:139], v[36:39], 0\n" \
  "v_mfma_f32_16x16x32_bf16 v[200:203], a[136:139], v[40:43], 0\n" \
  "v_mfma_f32_16x16x32_bf16 v[204:207], a[136:139], v[44:47], 0\n" \
  "v_mfma_f32_16x16x32_bf16 v[208:211], a[136:139], v[48:51], 0\n" \
  "v_mfma_f32_16x16x32_bf16 v[212:215], a[136:139], v[52:55], 0\n" \
  "v_mfma_f32_16x16x32_bf16 v[216:219], a[136:139], v[56:59], 0\n" \
  "v_mfma_f32_16x16x32_bf16 v[220:223], a[136:139], v[60:63], 0\n" \
  "buffer_load_dwordx4 a[136:139], %[l16], %[rs], s54 offen offset:2048 sc1\n" \
  "s_waitcnt vmcnt(7)\n" \
  "v_mfma_f32_16x16x32_bf16 v[224:227], a[140:143], v[32:35], 0\n" \
  "v_mfma_f32_16x16x32_bf16 v[228:231], a[140:143], v[36:39], 0\n" \
  "v_mfma_f32_16x16x32_bf16 v[232:235], a[140:143], v[40:43], 0\n" \
  "v_mfma_f32_16x16x32_bf16 v[236:239], a[140:143], v[44:47], 0\n" \
  "v_mfma_f32_16x16x32_bf16 v[240:243], a[140:143], v[48:51], 0\n" \
  "v_mfma_f32_16x16x32_bf16 v[244:247], a[140:143], v[52:55], 0\n" \
  "v_mfma_f32_16x16x32_bf16 v[248:251], a[140:143], v[56:59], 0\n" \
  "v_mfma_f32_16x16x32_bf16 v[252:255], a[140:143], v[60:63], 0\n" \
  "buffer_load_dwordx4 a[140:143], %[l16], %[rs], s55 offen offset:2048 sc1\n" \
  "s_waitcnt vmcnt(7) lgkmcnt(7)\n" \
  "v_mfma_f32_16x16x32_bf16 v[128:131], a[144:147], v[64:67], v[128:131]\n" \
  "s_waitcnt lgkmcnt(6)\n" \
  "v_mfma_f32_16x16x32_bf16 v[132:135], a[144:147], v[68:71], v[132:135]\n" \
  "s_waitcnt lgkmcnt(5)\n" \
  "v_mfma_f32_16x16x32_bf16 v[136:139], a[144:147], v[72:75], v[136:139]\n" \
  "s_waitcnt lgkmcnt(4)\n" \
  "v_mfma_f32_16x16x32_bf16 v[140:143], a[144:147], v[76:79], v[140:143]\n" \
  "s_waitcnt lgkmcnt(3)\n" \
  "v_mfma_f32_16x16x32_bf16 v[144:147], a[144:147], v[80:83], v[144:147]\n" \
  "s_waitcnt lgkmcnt(2)\n" \
  "v_mfma_f32_16x16x32_bf16 v[148:151], a[144:147], v[84:87], v[148:151]\n" \
  "s_waitcnt lgkmcnt(1)\n" \
  "v_mfma_f32_16x16x32_bf16 v[152:155], a[144:147], v[88:91], v[152:155]\n" \
  "s_waitcnt lgkmcnt(0)\n" \
  "v_mfma_f32_16x16x32_bf16 v[156:159], a[144:147], v[92:95], v[156:159]\n" \
  "buffer_load_dwordx4 a[144:147], %[l16], %[rs], s52 offen offset:3072 sc1\n" \
  "s_waitcnt vmcnt(7)\n" \
  "v_mfma_f32_16x16x32_bf16 v[160:163], a[148:151], v[64:67], v[160:163]\n" \
  "ds_read_b128 v[32:35], v120\n" \
  "v_mfma_f32_16x16x32_bf16 v[164:167], a[148:151], v[68:71], v[164:167]\n" \
  "ds_read_b128 v[36:39], v120 offset:14336\n" \
  "v_mfma_f32_16x16x32_bf16 v[168:171], a[148:151], v[72:75], v[168:171]\n" \
  "ds_read_b128 v[40:43], v120 offset:28672\n" \
  "v_mfma_f32_16x16x32_bf16 v[172:175], a[148:151], v[76:79], v[172:175]\n" \
  "ds_read_b128 v[44:47], v120 offset:43008\n" \
  "v_mfma_f32_16x16x32_bf16 v[176:179], a[148:151], v[80:83], v[176:179]\n" \
  "ds_read_b128 v[48:51], v121\n" \
  "v_mfma_f32_16x16x32_bf16 v[180:183], a[148:151], v[84:87], v[180:183]\n" \
  "ds_read_b128 v[52:55], v121 offset:14336\n" \
  "v_mfma_f32_16x16x32_bf16 v[184:187], a[148:151], v[88:91], v[184:187]\n" \
  "ds_read_b128 v[56:59], v121 offset:28672\n" \
  "v_mfma_f32_16x16x32_bf16 v[188:191], a[148:151], v[92:95], v[188:191]\n" \
  "ds_read_b128 v[60:63], v121 offset:43008\n" \
  "v_add_u32 v120, 128, v120\n" \
  "v_add_u32 v121, 128, v121\n" \
  "buffer_load_dwordx4 a[148:151], %[l16], %[rs], s53 offen offset:3072 sc1\n" \
  "s_waitcnt vmcnt(7)\n" \
  "v_mfma_f32_16x16x32_bf16 v[192:195], a[152:155], v[64:67], v[192:195]\n" \
  "v_mfma_f32_16x16x32_bf16 v[196:199], a[152:155], v[68:71], v[196:199]\n" \
  "v_mfma_f32_16x16x32_bf16 v[200:203], a[152:155], v[72:75], v[200:203]\n" \
  "v_mfma_f32_16x16x32_bf16 v[204:207], a[152:155], v[76:79], v[204:207]\n" \
  "v_mfma_f32_16x16x32_bf16 v[208:211], a[152:155], v[80:83], v[208:211]\n" \
  "v_mfma_f32_16x16x32_bf16 v[212:215], a[152:155], v[84:87], v[212:215]\n" \
  "v_mfma_f32_16x16x32_bf16 v[216:219], a[152:155], v[88:91], v[216:219]\n" \
  "v_mfma_f32_16x16x32_bf16 v[220:223], a[152:155], v[92:95], v[220:223]\n" \
  "buffer_load_dwordx4 a[152:155], %[l16], %[rs], s54 offen offset:3072 sc1\n" \
  "s_waitcnt vmcnt(7)\n" \
  "v_mfma_f32_16x16x32_bf16 v[224:227], a[156:159], v[64:67], v[224:227]\n" \
  "v_mfma_f32_16x16x32_bf16 v[228:231], a[156:159], v[68:71], v[228:231]\n" \
  "v_mfma_f32_16x16x32_bf16 v[232:235], a[156:159], v[72:75], v[232:235]\n" \
  "v_mfma_f32_16x16x32_bf16 v[236:239], a[156:159], v[76:79], v[236:239]\n" \
  "v_mfma_f32_16x16x32_bf16 v[240:243], a[156:159], v[80:83], v[240:243]\n" \
  "v_mfma_f32_16x16x32_bf16 v[244:247], a[156:159], v[84:87], v[244:247]\n" \
  "v_mfma_f32_16x16x32_bf16 v[248:251], a[156:159], v[88:91], v[248:251]\n" \
  "v_mfma_f32_16x16x32_bf16 v[252:255], a[156:159], v[92:95], v[252:255]\n" \
  "buffer_load_dwordx4 a[156:159], %[l16], %[rs], s55 offen offset:3072 sc1\n" \
  "s_add_u32 s52, s52, 2048\n" \
  "s_add_u32 s53, s53, 2048\n" \
  "s_add_u32 s54, s54, 2048\n" \
  "s_add_u32 s55, s55, 2048\n" \
  "s_mov_b32 s56, 6\n" \
  ".LL_k2%=:\n" \
  "s_waitcnt vmcnt(7) lgkmcnt(7)\n" \
  "v_mfma_f32_16x16x32_bf16 v[128:131], a[128:131], v[32:35], v[128:131]\n" \
  "s_waitcnt lgkmcnt(6)\n" \
  "v_mfma_f32_16x16x32_bf16 v[132:135], a[128:131], v[36:39], v[132:135]\n" \
  "s_waitcnt lgkmcnt(5)\n" \
  "v_mfma_f32_16x16x32_bf16 v[136:139], a[128:131], v[40:43], v[136:139]\n" \
  "s_waitcnt lgkmcnt(4)\n" \
  "v_mfma_f32_16x16x32_bf16 v[140:143], a[128:131], v[44:47], v[140:143]\n" \
  "s_waitcnt lgkmcnt(3)\n" \
  "v_mfma_f32_16x16x32_bf16 v[144:147], a[128:131], v[48:51], v[144:147]\n" \
  "s_waitcnt lgkmcnt(2)\n" \
  "v_mfma_f32_16x16x32_bf16 v[148:151], a[128:131], v[52:55], v[148:151]\n" \
  "s_waitcnt lgkmcnt(1)\n" \
  "v_mfma_f32_16x16x32_bf16 v[152:155], a[128:131], v[56:59], v[152:155]\n" \
  "s_waitcnt lgkmcnt(0)\n" \
  "v_mfma_f32_16x16x32_bf16 v[156:159], a[128:131], v[60:63], v[156:159]\n" \
  "buffer_load_dwordx4 a[128:131], %[l16], %[rs], s52 offen offset:2048 sc1\n" \
  "s_waitcnt vmcnt(7)\n" \
  "v_mfma_f32_16x16x32_bf16 v[160:163], a[132:135], v[32:35], v[160:163]\n" \
  "ds_read_b128 v[64:67], v122\n" \
  "v_mfma_f32_16x16x32_bf16 v[164:167], a[132:135], v[36:39], v[164:167]\n" \
  "ds_read_b128 v[68:71], v122 offset:14336\n" \
  "v_mfma_f32_16x16x32_bf16 v[168:171], a[132:135], v[40:43], v[168:171]\n" \
  "ds_read_b128 v[72:75], v122 offset:28672\n" \
  "v_mfma_f32_16x16x32_bf16 v[172:175], a[132:135], v[44:47], v[172:175]\n" \
  "ds_read_b128 v[76:79], v122 offset:43008\n" \
  "v_mfma_f32_16x16x32_bf16 v[176:179], a[132:135], v[48:51], v[176:179]\n" \
  "ds_read_b128 v[80:83], v123\n" \
  "v_mfma_f32_16x16x32_bf16 v[180:183], a[132:135], v[52:55], v[180:183]\n" \
  "ds_read_b128 v[84:87], v123 offset:14336\n" \
  "v_mfma_f32_16x16x32_bf16 v[184:187], a[132:135], v[56:59], v[184:187]\n" \
  "ds_read_b128 v[88:91], v123 offset:28672\n" \
  "v_mfma_f32_16x16x32_bf16 v[188:191], a[132:135], v[60:63], v[188:191]\n" \
  "ds_read_b128 v[92:95], v123 offset:43008\n" \
  "v_add_u32 v122, 128, v122\n" \
  "v_add_u32 v123, 128, v123\n" \
  "buffer_load_dwordx4 a[132:135], %[l16], %[rs], s53 offen offset:2048 sc1\n" \
  "s_waitcnt vmcnt(7)\n" \
  "v_mfma_f32_16x16x32_bf16 v[192:195], a[136:139], v[32:35], v[192:195]\n" \
  "v_mfma_f32_16x16x32_bf16 v[196:199], a[136:139], v[36:39], v[196:199]\n" \
  "v_mfma_f32_16x16x32_bf16 v[200:203], a[136:139], v[40:43], v[200:203]\n" \
  "v_mfma_f32_16x16x32_bf16 v[204:207], a[136:139], v[44:47], v[204:207]\n" \
  "v_mfma_f32_16x16x32_bf16 v[208:211], a[136:139], v[48:51], v[208:211]\n" \
  "v_mfma_f32_16x16x32_bf16 v[212:215], a[136:139], v[52:55], v[212:215]\n" \
  "v_mfma_f32_16x16x32_bf16 v[216:219], a[136:139], v[56:59], v[216:219]\n" \
  "v_mfma_f32_16x16x32_bf16 v[220:223], a[136:139], v[60:63], v[220:223]\n" \
  "buffer_load_dwordx4 a[136:139], %[l16], %[rs], s54 offen offset:2048 sc1\n" \
  "s_waitcnt vmcnt(7)\n" \
  "v_mfma_f32_16x16x32_bf16 v[224:227], a[140:143], v[32:35], v[224:227]\n" \
  "v_mfma_f32_16x16x32_bf16 v[228:231], a[140:143], v[36:39], v[228:231]\n" \
  "v_mfma_f32_16x16x32_bf16 v[232:235], a[140:143], v[40:43], v[232:235]\n" \
  "v_mfma_f32_16x16x32_bf16 v[236:239], a[140:143], v[44:47], v[236:239]\n" \
  "v_mfma_f32_16x16x32_bf16 v[240:243], a[140:143], v[48:51], v[240:243]\n" \
  "v_mfma_f32_16x16x32_bf16 v[244:247], a[140:143], v[52:55], v[244:247]\n" \
  "v_mfma_f32_16x16x32_bf16 v[248:251], a[140:143], v[56:59], v[248:251]\n" \
  "v_mfma_f32_16x16x32_bf16 v[252:255], a[140:143], v[60:63], v[252:255]\n" \
  "buffer_load_dwordx4 a[140:143], %[l16], %[rs], s55 offen offset:2048 sc1\n" \
  "s_waitcnt vmcnt(7) lgkmcnt(7)\n" \
  "v_mfma_f32_16x16x32_bf16 v[128:131], a[144:147], v[64:67], v[128:131]\n" \
  "s_waitcnt lgkmcnt(6)\n" \
  "v_mfma_f32_16x16x32_bf16 v[132:135], a[144:147], v[68:71], v[132:135]\n" \
  "s_waitcnt lgkmcnt(5)\n" \
  "v_mfma_f32_16x16x32_bf16 v[136:139], a[144:147], v[72:75], v[136:139]\n" \
  "s_waitcnt lgkmcnt(4)\n" \
  "v_mfma_f32_16x16x32_bf16 v[140:143], a[144:147], v[76:79], v[140:143]\n" \
  "s_waitcnt lgkmcnt(3)\n" \
  "v_mfma_f32_16x16x32_bf16 v[144:147], a[144:147], v[80:83], v[144:147]\n" \
  "s_waitcnt lgkmcnt(2)\n" \
  "v_mfma_f32_16x16x32_bf16 v[148:151], a[144:147], v[84:87], v[148:151]\n" \
  "s_waitcnt lgkmcnt(1)\n" \
  "v_mfma_f32_16x16x32_bf16 v[152:155], a[144:147], v[88:91], v[152:155]\n" \
  "s_waitcnt lgkmcnt(0)\n" \
  "v_mfma_f32_16x16x32_bf16 v[156:159], a[144:147], v[92:95], v[156:159]\n" \
  "buffer_load_dwordx4 a[144:147], %[l16], %[rs], s52 offen offset:3072 sc1\n" \
  "s_waitcnt vmcnt(7)\n" \
  "v_mfma_f32_16x16x32_bf16 v[160:163], a[148:151], v[64:67], v[160:163]\n" \
  "ds_read_b128 v[32:35], v120\n" \
  "v_mfma_f32_16x16x32_bf16 v[164:167], a[148:151], v[68:71], v[164:167]\n" \
  "ds_read_b128 v[36:39], v120 offset:14336\n" \
  "v_mfma_f32_16x16x32_bf16 v[168:171], a[148:151], v[72:75], v[168:171]\n" \
  "ds_read_b128 v[40:43], v120 offset:28672\n" \
  "v_mfma_f32_16x16x32_bf16 v[172:175], a[148:151], v[76:79], v[172:175]\n" \
  "ds_read_b128 v[44:47], v120 offset:43008\n" \
  "v_mfma_f32_16x16x32_bf16 v[176:179], a[148:151], v[80:83], v[176:179]\n" \
  "ds_read_b128 v[48:51], v121\n" \
  "v_mfma_f32_16x16x32_bf16 v[180:183], a[148:151], v[84:87], v[180:183]\n" \
  "ds_read_b128 v[52:55], v121 offset:14336\n" \
  "v_mfma_f32_16x16x32_bf16 v[184:187], a[148:151], v[88:91], v[184:187]\n" \
  "ds_read_b128 v[56:59], v121 offset:28672\n" \
  "v_mfma_f32_16x16x32_bf16 v[188:191], a[148:151], v[92:95], v[188:191]\n" \
  "ds_read_b128 v[60:63], v121 offset:43008\n" \
  "v_add_u32 v120, 128, v120\n" \
  "v_add_u32 v121, 128, v121\n" \
  "buffer_load_dwordx4 a[148:151], %[l16], %[rs], s53 offen offset:3072 sc1\n" \
  "s_waitcnt vmcnt(7)\n" \
  "v_mfma_f32_16x16x32_bf16 v[192:195], a[152:155], v[64:67], v[192:195]\n" \
  "v_mfma_f32_16x16x32_bf16 v[196:199], a[152:155], v[68:71], v[196:199]\n" \
  "v_mfma_f32_16x16x32_bf16 v[200:203], a[152:155], v[72:75], v[200:203]\n" \
  "v_mfma_f32_16x16x32_bf16 v[204:207], a[152:155], v[76:79], v[204:207]\n" \
  "v_mfma_f32_16x16x32_bf16 v[208:211], a[152:155], v[80:83], v[208:211]\n" \
  "v_mfma_f32_16x16x32_bf16 v[212:215], a[152:155], v[84:87], v[212:215]\n" \
  "v_mfma_f32_16x16x32_bf16 v[216:219], a[152:155], v[88:91], v[216:219]\n" \
  "v_mfma_f32_16x16x32_bf16 v[220:223], a[152:155], v[92:95], v[220:223]\n" \
  "buffer_load_dwordx4 a[152:155], %[l16], %[rs], s54 offen offset:3072 sc1\n" \
  "s_waitcnt vmcnt(7)\n" \
  "v_mfma_f32_16x16x32_bf16 v[224:227], a[156:159], v[64:67], v[224:227]\n" \
  "v_mfma_f32_16x16x32_bf16 v[228:231], a[156:159], v[68:71], v[228:231]\n" \
  "v_mfma_f32_16x16x32_bf16 v[232:235], a[156:159], v[72:75], v[232:235]\n" \
  "v_mfma_f32_16x16x32_bf16 v[236:239], a[156:159], v[76:79], v[236:239]\n" \
  "v_mfma_f32_16x16x32_bf16 v[240:243], a[156:159], v[80:83], v[240:243]\n" \
  "v_mfma_f32_16x16x32_bf16 v[244:247], a[156:159], v[84:87], v[244:247]\n" \
  "v_mfma_f32_16x16x32_bf16 v[248:251], a[156:159], v[88:91], v[248:251]\n" \
  "v_mfma_f32_16x16x32_bf16 v[252:255], a[156:159], v[92:95], v[252:255]\n" \
  "buffer_load_dwordx4 a[156:159], %[l16], %[rs], s55 offen offset:3072 sc1\n" \
  "s_add_u32 s52, s52, 2048\n" \
  "s_add_u32 s53, s53, 2048\n" \
  "s_add_u32 s54, s54, 2048\n" \
  "s_add_u32 s55, s55, 2048\n" \
  "s_sub_u32 s56, s56, 1\n" \
  "s_cmp_lg_u32 s56, 0\n" \
  "s_cbranch_scc1 .LL_k2%=\n" \
  "s_add_u32 s52, %[sWq], 57344\n" \
  "s_add_u32 s53, %[sWq], 71680\n" \
  "s_add_u32 s54, %[sWq], 86016\n" \
  "s_add_u32 s55, %[sWq], 100352\n" \
  "buffer_load_dwordx4 a[128:131], %[l16], %[rs], s52 offen sc1\n" \
  "buffer_load_dwordx4 a[132:135], %[l16], %[rs], s53 offen sc1\n" \
  "buffer_load_dwordx4 a[136:139], %[l16], %[rs], s54 offen sc1\n" \
  "buffer_load_dwordx4 a[140:143], %[l16], %[rs], s55 offen sc1\n" \
  "buffer_load_dwordx4 a[144:147], %[l16], %[rs], s52 offen offset:1024 sc1\n" \
  "buffer_load_dwordx4 a[148:151], %[l16], %[rs], s53 offen offset:1024 sc1\n" \
  "buffer_load_dwordx4 a[152:155], %[l16], %[rs], s54 offen offset:1024 sc1\n" \
  "buffer_load_dwordx4 a[156:159], %[l16], %[rs], s55 offen offset:1024 sc1\n" \
  "v_mov_b32 v120, %[rX]\n" \
  "v_add_u32 v121, 57344, %[rX]\n" \
  "v_xor_b32 v122, 64, v120\n" \
  "v_xor_b32 v123, 64, v121\n" \
  "ds_read_b128 v[32:35], v120\n" \
  "ds_read_b128 v[36:39], v120 offset:14336\n" \
  "ds_read_b128 v[40:43], v120 offset:28672\n" \
  "ds_read_b128 v[44:47], v120 offset:43008\n" \
  "ds_read_b128 v[48:51], v121\n" \
  "ds_read_b128 v[52:55], v121 offset:14336\n" \
  "ds_read_b128 v[56:59], v121 offset:28672\n" \
  "ds_read_b128 v[60:63], v121 offset:43008\n" \
  "v_add_u32 v120, 128, v120\n" \
  "v_add_u32 v121, 128, v121\n" \
  "v_accvgpr_mov_b32 a0, a64\n" \
  "v_accvgpr_mov_b32 a1, a65\n" \
  "v_accvgpr_mov_b32 a2, a66\n" \
  "v_accvgpr_mov_b32 a3, a67\n" \
  "v_accvgpr_mov_b32 a4, a68\n" \
  "v_accvgpr_mov_b32 a5, a69\n" \
  "v_accvgpr_mov_b32 a6, a70\n" \
  "v_accvgpr_mov_b32 a7, a71\n" \
  "v_accvgpr_mov_b32 a8, a72\n" \
  "v_accvgpr_mov_b32 a9, a73\n" \
  "v_accvgpr_mov_b32 a10, a74\n" \
  "v_accvgpr_mov_b32 a11, a75\n" \
  "v_accvgpr_mov_b32 a12, a76\n" \
  "v_accvgpr_mov_b32 a13, a77\n" \
  "v_accvgpr_mov_b32 a14, a78\n" \
  "v_accvgpr_mov_b32 a15, a79\n" \
  "v_accvgpr_mov_b32 a16, a80\n" \
  "v_accvgpr_mov_b32 a17, a81\n" \
  "v_accvgpr_mov_b32 a18, a82\n" \
  "v_accvgpr_mov_b32 a19, a83\n" \
  "v_accvgpr_mov_b32 a20, a84\n" \
  "v_accvgpr_mov_b32 a21, a85\n" \
  "v_accvgpr_mov_b32 a22, a86\n" \
  "v_accvgpr_mov_b32 a23, a87\n" \
  "v_accvgpr_mov_b32 a24, a88\n" \
  "v_accvgpr_mov_b32 a25, a89\n" \
  "v_accvgpr_mov_b32 a26, a90\n" \
  "v_accvgpr_mov_b32 a27, a91\n" \
  "v_accvgpr_mov_b32 a28, a92\n" \
  "v_accvgpr_mov_b32 a29, a93\n" \
  "v_accvgpr_mov_b32 a30, a94\n" \
  "v_accvgpr_mov_b32 a31, a95\n" \
  "v_accvgpr_mov_b32 a32, a96\n" \
  "v_accvgpr_mov_b32 a33, a97\n" \
  "v_accvgpr_mov_b32 a34, a98\n" \
  "v_accvgpr_mov_b32 a35, a99\n" \
  "v_accvgpr_mov_b32 a36, a100\n" \
  "v_accvgpr_mov_b32 a37, a101\n" \
  "v_accvgpr_mov_b32 a38, a102\n" \
  "v_accvgpr_mov_b32 a39, a103\n" \
  "v_accvgpr_mov_b32 a40, a104\n" \
  "v_accvgpr_mov_b32 a41, a105\n" \
  "v_accvgpr_mov_b32 a42, a106\n" \
  "v_accvgpr_mov_b32 a43, a107\n" \
  "v_accvgpr_mov_b32 a44, a108\n" \
  "v_accvgpr_mov_b32 a45, a109\n" \
  "v_accvgpr_mov_b32 a46, a110\n" \
  "v_accvgpr_mov_b32 a47, a111\n" \
  "v_accvgpr_mov_b32 a48, a112\n" \
  "v_accvgpr_mov_b32 a49, a113\n" \
  "v_accvgpr_mov_b32 a50, a114\n" \
  "v_accvgpr_mov_b32 a51, a115\n" \
  "v_accvgpr_mov_b32 a52, a116\n" \
  "v_accvgpr_mov_b32 a53, a117\n" \
  "v_accvgpr_mov_b32 a54, a118\n" \
  "v_accvgpr_mov_b32 a55, a119\n" \
  "v_accvgpr_mov_b32 a56, a120\n" \
  "v_accvgpr_mov_b32 a57, a121\n" \
  "v_accvgpr_mov_b32 a58, a122\n" \
  "v_accvgpr_mov_b32 a59, a123\n" \
  "v_accvgpr_mov_b32 a60, a124\n" \
  "v_accvgpr_mov_b32 a61, a125\n" \
  "v_accvgpr_mov_b32 a62, a126\n" \
  "v_accvgpr_mov_b32 a63, a127\n" \
  "v_mul_f32 v96, s58, v128\n" \
  "v_mul_f32 v97, s58, v129\n" \
  "v_mul_f32 v98, s58, v130\n" \
  "v_mul_f32 v99, s58, v131\n" \
  "v_exp_f32 v96, v96 clamp\n" \
  "v_exp_f32 v97, v97 clamp\n" \
  "v_exp_f32 v98, v98 clamp\n" \
  "v_exp_f32 v99, v99 clamp\n" \
  "v_add_f32 v128, s59, v128\n" \
  "v_add_f32 v129, s59, v129\n" \
  "v_add_f32 v130, s59, v130\n" \
  "v_add_f32 v131, s59, v131\n" \
  "v_max_f32 v128, s59, v128\n" \
  "v_max_f32 v129, s59, v129\n" \
  "v_max_f32 v130, s59, v130\n" \
  "v_max_f32 v131, s59, v131\n" \
  "v_add_f32 v128, v128, v96\n" \
  "v_add_f32 v129, v129, v97\n" \
  "v_add_f32 v130, v130, v98\n" \
  "v_add_f32 v131, v131, v99\n" \
  "v_mul_f32 v96, s58, v132\n" \
  "v_mul_f32 v97, s58, v133\n" \
  "v_mul_f32 v98, s58, v134\n" \
  "v_mul_f32 v99, s58, v135\n" \
  "v_exp_f32 v96, v96 clamp\n" \
  "v_exp_f32 v97, v97 clamp\n" \
  "v_exp_f32 v98, v98 clamp\n" \
  "v_exp_f32 v99, v99 clamp\n" \
  "v_add_f32 v132, s59, v132\n" \
  "v_add_f32 v133, s59, v133\n" \
  "v_add_f32 v134, s59, v134\n" \
  "v_add_f32 v135, s59, v135\n" \
  "v_max_f32 v132, s59, v132\n" \
  "v_max_f32 v133, s59, v133\n" \
  "v_max_f32 v134, s59, v134\n" \
  "v_max_f32 v135, s59, v135\n" \
  "v_add_f32 v132, v132, v96\n" \
  "v_add_f32 v133, v133, v97\n" \
  "v_add_f32 v134, v134, v98\n" \
  "v_add_f32 v135, v135, v99\n" \
  "v_mul_f32 v96, s58, v136\n" \
  "v_mul_f32 v97, s58, v137\n" \
  "v_mul_f32 v98, s58, v138\n" \
  "v_mul_f32 v99, s58, v139\n" \
  "v_exp_f32 v96, v96 clamp\n" \
  "v_exp_f32 v97, v97 clamp\n" \
  "v_exp_f32 v98, v98 clamp\n" \
  "v_exp_f32 v99, v99 clamp\n" \
  "v_add_f32 v136, s59, v136\n" \
  "v_add_f32 v137, s59, v137\n" \
  "v_add_f32 v138, s59, v138\n" \
  "v_add_f32 v139, s59, v139\n" \
  "v_max_f32 v136, s59, v136\n" \
  "v_max_f32 v137, s59, v137\n" \
  "v_max_f32 v138, s59, v138\n" \
  "v_max_f32 v139, s59, v139\n" \
  "v_add_f32 v136, v136, v96\n" \
  "v_add_f32 v137, v137, v97\n" \
  "v_add_f32 v138, v138, v98\n" \
  "v_add_f32 v139, v139, v99\n" \
  "v_mul_f32 v96, s58, v140\n" \
  "v_mul_f32 v97, s58, v141\n" \
  "v_mul_f32 v98, s58, v142\n" \
  "v_mul_f32 v99, s58, v143\n" \
  "v_exp_f32 v96, v96 clamp\n" \
  "v_exp_f32 v97, v97 clamp\n" \
  "v_exp_f32 v98, v98 clamp\n" \
  "v_exp_f32 v99, v99 clamp\n" \
  "v_add_f32 v140, s59, v140\n" \
  "v_add_f32 v141, s59, v141\n" \
  "v_add_f32 v142, s59, v142\n" \
  "v_add_f32 v143, s59, v143\n" \
  "v_max_f32 v140, s59, v140\n" \
  "v_max_f32 v141, s59, v141\n" \
  "v_max_f32 v142, s59, v142\n" \
  "v_max_f32 v143, s59, v143\n" \
  "v_add_f32 v140, v140, v96\n" \
  "v_add_f32 v141, v141, v97\n" \
  "v_add_f32 v142, v142, v98\n" \
  "v_add_f32 v143, v143, v99\n" \
  "v_mul_f32 v96, s58, v144\n" \
  "v_mul_f32 v97, s58, v145\n" \
  "v_mul_f32 v98, s58, v146\n" \
  "v_mul_f32 v99, s58, v147\n" \
  "v_exp_f32 v96, v96 clamp\n" \
  "v_exp_f32 v97, v97 clamp\n" \
  "v_exp_f32 v98, v98 clamp\n" \
  "v_exp_f32 v99, v99 clamp\n" \
  "v_add_f32 v144, s59, v144\n" \
  "v_add_f32 v145, s59, v145\n" \
  "v_add_f32 v146, s59, v146\n" \
  "v_add_f32 v147, s59, v147\n" \
  "v_max_f32 v144, s59, v144\n" \
  "v_max_f32 v145, s59, v145\n" \
  "v_max_f32 v146, s59, v146\n" \
  "v_max_f32 v147, s59, v147\n" \
  "v_add_f32 v144, v144, v96\n" \
  "v_add_f32 v145, v145, v97\n" \
  "v_add_f32 v146, v146, v98\n" \
  "v_add_f32 v147, v147, v99\n" \
  "v_mul_f32 v96, s58, v148\n" \
  "v_mul_f32 v97, s58, v149\n" \
  "v_mul_f32 v98, s58, v150\n" \
  "v_mul_f32 v99, s58, v151\n" \
  "v_exp_f32 v96, v96 clamp\n" \
  "v_exp_f32 v97, v97 clamp\n" \
  "v_exp_f32 v98, v98 clamp\n" \
  "v_exp_f32 v99, v99 clamp\n" \
  "v_add_f32 v148, s59, v148\n" \
  "v_add_f32 v149, s59, v149\n" \
  "v_add_f32 v150, s59, v150\n" \
  "v_add_f32 v151, s59, v151\n" \
  "v_max_f32 v148, s59, v148\n" \
  "v_max_f32 v149, s59, v149\n" \
  "v_max_f32 v150, s59, v150\n" \
  "v_max_f32 v151, s59, v151\n" \
  "v_add_f32 v148, v148, v96\n" \
  "v_add_f32 v149, v149, v97\n" \
  "v_add_f32 v150, v150, v98\n" \
  "v_add_f32 v151, v151, v99\n" \
  "v_mul_f32 v96, s58, v152\n" \
  "v_mul_f32 v97, s58, v153\n" \
  "v_mul_f32 v98, s58, v154\n" \
  "v_mul_f32 v99, s58, v155\n" \
  "v_exp_f32 v96, v96 clamp\n" \
  "v_exp_f32 v97, v97 clamp\n" \
  "v_exp_f32 v98, v98 clamp\n" \
  "v_exp_f32 v99, v99 clamp\n" \
  "v_add_f32 v152, s59, v152\n" \
  "v_add_f32 v153, s59, v153\n" \
  "v_add_f32 v154, s59, v154\n" \
  "v_add_f32 v155, s59, v155\n" \
  "v_max_f32 v152, s59, v152\n" \
  "v_max_f32 v153, s59, v153\n" \
  "v_max_f32 v154, s59, v154\n" \
  "v_max_f32 v155, s59, v155\n" \
  "v_add_f32 v152, v152, v96\n" \
  "v_add_f32 v153, v153, v97\n" \
  "v_add_f32 v154, v154, v98\n" \
  "v_add_f32 v155, v155, v99\n" \
  "v_mul_f32 v96, s58, v156\n" \
  "v_mul_f32 v97, s58, v157\n" \
  "v_mul_f32 v98, s58, v158\n" \
  "v_mul_f32 v99, s58, v159\n" \
  "v_exp_f32 v96, v96 clamp\n" \
  "v_exp_f32 v97, v97 clamp\n" \
  "v_exp_f32 v98, v98 clamp\n" \
  "v_exp_f32 v99, v99 clamp\n" \
  "v_add_f32 v156, s59, v156\n" \
  "v_add_f32 v157, s59, v157\n" \
  "v_add_f32 v158, s59, v158\n" \
  "v_add_f32 v159, s59, v159\n" \
  "v_max_f32 v156, s59, v156\n" \
  "v_max_f32 v157, s59, v157\n" \
  "v_max_f32 v158, s59, v158\n" \
  "v_max_f32 v159, s59, v159\n" \
  "v_add_f32 v156, v156, v96\n" \
  "v_add_f32 v157, v157, v97\n" \
  "v_add_f32 v158, v158, v98\n" \
  "v_add_f32 v159, v159, v99\n" \
  "v_mul_f32 v96, s58, v160\n" \
  "v_mul_f32 v97, s58, v161\n" \
  "v_mul_f32 v98, s58, v162\n" \
  "v_mul_f32 v99, s58, v163\n" \
  "v_exp_f32 v96, v96 clamp\n" \
  "v_exp_f32 v97, v97 clamp\n" \
  "v_exp_f32 v98, v98 clamp\n" \
  "v_exp_f32 v99, v99 clamp\n" \
  "v_add_f32 v160, s59, v160\n" \
  "v_add_f32 v161, s59, v161\n" \
  "v_add_f32 v162, s59, v162\n" \
  "v_add_f32 v163, s59, v163\n" \
  "v_max_f32 v160, s59, v160\n" \
  "v_max_f32 v161, s59, v161\n" \
  "v_max_f32 v162, s59, v162\n" \
  "v_max_f32 v163, s59, v163\n" \
  "v_add_f32 v160, v160, v96\n" \
  "v_add_f32 v161, v161, v97\n" \
  "v_add_f32 v162, v162, v98\n" \
  "v_add_f32 v163, v163, v99\n" \
  "v_mul_f32 v96, s58, v164\n" \
  "v_mul_f32 v97, s58, v165\n" \
  "v_mul_f32 v98, s58, v166\n" \
  "v_mul_f32 v99, s58, v167\n" \
  "v_exp_f32 v96, v96 clamp\n" \
  "v_exp_f32 v97, v97 clamp\n" \
  "v_exp_f32 v98, v98 clamp\n" \
  "v_exp_f32 v99, v99 clamp\n" \
  "v_add_f32 v164, s59, v164\n" \
  "v_add_f32 v165, s59, v165\n" \
  "v_add_f32 v166, s59, v166\n" \
  "v_add_f32 v167, s59, v167\n" \
  "v_max_f32 v164, s59, v164\n" \
  "v_max_f32 v165, s59, v165\n" \
  "v_max_f32 v166, s59, v166\n" \
  "v_max_f32 v167, s59, v167\n" \
  "v_add_f32 v164, v164, v96\n" \
  "v_add_f32 v165, v165, v97\n" \
  "v_add_f32 v166, v166, v98\n" \
  "v_add_f32 v167, v167, v99\n" \
  "v_mul_f32 v96, s58, v168\n" \
  "v_mul_f32 v97, s58, v169\n" \
  "v_mul_f32 v98, s58, v170\n" \
  "v_mul_f32 v99, s58, v171\n" \
  "v_exp_f32 v96, v96 clamp\n" \
  "v_exp_f32 v97, v97 clamp\n" \
  "v_exp_f32 v98, v98 clamp\n" \
  "v_exp_f32 v99, v99 clamp\n" \
  "v_add_f32 v168, s59, v168\n" \
  "v_add_f32 v169, s59, v169\n" \
  "v_add_f32 v170, s59, v170\n" \
  "v_add_f32 v171, s59, v171\n" \
  "v_max_f32 v168, s59, v168\n" \
  "v_max_f32 v169, s59, v169\n" \
  "v_max_f32 v170, s59, v170\n" \
  "v_max_f32 v171, s59, v171\n" \
  "v_add_f32 v168, v168, v96\n" \
  "v_add_f32 v169, v169, v97\n" \
  "v_add_f32 v170, v170, v98\n" \
  "v_add_f32 v171, v171, v99\n" \
  "v_mul_f32 v96, s58, v172\n" \
  "v_mul_f32 v97, s58, v173\n" \
  "v_mul_f32 v98, s58, v174\n" \
  "v_mul_f32 v99, s58, v175\n" \
  "v_exp_f32 v96, v96 clamp\n" \
  "v_exp_f32 v97, v97 clamp\n" \
  "v_exp_f32 v98, v98 clamp\n" \
  "v_exp_f32 v99, v99 clamp\n" \
  "v_add_f32 v172, s59, v172\n" \
  "v_add_f32 v173, s59, v173\n" \
  "v_add_f32 v174, s59, v174\n" \
  "v_add_f32 v175, s59, v175\n" \
  "v_max_f32 v172, s59, v172\n" \
  "v_max_f32 v173, s59, v173\n" \
  "v_max_f32 v174, s59, v174\n" \
  "v_max_f32 v175, s59, v175\n" \
  "v_add_f32 v172, v172, v96\n" \
  "v_add_f32 v173, v173, v97\n" \
  "v_add_f32 v174, v174, v98\n" \
  "v_add_f32 v175, v175, v99\n" \
  "v_mul_f32 v96, s58, v176\n" \
  "v_mul_f32 v97, s58, v177\n" \
  "v_mul_f32 v98, s58, v178\n" \
  "v_mul_f32 v99, s58, v179\n" \
  "v_exp_f32 v96, v96 clamp\n" \
  "v_exp_f32 v97, v97 clamp\n" \
  "v_exp_f32 v98, v98 clamp\n" \
  "v_exp_f32 v99, v99 clamp\n" \
  "v_add_f32 v176, s59, v176\n" \
  "v_add_f32 v177, s59, v177\n" \
  "v_add_f32 v178, s59, v178\n" \
  "v_add_f32 v179, s59, v179\n" \
  "v_max_f32 v176, s59, v176\n" \
  "v_max_f32 v177, s59, v177\n" \
  "v_max_f32 v178, s59, v178\n" \
  "v_max_f32 v179, s59, v179\n" \
  "v_add_f32 v176, v176, v96\n" \
  "v_add_f32 v177, v177, v97\n" \
  "v_add_f32 v178, v178, v98\n" \
  "v_add_f32 v179, v179, v99\n" \
  "v_mul_f32 v96, s58, v180\n" \
  "v_mul_f32 v97, s58, v181\n" \
  "v_mul_f32 v98, s58, v182\n" \
  "v_mul_f32 v99, s58, v183\n" \
  "v_exp_f32 v96, v96 clamp\n" \
  "v_exp_f32 v97, v97 clamp\n" \
  "v_exp_f32 v98, v98 clamp\n" \
  "v_exp_f32 v99, v99 clamp\n" \
  "v_add_f32 v180, s59, v180\n" \
  "v_add_f32 v181, s59, v181\n" \
  "v_add_f32 v182, s59, v182\n" \
  "v_add_f32 v183, s59, v183\n" \
  "v_max_f32 v180, s59, v180\n" \
  "v_max_f32 v181, s59, v181\n" \
  "v_max_f32 v182, s59, v182\n" \
  "v_max_f32 v183, s59, v183\n" \
  "v_add_f32 v180, v180, v96\n" \
  "v_add_f32 v181, v181, v97\n" \
  "v_add_f32 v182, v182, v98\n" \
  "v_add_f32 v183, v183, v99\n" \
  "v_mul_f32 v96, s58, v184\n" \
  "v_mul_f32 v97, s58, v185\n" \
  "v_mul_f32 v98, s58, v186\n" \
  "v_mul_f32 v99, s58, v187\n" \
  "v_exp_f32 v96, v96 clamp\n" \
  "v_exp_f32 v97, v97 clamp\n" \
  "v_exp_f32 v98, v98 clamp\n" \
  "v_exp_f32 v99, v99 clamp\n" \
  "v_add_f32 v184, s59, v184\n" \
  "v_add_f32 v185, s59, v185\n" \
  "v_add_f32 v186, s59, v186\n" \
  "v_add_f32 v187, s59, v187\n" \
  "v_max_f32 v184, s59, v184\n" \
  "v_max_f32 v185, s59, v185\n" \
  "v_max_f32 v186, s59, v186\n" \
  "v_max_f32 v187, s59, v187\n" \
  "v_add_f32 v184, v184, v96\n" \
  "v_add_f32 v185, v185, v97\n" \
  "v_add_f32 v186, v186, v98\n" \
  "v_add_f32 v187, v187, v99\n" \
  "v_mul_f32 v96, s58, v188\n" \
  "v_mul_f32 v97, s58, v189\n" \
  "v_mul_f32 v98, s58, v190\n" \
  "v_mul_f32 v99, s58, v191\n" \
  "v_exp_f32 v96, v96 clamp\n" \
  "v_exp_f32 v97, v97 clamp\n" \
  "v_exp_f32 v98, v98 clamp\n" \
  "v_exp_f32 v99, v99 clamp\n" \
  "v_add_f32 v188, s59, v188\n" \
  "v_add_f32 v189, s59, v189\n" \
  "v_add_f32 v190, s59, v190\n" \
  "v_add_f32 v191, s59, v191\n" \
  "v_max_f32 v188, s59, v188\n" \
  "v_max_f32 v189, s59, v189\n" \
  "v_max_f32 v190, s59, v190\n" \
  "v_max_f32 v191, s59, v191\n" \
  "v_add_f32 v188, v188, v96\n" \
  "v_add_f32 v189, v189, v97\n" \
  "v_add_f32 v190, v190, v98\n" \
  "v_add_f32 v191, v191, v99\n" \
  "v_mul_f32 v96, s58, v192\n" \
  "v_mul_f32 v97, s58, v193\n" \
  "v_mul_f32 v98, s58, v194\n" \
  "v_mul_f32 v99, s58, v195\n" \
  "v_exp_f32 v96, v96 clamp\n" \
  "v_exp_f32 v97, v97 clamp\n" \
  "v_exp_f32 v98, v98 clamp\n" \
  "v_exp_f32 v99, v99 clamp\n" \
  "v_add_f32 v192, s59, v192\n" \
  "v_add_f32 v193, s59, v193\n" \
  "v_add_f32 v194, s59, v194\n" \
  "v_add_f32 v195, s59, v195\n" \
  "v_max_f32 v192, s59, v192\n" \
  "v_max_f32 v193, s59, v193\n" \
  "v_max_f32 v194, s59, v194\n" \
  "v_max_f32 v195, s59, v195\n" \
  "v_add_f32 v192, v192, v96\n" \
  "v_add_f32 v193, v193, v97\n" \
  "v_add_f32 v194, v194, v98\n" \
  "v_add_f32 v195, v195, v99\n" \
  "v_mul_f32 v96, s58, v196\n" \
  "v_mul_f32 v97, s58, v197\n" \
  "v_mul_f32 v98, s58, v198\n" \
  "v_mul_f32 v99, s58, v199\n" \
  "v_exp_f32 v96, v96 clamp\n" \
  "v_exp_f32 v97, v97 clamp\n" \
  "v_exp_f32 v98, v98 clamp\n" \
  "v_exp_f32 v99, v99 clamp\n" \
  "v_add_f32 v196, s59, v196\n" \
  "v_add_f32 v197, s59, v197\n" \
  "v_add_f32 v198, s59, v198\n" \
  "v_add_f32 v199, s59, v199\n" \
  "v_max_f32 v196, s59, v196\n" \
  "v_max_f32 v197, s59, v197\n" \
  "v_max_f32 v198, s59, v198\n" \
  "v_max_f32 v199, s59, v199\n" \
  "v_add_f32 v196, v196, v96\n" \
  "v_add_f32 v197, v197, v97\n" \
  "v_add_f32 v198, v198, v98\n" \
  "v_add_f32 v199, v199, v99\n" \
  "v_mul_f32 v96, s58, v200\n" \
  "v_mul_f32 v97, s58, v201\n" \
  "v_mul_f32 v98, s58, v202\n" \
  "v_mul_f32 v99, s58, v203\n" \
  "v_exp_f32 v96, v96 clamp\n" \
  "v_exp_f32 v97, v97 clamp\n" \
  "v_exp_f32 v98, v98 clamp\n" \
  "v_exp_f32 v99, v99 clamp\n" \
  "v_add_f32 v200, s59, v200\n" \
  "v_add_f32 v201, s59, v201\n" \
  "v_add_f32 v202, s59, v202\n" \
  "v_add_f32 v203, s59, v203\n" \
  "v_max_f32 v200, s59, v200\n" \
  "v_max_f32 v201, s59, v201\n" \
  "v_max_f32 v202, s59, v202\n" \
  "v_max_f32 v203, s59, v203\n" \
  "v_add_f32 v200, v200, v96\n" \
  "v_add_f32 v201, v201, v97\n" \
  "v_add_f32 v202, v202, v98\n" \
  "v_add_f32 v203, v203, v99\n" \
  "v_mul_f32 v96, s58, v204\n" \
  "v_mul_f32 v97, s58, v205\n" \
  "v_mul_f32 v98, s58, v206\n" \
  "v_mul_f32 v99, s58, v207\n" \
  "v_exp_f32 v96, v96 clamp\n" \
  "v_exp_f32 v97, v97 clamp\n" \
  "v_exp_f32 v98, v98 clamp\n" \
  "v_exp_f32 v99, v99 clamp\n" \
  "v_add_f32 v204, s59, v204\n" \
  "v_add_f32 v205, s59, v205\n" \
  "v_add_f32 v206, s59, v206\n" \
  "v_add_f32 v207, s59, v207\n" \
  "v_max_f32 v204, s59, v204\n" \
  "v_max_f32 v205, s59, v205\n" \
  "v_max_f32 v206, s59, v206\n" \
  "v_max_f32 v207, s59, v207\n" \
  "v_add_f32 v204, v204, v96\n" \
  "v_add_f32 v205, v205, v97\n" \
  "v_add_f32 v206, v206, v98\n" \
  "v_add_f32 v207, v207, v99\n" \
  "v_mul_f32 v96, s58, v208\n" \
  "v_mul_f32 v97, s58, v209\n" \
  "v_mul_f32 v98, s58, v210\n" \
  "v_mul_f32 v99, s58, v211\n" \
  "v_exp_f32 v96, v96 clamp\n" \
  "v_exp_f32 v97, v97 clamp\n" \
  "v_exp_f32 v98, v98 clamp\n" \
  "v_exp_f32 v99, v99 clamp\n" \
  "v_add_f32 v208, s59, v208\n" \
  "v_add_f32 v209, s59, v209\n" \
  "v_add_f32 v210, s59, v210\n" \
  "v_add_f32 v211, s59, v211\n" \
  "v_max_f32 v208, s59, v208\n" \
  "v_max_f32 v209, s59, v209\n" \
  "v_max_f32 v210, s59, v210\n" \
  "v_max_f32 v211, s59, v211\n" \
  "v_add_f32 v208, v208, v96\n" \
  "v_add_f32 v209, v209, v97\n" \
  "v_add_f32 v210, v210, v98\n" \
  "v_add_f32 v211, v211, v99\n" \
  "v_mul_f32 v96, s58, v212\n" \
  "v_mul_f32 v97, s58, v213\n" \
  "v_mul_f32 v98, s58, v214\n" \
  "v_mul_f32 v99, s58, v215\n" \
  "v_exp_f32 v96, v96 clamp\n" \
  "v_exp_f32 v97, v97 clamp\n" \
  "v_exp_f32 v98, v98 clamp\n" \
  "v_exp_f32 v99, v99 clamp\n" \
  "v_add_f32 v212, s59, v212\n" \
  "v_add_f32 v213, s59, v213\n" \
  "v_add_f32 v214, s59, v214\n" \
  "v_add_f32 v215, s59, v215\n" \
  "v_max_f32 v212, s59, v212\n" \
  "v_max_f32 v213, s59, v213\n" \
  "v_max_f32 v214, s59, v214\n" \
  "v_max_f32 v215, s59, v215\n" \
  "v_add_f32 v212, v212, v96\n" \
  "v_add_f32 v213, v213, v97\n" \
  "v_add_f32 v214, v214, v98\n" \
  "v_add_f32 v215, v215, v99\n" \
  "v_mul_f32 v96, s58, v216\n" \
  "v_mul_f32 v97, s58, v217\n" \
  "v_mul_f32 v98, s58, v218\n" \
  "v_mul_f32 v99, s58, v219\n" \
  "v_exp_f32 v96, v96 clamp\n" \
  "v_exp_f32 v97, v97 clamp\n" \
  "v_exp_f32 v98, v98 clamp\n" \
  "v_exp_f32 v99, v99 clamp\n" \
  "v_add_f32 v216, s59, v216\n" \
  "v_add_f32 v217, s59, v217\n" \
  "v_add_f32 v218, s59, v218\n" \
  "v_add_f32 v219, s59, v219\n" \
  "v_max_f32 v216, s59, v216\n" \
  "v_max_f32 v217, s59, v217\n" \
  "v_max_f32 v218, s59, v218\n" \
  "v_max_f32 v219, s59, v219\n" \
  "v_add_f32 v216, v216, v96\n" \
  "v_add_f32 v217, v217, v97\n" \
  "v_add_f32 v218, v218, v98\n" \
  "v_add_f32 v219, v219, v99\n" \
  "v_mul_f32 v96, s58, v220\n" \
  "v_mul_f32 v97, s58, v221\n" \
  "v_mul_f32 v98, s58, v222\n" \
  "v_mul_f32 v99, s58, v223\n" \
  "v_exp_f32 v96, v96 clamp\n" \
  "v_exp_f32 v97, v97 clamp\n" \
  "v_exp_f32 v98, v98 clamp\n" \
  "v_exp_f32 v99, v99 clamp\n" \
  "v_add_f32 v220, s59, v220\n" \
  "v_add_f32 v221, s59, v221\n" \
  "v_add_f32 v222, s59, v222\n" \
  "v_add_f32 v223, s59, v223\n" \
  "v_max_f32 v220, s59, v220\n" \
  "v_max_f32 v221, s59, v221\n" \
  "v_max_f32 v222, s59, v222\n" \
  "v_max_f32 v223, s59, v223\n" \
  "v_add_f32 v220, v220, v96\n" \
  "v_add_f32 v221, v221, v97\n" \
  "v_add_f32 v222, v222, v98\n" \
  "v_add_f32 v223, v223, v99\n" \
  "v_mul_f32 v96, s58, v224\n" \
  "v_mul_f32 v97, s58, v225\n" \
  "v_mul_f32 v98, s58, v226\n" \
  "v_mul_f32 v99, s58, v227\n" \
  "v_exp_f32 v96, v96 clamp\n" \
  "v_exp_f32 v97, v97 clamp\n" \
  "v_exp_f32 v98, v98 clamp\n" \
  "v_exp_f32 v99, v99 clamp\n" \
  "v_add_f32 v224, s59, v224\n" \
  "v_add_f32 v225, s59, v225\n" \
  "v_add_f32 v226, s59, v226\n" \
  "v_add_f32 v227, s59, v227\n" \
  "v_max_f32 v224, s59, v224\n" \
  "v_max_f32 v225, s59, v225\n" \
  "v_max_f32 v226, s59, v226\n" \
  "v_max_f32 v227, s59, v227\n" \
  "v_add_f32 v224, v224, v96\n" \
  "v_add_f32 v225, v225, v97\n" \
  "v_add_f32 v226, v226, v98\n" \
  "v_add_f32 v227, v227, v99\n" \
  "v_mul_f32 v96, s58, v228\n" \
  "v_mul_f32 v97, s58, v229\n" \
  "v_mul_f32 v98, s58, v230\n" \
  "v_mul_f32 v99, s58, v231\n" \
  "v_exp_f32 v96, v96 clamp\n" \
  "v_exp_f32 v97, v97 clamp\n" \
  "v_exp_f32 v98, v98 clamp\n" \
  "v_exp_f32 v99, v99 clamp\n" \
  "v_add_f32 v228, s59, v228\n" \
  "v_add_f32 v229, s59, v229\n" \
  "v_add_f32 v230, s59, v230\n" \
  "v_add_f32 v231, s59, v231\n" \
  "v_max_f32 v228, s59, v228\n" \
  "v_max_f32 v229, s59, v229\n" \
  "v_max_f32 v230, s59, v230\n" \
  "v_max_f32 v231, s59, v231\n" \
  "v_add_f32 v228, v228, v96\n" \
  "v_add_f32 v229, v229, v97\n" \
  "v_add_f32 v230, v230, v98\n" \
  "v_add_f32 v231, v231, v99\n" \
  "v_mul_f32 v96, s58, v232\n" \
  "v_mul_f32 v97, s58, v233\n" \
  "v_mul_f32 v98, s58, v234\n" \
  "v_mul_f32 v99, s58, v235\n" \
  "v_exp_f32 v96, v96 clamp\n" \
  "v_exp_f32 v97, v97 clamp\n" \
  "v_exp_f32 v98, v98 clamp\n" \
  "v_exp_f32 v99, v99 clamp\n" \
  "v_add_f32 v232, s59, v232\n" \
  "v_add_f32 v233, s59, v233\n" \
  "v_add_f32 v234, s59, v234\n" \
  "v_add_f32 v235, s59, v235\n" \
  "v_max_f32 v232, s59, v232\n" \
  "v_max_f32 v233, s59, v233\n" \
  "v_max_f32 v234, s59, v234\n" \
  "v_max_f32 v235, s59, v235\n" \
  "v_add_f32 v232, v232, v96\n" \
  "v_add_f32 v233, v233, v97\n" \
  "v_add_f32 v234, v234, v98\n" \
  "v_add_f32 v235, v235, v99\n" \
  "v_mul_f32 v96, s58, v236\n" \
  "v_mul_f32 v97, s58, v237\n" \
  "v_mul_f32 v98, s58, v238\n" \
  "v_mul_f32 v99, s58, v239\n" \
  "v_exp_f32 v96, v96 clamp\n" \
  "v_exp_f32 v97, v97 clamp\n" \
  "v_exp_f32 v98, v98 clamp\n" \
  "v_exp_f32 v99, v99 clamp\n" \
  "v_add_f32 v236, s59, v236\n" \
  "v_add_f32 v237, s59, v237\n" \
  "v_add_f32 v238, s59, v238\n" \
  "v_add_f32 v239, s59, v239\n" \
  "v_max_f32 v236, s59, v236\n" \
  "v_max_f32 v237, s59, v237\n" \
  "v_max_f32 v238, s59, v238\n" \
  "v_max_f32 v239, s59, v239\n" \
  "v_add_f32 v236, v236, v96\n" \
  "v_add_f32 v237, v237, v97\n" \
  "v_add_f32 v238, v238, v98\n" \
  "v_add_f32 v239, v239, v99\n" \
  "v_mul_f32 v96, s58, v240\n" \
  "v_mul_f32 v97, s58, v241\n" \
  "v_mul_f32 v98, s58, v242\n" \
  "v_mul_f32 v99, s58, v243\n" \
  "v_exp_f32 v96, v96 clamp\n" \
  "v_exp_f32 v97, v97 clamp\n" \
  "v_exp_f32 v98, v98 clamp\n" \
  "v_exp_f32 v99, v99 clamp\n" \
  "v_add_f32 v240, s59, v240\n" \
  "v_add_f32 v241, s59, v241\n" \
  "v_add_f32 v242, s59, v242\n" \
  "v_add_f32 v243, s59, v243\n" \
  "v_max_f32 v240, s59, v240\n" \
  "v_max_f32 v241, s59, v241\n" \
  "v_max_f32 v242, s59, v242\n" \
  "v_max_f32 v243, s59, v243\n" \
  "v_add_f32 v240, v240, v96\n" \
  "v_add_f32 v241, v241, v97\n" \
  "v_add_f32 v242, v242, v98\n" \
  "v_add_f32 v243, v243, v99\n" \
  "v_mul_f32 v96, s58, v244\n" \
  "v_mul_f32 v97, s58, v245\n" \
  "v_mul_f32 v98, s58, v246\n" \
  "v_mul_f32 v99, s58, v247\n" \
  "v_exp_f32 v96, v96 clamp\n" \
  "v_exp_f32 v97, v97 clamp\n" \
  "v_exp_f32 v98, v98 clamp\n" \
  "v_exp_f32 v99, v99 clamp\n" \
  "v_add_f32 v244, s59, v244\n" \
  "v_add_f32 v245, s59, v245\n" \
  "v_add_f32 v246, s59, v246\n" \
  "v_add_f32 v247, s59, v247\n" \
  "v_max_f32 v244, s59, v244\n" \
  "v_max_f32 v245, s59, v245\n" \
  "v_max_f32 v246, s59, v246\n" \
  "v_max_f32 v247, s59, v247\n" \
  "v_add_f32 v244, v244, v96\n" \
  "v_add_f32 v245, v245, v97\n" \
  "v_add_f32 v246, v246, v98\n" \
  "v_add_f32 v247, v247, v99\n" \
  "v_mul_f32 v96, s58, v248\n" \
  "v_mul_f32 v97, s58, v249\n" \
  "v_mul_f32 v98, s58, v250\n" \
  "v_mul_f32 v99, s58, v251\n" \
  "v_exp_f32 v96, v96 clamp\n" \
  "v_exp_f32 v97, v97 clamp\n" \
  "v_exp_f32 v98, v98 clamp\n" \
  "v_exp_f32 v99, v99 clamp\n" \
  "v_add_f32 v248, s59, v248\n" \
  "v_add_f32 v249, s59, v249\n" \
  "v_add_f32 v250, s59, v250\n" \
  "v_add_f32 v251, s59, v251\n" \
  "v_max_f32 v248, s59, v248\n" \
  "v_max_f32 v249, s59, v249\n" \
  "v_max_f32 v250, s59, v250\n" \
  "v_max_f32 v251, s59, v251\n" \
  "v_add_f32 v248, v248, v96\n" \
  "v_add_f32 v249, v249, v97\n" \
  "v_add_f32 v250, v250, v98\n" \
  "v_add_f32 v251, v251, v99\n" \
  "v_mul_f32 v96, s58, v252\n" \
  "v_mul_f32 v97, s58, v253\n" \
  "v_mul_f32 v98, s58, v254\n" \
  "v_mul_f32 v99, s58, v255\n" \
  "v_exp_f32 v96, v96 clamp\n" \
  "v_exp_f32 v97, v97 clamp\n" \
  "v_exp_f32 v98, v98 clamp\n" \
  "v_exp_f32 v99, v99 clamp\n" \
  "v_add_f32 v252, s59, v252\n" \
  "v_add_f32 v253, s59, v253\n" \
  "v_add_f32 v254, s59, v254\n" \
  "v_add_f32 v255, s59, v255\n" \
  "v_max_f32 v252, s59, v252\n" \
  "v_max_f32 v253, s59, v253\n" \
  "v_max_f32 v254, s59, v254\n" \
  "v_max_f32 v255, s59, v255\n" \
  "v_add_f32 v252, v252, v96\n" \
  "v_add_f32 v253, v253, v97\n" \
  "v_add_f32 v254, v254, v98\n" \
  "v_add_f32 v255, v255, v99\n" \
  "v_cvt_pk_bf16_f32 v104, v128, v129\n" \
  "v_cvt_pk_bf16_f32 v105, v130, v131\n" \
  "v_cvt_pk_bf16_f32 v106, v160, v161\n" \
  "v_cvt_pk_bf16_f32 v107, v162, v163\n" \
  "v_accvgpr_write_b32 a64, v104\n" \
  "v_accvgpr_write_b32 a65, v105\n" \
  "v_accvgpr_write_b32 a66, v106\n" \
  "v_accvgpr_write_b32 a67, v107\n" \
  "v_cvt_pk_bf16_f32 v104, v132, v133\n" \
  "v_cvt_pk_bf16_f32 v105, v134, v135\n" \
  "v_cvt_pk_bf16_f32 v106, v164, v165\n" \
  "v_cvt_pk_bf16_f32 v107, v166, v167\n" \
  "v_accvgpr_write_b32 a68, v104\n" \
  "v_accvgpr_write_b32 a69, v105\n" \
  "v_accvgpr_write_b32 a70, v106\n" \
  "v_accvgpr_write_b32 a71, v107\n" \
  "v_cvt_pk_bf16_f32 v104, v136, v137\n" \
  "v_cvt_pk_bf16_f32 v105, v138, v139\n" \
  "v_cvt_pk_bf16_f32 v106, v168, v169\n" \
  "v_cvt_pk_bf16_f32 v107, v170, v171\n" \
  "v_accvgpr_write_b32 a72, v104\n" \
  "v_accvgpr_write_b32 a73, v105\n" \
  "v_accvgpr_write_b32 a74, v106\n" \
  "v_accvgpr_write_b32 a75, v107\n" \
  "v_cvt_pk_bf16_f32 v104, v140, v141\n" \
  "v_cvt_pk_bf16_f32 v105, v142, v143\n" \
  "v_cvt_pk_bf16_f32 v106, v172, v173\n" \
  "v_cvt_pk_bf16_f32 v107, v174, v175\n" \
  "v_accvgpr_write_b32 a76, v104\n" \
  "v_accvgpr_write_b32 a77, v105\n" \
  "v_accvgpr_write_b32 a78, v106\n" \
  "v_accvgpr_write_b32 a79, v107\n" \
  "v_cvt_pk_bf16_f32 v104, v144, v145\n" \
  "v_cvt_pk_bf16_f32 v105, v146, v147\n" \
  "v_cvt_pk_bf16_f32 v106, v176, v177\n" \
  "v_cvt_pk_bf16_f32 v107, v178, v179\n" \
  "v_accvgpr_write_b32 a80, v104\n" \
  "v_accvgpr_write_b32 a81, v105\n" \
  "v_accvgpr_write_b32 a82, v106\n" \
  "v_accvgpr_write_b32 a83, v107\n" \
  "v_cvt_pk_bf16_f32 v104, v148, v149\n" \
  "v_cvt_pk_bf16_f32 v105, v150, v151\n" \
  "v_cvt_pk_bf16_f32 v106, v180, v181\n" \
  "v_cvt_pk_bf16_f32 v107, v182, v183\n" \
  "v_accvgpr_write_b32 a84, v104\n" \
  "v_accvgpr_write_b32 a85, v105\n" \
  "v_accvgpr_write_b32 a86, v106\n" \
  "v_accvgpr_write_b32 a87, v107\n" \
  "v_cvt_pk_bf16_f32 v104, v152, v153\n" \
  "v_cvt_pk_bf16_f32 v105, v154, v155\n" \
  "v_cvt_pk_bf16_f32 v106, v184, v185\n" \
  "v_cvt_pk_bf16_f32 v107, v186, v187\n" \
  "v_accvgpr_write_b32 a88, v104\n" \
  "v_accvgpr_write_b32 a89, v105\n" \
  "v_accvgpr_write_b32 a90, v106\n" \
  "v_accvgpr_write_b32 a91, v107\n" \
  "v_cvt_pk_bf16_f32 v104, v156, v157\n" \
  "v_cvt_pk_bf16_f32 v105, v158, v159\n" \
  "v_cvt_pk_bf16_f32 v106, v188, v189\n" \
  "v_cvt_pk_bf16_f32 v107, v190, v191\n" \
  "v_accvgpr_write_b32 a92, v104\n" \
  "v_accvgpr_write_b32 a93, v105\n" \
  "v_accvgpr_write_b32 a94, v106\n" \
  "v_accvgpr_write_b32 a95, v107\n" \
  "v_cvt_pk_bf16_f32 v104, v192, v193\n" \
  "v_cvt_pk_bf16_f32 v105, v194, v195\n" \
  "v_cvt_pk_bf16_f32 v106, v224, v225\n" \
  "v_cvt_pk_bf16_f32 v107, v226, v227\n" \
  "v_accvgpr_write_b32 a96, v104\n" \
  "v_accvgpr_write_b32 a97, v105\n" \
  "v_accvgpr_write_b32 a98, v106\n" \
  "v_accvgpr_write_b32 a99, v107\n" \
  "v_cvt_pk_bf16_f32 v104, v196, v197\n" \
  "v_cvt_pk_bf16_f32 v105, v198, v199\n" \
  "v_cvt_pk_bf16_f32 v106, v228, v229\n" \
  "v_cvt_pk_bf16_f32 v107, v230, v231\n" \
  "v_accvgpr_write_b32 a100, v104\n" \
  "v_accvgpr_write_b32 a101, v105\n" \
  "v_accvgpr_write_b32 a102, v106\n" \
  "v_accvgpr_write_b32 a103, v107\n" \
  "v_cvt_pk_bf16_f32 v104, v200, v201\n" \
  "v_cvt_pk_bf16_f32 v105, v202, v203\n" \
  "v_cvt_pk_bf16_f32 v106, v232, v233\n" \
  "v_cvt_pk_bf16_f32 v107, v234, v235\n" \
  "v_accvgpr_write_b32 a104, v104\n" \
  "v_accvgpr_write_b32 a105, v105\n" \
  "v_accvgpr_write_b32 a106, v106\n" \
  "v_accvgpr_write_b32 a107, v107\n" \
  "v_cvt_pk_bf16_f32 v104, v204, v205\n" \
  "v_cvt_pk_bf16_f32 v105, v206, v207\n" \
  "v_cvt_pk_bf16_f32 v106, v236, v237\n" \
  "v_cvt_pk_bf16_f32 v107, v238, v239\n" \
  "v_accvgpr_write_b32 a108, v104\n" \
  "v_accvgpr_write_b32 a109, v105\n" \
  "v_accvgpr_write_b32 a110, v106\n" \
  "v_accvgpr_write_b32 a111, v107\n" \
  "v_cvt_pk_bf16_f32 v104, v208, v209\n" \
  "v_cvt_pk_bf16_f32 v105, v210, v211\n" \
  "v_cvt_pk_bf16_f32 v106, v240, v241\n" \
  "v_cvt_pk_bf16_f32 v107, v242, v243\n" \
  "v_accvgpr_write_b32 a112, v104\n" \
  "v_accvgpr_write_b32 a113, v105\n" \
  "v_accvgpr_write_b32 a114, v106\n" \
  "v_accvgpr_write_b32 a115, v107\n" \
  "v_cvt_pk_bf16_f32 v104, v212, v213\n" \
  "v_cvt_pk_bf16_f32 v105, v214, v215\n" \
  "v_cvt_pk_bf16_f32 v106, v244, v245\n" \
  "v_cvt_pk_bf16_f32 v107, v246, v247\n" \
  "v_accvgpr_write_b32 a116, v104\n" \
  "v_accvgpr_write_b32 a117, v105\n" \
  "v_accvgpr_write_b32 a118, v106\n" \
  "v_accvgpr_write_b32 a119, v107\n" \
  "v_cvt_pk_bf16_f32 v104, v216, v217\n" \
  "v_cvt_pk_bf16_f32 v105, v218, v219\n" \
  "v_cvt_pk_bf16_f32 v106, v248, v249\n" \
  "v_cvt_pk_bf16_f32 v107, v250, v251\n" \
  "v_accvgpr_write_b32 a120, v104\n" \
  "v_accvgpr_write_b32 a121, v105\n" \
  "v_accvgpr_write_b32 a122, v106\n" \
  "v_accvgpr_write_b32 a123, v107\n" \
  "v_cvt_pk_bf16_f32 v104, v220, v221\n" \
  "v_cvt_pk_bf16_f32 v105, v222, v223\n" \
  "v_cvt_pk_bf16_f32 v106, v252, v253\n" \
  "v_cvt_pk_bf16_f32 v107, v254, v255\n" \
  "v_accvgpr_write_b32 a124, v104\n" \
  "v_accvgpr_write_b32 a125, v105\n" \
  "v_accvgpr_write_b32 a126, v106\n" \
  "v_accvgpr_write_b32 a127, v107\n" \
  "s_add_u32 s57, s57, 1\n" \
  "s_cmp_lt_u32 s57, 2\n" \
  "s_cbranch_scc1 .LL_q%=\n" \
  "s_mov_b32 s44, 262144\n" \
  "s_mov_b32 exec_hi, 0\n" \
  "s_mov_b32 exec_lo, 0xffff\n" \
  "s_branch .LL_w1_in%=\n" \
  ".LL_w1%=:\n" \
  "s_sleep 2\n" \
  "s_sub_u32 s44, s44, 1\n" \
  "s_cbranch_scc1 .LL_abort%=\n" \
  ".LL_w1_in%=:\n" \
  "buffer_load_dword v96, %[pL1], %[rs], 0 offen sc1\n" \
  "s_waitcnt vmcnt(0)\n" \
  "v_cmp_gt_i32 vcc, 1, v96\n" \
  "s_cbranch_vccnz .LL_w1%=\n" \
  "s_mov_b64 exec, -1\n" \
  "s_add_u32 s46, %[sKVR], 0\n" \
  "buffer_load_dwordx4 v[128:131], %[l16], %[rs], s46 offen sc1\n" \
  "buffer_load_dwordx4 v[132:135], %[l16], %[rs], s46 offen offset:2048 sc1\n" \
  "s_add_u32 s46, %[sKVR], 4096\n" \
  "buffer_load_dwordx4 v[136:139], %[l16], %[rs], s46 offen sc1\n" \
  "buffer_load_dwordx4 v[140:143], %[l16], %[rs], s46 offen offset:2048 sc1\n" \
  "s_add_u32 s46, %[sKVR], 8192\n" \
  "buffer_load_dwordx4 v[144:147], %[l16], %[rs], s46 offen sc1\n" \
  "buffer_load_dwordx4 v[148:151], %[l16], %[rs], s46 offen offset:2048 sc1\n" \
  "s_add_u32 s46, %[sKVR], 12288\n" \
  "buffer_load_dwordx4 v[152:155], %[l16], %[rs], s46 offen sc1\n" \
  "buffer_load_dwordx4 v[156:159], %[l16], %[rs], s46 offen offset:2048 sc1\n" \
  "s_add_u32 s46, %[sKVR], 16384\n" \
  "buffer_load_dwordx4 v[160:163], %[l16], %[rs], s46 offen sc1\n" \
  "buffer_load_dwordx4 v[164:167], %[l16], %[rs], s46 offen offset:2048 sc1\n" \
  "s_add_u32 s46, %[sKVR], 20480\n" \
  "buffer_load_dwordx4 v[168:171], %[l16], %[rs], s46 offen sc1\n" \
  "buffer_load_dwordx4 v[172:175], %[l16], %[rs], s46 offen offset:2048 sc1\n" \
  "s_add_u32 s46, %[sKVR], 24576\n" \
  "buffer_load_dwordx4 v[176:179], %[l16], %[rs], s46 offen sc1\n" \
  "buffer_load_dwordx4 v[180:183], %[l16], %[rs], s46 offen offset:2048 sc1\n" \
  "s_add_u32 s46, %[sKVR], 28672\n" \
  "buffer_load_dwordx4 v[184:187], %[l16], %[rs], s46 offen sc1\n" \
  "buffer_load_dwordx4 v[188:191], %[l16], %[rs], s46 offen offset:2048 sc1\n" \
  "s_add_u32 s46, %[sKVR], 32768\n" \
  "buffer_load_dwordx4 v[192:195], %[l16], %[rs], s46 offen sc1\n" \
  "buffer_load_dwordx4 v[196:199], %[l16], %[rs], s46 offen offset:2048 sc1\n" \
  "s_add_u32 s46, %[sKVR], 36864\n" \
  "buffer_load_dwordx4 v[200:203], %[l16], %[rs], s46 offen sc1\n" \
  "buffer_load_dwordx4 v[204:207], %[l16], %[rs], s46 offen offset:2048 sc1\n" \
  "s_add_u32 s46, %[sKVR], 40960\n" \
  "buffer_load_dwordx4 v[208:211], %[l16], %[rs], s46 offen sc1\n" \
  "buffer_load_dwordx4 v[212:215], %[l16], %[rs], s46 offen offset:2048 sc1\n" \
  "s_add_u32 s46, %[sKVR], 45056\n" \
  "buffer_load_dwordx4 v[216:219], %[l16], %[rs], s46 offen sc1\n" \
  "buffer_load_dwordx4 v[220:223], %[l16], %[rs], s46 offen offset:2048 sc1\n" \
  "s_add_u32 s46, %[sKVR], 49152\n" \
  "buffer_load_dwordx4 v[224:227], %[l16], %[rs], s46 offen sc1\n" \
  "buffer_load_dwordx4 v[228:231], %[l16], %[rs], s46 offen offset:2048 sc1\n" \
  "s_add_u32 s46, %[sKVR], 53248\n" \
  "buffer_load_dwordx4 v[232:235], %[l16], %[rs], s46 offen sc1\n" \
  "buffer_load_dwordx4 v[236:239], %[l16], %[rs], s46 offen offset:2048 sc1\n" \
  "s_add_u32 s46, %[sKVR], 57344\n" \
  "buffer_load_dwordx4 v[240:243], %[l16], %[rs], s46 offen sc1\n" \
  "buffer_load_dwordx4 v[244:247], %[l16], %[rs], s46 offen offset:2048 sc1\n" \
  "s_add_u32 s46, %[sKVR], 61440\n" \
  "buffer_load_dwordx4 v[248:251], %[l16], %[rs], s46 offen sc1\n" \
  "buffer_load_dwordx4 v[252:255], %[l16], %[rs], s46 offen offset:2048 sc1\n" \
  "s_waitcnt vmcnt(29)\n" \
  "v_add_f32 v128, v128, v136\n" \
  "v_add_f32 v129, v129, v137\n" \
  "v_add_f32 v130, v130, v138\n" \
  "v_add_f32 v131, v131, v139\n" \
  "s_waitcnt vmcnt(28)\n" \
  "v_add_f32 v132, v132, v140\n" \
  "v_add_f32 v133, v133, v141\n" \
  "v_add_f32 v134, v134, v142\n" \
  "v_add_f32 v135, v135, v143\n" \
  "s_waitcnt vmcnt(27)\n" \
  "v_add_f32 v128, v128, v144\n" \
  "v_add_f32 v129, v129, v145\n" \
  "v_add_f32 v130, v130, v146\n" \
  "v_add_f32 v131, v131, v147\n" \
  "s_waitcnt vmcnt(26)\n" \
  "v_add_f32 v132, v132, v148\n" \
  "v_add_f32 v133, v133, v149\n" \
  "v_add_f32 v134, v134, v150\n" \
  "v_add_f32 v135, v135, v151\n" \
  "s_waitcnt vmcnt(25)\n" \
  "v_add_f32 v128, v128, v152\n" \
  "v_add_f32 v129, v129, v153\n" \
  "v_add_f32 v130, v130, v154\n" \
  "v_add_f32 v131, v131, v155\n" \
  "s_waitcnt vmcnt(24)\n" \
  "v_add_f32 v132, v132, v156\n" \
  "v_add_f32 v133, v133, v157\n" \
  "v_add_f32 v134, v134, v158\n" \
  "v_add_f32 v135, v135, v159\n" \
  "s_waitcnt vmcnt(23)\n" \
  "v_add_f32 v128, v128, v160\n" \
  "v_add_f32 v129, v129, v161\n" \
  "v_add_f32 v130, v130, v162\n" \
  "v_add_f32 v131, v131, v163\n" \
  "s_waitcnt vmcnt(22)\n" \
  "v_add_f32 v132, v132, v164\n" \
  "v_add_f32 v133, v133, v165\n" \
  "v_add_f32 v134, v134, v166\n" \
  "v_add_f32 v135, v135, v167\n" \
  "s_waitcnt vmcnt(21)\n" \
  "v_add_f32 v128, v128, v168\n" \
  "v_add_f32 v129, v129, v169\n" \
  "v_add_f32 v130, v130, v170\n" \
  "v_add_f32 v131, v131, v171\n" \
  "s_waitcnt vmcnt(20)\n" \
  "v_add_f32 v132, v132, v172\n" \
  "v_add_f32 v133, v133, v173\n" \
  "v_add_f32 v134, v134, v174\n" \
  "v_add_f32 v135, v135, v175\n" \
  "s_waitcnt vmcnt(19)\n" \
  "v_add_f32 v128, v128, v176\n" \
  "v_add_f32 v129, v129, v177\n" \
  "v_add_f32 v130, v130, v178\n" \
  "v_add_f32 v131, v131, v179\n" \
  "s_waitcnt vmcnt(18)\n" \
  "v_add_f32 v132, v132, v180\n" \
  "v_add_f32 v133, v133, v181\n" \
  "v_add_f32 v134, v134, v182\n" \
  "v_add_f32 v135, v135, v183\n" \
  "s_waitcnt vmcnt(17)\n" \
  "v_add_f32 v128, v128, v184\n" \
  "v_add_f32 v129, v129, v185\n" \
  "v_add_f32 v130, v130, v186\n" \
  "v_add_f32 v131, v131, v187\n" \
  "s_waitcnt vmcnt(16)\n" \
  "v_add_f32 v132, v132, v188\n" \
  "v_add_f32 v133, v133, v189\n" \
  "v_add_f32 v134, v134, v190\n" \
  "v_add_f32 v135, v135, v191\n" \
  "s_waitcnt vmcnt(15)\n" \
  "v_add_f32 v128, v128, v192\n" \
  "v_add_f32 v129, v129, v193\n" \
  "v_add_f32 v130, v130, v194\n" \
  "v_add_f32 v131, v131, v195\n" \
  "s_waitcnt vmcnt(14)\n" \
  "v_add_f32 v132, v132, v196\n" \
  "v_add_f32 v133, v133, v197\n" \
  "v_add_f32 v134, v134, v198\n" \
  "v_add_f32 v135, v135, v199\n" \
  "s_waitcnt vmcnt(13)\n" \
  "v_add_f32 v128, v128, v200\n" \
  "v_add_f32 v129, v129, v201\n" \
  "v_add_f32 v130, v130, v202\n" \
  "v_add_f32 v131, v131, v203\n" \
  "s_waitcnt vmcnt(12)\n" \
  "v_add_f32 v132, v132, v204\n" \
  "v_add_f32 v133, v133, v205\n" \
  "v_add_f32 v134, v134, v206\n" \
  "v_add_f32 v135, v135, v207\n" \
  "s_waitcnt vmcnt(11)\n" \
  "v_add_f32 v128, v128, v208\n" \
  "v_add_f32 v129, v129, v209\n" \
  "v_add_f32 v130, v130, v210\n" \
  "v_add_f32 v131, v131, v211\n" \
  "s_waitcnt vmcnt(10)\n" \
  "v_add_f32 v132, v132, v212\n" \
  "v_add_f32 v133, v133, v213\n" \
  "v_add_f32 v134, v134, v214\n" \
  "v_add_f32 v135, v135, v215\n" \
  "s_waitcnt vmcnt(9)\n" \
  "v_add_f32 v128, v128, v216\n" \
  "v_add_f32 v129, v129, v217\n" \
  "v_add_f32 v130, v130, v218\n" \
  "v_add_f32 v131, v131, v219\n" \
  "s_waitcnt vmcnt(8)\n" \
  "v_add_f32 v132, v132, v220\n" \
  "v_add_f32 v133, v133, v221\n" \
  "v_add_f32 v134, v134, v222\n" \
  "v_add_f32 v135, v135, v223\n" \
  "s_waitcnt vmcnt(7)\n" \
  "v_add_f32 v128, v128, v224\n" \
  "v_add_f32 v129, v129, v225\n" \
  "v_add_f32 v130, v130, v226\n" \
  "v_add_f32 v131, v131, v227\n" \
  "s_waitcnt vmcnt(6)\n" \
  "v_add_f32 v132, v132, v228\n" \
  "v_add_f32 v133, v133, v229\n" \
  "v_add_f32 v134, v134, v230\n" \
  "v_add_f32 v135, v135, v231\n" \
  "s_waitcnt vmcnt(5)\n" \
  "v_add_f32 v128, v128, v232\n" \
  "v_add_f32 v129, v129, v233\n" \
  "v_add_f32 v130, v130, v234\n" \
  "v_add_f32 v131, v131, v235\n" \
  "s_waitcnt vmcnt(4)\n" \
  "v_add_f32 v132, v132, v236\n" \
  "v_add_f32 v133, v133, v237\n" \
  "v_add_f32 v134, v134, v238\n" \
  "v_add_f32 v135, v135, v239\n" \
  "s_waitcnt vmcnt(3)\n" \
  "v_add_f32 v128, v128, v240\n" \
  "v_add_f32 v129, v129, v241\n" \
  "v_add_f32 v130, v130, v242\n" \
  "v_add_f32 v131, v131, v243\n" \
  "s_waitcnt vmcnt(2)\n" \
  "v_add_f32 v132, v132, v244\n" \
  "v_add_f32 v133, v133, v245\n" \
  "v_add_f32 v134, v134, v246\n" \
  "v_add_f32 v135, v135, v247\n" \
  "s_waitcnt vmcnt(1)\n" \
  "v_add_f32 v128, v128, v248\n" \
  "v_add_f32 v129, v129, v249\n" \
  "v_add_f32 v130, v130, v250\n" \
  "v_add_f32 v131, v131, v251\n" \
  "s_waitcnt vmcnt(0)\n" \
  "v_add_f32 v132, v132, v252\n" \
  "v_add_f32 v133, v133, v253\n" \
  "v_add_f32 v134, v134, v254\n" \
  "v_add_f32 v135, v135, v255\n" \
  "v_cvt_pk_bf16_f32 v96, v128, v129\n" \
  "v_cvt_pk_bf16_f32 v97, v130, v131\n" \
  "v_cvt_pk_bf16_f32 v98, v132, v133\n" \
  "v_cvt_pk_bf16_f32 v99, v134, v135\n" \
  "s_mov_b32 exec_lo, 0\n" \
  "s_mov_b32 exec_hi, 0xffff0000\n" \
  "v_mov_b32 v98, 0\n" \
  "v_mov_b32 v99, 0\n" \
  "s_mov_b64 exec, -1\n" \
  "v_mov_b32_dpp v100, v96 row_newbcast:12 row_mask:0xf bank_mask:0xf\n" \
  "v_mov_b32_dpp v101, v97 row_newbcast:12 row_mask:0xf bank_mask:0xf\n" \
  "v_mov_b32_dpp v102, v98 row_newbcast:12 row_mask:0xf bank_mask:0xf\n" \
  "v_mov_b32_dpp v103, v99 row_newbcast:12 row_mask:0xf bank_mask:0xf\n" \
  "buffer_store_dwordx4 v[96:99], %[oK0], %[rs], 0 offen sc1\n" \
  "buffer_store_dwordx4 v[100:103], %[oKz], %[rs], 0 offen sc1\n" \
  "s_waitcnt vmcnt(0) lgkmcnt(0)\n" \
  "s_barrier\n" \
  "buffer_store_dword v127, %[oL2], %[rs], 0 offen sc1\n" \
  "s_mov_b32 s44, 262144\n" \
  "s_mov_b32 exec_hi, 0\n" \
  "s_mov_b32 exec_lo, 0xf\n" \
  "s_branch .LL_w2_in%=\n" \
  ".LL_w2%=:\n" \
  "s_sleep 2\n" \
  "s_sub_u32 s44, s44, 1\n" \
  "s_cbranch_scc1 .LL_abort%=\n" \
  ".LL_w2_in%=:\n" \
  "buffer_load_dword v96, %[pL2], %[rs], 0 offen sc1\n" \
  "s_waitcnt vmcnt(0)\n" \
  "v_cmp_gt_i32 vcc, 1, v96\n" \
  "s_cbranch_vccnz .LL_w2%=\n" \
  "s_mov_b64 exec, -1\n" \
  "s_add_u32 s46, %[sKVF], 0\n" \
  "buffer_load_dwordx4 v[32:35], %[l16], %[rs], s46 offen sc1\n" \
  "buffer_load_dwordx4 v[36:39], %[l16], %[rs], s46 offen offset:1024 sc1\n" \
  "buffer_load_dwordx4 v[40:43], %[l16], %[rs], s46 offen offset:2048 sc1\n" \
  "s_add_u32 s46, %[sKVF], 3072\n" \
  "buffer_load_dwordx4 v[44:47], %[l16], %[rs], s46 offen sc1\n" \
  "buffer_load_dwordx4 v[48:51], %[l16], %[rs], s46 offen offset:1024 sc1\n" \
  "buffer_load_dwordx4 v[52:55], %[l16], %[rs], s46 offen offset:2048 sc1\n" \
  "s_add_u32 s46, %[sKVF], 6144\n" \
  "buffer_load_dwordx4 v[56:59], %[l16], %[rs], s46 offen sc1\n" \
  "buffer_load_dwordx4 v[60:63], %[l16], %[rs], s46 offen offset:1024 sc1\n" \
  "buffer_load_dwordx4 v[64:67], %[l16], %[rs], s46 offen offset:2048 sc1\n" \
  "s_add_u32 s46, %[sKVF], 9216\n" \
  "buffer_load_dwordx4 v[68:71], %[l16], %[rs], s46 offen sc1\n" \
  "buffer_load_dwordx4 v[72:75], %[l16], %[rs], s46 offen offset:1024 sc1\n" \
  "buffer_load_dwordx4 v[76:79], %[l16], %[rs], s46 offen offset:2048 sc1\n" \
  "s_waitcnt vmcnt(9)\n" \
  "v_mfma_f32_16x16x32_bf16 v[136:139], v[40:43], a[0:3], 0\n" \
  "v_mfma_f32_16x16x32_bf16 v[128:131], v[32:35], a[0:3], 0\n" \
  "v_mfma_f32_16x16x32_bf16 v[132:135], v[36:39], a[0:3], 0\n" \
  "v_mfma_f32_16x16x32_bf16 v[152:155], v[40:43], a[4:7], 0\n" \
  "v_mfma_f32_16x16x32_bf16 v[144:147], v[32:35], a[4:7], 0\n" \
  "v_mfma_f32_16x16x32_bf16 v[148:151], v[36:39], a[4:7], 0\n" \
  "v_mfma_f32_16x16x32_bf16 v[168:171], v[40:43], a[8:11], 0\n" \
  "v_mfma_f32_16x16x32_bf16 v[160:163], v[32:35], a[8:11], 0\n" \
  "v_mfma_f32_16x16x32_bf16 v[164:167], v[36:39], a[8:11], 0\n" \
  "v_add_f32 v136, s59, v136\n" \
  "v_rcp_f32 v136, v136\n" \
  "s_nop 0\n" \
  "v_mul_f32 v128, v128, v136\n" \
  "v_mul_f32 v129, v129, v136\n" \
  "v_mul_f32 v130, v130, v136\n" \
  "v_mul_f32 v131, v131, v136\n" \
  "v_mul_f32 v132, v132, v136\n" \
  "v_mul_f32 v133, v133, v136\n" \
  "v_mul_f32 v134, v134, v136\n" \
  "v_mul_f32 v135, v135, v136\n" \
  "v_cvt_pk_bf16_f32 v140, v128, v129\n" \
  "v_cvt_pk_bf16_f32 v141, v130, v131\n" \
  "v_cvt_pk_bf16_f32 v142, v132, v133\n" \
  "v_cvt_pk_bf16_f32 v143, v134, v135\n" \
  "s_add_u32 s46, %[sAO], 0\n" \
  "buffer_store_dwordx4 v[140:143], %[l16], %[rs], s46 offen sc1\n" \
  "v_mfma_f32_16x16x32_bf16 v[184:187], v[40:43], a[12:15], 0\n" \
  "v_mfma_f32_16x16x32_bf16 v[176:179], v[32:35], a[12:15], 0\n" \
  "v_mfma_f32_16x16x32_bf16 v[180:183], v[36:39], a[12:15], 0\n" \
  "v_add_f32 v152, s59, v152\n" \
  "v_rcp_f32 v152, v152\n" \
  "s_nop 0\n" \
  "v_mul_f32 v144, v144, v152\n" \
  "v_mul_f32 v145, v145, v152\n" \
  "v_mul_f32 v146, v146, v152\n" \
  "v_mul_f32 v147, v147, v152\n" \
  "v_mul_f32 v148, v148, v152\n" \
  "v_mul_f32 v149, v149, v152\n" \
  "v_mul_f32 v150, v150, v152\n" \
  "v_mul_f32 v151, v151, v152\n" \
  "v_cvt_pk_bf16_f32 v156, v144, v145\n" \
  "v_cvt_pk_bf16_f32 v157, v146, v147\n" \
  "v_cvt_pk_bf16_f32 v158, v148, v149\n" \
  "v_cvt_pk_bf16_f32 v159, v150, v151\n" \
  "s_add_u32 s46, %[sAO], 16384\n" \
  "buffer_store_dwordx4 v[156:159], %[l16], %[rs], s46 offen sc1\n" \
  "v_mfma_f32_16x16x32_bf16 v[136:139], v[40:43], a[16:19], 0\n" \
  "v_mfma_f32_16x16x32_bf16 v[128:131], v[32:35], a[16:19], 0\n" \
  "v_mfma_f32_16x16x32_bf16 v[132:135], v[36:39], a[16:19], 0\n" \
  "v_add_f32 v168, s59, v168\n" \
  "v_rcp_f32 v168, v168\n" \
  "s_nop 0\n" \
  "v_mul_f32 v160, v160, v168\n" \
  "v_mul_f32 v161, v161, v168\n" \
  "v_mul_f32 v162, v162, v168\n" \
  "v_mul_f32 v163, v163, v168\n" \
  "v_mul_f32 v164, v164, v168\n" \
  "v_mul_f32 v165, v165, v168\n" \
  "v_mul_f32 v166, v166, v168\n" \
  "v_mul_f32 v167, v167, v168\n" \
  "v_cvt_pk_bf16_f32 v172, v160, v161\n" \
  "v_cvt_pk_bf16_f32 v173, v162, v163\n" \
  "v_cvt_pk_bf16_f32 v174, v164, v165\n" \
  "v_cvt_pk_bf16_f32 v175, v166, v167\n" \
  "s_add_u32 s46, %[sAO], 32768\n" \
  "buffer_store_dwordx4 v[172:175], %[l16], %[rs], s46 offen sc1\n" \
  "v_mfma_f32_16x16x32_bf16 v[152:155], v[40:43], a[20:23], 0\n" \
  "v_mfma_f32_16x16x32_bf16 v[144:147], v[32:35], a[20:23], 0\n" \
  "v_mfma_f32_16x16x32_bf16 v[148:151], v[36:39], a[20:23], 0\n" \
  "v_add_f32 v184, s59, v184\n" \
  "v_rcp_f32 v184, v184\n" \
  "s_nop 0\n" \
  "v_mul_f32 v176, v176, v184\n" \
  "v_mul_f32 v177, v177, v184\n" \
  "v_mul_f32 v178, v178, v184\n" \
  "v_mul_f32 v179, v179, v184\n" \
  "v_mul_f32 v180, v180, v184\n" \
  "v_mul_f32 v181, v181, v184\n" \
  "v_mul_f32 v182, v182, v184\n" \
  "v_mul_f32 v183, v183, v184\n" \
  "v_cvt_pk_bf16_f32 v188, v176, v177\n" \
  "v_cvt_pk_bf16_f32 v189, v178, v179\n" \
  "v_cvt_pk_bf16_f32 v190, v180, v181\n" \
  "v_cvt_pk_bf16_f32 v191, v182, v183\n" \
  "s_add_u32 s46, %[sAO], 49152\n" \
  "buffer_store_dwordx4 v[188:191], %[l16], %[rs], s46 offen sc1\n" \
  "v_mfma_f32_16x16x32_bf16 v[168:171], v[40:43], a[24:27], 0\n" \
  "v_mfma_f32_16x16x32_bf16 v[160:163], v[32:35], a[24:27], 0\n" \
  "v_mfma_f32_16x16x32_bf16 v[164:167], v[36:39], a[24:27], 0\n" \
  "v_add_f32 v136, s59, v136\n" \
  "v_rcp_f32 v136, v136\n" \
  "s_nop 0\n" \
  "v_mul_f32 v128, v128, v136\n" \
  "v_mul_f32 v129, v129, v136\n" \
  "v_mul_f32 v130, v130, v136\n" \
  "v_mul_f32 v131, v131, v136\n" \
  "v_mul_f32 v132, v132, v136\n" \
  "v_mul_f32 v133, v133, v136\n" \
  "v_mul_f32 v134, v134, v136\n" \
  "v_mul_f32 v135, v135, v136\n" \
  "v_cvt_pk_bf16_f32 v140, v128, v129\n" \
  "v_cvt_pk_bf16_f32 v141, v130, v131\n" \
  "v_cvt_pk_bf16_f32 v142, v132, v133\n" \
  "v_cvt_pk_bf16_f32 v143, v134, v135\n" \
  "s_add_u32 s46, %[sAO], 65536\n" \
  "buffer_store_dwordx4 v[140:143], %[l16], %[rs], s46 offen sc1\n" \
  "v_mfma_f32_16x16x32_bf16 v[184:187], v[40:43], a[28:31], 0\n" \
  "v_mfma_f32_16x16x32_bf16 v[176:179], v[32:35], a[28:31], 0\n" \
  "v_mfma_f32_16x16x32_bf16 v[180:183], v[36:39], a[28:31], 0\n" \
  "v_add_f32 v152, s59, v152\n" \
  "v_rcp_f32 v152, v152\n" \
  "s_nop 0\n" \
  "v_mul_f32 v144, v144, v152\n" \
  "v_mul_f32 v145, v145, v152\n" \
  "v_mul_f32 v146, v146, v152\n" \
  "v_mul_f32 v147, v147, v152\n" \
  "v_mul_f32 v148, v148, v152\n" \
  "v_mul_f32 v149, v149, v152\n" \
  "v_mul_f32 v150, v150, v152\n" \
  "v_mul_f32 v151, v151, v152\n" \
  "v_cvt_pk_bf16_f32 v156, v144, v145\n" \
  "v_cvt_pk_bf16_f32 v157, v146, v147\n" \
  "v_cvt_pk_bf16_f32 v158, v148, v149\n" \
  "v_cvt_pk_bf16_f32 v159, v150, v151\n" \
  "s_add_u32 s46, %[sAO], 81920\n" \
  "buffer_store_dwordx4 v[156:159], %[l16], %[rs], s46 offen sc1\n" \
  "s_waitcnt vmcnt(12)\n" \
  "v_mfma_f32_16x16x32_bf16 v[136:139], v[52:55], a[32:35], 0\n" \
  "v_mfma_f32_16x16x32_bf16 v[128:131], v[44:47], a[32:35], 0\n" \
  "v_mfma_f32_16x16x32_bf16 v[132:135], v[48:51], a[32:35], 0\n" \
  "v_add_f32 v168, s59, v168\n" \
  "v_rcp_f32 v168, v168\n" \
  "s_nop 0\n" \
  "v_mul_f32 v160, v160, v168\n" \
  "v_mul_f32 v161, v161, v168\n" \
  "v_mul_f32 v162, v162, v168\n" \
  "v_mul_f32 v163, v163, v168\n" \
  "v_mul_f32 v164, v164, v168\n" \
  "v_mul_f32 v165, v165, v168\n" \
  "v_mul_f32 v166, v166, v168\n" \
  "v_mul_f32 v167, v167, v168\n" \
  "v_cvt_pk_bf16_f32 v172, v160, v161\n" \
  "v_cvt_pk_bf16_f32 v173, v162, v163\n" \
  "v_cvt_pk_bf16_f32 v174, v164, v165\n" \
  "v_cvt_pk_bf16_f32 v175, v166, v167\n" \
  "s_add_u32 s46, %[sAO], 98304\n" \
  "buffer_store_dwordx4 v[172:175], %[l16], %[rs], s46 offen sc1\n" \
  "v_mfma_f32_16x16x32_bf16 v[152:155], v[52:55], a[36:39], 0\n" \
  "v_mfma_f32_16x16x32_bf16 v[144:147], v[44:47], a[36:39], 0\n" \
  "v_mfma_f32_16x16x32_bf16 v[148:151], v[48:51], a[36:39], 0\n" \
  "v_add_f32 v184, s59, v184\n" \
  "v_rcp_f32 v184, v184\n" \
  "s_nop 0\n" \
  "v_mul_f32 v176, v176, v184\n" \
  "v_mul_f32 v177, v177, v184\n" \
  "v_mul_f32 v178, v178, v184\n" \
  "v_mul_f32 v179, v179, v184\n" \
  "v_mul_f32 v180, v180, v184\n" \
  "v_mul_f32 v181, v181, v184\n" \
  "v_mul_f32 v182, v182, v184\n" \
  "v_mul_f32 v183, v183, v184\n" \
  "v_cvt_pk_bf16_f32 v188, v176, v177\n" \
  "v_cvt_pk_bf16_f32 v189, v178, v179\n" \
  "v_cvt_pk_bf16_f32 v190, v180, v181\n" \
  "v_cvt_pk_bf16_f32 v191, v182, v183\n" \
  "s_add_u32 s46, %[sAO], 114688\n" \
  "buffer_store_dwordx4 v[188:191], %[l16], %[rs], s46 offen sc1\n" \
  "v_mfma_f32_16x16x32_bf16 v[168:171], v[52:55], a[40:43], 0\n" \
  "v_mfma_f32_16x16x32_bf16 v[160:163], v[44:47], a[40:43], 0\n" \
  "v_mfma_f32_16x16x32_bf16 v[164:167], v[48:51], a[40:43], 0\n" \
  "v_add_f32 v136, s59, v136\n" \
  "v_rcp_f32 v136, v136\n" \
  "s_nop 0\n" \
  "v_mul_f32 v128, v128, v136\n" \
  "v_mul_f32 v129, v129, v136\n" \
  "v_mul_f32 v130, v130, v136\n" \
  "v_mul_f32 v131, v131, v136\n" \
  "v_mul_f32 v132, v132, v136\n" \
  "v_mul_f32 v133, v133, v136\n" \
  "v_mul_f32 v134, v134, v136\n" \
  "v_mul_f32 v135, v135, v136\n" \
  "v_cvt_pk_bf16_f32 v140, v128, v129\n" \
  "v_cvt_pk_bf16_f32 v141, v130, v131\n" \
  "v_cvt_pk_bf16_f32 v142, v132, v133\n" \
  "v_cvt_pk_bf16_f32 v143, v134, v135\n" \
  "s_add_u32 s46, %[sAO], 0\n" \
  "buffer_store_dwordx4 v[140:143], %[l16], %[rs], s46 offen offset:1024 sc1\n" \
  "v_mfma_f32_16x16x32_bf16 v[184:187], v[52:55], a[44:47], 0\n" \
  "v_mfma_f32_16x16x32_bf16 v[176:179], v[44:47], a[44:47], 0\n" \
  "v_mfma_f32_16x16x32_bf16 v[180:183], v[48:51], a[44:47], 0\n" \
  "v_add_f32 v152, s59, v152\n" \
  "v_rcp_f32 v152, v152\n" \
  "s_nop 0\n" \
  "v_mul_f32 v144, v144, v152\n" \
  "v_mul_f32 v145, v145, v152\n" \
  "v_mul_f32 v146, v146, v152\n" \
  "v_mul_f32 v147, v147, v152\n" \
  "v_mul_f32 v148, v148, v152\n" \
  "v_mul_f32 v149, v149, v152\n" \
  "v_mul_f32 v150, v150, v152\n" \
  "v_mul_f32 v151, v151, v152\n" \
  "v_cvt_pk_bf16_f32 v156, v144, v145\n" \
  "v_cvt_pk_bf16_f32 v157, v146, v147\n" \
  "v_cvt_pk_bf16_f32 v158, v148, v149\n" \
  "v_cvt_pk_bf16_f32 v159, v150, v151\n" \
  "s_add_u32 s46, %[sAO], 16384\n" \
  "buffer_store_dwordx4 v[156:159], %[l16], %[rs], s46 offen offset:1024 sc1\n" \
  "v_mfma_f32_16x16x32_bf16 v[136:139], v[52:55], a[48:51], 0\n" \
  "v_mfma_f32_16x16x32_bf16 v[128:131], v[44:47], a[48:51], 0\n" \
  "v_mfma_f32_16x16x32_bf16 v[132:135], v[48:51], a[48:51], 0\n" \
  "v_add_f32 v168, s59, v168\n" \
  "v_rcp_f32 v168, v168\n" \
  "s_nop 0\n" \
  "v_mul_f32 v160, v160, v168\n" \
  "v_mul_f32 v161, v161, v168\n" \
  "v_mul_f32 v162, v162, v168\n" \
  "v_mul_f32 v163, v163, v168\n" \
  "v_mul_f32 v164, v164, v168\n" \
  "v_mul_f32 v165, v165, v168\n" \
  "v_mul_f32 v166, v166, v168\n" \
  "v_mul_f32 v167, v167, v168\n" \
  "v_cvt_pk_bf16_f32 v172, v160, v161\n" \
  "v_cvt_pk_bf16_f32 v173, v162, v163\n" \
  "v_cvt_pk_bf16_f32 v174, v164, v165\n" \
  "v_cvt_pk_bf16_f32 v175, v166, v167\n" \
  "s_add_u32 s46, %[sAO], 32768\n" \
  "buffer_store_dwordx4 v[172:175], %[l16], %[rs], s46 offen offset:1024 sc1\n" \
  "v_mfma_f32_16x16x32_bf16 v[152:155], v[52:55], a[52:55], 0\n" \
  "v_mfma_f32_16x16x32_bf16 v[144:147], v[44:47], a[52:55], 0\n" \
  "v_mfma_f32_16x16x32_bf16 v[148:151], v[48:51], a[52:55], 0\n" \
  "v_add_f32 v184, s59, v184\n" \
  "v_rcp_f32 v184, v184\n" \
  "s_nop 0\n" \
  "v_mul_f32 v176, v176, v184\n" \
  "v_mul_f32 v177, v177, v184\n" \
  "v_mul_f32 v178, v178, v184\n" \
  "v_mul_f32 v179, v179, v184\n" \
  "v_mul_f32 v180, v180, v184\n" \
  "v_mul_f32 v181, v181, v184\n" \
  "v_mul_f32 v182, v182, v184\n" \
  "v_mul_f32 v183, v183, v184\n" \
  "v_cvt_pk_bf16_f32 v188, v176, v177\n" \
  "v_cvt_pk_bf16_f32 v189, v178, v179\n" \
  "v_cvt_pk_bf16_f32 v190, v180, v181\n" \
  "v_cvt_pk_bf16_f32 v191, v182, v183\n" \
  "s_add_u32 s46, %[sAO], 49152\n" \
  "buffer_store_dwordx4 v[188:191], %[l16], %[rs], s46 offen offset:1024 sc1\n" \
  "v_mfma_f32_16x16x32_bf16 v[168:171], v[52:55], a[56:59], 0\n" \
  "v_mfma_f32_16x16x32_bf16 v[160:163], v[44:47], a[56:59], 0\n" \
  "v_mfma_f32_16x16x32_bf16 v[164:167], v[48:51], a[56:59], 0\n" \
  "v_add_f32 v136, s59, v136\n" \
  "v_rcp_f32 v136, v136\n" \
  "s_nop 0\n" \
  "v_mul_f32 v128, v128, v136\n" \
  "v_mul_f32 v129, v129, v136\n" \
  "v_mul_f32 v130, v130, v136\n" \
  "v_mul_f32 v131, v131, v136\n" \
  "v_mul_f32 v132, v132, v136\n" \
  "v_mul_f32 v133, v133, v136\n" \
  "v_mul_f32 v134, v134, v136\n" \
  "v_mul_f32 v135, v135, v136\n" \
  "v_cvt_pk_bf16_f32 v140, v128, v129\n" \
  "v_cvt_pk_bf16_f32 v141, v130, v131\n" \
  "v_cvt_pk_bf16_f32 v142, v132, v133\n" \
  "v_cvt_pk_bf16_f32 v143, v134, v135\n" \
  "s_add_u32 s46, %[sAO], 65536\n" \
  "buffer_store_dwordx4 v[140:143], %[l16], %[rs], s46 offen offset:1024 sc1\n" \
  "v_mfma_f32_16x16x32_bf16 v[184:187], v[52:55], a[60:63], 0\n" \
  "v_mfma_f32_16x16x32_bf16 v[176:179], v[44:47], a[60:63], 0\n" \
  "v_mfma_f32_16x16x32_bf16 v[180:183], v[48:51], a[60:63], 0\n" \
  "v_add_f32 v152, s59, v152\n" \
  "v_rcp_f32 v152, v152\n" \
  "s_nop 0\n" \
  "v_mul_f32 v144, v144, v152\n" \
  "v_mul_f32 v145, v145, v152\n" \
  "v_mul_f32 v146, v146, v152\n" \
  "v_mul_f32 v147, v147, v152\n" \
  "v_mul_f32 v148, v148, v152\n" \
  "v_mul_f32 v149, v149, v152\n" \
  "v_mul_f32 v150, v150, v152\n" \
  "v_mul_f32 v151, v151, v152\n" \
  "v_cvt_pk_bf16_f32 v156, v144, v145\n" \
  "v_cvt_pk_bf16_f32 v157, v146, v147\n" \
  "v_cvt_pk_bf16_f32 v158, v148, v149\n" \
  "v_cvt_pk_bf16_f32 v159, v150, v151\n" \
  "s_add_u32 s46, %[sAO], 81920\n" \
  "buffer_store_dwordx4 v[156:159], %[l16], %[rs], s46 offen offset:1024 sc1\n" \
  "s_waitcnt vmcnt(17)\n" \
  "v_mfma_f32_16x16x32_bf16 v[136:139], v[64:67], a[64:67], 0\n" \
  "v_mfma_f32_16x16x32_bf16 v[128:131], v[56:59], a[64:67], 0\n" \
  "v_mfma_f32_16x16x32_bf16 v[132:135], v[60:63], a[64:67], 0\n" \
  "v_add_f32 v168, s59, v168\n" \
  "v_rcp_f32 v168, v168\n" \
  "s_nop 0\n" \
  "v_mul_f32 v160, v160, v168\n" \
  "v_mul_f32 v161, v161, v168\n" \
  "v_mul_f32 v162, v162, v168\n" \
  "v_mul_f32 v163, v163, v168\n" \
  "v_mul_f32 v164, v164, v168\n" \
  "v_mul_f32 v165, v165, v168\n" \
  "v_mul_f32 v166, v166, v168\n" \
  "v_mul_f32 v167, v167, v168\n" \
  "v_cvt_pk_bf16_f32 v172, v160, v161\n" \
  "v_cvt_pk_bf16_f32 v173, v162, v163\n" \
  "v_cvt_pk_bf16_f32 v174, v164, v165\n" \
  "v_cvt_pk_bf16_f32 v175, v166, v167\n" \
  "s_add_u32 s46, %[sAO], 98304\n" \
  "buffer_store_dwordx4 v[172:175], %[l16], %[rs], s46 offen offset:1024 sc1\n" \
  "v_mfma_f32_16x16x32_bf16 v[152:155], v[64:67], a[68:71], 0\n" \
  "v_mfma_f32_16x16x32_bf16 v[144:147], v[56:59], a[68:71], 0\n" \
  "v_mfma_f32_16x16x32_bf16 v[148:151], v[60:63], a[68:71], 0\n" \
  "v_add_f32 v184, s59, v184\n" \
  "v_rcp_f32 v184, v184\n" \
  "s_nop 0\n" \
  "v_mul_f32 v176, v176, v184\n" \
  "v_mul_f32 v177, v177, v184\n" \
  "v_mul_f32 v178, v178, v184\n" \
  "v_mul_f32 v179, v179, v184\n" \
  "v_mul_f32 v180, v180, v184\n" \
  "v_mul_f32 v181, v181, v184\n" \
  "v_mul_f32 v182, v182, v184\n" \
  "v_mul_f32 v183, v183, v184\n" \
  "v_cvt_pk_bf16_f32 v188, v176, v177\n" \
  "v_cvt_pk_bf16_f32 v189, v178, v179\n" \
  "v_cvt_pk_bf16_f32 v190, v180, v181\n" \
  "v_cvt_pk_bf16_f32 v191, v182, v183\n" \
  "s_add_u32 s46, %[sAO], 114688\n" \
  "buffer_store_dwordx4 v[188:191], %[l16], %[rs], s46 offen offset:1024 sc1\n" \
  "v_mfma_f32_16x16x32_bf16 v[168:171], v[64:67], a[72:75], 0\n" \
  "v_mfma_f32_16x16x32_bf16 v[160:163], v[56:59], a[72:75], 0\n" \
  "v_mfma_f32_16x16x32_bf16 v[164:167], v[60:63], a[72:75], 0\n" \
  "v_add_f32 v136, s59, v136\n" \
  "v_rcp_f32 v136, v136\n" \
  "s_nop 0\n" \
  "v_mul_f32 v128, v128, v136\n" \
  "v_mul_f32 v129, v129, v136\n" \
  "v_mul_f32 v130, v130, v136\n" \
  "v_mul_f32 v131, v131, v136\n" \
  "v_mul_f32 v132, v132, v136\n" \
  "v_mul_f32 v133, v133, v136\n" \
  "v_mul_f32 v134, v134, v136\n" \
  "v_mul_f32 v135, v135, v136\n" \
  "v_cvt_pk_bf16_f32 v140, v128, v129\n" \
  "v_cvt_pk_bf16_f32 v141, v130, v131\n" \
  "v_cvt_pk_bf16_f32 v142, v132, v133\n" \
  "v_cvt_pk_bf16_f32 v143, v134, v135\n" \
  "s_add_u32 s46, %[sAO], 0\n" \
  "buffer_store_dwordx4 v[140:143], %[l16], %[rs], s46 offen offset:2048 sc1\n" \
  "v_mfma_f32_16x16x32_bf16 v[184:187], v[64:67], a[76:79], 0\n" \
  "v_mfma_f32_16x16x32_bf16 v[176:179], v[56:59], a[76:79], 0\n" \
  "v_mfma_f32_16x16x32_bf16 v[180:183], v[60:63], a[76:79], 0\n" \
  "v_add_f32 v152, s59, v152\n" \
  "v_rcp_f32 v152, v152\n" \
  "s_nop 0\n" \
  "v_mul_f32 v144, v144, v152\n" \
  "v_mul_f32 v145, v145, v152\n" \
  "v_mul_f32 v146, v146, v152\n" \
  "v_mul_f32 v147, v147, v152\n" \
  "v_mul_f32 v148, v148, v152\n" \
  "v_mul_f32 v149, v149, v152\n" \
  "v_mul_f32 v150, v150, v152\n" \
  "v_mul_f32 v151, v151, v152\n" \
  "v_cvt_pk_bf16_f32 v156, v144, v145\n" \
  "v_cvt_pk_bf16_f32 v157, v146, v147\n" \
  "v_cvt_pk_bf16_f32 v158, v148, v149\n" \
  "v_cvt_pk_bf16_f32 v159, v150, v151\n" \
  "s_add_u32 s46, %[sAO], 16384\n" \
  "buffer_store_dwordx4 v[156:159], %[l16], %[rs], s46 offen offset:2048 sc1\n" \
  "v_mfma_f32_16x16x32_bf16 v[136:139], v[64:67], a[80:83], 0\n" \
  "v_mfma_f32_16x16x32_bf16 v[128:131], v[56:59], a[80:83], 0\n" \
  "v_mfma_f32_16x16x32_bf16 v[132:135], v[60:63], a[80:83], 0\n" \
  "v_add_f32 v168, s59, v168\n" \
  "v_rcp_f32 v168, v168\n" \
  "s_nop 0\n" \
  "v_mul_f32 v160, v160, v168\n" \
  "v_mul_f32 v161, v161, v168\n" \
  "v_mul_f32 v162, v162, v168\n" \
  "v_mul_f32 v163, v163, v168\n" \
  "v_mul_f32 v164, v164, v168\n" \
  "v_mul_f32 v165, v165, v168\n" \
  "v_mul_f32 v166, v166, v168\n" \
  "v_mul_f32 v167, v167, v168\n" \
  "v_cvt_pk_bf16_f32 v172, v160, v161\n" \
  "v_cvt_pk_bf16_f32 v173, v162, v163\n" \
  "v_cvt_pk_bf16_f32 v174, v164, v165\n" \
  "v_cvt_pk_bf16_f32 v175, v166, v167\n" \
  "s_add_u32 s46, %[sAO], 32768\n" \
  "buffer_store_dwordx4 v[172:175], %[l16], %[rs], s46 offen offset:2048 sc1\n" \
  "v_mfma_f32_16x16x32_bf16 v[152:155], v[64:67], a[84:87], 0\n" \
  "v_mfma_f32_16x16x32_bf16 v[144:147], v[56:59], a[84:87], 0\n" \
  "v_mfma_f32_16x16x32_bf16 v[148:151], v[60:63], a[84:87], 0\n" \
  "v_add_f32 v184, s59, v184\n" \
  "v_rcp_f32 v184, v184\n" \
  "s_nop 0\n" \
  "v_mul_f32 v176, v176, v184\n" \
  "v_mul_f32 v177, v177, v184\n" \
  "v_mul_f32 v178, v178, v184\n" \
  "v_mul_f32 v179, v179, v184\n" \
  "v_mul_f32 v180, v180, v184\n" \
  "v_mul_f32 v181, v181, v184\n" \
  "v_mul_f32 v182, v182, v184\n" \
  "v_mul_f32 v183, v183, v184\n" \
  "v_cvt_pk_bf16_f32 v188, v176, v177\n" \
  "v_cvt_pk_bf16_f32 v189, v178, v179\n" \
  "v_cvt_pk_bf16_f32 v190, v180, v181\n" \
  "v_cvt_pk_bf16_f32 v191, v182, v183\n" \
  "s_add_u32 s46, %[sAO], 49152\n" \
  "buffer_store_dwordx4 v[188:191], %[l16], %[rs], s46 offen offset:2048 sc1\n" \
  "v_mfma_f32_16x16x32_bf16 v[168:171], v[64:67], a[88:91], 0\n" \
  "v_mfma_f32_16x16x32_bf16 v[160:163], v[56:59], a[88:91], 0\n" \
  "v_mfma_f32_16x16x32_bf16 v[164:167], v[60:63], a[88:91], 0\n" \
  "v_add_f32 v136, s59, v136\n" \
  "v_rcp_f32 v136, v136\n" \
  "s_nop 0\n" \
  "v_mul_f32 v128, v128, v136\n" \
  "v_mul_f32 v129, v129, v136\n" \
  "v_mul_f32 v130, v130, v136\n" \
  "v_mul_f32 v131, v131, v136\n" \
  "v_mul_f32 v132, v132, v136\n" \
  "v_mul_f32 v133, v133, v136\n" \
  "v_mul_f32 v134, v134, v136\n" \
  "v_mul_f32 v135, v135, v136\n" \
  "v_cvt_pk_bf16_f32 v140, v128, v129\n" \
  "v_cvt_pk_bf16_f32 v141, v130, v131\n" \
  "v_cvt_pk_bf16_f32 v142, v132, v133\n" \
  "v_cvt_pk_bf16_f32 v143, v134, v135\n" \
  "s_add_u32 s46, %[sAO], 65536\n" \
  "buffer_store_dwordx4 v[140:143], %[l16], %[rs], s46 offen offset:2048 sc1\n" \
  "v_mfma_f32_16x16x32_bf16 v[184:187], v[64:67], a[92:95], 0\n" \
  "v_mfma_f32_16x16x32_bf16 v[176:179], v[56:59], a[92:95], 0\n" \
  "v_mfma_f32_16x16x32_bf16 v[180:183], v[60:63], a[92:95], 0\n" \
  "v_add_f32 v152, s59, v152\n" \
  "v_rcp_f32 v152, v152\n" \
  "s_nop 0\n" \
  "v_mul_f32 v144, v144, v152\n" \
  "v_mul_f32 v145, v145, v152\n" \
  "v_mul_f32 v146, v146, v152\n" \
  "v_mul_f32 v147, v147, v152\n" \
  "v_mul_f32 v148, v148, v152\n" \
  "v_mul_f32 v149, v149, v152\n" \
  "v_mul_f32 v150, v150, v152\n" \
  "v_mul_f32 v151, v151, v152\n" \
  "v_cvt_pk_bf16_f32 v156, v144, v145\n" \
  "v_cvt_pk_bf16_f32 v157, v146, v147\n" \
  "v_cvt_pk_bf16_f32 v158, v148, v149\n" \
  "v_cvt_pk_bf16_f32 v159, v150, v151\n" \
  "s_add_u32 s46, %[sAO], 81920\n" \
  "buffer_store_dwordx4 v[156:159], %[l16], %[rs], s46 offen offset:2048 sc1\n" \
  "s_waitcnt vmcnt(22)\n" \
  "v_mfma_f32_16x16x32_bf16 v[136:139], v[76:79], a[96:99], 0\n" \
  "v_mfma_f32_16x16x32_bf16 v[128:131], v[68:71], a[96:99], 0\n" \
  "v_mfma_f32_16x16x32_bf16 v[132:135], v[72:75], a[96:99], 0\n" \
  "v_add_f32 v168, s59, v168\n" \
  "v_rcp_f32 v168, v168\n" \
  "s_nop 0\n" \
  "v_mul_f32 v160, v160, v168\n" \
  "v_mul_f32 v161, v161, v168\n" \
  "v_mul_f32 v162, v162, v168\n" \
  "v_mul_f32 v163, v163, v168\n" \
  "v_mul_f32 v164, v164, v168\n" \
  "v_mul_f32 v165, v165, v168\n" \
  "v_mul_f32 v166, v166, v168\n" \
  "v_mul_f32 v167, v167, v168\n" \
  "v_cvt_pk_bf16_f32 v172, v160, v161\n" \
  "v_cvt_pk_bf16_f32 v173, v162, v163\n" \
  "v_cvt_pk_bf16_f32 v174, v164, v165\n" \
  "v_cvt_pk_bf16_f32 v175, v166, v167\n" \
  "s_add_u32 s46, %[sAO], 98304\n" \
  "buffer_store_dwordx4 v[172:175], %[l16], %[rs], s46 offen offset:2048 sc1\n" \
  "v_mfma_f32_16x16x32_bf16 v[152:155], v[76:79], a[100:103], 0\n" \
  "v_mfma_f32_16x16x32_bf16 v[144:147], v[68:71], a[100:103], 0\n" \
  "v_mfma_f32_16x16x32_bf16 v[148:151], v[72:75], a[100:103], 0\n" \
  "v_add_f32 v184, s59, v184\n" \
  "v_rcp_f32 v184, v184\n" \
  "s_nop 0\n" \
  "v_mul_f32 v176, v176, v184\n" \
  "v_mul_f32 v177, v177, v184\n" \
  "v_mul_f32 v178, v178, v184\n" \
  "v_mul_f32 v179, v179, v184\n" \
  "v_mul_f32 v180, v180, v184\n" \
  "v_mul_f32 v181, v181, v184\n" \
  "v_mul_f32 v182, v182, v184\n" \
  "v_mul_f32 v183, v183, v184\n" \
  "v_cvt_pk_bf16_f32 v188, v176, v177\n" \
  "v_cvt_pk_bf16_f32 v189, v178, v179\n" \
  "v_cvt_pk_bf16_f32 v190, v180, v181\n" \
  "v_cvt_pk_bf16_f32 v191, v182, v183\n" \
  "s_add_u32 s46, %[sAO], 114688\n" \
  "buffer_store_dwordx4 v[188:191], %[l16], %[rs], s46 offen offset:2048 sc1\n" \
  "v_mfma_f32_16x16x32_bf16 v[168:171], v[76:79], a[104:107], 0\n" \
  "v_mfma_f32_16x16x32_bf16 v[160:163], v[68:71], a[104:107], 0\n" \
  "v_mfma_f32_16x16x32_bf16 v[164:167], v[72:75], a[104:107], 0\n" \
  "v_add_f32 v136, s59, v136\n" \
  "v_rcp_f32 v136, v136\n" \
  "s_nop 0\n" \
  "v_mul_f32 v128, v128, v136\n" \
  "v_mul_f32 v129, v129, v136\n" \
  "v_mul_f32 v130, v130, v136\n" \
  "v_mul_f32 v131, v131, v136\n" \
  "v_mul_f32 v132, v132, v136\n" \
  "v_mul_f32 v133, v133, v136\n" \
  "v_mul_f32 v134, v134, v136\n" \
  "v_mul_f32 v135, v135, v136\n" \
  "v_cvt_pk_bf16_f32 v140, v128, v129\n" \
  "v_cvt_pk_bf16_f32 v141, v130, v131\n" \
  "v_cvt_pk_bf16_f32 v142, v132, v133\n" \
  "v_cvt_pk_bf16_f32 v143, v134, v135\n" \
  "s_add_u32 s46, %[sAO], 0\n" \
  "buffer_store_dwordx4 v[140:143], %[l16], %[rs], s46 offen offset:3072 sc1\n" \
  "v_mfma_f32_16x16x32_bf16 v[184:187], v[76:79], a[108:111], 0\n" \
  "v_mfma_f32_16x16x32_bf16 v[176:179], v[68:71], a[108:111], 0\n" \
  "v_mfma_f32_16x16x32_bf16 v[180:183], v[72:75], a[108:111], 0\n" \
  "v_add_f32 v152, s59, v152\n" \
  "v_rcp_f32 v152, v152\n" \
  "s_nop 0\n" \
  "v_mul_f32 v144, v144, v152\n" \
  "v_mul_f32 v145, v145, v152\n" \
  "v_mul_f32 v146, v146, v152\n" \
  "v_mul_f32 v147, v147, v152\n" \
  "v_mul_f32 v148, v148, v152\n" \
  "v_mul_f32 v149, v149, v152\n" \
  "v_mul_f32 v150, v150, v152\n" \
  "v_mul_f32 v151, v151, v152\n" \
  "v_cvt_pk_bf16_f32 v156, v144, v145\n" \
  "v_cvt_pk_bf16_f32 v157, v146, v147\n" \
  "v_cvt_pk_bf16_f32 v158, v148, v149\n" \
  "v_cvt_pk_bf16_f32 v159, v150, v151\n" \
  "s_add_u32 s46, %[sAO], 16384\n" \
  "buffer_store_dwordx4 v[156:159], %[l16], %[rs], s46 offen offset:3072 sc1\n" \
  "v_mfma_f32_16x16x32_bf16 v[136:139], v[76:79], a[112:115], 0\n" \
  "v_mfma_f32_16x16x32_bf16 v[128:131], v[68:71], a[112:115], 0\n" \
  "v_mfma_f32_16x16x32_bf16 v[132:135], v[72:75], a[112:115], 0\n" \
  "v_add_f32 v168, s59, v168\n" \
  "v_rcp_f32 v168, v168\n" \
  "s_nop 0\n" \
  "v_mul_f32 v160, v160, v168\n" \
  "v_mul_f32 v161, v161, v168\n" \
  "v_mul_f32 v162, v162, v168\n" \
  "v_mul_f32 v163, v163, v168\n" \
  "v_mul_f32 v164, v164, v168\n" \
  "v_mul_f32 v165, v165, v168\n" \
  "v_mul_f32 v166, v166, v168\n" \
  "v_mul_f32 v167, v167, v168\n" \
  "v_cvt_pk_bf16_f32 v172, v160, v161\n" \
  "v_cvt_pk_bf16_f32 v173, v162, v163\n" \
  "v_cvt_pk_bf16_f32 v174, v164, v165\n" \
  "v_cvt_pk_bf16_f32 v175, v166, v167\n" \
  "s_add_u32 s46, %[sAO], 32768\n" \
  "buffer_store_dwordx4 v[172:175], %[l16], %[rs], s46 offen offset:3072 sc1\n" \
  "v_mfma_f32_16x16x32_bf16 v[152:155], v[76:79], a[116:119], 0\n" \
  "v_mfma_f32_16x16x32_bf16 v[144:147], v[68:71], a[116:119], 0\n" \
  "v_mfma_f32_16x16x32_bf16 v[148:151], v[72:75], a[116:119], 0\n" \
  "v_add_f32 v184, s59, v184\n" \
  "v_rcp_f32 v184, v184\n" \
  "s_nop 0\n" \
  "v_mul_f32 v176, v176, v184\n" \
  "v_mul_f32 v177, v177, v184\n" \
  "v_mul_f32 v178, v178, v184\n" \
  "v_mul_f32 v179, v179, v184\n" \
  "v_mul_f32 v180, v180, v184\n" \
  "v_mul_f32 v181, v181, v184\n" \
  "v_mul_f32 v182, v182, v184\n" \
  "v_mul_f32 v183, v183, v184\n" \
  "v_cvt_pk_bf16_f32 v188, v176, v177\n" \
  "v_cvt_pk_bf16_f32 v189, v178, v179\n" \
  "v_cvt_pk_bf16_f32 v190, v180, v181\n" \
  "v_cvt_pk_bf16_f32 v191, v182, v183\n" \
  "s_add_u32 s46, %[sAO], 49152\n" \
  "buffer_store_dwordx4 v[188:191], %[l16], %[rs], s46 offen offset:3072 sc1\n" \
  "v_mfma_f32_16x16x32_bf16 v[168:171], v[76:79], a[120:123], 0\n" \
  "v_mfma_f32_16x16x32_bf16 v[160:163], v[68:71], a[120:123], 0\n" \
  "v_mfma_f32_16x16x32_bf16 v[164:167], v[72:75], a[120:123], 0\n" \
  "v_add_f32 v136, s59, v136\n" \
  "v_rcp_f32 v136, v136\n" \
  "s_nop 0\n" \
  "v_mul_f32 v128, v128, v136\n" \
  "v_mul_f32 v129, v129, v136\n" \
  "v_mul_f32 v130, v130, v136\n" \
  "v_mul_f32 v131, v131, v136\n" \
  "v_mul_f32 v132, v132, v136\n" \
  "v_mul_f32 v133, v133, v136\n" \
  "v_mul_f32 v134, v134, v136\n" \
  "v_mul_f32 v135, v135, v136\n" \
  "v_cvt_pk_bf16_f32 v140, v128, v129\n" \
  "v_cvt_pk_bf16_f32 v141, v130, v131\n" \
  "v_cvt_pk_bf16_f32 v142, v132, v133\n" \
  "v_cvt_pk_bf16_f32 v143, v134, v135\n" \
  "s_add_u32 s46, %[sAO], 65536\n" \
  "buffer_store_dwordx4 v[140:143], %[l16], %[rs], s46 offen offset:3072 sc1\n" \
  "v_mfma_f32_16x16x32_bf16 v[184:187], v[76:79], a[124:127], 0\n" \
  "v_mfma_f32_16x16x32_bf16 v[176:179], v[68:71], a[124:127], 0\n" \
  "v_mfma_f32_16x16x32_bf16 v[180:183], v[72:75], a[124:127], 0\n" \
  "v_add_f32 v152, s59, v152\n" \
  "v_rcp_f32 v152, v152\n" \
  "s_nop 0\n" \
  "v_mul_f32 v144, v144, v152\n" \
  "v_mul_f32 v145, v145, v152\n" \
  "v_mul_f32 v146, v146, v152\n" \
  "v_mul_f32 v147, v147, v152\n" \
  "v_mul_f32 v148, v148, v152\n" \
  "v_mul_f32 v149, v149, v152\n" \
  "v_mul_f32 v150, v150, v152\n" \
  "v_mul_f32 v151, v151, v152\n" \
  "v_cvt_pk_bf16_f32 v156, v144, v145\n" \
  "v_cvt_pk_bf16_f32 v157, v146, v147\n" \
  "v_cvt_pk_bf16_f32 v158, v148, v149\n" \
  "v_cvt_pk_bf16_f32 v159, v150, v151\n" \
  "s_add_u32 s46, %[sAO], 81920\n" \
  "buffer_store_dwordx4 v[156:159], %[l16], %[rs], s46 offen offset:3072 sc1\n" \
  "v_add_f32 v168, s59, v168\n" \
  "v_rcp_f32 v168, v168\n" \
  "s_nop 0\n" \
  "v_mul_f32 v160, v160, v168\n" \
  "v_mul_f32 v161, v161, v168\n" \
  "v_mul_f32 v162, v162, v168\n" \
  "v_mul_f32 v163, v163, v168\n" \
  "v_mul_f32 v164, v164, v168\n" \
  "v_mul_f32 v165, v165, v168\n" \
  "v_mul_f32 v166, v166, v168\n" \
  "v_mul_f32 v167, v167, v168\n" \
  "v_cvt_pk_bf16_f32 v172, v160, v161\n" \
  "v_cvt_pk_bf16_f32 v173, v162, v163\n" \
  "v_cvt_pk_bf16_f32 v174, v164, v165\n" \
  "v_cvt_pk_bf16_f32 v175, v166, v167\n" \
  "s_add_u32 s46, %[sAO], 98304\n" \
  "buffer_store_dwordx4 v[172:175], %[l16], %[rs], s46 offen offset:3072 sc1\n" \
  "v_add_f32 v184, s59, v184\n" \
  "v_rcp_f32 v184, v184\n" \
  "s_nop 0\n" \
  "v_mul_f32 v176, v176, v184\n" \
  "v_mul_f32 v177, v177, v184\n" \
  "v_mul_f32 v178, v178, v184\n" \
  "v_mul_f32 v179, v179, v184\n" \
  "v_mul_f32 v180, v180, v184\n" \
  "v_mul_f32 v181, v181, v184\n" \
  "v_mul_f32 v182, v182, v184\n" \
  "v_mul_f32 v183, v183, v184\n" \
  "v_cvt_pk_bf16_f32 v188, v176, v177\n" \
  "v_cvt_pk_bf16_f32 v189, v178, v179\n" \
  "v_cvt_pk_bf16_f32 v190, v180, v181\n" \
  "v_cvt_pk_bf16_f32 v191, v182, v183\n" \
  "s_add_u32 s46, %[sAO], 114688\n" \
  "buffer_store_dwordx4 v[188:191], %[l16], %[rs], s46 offen offset:3072 sc1\n" \
  "s_waitcnt vmcnt(0) lgkmcnt(0)\n" \
  "s_barrier\n" \
  "buffer_store_dword v127, %[oL3], %[rs], 0 offen sc1\n" \
  "s_branch .LL_end%=\n" \
  ".LL_abort%=:\n" \
  "s_waitcnt vmcnt(0) lgkmcnt(0)\n" \
  "s_mov_b64 exec, -1\n" \
  "v_mov_b32 v96, 1\n" \
  "v_mov_b32 v97, 0x2913000\n" \
  "buffer_store_dword v96, v97, %[rs], 0 offen sc1\n" \
  ".LL_end%=:\n" \
  "s_waitcnt vmcnt(0) lgkmcnt(0)\n" \
  ".LL_zend%=:\n" \
  ""
#define ASM_L_CLOB "memory", "scc", "vcc", "a0", "a1", "a2", "a3", "a4", "a5", "a6", "a7", "a8", "a9", "a10", "a11", "a12", "a13", "a14", "a15", "a16", "a17", "a18", "a19", "a20", "a21", "a22", "a23", "a24", "a25", "a26", "a27", "a28", "a29", "a30", "a31", "a32", "a33", "a34", "a35", "a36", "a37", "a38", "a39", "a40", "a41", "a42", "a43", "a44", "a45", "a46", "a47", "a48", "a49", "a50", "a51", "a52", "a53", "a54", "a55", "a56", "a57", "a58", "a59", "a60", "a61", "a62", "a63", "a64", "a65", "a66", "a67", "a68", "a69", "a70", "a71", "a72", "a73", "a74", "a75", "a76", "a77", "a78", "a79", "a80", "a81", "a82", "a83", "a84", "a85", "a86", "a87", "a88", "a89", "a90", "a91", "a92", "a93", "a94", "a95", "a96", "a97", "a98", "a99", "a100", "a101", "a102", "a103", "a104", "a105", "a106", "a107", "a108", "a109", "a110", "a111", "a112", "a113", "a114", "a115", "a116", "a117", "a118", "a119", "a120", "a121", "a122", "a123", "a124", "a125", "a126", "a127", "a128", "a129", "a130", "a131", "a132", "a133", "a134", "a135", "a136", "a137", "a138", "a139", "a140", "a141", "a142", "a143", "a144", "a145", "a146", "a147", "a148", "a149", "a150", "a151", "a152", "a153", "a154", "a155", "a156", "a157", "a158", "a159", "s44", "s46", "s47", "s52", "s53", "s54", "s55", "s56", "s57", "s58", "s59", "s60", "s62", "s63", "v32", "v33", "v34", "v35", "v36", "v37", "v38", "v39", "v40", "v41", "v42", "v43", "v44", "v45", "v46", "v47", "v48", "v49", "v50", "v51", "v52", "v53", "v54", "v55", "v56", "v57", "v58", "v59", "v60", "v61", "v62", "v63", "v64", "v65", "v66", "v67", "v68", "v69", "v70", "v71", "v72", "v73", "v74", "v75", "v76", "v77", "v78", "v79", "v80", "v81", "v82", "v83", "v84", "v85", "v86", "v87", "v88", "v89", "v90", "v91", "v92", "v93", "v94", "v95", "v96", "v97", "v98", "v99", "v100", "v101", "v102", "v103", "v104", "v105", "v106", "v107", "v108", "v109", "v110", "v111", "v112", "v113", "v114", "v115", "v116", "v117", "v118", "v119", "v120", "v121", "v122", "v123", "v126", "v127", "v128", "v129", "v130", "v131", "v132", "v133", "v134", "v135", "v136", "v137", "v138", "v139", "v140", "v141", "v142", "v143", "v144", "v145", "v146", "v147", "v148", "v149", "v150", "v151", "v152", "v153", "v154", "v155", "v156", "v157", "v158", "v159", "v160", "v161", "v162", "v163", "v164", "v165", "v166", "v167", "v168", "v169", "v170", "v171", "v172", "v173", "v174", "v175", "v176", "v177", "v178", "v179", "v180", "v181", "v182", "v183", "v184", "v185", "v186", "v187", "v188", "v189", "v190", "v191", "v192", "v193", "v194", "v195", "v196", "v197", "v198", "v199", "v200", "v201", "v202", "v203", "v204", "v205", "v206", "v207", "v208", "v209", "v210", "v211", "v212", "v213", "v214", "v215", "v216", "v217", "v218", "v219", "v220", "v221", "v222", "v223", "v224", "v225", "v226", "v227", "v228", "v229", "v230", "v231", "v232", "v233", "v234", "v235", "v236", "v237", "v238", "v239", "v240", "v241", "v242", "v243", "v244", "v245", "v246", "v247", "v248", "v249", "v250", "v251", "v252", "v253", "v254", "v255"
#define ASM_F \
  "s_nop 4\n" \
  "buffer_load_dwordx4 v[208:211], %[oCal], %[rs], 0 offen\n" \
  "buffer_load_dwordx4 v[212:215], %[oCal], %[rs], 0 offen offset:128\n" \
  "buffer_load_dwordx4 v[216:219], %[oCal], %[rs], 0 offen offset:256\n" \
  "buffer_load_dwordx4 v[220:223], %[oCal], %[rs], 0 offen offset:384\n" \
  "s_waitcnt vmcnt(0)\n" \
  "v_add3_u32 v208, v208, v212, v216\n" \
  "v_add_u32 v208, v208, v220\n" \
  "v_lshl_or_b32 v208, v208, 2, 0\n" \
  "v_add3_u32 v209, v209, v213, v217\n" \
  "v_add_u32 v209, v209, v221\n" \
  "v_lshl_or_b32 v209, v209, 2, 1\n" \
  "v_add3_u32 v210, v210, v214, v218\n" \
  "v_add_u32 v210, v210, v222\n" \
  "v_lshl_or_b32 v210, v210, 2, 2\n" \
  "v_add3_u32 v211, v211, v215, v219\n" \
  "v_add_u32 v211, v211, v223\n" \
  "v_lshl_or_b32 v211, v211, 2, 3\n" \
  "v_min3_u32 v208, v208, v209, v210\n" \
  "v_min_u32 v208, v208, v211\n" \
  "v_and_b32 v208, 3, v208\n" \
  "v_lshlrev_b32 v208, 13, v208\n" \
  "v_lshlrev_b32 v209, 9, %[l16]\n" \
  "v_and_b32 v209, 0x4000, v209\n" \
  "v_xor_b32 v30, v208, v209\n" \
  "s_nop 0\n" \
  "v_readlane_b32 s88, v30, 0\n" \
  "s_nop 0\n" \
  "s_and_b32 s88, s88, 8192\n" \
  "s_xor_b32 s88, s88, 8192\n" \
  "s_mov_b32 s52, %[s0]\n" \
  "s_mov_b32 s55, 0\n" \
  "s_mov_b32 s56, 0xceabfb8f\n" \
  "v_cmp_gt_i32 s[58:59], 0, %[cm]\n" \
  "v_cmp_gt_i32 s[60:61], 1, %[cm]\n" \
  "v_cmp_gt_i32 s[62:63], 2, %[cm]\n" \
  "v_cmp_gt_i32 s[64:65], 3, %[cm]\n" \
  ".LF_job%=:\n" \
  "s_add_u32 s53, s52, 1\n" \
  "s_mul_i32 s54, s52, 28672\n" \
  "s_add_u32 s54, s54, %[sX]\n" \
  "v_add_u32 v151, 0, %[l16]\n" \
  "v_lshrrev_b32 v151, 8, v151\n" \
  "v_mul_u32_u24 v151, 9363, v151\n" \
  "v_lshrrev_b32 v151, 12, v151\n" \
  "v_and_b32 v151, 0x70, v151\n" \
  "v_xor_b32 v144, %[l16], v151\n" \
  "v_xor_b32 v144, %[odd], v144\n" \
  "v_add_u32 v151, 1024, %[l16]\n" \
  "v_lshrrev_b32 v151, 8, v151\n" \
  "v_mul_u32_u24 v151, 9363, v151\n" \
  "v_lshrrev_b32 v151, 12, v151\n" \
  "v_and_b32 v151, 0x70, v151\n" \
  "v_xor_b32 v145, %[l16], v151\n" \
  "v_xor_b32 v145, %[odd], v145\n" \
  "v_add_u32 v151, 2048, %[l16]\n" \
  "v_lshrrev_b32 v151, 8, v151\n" \
  "v_mul_u32_u24 v151, 9363, v151\n" \
  "v_lshrrev_b32 v151, 12, v151\n" \
  "v_and_b32 v151, 0x70, v151\n" \
  "v_xor_b32 v146, %[l16], v151\n" \
  "v_xor_b32 v146, %[odd], v146\n" \
  "v_add_u32 v151, 3072, %[l16]\n" \
  "v_lshrrev_b32 v151, 8, v151\n" \
  "v_mul_u32_u24 v151, 9363, v151\n" \
  "v_lshrrev_b32 v151, 12, v151\n" \
  "v_and_b32 v151, 0x70, v151\n" \
  "v_xor_b32 v147, %[l16], v151\n" \
  "v_xor_b32 v147, %[odd], v147\n" \
  "v_add_u32 v151, 4096, %[l16]\n" \
  "v_lshrrev_b32 v151, 8, v151\n" \
  "v_mul_u32_u24 v151, 9363, v151\n" \
  "v_lshrrev_b32 v151, 12, v151\n" \
  "v_and_b32 v151, 0x70, v151\n" \
  "v_xor_b32 v148, %[l16], v151\n" \
  "v_xor_b32 v148, %[odd], v148\n" \
  "v_add_u32 v151, 5120, %[l16]\n" \
  "v_lshrrev_b32 v151, 8, v151\n" \
  "v_mul_u32_u24 v151, 9363, v151\n" \
  "v_lshrrev_b32 v151, 12, v151\n" \
  "v_and_b32 v151, 0x70, v151\n" \
  "v_xor_b32 v149, %[l16], v151\n" \
  "v_xor_b32 v149, %[odd], v149\n" \
  "v_add_u32 v151, 6144, %[l16]\n" \
  "v_lshrrev_b32 v151, 8, v151\n" \
  "v_mul_u32_u24 v151, 9363, v151\n" \
  "v_lshrrev_b32 v151, 12, v151\n" \
  "v_and_b32 v151, 0x70, v151\n" \
  "v_xor_b32 v150, %[l16], v151\n" \
  "v_xor_b32 v150, %[odd], v150\n" \
  "s_add_u32 m0, %[sX], 49152\n" \
  "s_nop 0\n" \
  "buffer_load_dwordx4 v144, %[rx], s54 offen lds\n" \
  "buffer_load_dwordx4 v145, %[rx], s54 offen offset:1024 lds\n" \
  "buffer_load_dwordx4 v146, %[rx], s54 offen offset:2048 lds\n" \
  "buffer_load_dwordx4 v147, %[rx], s54 offen offset:3072 lds\n" \
  "s_add_u32 m0, m0, 4096\n" \
  "s_add_u32 s54, s54, 4096\n" \
  "buffer_load_dwordx4 v148, %[rx], s54 offen lds\n" \
  "buffer_load_dwordx4 v149, %[rx], s54 offen offset:1024 lds\n" \
  "buffer_load_dwordx4 v150, %[rx], s54 offen offset:2048 lds\n" \
  "buffer_load_dwordx4 v[32:35], %[g16], %[rg], 0 offen\n" \
  "buffer_load_dwordx4 v[36:39], %[g16], %[rg], 0 offen offset:64\n" \
  "buffer_load_dwordx4 v[40:43], %[g16], %[rg], 0 offen offset:128\n" \
  "buffer_load_dwordx4 v[44:47], %[g16], %[rg], 0 offen offset:192\n" \
  "buffer_load_dwordx4 v[48:51], %[g16], %[rg], 0 offen offset:256\n" \
  "buffer_load_dwordx4 v[52:55], %[g16], %[rg], 0 offen offset:320\n" \
  "buffer_load_dwordx4 v[56:59], %[g16], %[rg], 0 offen offset:384\n" \
  "buffer_load_dwordx4 v[60:63], %[g16], %[rg], 0 offen offset:448\n" \
  "buffer_load_dwordx4 v[64:67], %[g16], %[rg], 0 offen offset:512\n" \
  "buffer_load_dwordx4 v[68:71], %[g16], %[rg], 0 offen offset:576\n" \
  "buffer_load_dwordx4 v[72:75], %[g16], %[rg], 0 offen offset:640\n" \
  "buffer_load_dwordx4 v[76:79], %[g16], %[rg], 0 offen offset:704\n" \
  "buffer_load_dwordx4 v[80:83], %[g16], %[rg], 0 offen offset:768\n" \
  "buffer_load_dwordx4 v[84:87], %[g16], %[rg], 0 offen offset:832\n" \
  "v_add_u32 v144, 49152, %[rX]\n" \
  "v_xor_b32 v145, 64, v144\n" \
  "s_waitcnt vmcnt(14)\n" \
  "s_barrier\n" \
  "ds_read_b128 a[0:3], v144\n" \
  "ds_read_b128 a[56:59], v144 offset:14336\n" \
  "ds_read_b128 a[4:7], v145\n" \
  "ds_read_b128 a[60:63], v145 offset:14336\n" \
  "ds_read_b128 a[8:11], v144 offset:128\n" \
  "ds_read_b128 a[64:67], v144 offset:14464\n" \
  "ds_read_b128 a[12:15], v145 offset:128\n" \
  "ds_read_b128 a[68:71], v145 offset:14464\n" \
  "ds_read_b128 a[16:19], v144 offset:256\n" \
  "ds_read_b128 a[72:75], v144 offset:14592\n" \
  "ds_read_b128 a[20:23], v145 offset:256\n" \
  "ds_read_b128 a[76:79], v145 offset:14592\n" \
  "ds_read_b128 a[24:27], v144 offset:384\n" \
  "ds_read_b128 a[80:83], v144 offset:14720\n" \
  "ds_read_b128 a[28:31], v145 offset:384\n" \
  "ds_read_b128 a[84:87], v145 offset:14720\n" \
  "ds_read_b128 a[32:35], v144 offset:512\n" \
  "ds_read_b128 a[88:91], v144 offset:14848\n" \
  "ds_read_b128 a[36:39], v145 offset:512\n" \
  "ds_read_b128 a[92:95], v145 offset:14848\n" \
  "ds_read_b128 a[40:43], v144 offset:640\n" \
  "ds_read_b128 a[96:99], v144 offset:14976\n" \
  "ds_read_b128 a[44:47], v145 offset:640\n" \
  "ds_read_b128 a[100:103], v145 offset:14976\n" \
  "ds_read_b128 a[48:51], v144 offset:768\n" \
  "ds_read_b128 a[104:107], v144 offset:15104\n" \
  "ds_read_b128 a[52:55], v145 offset:768\n" \
  "ds_read_b128 a[108:111], v145 offset:15104\n" \
  "s_waitcnt vmcnt(13) lgkmcnt(15)\n" \
  "v_mfma_f32_16x16x32_bf16 v[208:211], v[32:35], a[0:3], 0\n" \
  "v_mfma_f32_16x16x32_bf16 v[212:215], v[32:35], a[56:59], 0\n" \
  "s_waitcnt vmcnt(12)\n" \
  "v_mfma_f32_16x16x32_bf16 v[208:211], v[36:39], a[4:7], v[208:211]\n" \
  "v_mfma_f32_16x16x32_bf16 v[212:215], v[36:39], a[60:63], v[212:215]\n" \
  "s_waitcnt vmcnt(11)\n" \
  "v_mfma_f32_16x16x32_bf16 v[208:211], v[40:43], a[8:11], v[208:211]\n" \
  "v_mfma_f32_16x16x32_bf16 v[212:215], v[40:43], a[64:67], v[212:215]\n" \
  "s_waitcnt vmcnt(10)\n" \
  "v_mfma_f32_16x16x32_bf16 v[208:211], v[44:47], a[12:15], v[208:211]\n" \
  "v_mfma_f32_16x16x32_bf16 v[212:215], v[44:47], a[68:71], v[212:215]\n" \
  "s_waitcnt vmcnt(9)\n" \
  "v_mfma_f32_16x16x32_bf16 v[208:211], v[48:51], a[16:19], v[208:211]\n" \
  "v_mfma_f32_16x16x32_bf16 v[212:215], v[48:51], a[72:75], v[212:215]\n" \
  "s_waitcnt vmcnt(8)\n" \
  "v_mfma_f32_16x16x32_bf16 v[208:211], v[52:55], a[20:23], v[208:211]\n" \
  "v_mfma_f32_16x16x32_bf16 v[212:215], v[52:55], a[76:79], v[212:215]\n" \
  "s_waitcnt vmcnt(7)\n" \
  "v_mfma_f32_16x16x32_bf16 v[208:211], v[56:59], a[24:27], v[208:211]\n" \
  "s_waitcnt lgkmcnt(14)\n" \
  "v_mfma_f32_16x16x32_bf16 v[212:215], v[56:59], a[80:83], v[212:215]\n" \
  "s_waitcnt vmcnt(6) lgkmcnt(13)\n" \
  "v_mfma_f32_16x16x32_bf16 v[208:211], v[60:63], a[28:31], v[208:211]\n" \
  "s_waitcnt lgkmcnt(12)\n" \
  "v_mfma_f32_16x16x32_bf16 v[212:215], v[60:63], a[84:87], v[212:215]\n" \
  "s_waitcnt vmcnt(5) lgkmcnt(11)\n" \
  "v_mfma_f32_16x16x32_bf16 v[208:211], v[64:67], a[32:35], v[208:211]\n" \
  "s_waitcnt lgkmcnt(10)\n" \
  "v_mfma_f32_16x16x32_bf16 v[212:215], v[64:67], a[88:91], v[212:215]\n" \
  "s_waitcnt vmcnt(4) lgkmcnt(9)\n" \
  "v_mfma_f32_16x16x32_bf16 v[208:211], v[68:71], a[36:39], v[208:211]\n" \
  "s_waitcnt lgkmcnt(8)\n" \
  "v_mfma_f32_16x16x32_bf16 v[212:215], v[68:71], a[92:95], v[212:215]\n" \
  "s_waitcnt vmcnt(3) lgkmcnt(7)\n" \
  "v_mfma_f32_16x16x32_bf16 v[208:211], v[72:75], a[40:43], v[208:211]\n" \
  "s_waitcnt lgkmcnt(6)\n" \
  "v_mfma_f32_16x16x32_bf16 v[212:215], v[72:75], a[96:99], v[212:215]\n" \
  "s_waitcnt vmcnt(2) lgkmcnt(5)\n" \
  "v_mfma_f32_16x16x32_bf16 v[208:211], v[76:79], a[44:47], v[208:211]\n" \
  "s_waitcnt lgkmcnt(4)\n" \
  "v_mfma_f32_16x16x32_bf16 v[212:215], v[76:79], a[100:103], v[212:215]\n" \
  "s_waitcnt vmcnt(1) lgkmcnt(3)\n" \
  "v_mfma_f32_16x16x32_bf16 v[208:211], v[80:83], a[48:51], v[208:211]\n" \
  "s_waitcnt lgkmcnt(2)\n" \
  "v_mfma_f32_16x16x32_bf16 v[212:215], v[80:83], a[104:107], v[212:215]\n" \
  "s_waitcnt vmcnt(0) lgkmcnt(1)\n" \
  "v_mfma_f32_16x16x32_bf16 v[208:211], v[84:87], a[52:55], v[208:211]\n" \
  "s_waitcnt lgkmcnt(0)\n" \
  "v_mfma_f32_16x16x32_bf16 v[212:215], v[84:87], a[108:111], v[212:215]\n" \
  "s_mov_b32 s44, 262144\n" \
  "s_mov_b32 exec_hi, 0\n" \
  "s_mov_b32 exec_lo, 0x3fff\n" \
  "s_branch .LF_wk_in%=\n" \
  ".LF_wk%=:\n" \
  "s_sleep 8\n" \
  "s_sub_u32 s44, s44, 1\n" \
  "s_cbranch_scc1 .LF_abort%=\n" \
  ".LF_wk_in%=:\n" \
  "buffer_load_dword v234, %[pKS], %[rs], 0 offen sc1\n" \
  "s_waitcnt vmcnt(0)\n" \
  "v_cmp_gt_i32 vcc, s53, v234\n" \
  "s_cbranch_vccnz .LF_wk%=\n" \
  "s_mov_b64 exec, -1\n" \
  "s_lshl_b32 s49, s52, 12\n" \
  "s_and_b32 s49, s49, 8192\n" \
  "s_xor_b32 s49, s49, s88\n" \
  "s_cmp_lt_u32 s52, 2\n" \
  "s_cselect_b32 s49, 0, s49\n" \
  "s_lshl_b32 s54, s52, 20\n" \
  "s_add_u32 s54, s54, %[sKX]\n" \
  "s_xor_b32 s54, s54, s49\n" \
  "s_xor_b32 s47, s54, 0\n" \
  "buffer_load_dwordx4 v[32:35], %[l16], %[rs], s47 offen sc1\n" \
  "buffer_load_dwordx4 v[36:39], %[l16], %[rs], s47 offen offset:1024 sc1\n" \
  "buffer_load_dwordx4 v[40:43], %[l16], %[rs], s47 offen offset:2048 sc1\n" \
  "buffer_load_dwordx4 v[44:47], %[l16], %[rs], s47 offen offset:3072 sc1\n" \
  "s_xor_b32 s47, s54, 4096\n" \
  "buffer_load_dwordx4 v[48:51], %[l16], %[rs], s47 offen sc1\n" \
  "buffer_load_dwordx4 v[52:55], %[l16], %[rs], s47 offen offset:1024 sc1\n" \
  "buffer_load_dwordx4 v[56:59], %[l16], %[rs], s47 offen offset:2048 sc1\n" \
  "buffer_load_dwordx4 v[60:63], %[l16], %[rs], s47 offen offset:3072 sc1\n" \
  "s_xor_b32 s47, s54, 16384\n" \
  "buffer_load_dwordx4 v[64:67], %[l16], %[rs], s47 offen sc1\n" \
  "buffer_load_dwordx4 v[68:71], %[l16], %[rs], s47 offen offset:1024 sc1\n" \
  "buffer_load_dwordx4 v[72:75], %[l16], %[rs], s47 offen offset:2048 sc1\n" \
  "buffer_load_dwordx4 v[76:79], %[l16], %[rs], s47 offen offset:3072 sc1\n" \
  "s_xor_b32 s47, s54, 20480\n" \
  "buffer_load_dwordx4 v[80:83], %[l16], %[rs], s47 offen sc1\n" \
  "buffer_load_dwordx4 v[84:87], %[l16], %[rs], s47 offen offset:1024 sc1\n" \
  "buffer_load_dwordx4 v[88:91], %[l16], %[rs], s47 offen offset:2048 sc1\n" \
  "buffer_load_dwordx4 v[92:95], %[l16], %[rs], s47 offen offset:3072 sc1\n" \
  "s_xor_b32 s47, s54, 32768\n" \
  "buffer_load_dwordx4 v[96:99], %[l16], %[rs], s47 offen sc1\n" \
  "buffer_load_dwordx4 v[100:103], %[l16], %[rs], s47 offen offset:1024 sc1\n" \
  "buffer_load_dwordx4 v[104:107], %[l16], %[rs], s47 offen offset:2048 sc1\n" \
  "buffer_load_dwordx4 v[108:111], %[l16], %[rs], s47 offen offset:3072 sc1\n" \
  "s_xor_b32 s47, s54, 36864\n" \
  "buffer_load_dwordx4 v[112:115], %[l16], %[rs], s47 offen sc1\n" \
  "buffer_load_dwordx4 v[116:119], %[l16], %[rs], s47 offen offset:1024 sc1\n" \
  "buffer_load_dwordx4 v[120:123], %[l16], %[rs], s47 offen offset:2048 sc1\n" \
  "buffer_load_dwordx4 v[124:127], %[l16], %[rs], s47 offen offset:3072 sc1\n" \
  "s_xor_b32 s47, s54, 49152\n" \
  "buffer_load_dwordx4 v[128:131], %[l16], %[rs], s47 offen sc1\n" \
  "buffer_load_dwordx4 v[132:135], %[l16], %[rs], s47 offen offset:1024 sc1\n" \
  "buffer_load_dwordx4 v[136:139], %[l16], %[rs], s47 offen offset:2048 sc1\n" \
  "buffer_load_dwordx4 v[140:143], %[l16], %[rs], s47 offen offset:3072 sc1\n" \
  "v_mul_f32 v232, 0xbfb8aa3b, v208\n" \
  "v_exp_f32 v232, v232\n" \
  "s_nop 0\n" \
  "v_add_f32 v232, 1.0, v232\n" \
  "v_rcp_f32 v232, v232\n" \
  "v_mul_f32 v233, 0xbfb8aa3b, v212\n" \
  "v_exp_f32 v233, v233\n" \
  "s_nop 0\n" \
  "v_add_f32 v233, 1.0, v233\n" \
  "v_rcp_f32 v233, v233\n" \
  "s_add_u32 s54, s54, 65536\n" \
  "s_waitcnt vmcnt(27)\n" \
  "v_mfma_f32_16x16x32_bf16 v[144:147], v[32:35], a[0:3], 0\n" \
  "v_mfma_f32_16x16x32_bf16 v[152:155], v[32:35], a[56:59], 0\n" \
  "s_waitcnt vmcnt(26)\n" \
  "v_mfma_f32_16x16x32_bf16 v[156:159], v[36:39], a[56:59], 0\n" \
  "s_xor_b32 s47, s54, 0\n" \
  "buffer_load_dwordx4 v[32:35], %[l16], %[rs], s47 offen sc1\n" \
  "buffer_load_dwordx4 v[36:39], %[l16], %[rs], s47 offen offset:1024 sc1\n" \
  "s_waitcnt vmcnt(27)\n" \
  "v_mfma_f32_16x16x32_bf16 v[144:147], v[40:43], a[4:7], v[144:147]\n" \
  "v_mfma_f32_16x16x32_bf16 v[152:155], v[40:43], a[60:63], v[152:155]\n" \
  "s_waitcnt vmcnt(26)\n" \
  "v_mfma_f32_16x16x32_bf16 v[156:159], v[44:47], a[60:63], v[156:159]\n" \
  "buffer_load_dwordx4 v[40:43], %[l16], %[rs], s47 offen offset:2048 sc1\n" \
  "buffer_load_dwordx4 v[44:47], %[l16], %[rs], s47 offen offset:3072 sc1\n" \
  "s_waitcnt vmcnt(27)\n" \
  "v_mfma_f32_16x16x32_bf16 v[144:147], v[48:51], a[8:11], v[144:147]\n" \
  "v_mfma_f32_16x16x32_bf16 v[152:155], v[48:51], a[64:67], v[152:155]\n" \
  "s_waitcnt vmcnt(26)\n" \
  "v_mfma_f32_16x16x32_bf16 v[156:159], v[52:55], a[64:67], v[156:159]\n" \
  "s_xor_b32 s47, s54, 4096\n" \
  "buffer_load_dwordx4 v[48:51], %[l16], %[rs], s47 offen sc1\n" \
  "buffer_load_dwordx4 v[52:55], %[l16], %[rs], s47 offen offset:1024 sc1\n" \
  "s_waitcnt vmcnt(27)\n" \
  "v_mfma_f32_16x16x32_bf16 v[144:147], v[56:59], a[12:15], v[144:147]\n" \
  "v_mfma_f32_16x16x32_bf16 v[152:155], v[56:59], a[68:71], v[152:155]\n" \
  "s_waitcnt vmcnt(26)\n" \
  "v_mfma_f32_16x16x32_bf16 v[156:159], v[60:63], a[68:71], v[156:159]\n" \
  "buffer_load_dwordx4 v[56:59], %[l16], %[rs], s47 offen offset:2048 sc1\n" \
  "buffer_load_dwordx4 v[60:63], %[l16], %[rs], s47 offen offset:3072 sc1\n" \
  "s_waitcnt vmcnt(27)\n" \
  "v_mfma_f32_16x16x32_bf16 v[144:147], v[64:67], a[16:19], v[144:147]\n" \
  "v_mfma_f32_16x16x32_bf16 v[152:155], v[64:67], a[72:75], v[152:155]\n" \
  "s_waitcnt vmcnt(26)\n" \
  "v_mfma_f32_16x16x32_bf16 v[156:159], v[68:71], a[72:75], v[156:159]\n" \
  "s_xor_b32 s47, s54, 16384\n" \
  "buffer_load_dwordx4 v[64:67], %[l16], %[rs], s47 offen sc1\n" \
  "buffer_load_dwordx4 v[68:71], %[l16], %[rs], s47 offen offset:1024 sc1\n" \
  "s_waitcnt vmcnt(27)\n" \
  "v_mfma_f32_16x16x32_bf16 v[144:147], v[72:75], a[20:23], v[144:147]\n" \
  "v_mfma_f32_16x16x32_bf16 v[152:155], v[72:75], a[76:79], v[152:155]\n" \
  "s_waitcnt vmcnt(26)\n" \
  "v_mfma_f32_16x16x32_bf16 v[156:159], v[76:79], a[76:79], v[156:159]\n" \
  "buffer_load_dwordx4 v[72:75], %[l16], %[rs], s47 offen offset:2048 sc1\n" \
  "buffer_load_dwordx4 v[76:79], %[l16], %[rs], s47 offen offset:3072 sc1\n" \
  "s_waitcnt vmcnt(27)\n" \
  "v_mfma_f32_16x16x32_bf16 v[144:147], v[80:83], a[24:27], v[144:147]\n" \
  "v_mfma_f32_16x16x32_bf16 v[152:155], v[80:83], a[80:83], v[152:155]\n" \
  "s_waitcnt vmcnt(26)\n" \
  "v_mfma_f32_16x16x32_bf16 v[156:159], v[84:87], a[80:83], v[156:159]\n" \
  "s_xor_b32 s47, s54, 20480\n" \
  "buffer_load_dwordx4 v[80:83], %[l16], %[rs], s47 offen sc1\n" \
  "buffer_load_dwordx4 v[84:87], %[l16], %[rs], s47 offen offset:1024 sc1\n" \
  "s_waitcnt vmcnt(27)\n" \
  "v_mfma_f32_16x16x32_bf16 v[144:147], v[88:91], a[28:31], v[144:147]\n" \
  "v_mfma_f32_16x16x32_bf16 v[152:155], v[88:91], a[84:87], v[152:155]\n" \
  "s_waitcnt vmcnt(26)\n" \
  "v_mfma_f32_16x16x32_bf16 v[156:159], v[92:95], a[84:87], v[156:159]\n" \
  "buffer_load_dwordx4 v[88:91], %[l16], %[rs], s47 offen offset:2048 sc1\n" \
  "buffer_load_dwordx4 v[92:95], %[l16], %[rs], s47 offen offset:3072 sc1\n" \
  "s_waitcnt vmcnt(27)\n" \
  "v_mfma_f32_16x16x32_bf16 v[144:147], v[96:99], a[32:35], v[144:147]\n" \
  "v_mfma_f32_16x16x32_bf16 v[152:155], v[96:99], a[88:91], v[152:155]\n" \
  "s_waitcnt vmcnt(26)\n" \
  "v_mfma_f32_16x16x32_bf16 v[156:159], v[100:103], a[88:91], v[156:159]\n" \
  "s_xor_b32 s47, s54, 32768\n" \
  "buffer_load_dwordx4 v[96:99], %[l16], %[rs], s47 offen sc1\n" \
  "buffer_load_dwordx4 v[100:103], %[l16], %[rs], s47 offen offset:1024 sc1\n" \
  "s_waitcnt vmcnt(27)\n" \
  "v_mfma_f32_16x16x32_bf16 v[144:147], v[104:107], a[36:39], v[144:147]\n" \
  "v_mfma_f32_16x16x32_bf16 v[152:155], v[104:107], a[92:95], v[152:155]\n" \
  "s_waitcnt vmcnt(26)\n" \
  "v_mfma_f32_16x16x32_bf16 v[156:159], v[108:111], a[92:95], v[156:159]\n" \
  "buffer_load_dwordx4 v[104:107], %[l16], %[rs], s47 offen offset:2048 sc1\n" \
  "buffer_load_dwordx4 v[108:111], %[l16], %[rs], s47 offen offset:3072 sc1\n" \
  "s_waitcnt vmcnt(27)\n" \
  "v_mfma_f32_16x16x32_bf16 v[144:147], v[112:115], a[40:43], v[144:147]\n" \
  "v_mfma_f32_16x16x32_bf16 v[152:155], v[112:115], a[96:99], v[152:155]\n" \
  "s_waitcnt vmcnt(26)\n" \
  "v_mfma_f32_16x16x32_bf16 v[156:159], v[116:119], a[96:99], v[156:159]\n" \
  "s_xor_b32 s47, s54, 36864\n" \
  "buffer_load_dwordx4 v[112:115], %[l16], %[rs], s47 offen sc1\n" \
  "buffer_load_dwordx4 v[116:119], %[l16], %[rs], s47 offen offset:1024 sc1\n" \
  "s_waitcnt vmcnt(27)\n" \
  "v_mfma_f32_16x16x32_bf16 v[144:147], v[120:123], a[44:47], v[144:147]\n" \
  "v_mfma_f32_16x16x32_bf16 v[152:155], v[120:123], a[100:103], v[152:155]\n" \
  "s_waitcnt vmcnt(26)\n" \
  "v_mfma_f32_16x16x32_bf16 v[156:159], v[124:127], a[100:103], v[156:159]\n" \
  "buffer_load_dwordx4 v[120:123], %[l16], %[rs], s47 offen offset:2048 sc1\n" \
  "buffer_load_dwordx4 v[124:127], %[l16], %[rs], s47 offen offset:3072 sc1\n" \
  "s_waitcnt vmcnt(27)\n" \
  "v_mfma_f32_16x16x32_bf16 v[144:147], v[128:131], a[48:51], v[144:147]\n" \
  "v_mfma_f32_16x16x32_bf16 v[152:155], v[128:131], a[104:107], v[152:155]\n" \
  "s_waitcnt vmcnt(26)\n" \
  "v_mfma_f32_16x16x32_bf16 v[156:159], v[132:135], a[104:107], v[156:159]\n" \
  "s_xor_b32 s47, s54, 49152\n" \
  "buffer_load_dwordx4 v[128:131], %[l16], %[rs], s47 offen sc1\n" \
  "buffer_load_dwordx4 v[132:135], %[l16], %[rs], s47 offen offset:1024 sc1\n" \
  "s_waitcnt vmcnt(27)\n" \
  "v_mfma_f32_16x16x32_bf16 v[144:147], v[136:139], a[52:55], v[144:147]\n" \
  "v_mfma_f32_16x16x32_bf16 v[152:155], v[136:139], a[108:111], v[152:155]\n" \
  "s_waitcnt vmcnt(26)\n" \
  "v_mfma_f32_16x16x32_bf16 v[156:159], v[140:143], a[108:111], v[156:159]\n" \
  "buffer_load_dwordx4 v[136:139], %[l16], %[rs], s47 offen offset:2048 sc1\n" \
  "buffer_load_dwordx4 v[140:143], %[l16], %[rs], s47 offen offset:3072 sc1\n" \
  "v_mov_b32 v236, s56\n" \
  "v_mov_b32 v148, s56\n" \
  "v_mov_b32 v149, s56\n" \
  "v_mov_b32 v150, s56\n" \
  "v_mov_b32 v151, s56\n" \
  "v_cndmask_b32 v144, v144, v236, s[58:59]\n" \
  "v_cndmask_b32 v145, v145, v236, s[60:61]\n" \
  "v_cndmask_b32 v146, v146, v236, s[62:63]\n" \
  "v_cndmask_b32 v147, v147, v236, s[64:65]\n" \
  "v_max3_f32 v234, v144, v145, v146\n" \
  "v_max3_f32 v234, v234, v147, v148\n" \
  "v_max3_f32 v234, v234, v149, v150\n" \
  "v_max_f32 v234, v234, v151\n" \
  "v_mov_b32 v235, v234\n" \
  "s_nop 1\n" \
  "v_permlane16_swap_b32 v235, v234\n" \
  "v_max_f32 v234, v234, v235\n" \
  "v_mov_b32 v235, v234\n" \
  "s_nop 1\n" \
  "v_permlane32_swap_b32 v235, v234\n" \
  "v_max_f32 v234, v234, v235\n" \
  "v_sub_f32 v144, v144, v234\n" \
  "v_sub_f32 v145, v145, v234\n" \
  "v_sub_f32 v146, v146, v234\n" \
  "v_sub_f32 v147, v147, v234\n" \
  "v_sub_f32 v148, v148, v234\n" \
  "v_sub_f32 v149, v149, v234\n" \
  "v_sub_f32 v150, v150, v234\n" \
  "v_sub_f32 v151, v151, v234\n" \
  "v_exp_f32 v144, v144\n" \
  "v_exp_f32 v145, v145\n" \
  "v_exp_f32 v146, v146\n" \
  "v_exp_f32 v147, v147\n" \
  "v_exp_f32 v148, v148\n" \
  "v_exp_f32 v149, v149\n" \
  "v_exp_f32 v150, v150\n" \
  "v_exp_f32 v151, v151\n" \
  "v_add_f32 v237, v144, v145\n" \
  "v_add_f32 v237, v237, v146\n" \
  "v_add_f32 v237, v237, v147\n" \
  "v_add_f32 v237, v237, v148\n" \
  "v_add_f32 v237, v237, v149\n" \
  "v_add_f32 v237, v237, v150\n" \
  "v_add_f32 v237, v237, v151\n" \
  "v_mov_b32 v235, v237\n" \
  "s_nop 1\n" \
  "v_permlane16_swap_b32 v235, v237\n" \
  "v_add_f32 v237, v237, v235\n" \
  "v_mov_b32 v235, v237\n" \
  "s_nop 1\n" \
  "v_permlane32_swap_b32 v235, v237\n" \
  "v_add_f32 v237, v237, v235\n" \
  "v_mov_b32 v224, v234\n" \
  "v_mov_b32 v228, v237\n" \
  "v_cndmask_b32 v156, v156, v236, s[58:59]\n" \
  "v_cndmask_b32 v157, v157, v236, s[60:61]\n" \
  "v_cndmask_b32 v158, v158, v236, s[62:63]\n" \
  "v_cndmask_b32 v159, v159, v236, s[64:65]\n" \
  "v_max3_f32 v234, v152, v153, v154\n" \
  "v_max3_f32 v234, v234, v155, v156\n" \
  "v_max3_f32 v234, v234, v157, v158\n" \
  "v_max_f32 v234, v234, v159\n" \
  "v_mov_b32 v235, v234\n" \
  "s_nop 1\n" \
  "v_permlane16_swap_b32 v235, v234\n" \
  "v_max_f32 v234, v234, v235\n" \
  "v_mov_b32 v235, v234\n" \
  "s_nop 1\n" \
  "v_permlane32_swap_b32 v235, v234\n" \
  "v_max_f32 v234, v234, v235\n" \
  "v_sub_f32 v152, v152, v234\n" \
  "v_sub_f32 v153, v153, v234\n" \
  "v_sub_f32 v154, v154, v234\n" \
  "v_sub_f32 v155, v155, v234\n" \
  "v_sub_f32 v156, v156, v234\n" \
  "v_sub_f32 v157, v157, v234\n" \
  "v_sub_f32 v158, v158, v234\n" \
  "v_sub_f32 v159, v159, v234\n" \
  "v_exp_f32 v152, v152\n" \
  "v_exp_f32 v153, v153\n" \
  "v_exp_f32 v154, v154\n" \
  "v_exp_f32 v155, v155\n" \
  "v_exp_f32 v156, v156\n" \
  "v_exp_f32 v157, v157\n" \
  "v_exp_f32 v158, v158\n" \
  "v_exp_f32 v159, v159\n" \
  "v_add_f32 v237, v152, v153\n" \
  "v_add_f32 v237, v237, v154\n" \
  "v_add_f32 v237, v237, v155\n" \
  "v_add_f32 v237, v237, v156\n" \
  "v_add_f32 v237, v237, v157\n" \
  "v_add_f32 v237, v237, v158\n" \
  "v_add_f32 v237, v237, v159\n" \
  "v_mov_b32 v235, v237\n" \
  "s_nop 1\n" \
  "v_permlane16_swap_b32 v235, v237\n" \
  "v_add_f32 v237, v237, v235\n" \
  "v_mov_b32 v235, v237\n" \
  "s_nop 1\n" \
  "v_permlane32_swap_b32 v235, v237\n" \
  "v_add_f32 v237, v237, v235\n" \
  "v_mov_b32 v225, v234\n" \
  "v_mov_b32 v229, v237\n" \
  "s_add_u32 s54, s54, 65536\n" \
  "s_waitcnt vmcnt(27)\n" \
  "v_mfma_f32_16x16x32_bf16 v[160:163], v[32:35], a[0:3], 0\n" \
  "v_mfma_f32_16x16x32_bf16 v[168:171], v[32:35], a[56:59], 0\n" \
  "s_waitcnt vmcnt(26)\n" \
  "v_mfma_f32_16x16x32_bf16 v[172:175], v[36:39], a[56:59], 0\n" \
  "s_xor_b32 s47, s54, 0\n" \
  "buffer_load_dwordx4 v[32:35], %[l16], %[rs], s47 offen sc1\n" \
  "buffer_load_dwordx4 v[36:39], %[l16], %[rs], s47 offen offset:1024 sc1\n" \
  "s_waitcnt vmcnt(27)\n" \
  "v_mfma_f32_16x16x32_bf16 v[160:163], v[40:43], a[4:7], v[160:163]\n" \
  "v_mfma_f32_16x16x32_bf16 v[168:171], v[40:43], a[60:63], v[168:171]\n" \
  "s_waitcnt vmcnt(26)\n" \
  "v_mfma_f32_16x16x32_bf16 v[172:175], v[44:47], a[60:63], v[172:175]\n" \
  "buffer_load_dwordx4 v[40:43], %[l16], %[rs], s47 offen offset:2048 sc1\n" \
  "buffer_load_dwordx4 v[44:47], %[l16], %[rs], s47 offen offset:3072 sc1\n" \
  "s_waitcnt vmcnt(27)\n" \
  "v_mfma_f32_16x16x32_bf16 v[160:163], v[48:51], a[8:11], v[160:163]\n" \
  "v_mfma_f32_16x16x32_bf16 v[168:171], v[48:51], a[64:67], v[168:171]\n" \
  "s_waitcnt vmcnt(26)\n" \
  "v_mfma_f32_16x16x32_bf16 v[172:175], v[52:55], a[64:67], v[172:175]\n" \
  "s_xor_b32 s47, s54, 4096\n" \
  "buffer_load_dwordx4 v[48:51], %[l16], %[rs], s47 offen sc1\n" \
  "buffer_load_dwordx4 v[52:55], %[l16], %[rs], s47 offen offset:1024 sc1\n" \
  "s_waitcnt vmcnt(27)\n" \
  "v_mfma_f32_16x16x32_bf16 v[160:163], v[56:59], a[12:15], v[160:163]\n" \
  "v_mfma_f32_16x16x32_bf16 v[168:171], v[56:59], a[68:71], v[168:171]\n" \
  "s_waitcnt vmcnt(26)\n" \
  "v_mfma_f32_16x16x32_bf16 v[172:175], v[60:63], a[68:71], v[172:175]\n" \
  "buffer_load_dwordx4 v[56:59], %[l16], %[rs], s47 offen offset:2048 sc1\n" \
  "buffer_load_dwordx4 v[60:63], %[l16], %[rs], s47 offen offset:3072 sc1\n" \
  "s_waitcnt vmcnt(27)\n" \
  "v_mfma_f32_16x16x32_bf16 v[160:163], v[64:67], a[16:19], v[160:163]\n" \
  "v_mfma_f32_16x16x32_bf16 v[168:171], v[64:67], a[72:75], v[168:171]\n" \
  "s_waitcnt vmcnt(26)\n" \
  "v_mfma_f32_16x16x32_bf16 v[172:175], v[68:71], a[72:75], v[172:175]\n" \
  "s_xor_b32 s47, s54, 16384\n" \
  "buffer_load_dwordx4 v[64:67], %[l16], %[rs], s47 offen sc1\n" \
  "buffer_load_dwordx4 v[68:71], %[l16], %[rs], s47 offen offset:1024 sc1\n" \
  "s_waitcnt vmcnt(27)\n" \
  "v_mfma_f32_16x16x32_bf16 v[160:163], v[72:75], a[20:23], v[160:163]\n" \
  "v_mfma_f32_16x16x32_bf16 v[168:171], v[72:75], a[76:79], v[168:171]\n" \
  "s_waitcnt vmcnt(26)\n" \
  "v_mfma_f32_16x16x32_bf16 v[172:175], v[76:79], a[76:79], v[172:175]\n" \
  "buffer_load_dwordx4 v[72:75], %[l16], %[rs], s47 offen offset:2048 sc1\n" \
  "buffer_load_dwordx4 v[76:79], %[l16], %[rs], s47 offen offset:3072 sc1\n" \
  "s_waitcnt vmcnt(27)\n" \
  "v_mfma_f32_16x16x32_bf16 v[160:163], v[80:83], a[24:27], v[160:163]\n" \
  "v_mfma_f32_16x16x32_bf16 v[168:171], v[80:83], a[80:83], v[168:171]\n" \
  "s_waitcnt vmcnt(26)\n" \
  "v_mfma_f32_16x16x32_bf16 v[172:175], v[84:87], a[80:83], v[172:175]\n" \
  "s_xor_b32 s47, s54, 20480\n" \
  "buffer_load_dwordx4 v[80:83], %[l16], %[rs], s47 offen sc1\n" \
  "buffer_load_dwordx4 v[84:87], %[l16], %[rs], s47 offen offset:1024 sc1\n" \
  "s_waitcnt vmcnt(27)\n" \
  "v_mfma_f32_16x16x32_bf16 v[160:163], v[88:91], a[28:31], v[160:163]\n" \
  "v_mfma_f32_16x16x32_bf16 v[168:171], v[88:91], a[84:87], v[168:171]\n" \
  "s_waitcnt vmcnt(26)\n" \
  "v_mfma_f32_16x16x32_bf16 v[172:175], v[92:95], a[84:87], v[172:175]\n" \
  "buffer_load_dwordx4 v[88:91], %[l16], %[rs], s47 offen offset:2048 sc1\n" \
  "buffer_load_dwordx4 v[92:95], %[l16], %[rs], s47 offen offset:3072 sc1\n" \
  "s_waitcnt vmcnt(27)\n" \
  "v_mfma_f32_16x16x32_bf16 v[160:163], v[96:99], a[32:35], v[160:163]\n" \
  "v_mfma_f32_16x16x32_bf16 v[168:171], v[96:99], a[88:91], v[168:171]\n" \
  "s_waitcnt vmcnt(26)\n" \
  "v_mfma_f32_16x16x32_bf16 v[172:175], v[100:103], a[88:91], v[172:175]\n" \
  "s_xor_b32 s47, s54, 32768\n" \
  "buffer_load_dwordx4 v[96:99], %[l16], %[rs], s47 offen sc1\n" \
  "buffer_load_dwordx4 v[100:103], %[l16], %[rs], s47 offen offset:1024 sc1\n" \
  "s_waitcnt vmcnt(27)\n" \
  "v_mfma_f32_16x16x32_bf16 v[160:163], v[104:107], a[36:39], v[160:163]\n" \
  "v_mfma_f32_16x16x32_bf16 v[168:171], v[104:107], a[92:95], v[168:171]\n" \
  "s_waitcnt vmcnt(26)\n" \
  "v_mfma_f32_16x16x32_bf16 v[172:175], v[108:111], a[92:95], v[172:175]\n" \
  "buffer_load_dwordx4 v[104:107], %[l16], %[rs], s47 offen offset:2048 sc1\n" \
  "buffer_load_dwordx4 v[108:111], %[l16], %[rs], s47 offen offset:3072 sc1\n" \
  "s_waitcnt vmcnt(27)\n" \
  "v_mfma_f32_16x16x32_bf16 v[160:163], v[112:115], a[40:43], v[160:163]\n" \
  "v_mfma_f32_16x16x32_bf16 v[168:171], v[112:115], a[96:99], v[168:171]\n" \
  "s_waitcnt vmcnt(26)\n" \
  "v_mfma_f32_16x16x32_bf16 v[172:175], v[116:119], a[96:99], v[172:175]\n" \
  "s_xor_b32 s47, s54, 36864\n" \
  "buffer_load_dwordx4 v[112:115], %[l16], %[rs], s47 offen sc1\n" \
  "buffer_load_dwordx4 v[116:119], %[l16], %[rs], s47 offen offset:1024 sc1\n" \
  "s_waitcnt vmcnt(27)\n" \
  "v_mfma_f32_16x16x32_bf16 v[160:163], v[120:123], a[44:47], v[160:163]\n" \
  "v_mfma_f32_16x16x32_bf16 v[168:171], v[120:123], a[100:103], v[168:171]\n" \
  "s_waitcnt vmcnt(26)\n" \
  "v_mfma_f32_16x16x32_bf16 v[172:175], v[124:127], a[100:103], v[172:175]\n" \
  "buffer_load_dwordx4 v[120:123], %[l16], %[rs], s47 offen offset:2048 sc1\n" \
  "buffer_load_dwordx4 v[124:127], %[l16], %[rs], s47 offen offset:3072 sc1\n" \
  "s_waitcnt vmcnt(27)\n" \
  "v_mfma_f32_16x16x32_bf16 v[160:163], v[128:131], a[48:51], v[160:163]\n" \
  "v_mfma_f32_16x16x32_bf16 v[168:171], v[128:131], a[104:107], v[168:171]\n" \
  "s_waitcnt vmcnt(26)\n" \
  "v_mfma_f32_16x16x32_bf16 v[172:175], v[132:135], a[104:107], v[172:175]\n" \
  "s_xor_b32 s47, s54, 49152\n" \
  "buffer_load_dwordx4 v[128:131], %[l16], %[rs], s47 offen sc1\n" \
  "buffer_load_dwordx4 v[132:135], %[l16], %[rs], s47 offen offset:1024 sc1\n" \
  "s_waitcnt vmcnt(27)\n" \
  "v_mfma_f32_16x16x32_bf16 v[160:163], v[136:139], a[52:55], v[160:163]\n" \
  "v_mfma_f32_16x16x32_bf16 v[168:171], v[136:139], a[108:111], v[168:171]\n" \
  "s_waitcnt vmcnt(26)\n" \
  "v_mfma_f32_16x16x32_bf16 v[172:175], v[140:143], a[108:111], v[172:175]\n" \
  "buffer_load_dwordx4 v[136:139], %[l16], %[rs], s47 offen offset:2048 sc1\n" \
  "buffer_load_dwordx4 v[140:143], %[l16], %[rs], s47 offen offset:3072 sc1\n" \
  "v_mov_b32 v236, s56\n" \
  "v_mov_b32 v164, s56\n" \
  "v_mov_b32 v165, s56\n" \
  "v_mov_b32 v166, s56\n" \
  "v_mov_b32 v167, s56\n" \
  "v_cndmask_b32 v160, v160, v236, s[58:59]\n" \
  "v_cndmask_b32 v161, v161, v236, s[60:61]\n" \
  "v_cndmask_b32 v162, v162, v236, s[62:63]\n" \
  "v_cndmask_b32 v163, v163, v236, s[64:65]\n" \
  "v_max3_f32 v234, v160, v161, v162\n" \
  "v_max3_f32 v234, v234, v163, v164\n" \
  "v_max3_f32 v234, v234, v165, v166\n" \
  "v_max_f32 v234, v234, v167\n" \
  "v_mov_b32 v235, v234\n" \
  "s_nop 1\n" \
  "v_permlane16_swap_b32 v235, v234\n" \
  "v_max_f32 v234, v234, v235\n" \
  "v_mov_b32 v235, v234\n" \
  "s_nop 1\n" \
  "v_permlane32_swap_b32 v235, v234\n" \
  "v_max_f32 v234, v234, v235\n" \
  "v_sub_f32 v160, v160, v234\n" \
  "v_sub_f32 v161, v161, v234\n" \
  "v_sub_f32 v162, v162, v234\n" \
  "v_sub_f32 v163, v163, v234\n" \
  "v_sub_f32 v164, v164, v234\n" \
  "v_sub_f32 v165, v165, v234\n" \
  "v_sub_f32 v166, v166, v234\n" \
  "v_sub_f32 v167, v167, v234\n" \
  "v_exp_f32 v160, v160\n" \
  "v_exp_f32 v161, v161\n" \
  "v_exp_f32 v162, v162\n" \
  "v_exp_f32 v163, v163\n" \
  "v_exp_f32 v164, v164\n" \
  "v_exp_f32 v165, v165\n" \
  "v_exp_f32 v166, v166\n" \
  "v_exp_f32 v167, v167\n" \
  "v_add_f32 v237, v160, v161\n" \
  "v_add_f32 v237, v237, v162\n" \
  "v_add_f32 v237, v237, v163\n" \
  "v_add_f32 v237, v237, v164\n" \
  "v_add_f32 v237, v237, v165\n" \
  "v_add_f32 v237, v237, v166\n" \
  "v_add_f32 v237, v237, v167\n" \
  "v_mov_b32 v235, v237\n" \
  "s_nop 1\n" \
  "v_permlane16_swap_b32 v235, v237\n" \
  "v_add_f32 v237, v237, v235\n" \
  "v_mov_b32 v235, v237\n" \
  "s_nop 1\n" \
  "v_permlane32_swap_b32 v235, v237\n" \
  "v_add_f32 v237, v237, v235\n" \
  "v_mov_b32 v226, v234\n" \
  "v_mov_b32 v230, v237\n" \
  "v_cndmask_b32 v172, v172, v236, s[58:59]\n" \
  "v_cndmask_b32 v173, v173, v236, s[60:61]\n" \
  "v_cndmask_b32 v174, v174, v236, s[62:63]\n" \
  "v_cndmask_b32 v175, v175, v236, s[64:65]\n" \
  "v_max3_f32 v234, v168, v169, v170\n" \
  "v_max3_f32 v234, v234, v171, v172\n" \
  "v_max3_f32 v234, v234, v173, v174\n" \
  "v_max_f32 v234, v234, v175\n" \
  "v_mov_b32 v235, v234\n" \
  "s_nop 1\n" \
  "v_permlane16_swap_b32 v235, v234\n" \
  "v_max_f32 v234, v234, v235\n" \
  "v_mov_b32 v235, v234\n" \
  "s_nop 1\n" \
  "v_permlane32_swap_b32 v235, v234\n" \
  "v_max_f32 v234, v234, v235\n" \
  "v_sub_f32 v168, v168, v234\n" \
  "v_sub_f32 v169, v169, v234\n" \
  "v_sub_f32 v170, v170, v234\n" \
  "v_sub_f32 v171, v171, v234\n" \
  "v_sub_f32 v172, v172, v234\n" \
  "v_sub_f32 v173, v173, v234\n" \
  "v_sub_f32 v174, v174, v234\n" \
  "v_sub_f32 v175, v175, v234\n" \
  "v_exp_f32 v168, v168\n" \
  "v_exp_f32 v169, v169\n" \
  "v_exp_f32 v170, v170\n" \
  "v_exp_f32 v171, v171\n" \
  "v_exp_f32 v172, v172\n" \
  "v_exp_f32 v173, v173\n" \
  "v_exp_f32 v174, v174\n" \
  "v_exp_f32 v175, v175\n" \
  "v_add_f32 v237, v168, v169\n" \
  "v_add_f32 v237, v237, v170\n" \
  "v_add_f32 v237, v237, v171\n" \
  "v_add_f32 v237, v237, v172\n" \
  "v_add_f32 v237, v237, v173\n" \
  "v_add_f32 v237, v237, v174\n" \
  "v_add_f32 v237, v237, v175\n" \
  "v_mov_b32 v235, v237\n" \
  "s_nop 1\n" \
  "v_permlane16_swap_b32 v235, v237\n" \
  "v_add_f32 v237, v237, v235\n" \
  "v_mov_b32 v235, v237\n" \
  "s_nop 1\n" \
  "v_permlane32_swap_b32 v235, v237\n" \
  "v_add_f32 v237, v237, v235\n" \
  "v_mov_b32 v227, v234\n" \
  "v_mov_b32 v231, v237\n" \
  "s_add_u32 s54, s54, 65536\n" \
  "s_waitcnt vmcnt(27)\n" \
  "v_mfma_f32_16x16x32_bf16 v[176:179], v[32:35], a[0:3], 0\n" \
  "v_mfma_f32_16x16x32_bf16 v[184:187], v[32:35], a[56:59], 0\n" \
  "s_waitcnt vmcnt(26)\n" \
  "v_mfma_f32_16x16x32_bf16 v[188:191], v[36:39], a[56:59], 0\n" \
  "s_xor_b32 s47, s54, 0\n" \
  "buffer_load_dwordx4 v[32:35], %[l16], %[rs], s47 offen sc1\n" \
  "buffer_load_dwordx4 v[36:39], %[l16], %[rs], s47 offen offset:1024 sc1\n" \
  "s_waitcnt vmcnt(27)\n" \
  "v_mfma_f32_16x16x32_bf16 v[176:179], v[40:43], a[4:7], v[176:179]\n" \
  "v_mfma_f32_16x16x32_bf16 v[184:187], v[40:43], a[60:63], v[184:187]\n" \
  "s_waitcnt vmcnt(26)\n" \
  "v_mfma_f32_16x16x32_bf16 v[188:191], v[44:47], a[60:63], v[188:191]\n" \
  "buffer_load_dwordx4 v[40:43], %[l16], %[rs], s47 offen offset:2048 sc1\n" \
  "buffer_load_dwordx4 v[44:47], %[l16], %[rs], s47 offen offset:3072 sc1\n" \
  "s_waitcnt vmcnt(27)\n" \
  "v_mfma_f32_16x16x32_bf16 v[176:179], v[48:51], a[8:11], v[176:179]\n" \
  "v_mfma_f32_16x16x32_bf16 v[184:187], v[48:51], a[64:67], v[184:187]\n" \
  "s_waitcnt vmcnt(26)\n" \
  "v_mfma_f32_16x16x32_bf16 v[188:191], v[52:55], a[64:67], v[188:191]\n" \
  "s_xor_b32 s47, s54, 4096\n" \
  "buffer_load_dwordx4 v[48:51], %[l16], %[rs], s47 offen sc1\n" \
  "buffer_load_dwordx4 v[52:55], %[l16], %[rs], s47 offen offset:1024 sc1\n" \
  "s_waitcnt vmcnt(27)\n" \
  "v_mfma_f32_16x16x32_bf16 v[176:179], v[56:59], a[12:15], v[176:179]\n" \
  "v_mfma_f32_16x16x32_bf16 v[184:187], v[56:59], a[68:71], v[184:187]\n" \
  "s_waitcnt vmcnt(26)\n" \
  "v_mfma_f32_16x16x32_bf16 v[188:191], v[60:63], a[68:71], v[188:191]\n" \
  "buffer_load_dwordx4 v[56:59], %[l16], %[rs], s47 offen offset:2048 sc1\n" \
  "buffer_load_dwordx4 v[60:63], %[l16], %[rs], s47 offen offset:3072 sc1\n" \
  "s_waitcnt vmcnt(27)\n" \
  "v_mfma_f32_16x16x32_bf16 v[176:179], v[64:67], a[16:19], v[176:179]\n" \
  "v_mfma_f32_16x16x32_bf16 v[184:187], v[64:67], a[72:75], v[184:187]\n" \
  "s_waitcnt vmcnt(26)\n" \
  "v_mfma_f32_16x16x32_bf16 v[188:191], v[68:71], a[72:75], v[188:191]\n" \
  "s_xor_b32 s47, s54, 16384\n" \
  "buffer_load_dwordx4 v[64:67], %[l16], %[rs], s47 offen sc1\n" \
  "buffer_load_dwordx4 v[68:71], %[l16], %[rs], s47 offen offset:1024 sc1\n" \
  "s_waitcnt vmcnt(27)\n" \
  "v_mfma_f32_16x16x32_bf16 v[176:179], v[72:75], a[20:23], v[176:179]\n" \
  "v_mfma_f32_16x16x32_bf16 v[184:187], v[72:75], a[76:79], v[184:187]\n" \
  "s_waitcnt vmcnt(26)\n" \
  "v_mfma_f32_16x16x32_bf16 v[188:191], v[76:79], a[76:79], v[188:191]\n" \
  "buffer_load_dwordx4 v[72:75], %[l16], %[rs], s47 offen offset:2048 sc1\n" \
  "buffer_load_dwordx4 v[76:79], %[l16], %[rs], s47 offen offset:3072 sc1\n" \
  "s_waitcnt vmcnt(27)\n" \
  "v_mfma_f32_16x16x32_bf16 v[176:179], v[80:83], a[24:27], v[176:179]\n" \
  "v_mfma_f32_16x16x32_bf16 v[184:187], v[80:83], a[80:83], v[184:187]\n" \
  "s_waitcnt vmcnt(26)\n" \
  "v_mfma_f32_16x16x32_bf16 v[188:191], v[84:87], a[80:83], v[188:191]\n" \
  "s_xor_b32 s47, s54, 20480\n" \
  "buffer_load_dwordx4 v[80:83], %[l16], %[rs], s47 offen sc1\n" \
  "buffer_load_dwordx4 v[84:87], %[l16], %[rs], s47 offen offset:1024 sc1\n" \
  "s_waitcnt vmcnt(27)\n" \
  "v_mfma_f32_16x16x32_bf16 v[176:179], v[88:91], a[28:31], v[176:179]\n" \
  "v_mfma_f32_16x16x32_bf16 v[184:187], v[88:91], a[84:87], v[184:187]\n" \
  "s_waitcnt vmcnt(26)\n" \
  "v_mfma_f32_16x16x32_bf16 v[188:191], v[92:95], a[84:87], v[188:191]\n" \
  "buffer_load_dwordx4 v[88:91], %[l16], %[rs], s47 offen offset:2048 sc1\n" \
  "buffer_load_dwordx4 v[92:95], %[l16], %[rs], s47 offen offset:3072 sc1\n" \
  "s_waitcnt vmcnt(27)\n" \
  "v_mfma_f32_16x16x32_bf16 v[176:179], v[96:99], a[32:35], v[176:179]\n" \
  "v_mfma_f32_16x16x32_bf16 v[184:187], v[96:99], a[88:91], v[184:187]\n" \
  "s_waitcnt vmcnt(26)\n" \
  "v_mfma_f32_16x16x32_bf16 v[188:191], v[100:103], a[88:91], v[188:191]\n" \
  "s_xor_b32 s47, s54, 32768\n" \
  "buffer_load_dwordx4 v[96:99], %[l16], %[rs], s47 offen sc1\n" \
  "buffer_load_dwordx4 v[100:103], %[l16], %[rs], s47 offen offset:1024 sc1\n" \
  "s_waitcnt vmcnt(27)\n" \
  "v_mfma_f32_16x16x32_bf16 v[176:179], v[104:107], a[36:39], v[176:179]\n" \
  "v_mfma_f32_16x16x32_bf16 v[184:187], v[104:107], a[92:95], v[184:187]\n" \
  "s_waitcnt vmcnt(26)\n" \
  "v_mfma_f32_16x16x32_bf16 v[188:191], v[108:111], a[92:95], v[188:191]\n" \
  "buffer_load_dwordx4 v[104:107], %[l16], %[rs], s47 offen offset:2048 sc1\n" \
  "buffer_load_dwordx4 v[108:111], %[l16], %[rs], s47 offen offset:3072 sc1\n" \
  "s_waitcnt vmcnt(27)\n" \
  "v_mfma_f32_16x16x32_bf16 v[176:179], v[112:115], a[40:43], v[176:179]\n" \
  "v_mfma_f32_16x16x32_bf16 v[184:187], v[112:115], a[96:99], v[184:187]\n" \
  "s_waitcnt vmcnt(26)\n" \
  "v_mfma_f32_16x16x32_bf16 v[188:191], v[116:119], a[96:99], v[188:191]\n" \
  "s_xor_b32 s47, s54, 36864\n" \
  "buffer_load_dwordx4 v[112:115], %[l16], %[rs], s47 offen sc1\n" \
  "buffer_load_dwordx4 v[116:119], %[l16], %[rs], s47 offen offset:1024 sc1\n" \
  "s_waitcnt vmcnt(27)\n" \
  "v_mfma_f32_16x16x32_bf16 v[176:179], v[120:123], a[44:47], v[176:179]\n" \
  "v_mfma_f32_16x16x32_bf16 v[184:187], v[120:123], a[100:103], v[184:187]\n" \
  "s_waitcnt vmcnt(26)\n" \
  "v_mfma_f32_16x16x32_bf16 v[188:191], v[124:127], a[100:103], v[188:191]\n" \
  "buffer_load_dwordx4 v[120:123], %[l16], %[rs], s47 offen offset:2048 sc1\n" \
  "buffer_load_dwordx4 v[124:127], %[l16], %[rs], s47 offen offset:3072 sc1\n" \
  "s_waitcnt vmcnt(27)\n" \
  "v_mfma_f32_16x16x32_bf16 v[176:179], v[128:131], a[48:51], v[176:179]\n" \
  "v_mfma_f32_16x16x32_bf16 v[184:187], v[128:131], a[104:107], v[184:187]\n" \
  "s_waitcnt vmcnt(26)\n" \
  "v_mfma_f32_16x16x32_bf16 v[188:191], v[132:135], a[104:107], v[188:191]\n" \
  "s_xor_b32 s47, s54, 49152\n" \
  "buffer_load_dwordx4 v[128:131], %[l16], %[rs], s47 offen sc1\n" \
  "buffer_load_dwordx4 v[132:135], %[l16], %[rs], s47 offen offset:1024 sc1\n" \
  "s_waitcnt vmcnt(27)\n" \
  "v_mfma_f32_16x16x32_bf16 v[176:179], v[136:139], a[52:55], v[176:179]\n" \
  "v_mfma_f32_16x16x32_bf16 v[184:187], v[136:139], a[108:111], v[184:187]\n" \
  "s_waitcnt vmcnt(26)\n" \
  "v_mfma_f32_16x16x32_bf16 v[188:191], v[140:143], a[108:111], v[188:191]\n" \
  "buffer_load_dwordx4 v[136:139], %[l16], %[rs], s47 offen offset:2048 sc1\n" \
  "buffer_load_dwordx4 v[140:143], %[l16], %[rs], s47 offen offset:3072 sc1\n" \
  "v_mov_b32 v236, s56\n" \
  "v_mov_b32 v180, s56\n" \
  "v_mov_b32 v181, s56\n" \
  "v_mov_b32 v182, s56\n" \
  "v_mov_b32 v183, s56\n" \
  "v_cndmask_b32 v176, v176, v236, s[58:59]\n" \
  "v_cndmask_b32 v177, v177, v236, s[60:61]\n" \
  "v_cndmask_b32 v178, v178, v236, s[62:63]\n" \
  "v_cndmask_b32 v179, v179, v236, s[64:65]\n" \
  "v_max3_f32 v234, v176, v177, v178\n" \
  "v_max3_f32 v234, v234, v179, v180\n" \
  "v_max3_f32 v234, v234, v181, v182\n" \
  "v_max_f32 v234, v234, v183\n" \
  "v_mov_b32 v235, v234\n" \
  "s_nop 1\n" \
  "v_permlane16_swap_b32 v235, v234\n" \
  "v_max_f32 v234, v234, v235\n" \
  "v_mov_b32 v235, v234\n" \
  "s_nop 1\n" \
  "v_permlane32_swap_b32 v235, v234\n" \
  "v_max_f32 v234, v234, v235\n" \
  "v_sub_f32 v176, v176, v234\n" \
  "v_sub_f32 v177, v177, v234\n" \
  "v_sub_f32 v178, v178, v234\n" \
  "v_sub_f32 v179, v179, v234\n" \
  "v_sub_f32 v180, v180, v234\n" \
  "v_sub_f32 v181, v181, v234\n" \
  "v_sub_f32 v182, v182, v234\n" \
  "v_sub_f32 v183, v183, v234\n" \
  "v_exp_f32 v176, v176\n" \
  "v_exp_f32 v177, v177\n" \
  "v_exp_f32 v178, v178\n" \
  "v_exp_f32 v179, v179\n" \
  "v_exp_f32 v180, v180\n" \
  "v_exp_f32 v181, v181\n" \
  "v_exp_f32 v182, v182\n" \
  "v_exp_f32 v183, v183\n" \
  "v_add_f32 v237, v176, v177\n" \
  "v_add_f32 v237, v237, v178\n" \
  "v_add_f32 v237, v237, v179\n" \
  "v_add_f32 v237, v237, v180\n" \
  "v_add_f32 v237, v237, v181\n" \
  "v_add_f32 v237, v237, v182\n" \
  "v_add_f32 v237, v237, v183\n" \
  "v_mov_b32 v235, v237\n" \
  "s_nop 1\n" \
  "v_permlane16_swap_b32 v235, v237\n" \
  "v_add_f32 v237, v237, v235\n" \
  "v_mov_b32 v235, v237\n" \
  "s_nop 1\n" \
  "v_permlane32_swap_b32 v235, v237\n" \
  "v_add_f32 v237, v237, v235\n" \
  "s_mov_b32 exec_lo, 0\n" \
  "v_mov_b32 v224, v234\n" \
  "v_mov_b32 v228, v237\n" \
  "s_mov_b32 exec_lo, -1\n" \
  "v_cndmask_b32 v188, v188, v236, s[58:59]\n" \
  "v_cndmask_b32 v189, v189, v236, s[60:61]\n" \
  "v_cndmask_b32 v190, v190, v236, s[62:63]\n" \
  "v_cndmask_b32 v191, v191, v236, s[64:65]\n" \
  "v_max3_f32 v234, v184, v185, v186\n" \
  "v_max3_f32 v234, v234, v187, v188\n" \
  "v_max3_f32 v234, v234, v189, v190\n" \
  "v_max_f32 v234, v234, v191\n" \
  "v_mov_b32 v235, v234\n" \
  "s_nop 1\n" \
  "v_permlane16_swap_b32 v235, v234\n" \
  "v_max_f32 v234, v234, v235\n" \
  "v_mov_b32 v235, v234\n" \
  "s_nop 1\n" \
  "v_permlane32_swap_b32 v235, v234\n" \
  "v_max_f32 v234, v234, v235\n" \
  "v_sub_f32 v184, v184, v234\n" \
  "v_sub_f32 v185, v185, v234\n" \
  "v_sub_f32 v186, v186, v234\n" \
  "v_sub_f32 v187, v187, v234\n" \
  "v_sub_f32 v188, v188, v234\n" \
  "v_sub_f32 v189, v189, v234\n" \
  "v_sub_f32 v190, v190, v234\n" \
  "v_sub_f32 v191, v191, v234\n" \
  "v_exp_f32 v184, v184\n" \
  "v_exp_f32 v185, v185\n" \
  "v_exp_f32 v186, v186\n" \
  "v_exp_f32 v187, v187\n" \
  "v_exp_f32 v188, v188\n" \
  "v_exp_f32 v189, v189\n" \
  "v_exp_f32 v190, v190\n" \
  "v_exp_f32 v191, v191\n" \
  "v_add_f32 v237, v184, v185\n" \
  "v_add_f32 v237, v237, v186\n" \
  "v_add_f32 v237, v237, v187\n" \
  "v_add_f32 v237, v237, v188\n" \
  "v_add_f32 v237, v237, v189\n" \
  "v_add_f32 v237, v237, v190\n" \
  "v_add_f32 v237, v237, v191\n" \
  "v_mov_b32 v235, v237\n" \
  "s_nop 1\n" \
  "v_permlane16_swap_b32 v235, v237\n" \
  "v_add_f32 v237, v237, v235\n" \
  "v_mov_b32 v235, v237\n" \
  "s_nop 1\n" \
  "v_permlane32_swap_b32 v235, v237\n" \
  "v_add_f32 v237, v237, v235\n" \
  "s_mov_b32 exec_lo, 0\n" \
  "v_mov_b32 v225, v234\n" \
  "v_mov_b32 v229, v237\n" \
  "s_mov_b32 exec_lo, -1\n" \
  "s_add_u32 s54, s54, 65536\n" \
  "s_waitcnt vmcnt(27)\n" \
  "v_mfma_f32_16x16x32_bf16 v[192:195], v[32:35], a[0:3], 0\n" \
  "v_mfma_f32_16x16x32_bf16 v[200:203], v[32:35], a[56:59], 0\n" \
  "s_waitcnt vmcnt(26)\n" \
  "v_mfma_f32_16x16x32_bf16 v[204:207], v[36:39], a[56:59], 0\n" \
  "s_waitcnt vmcnt(25)\n" \
  "v_mfma_f32_16x16x32_bf16 v[192:195], v[40:43], a[4:7], v[192:195]\n" \
  "v_mfma_f32_16x16x32_bf16 v[200:203], v[40:43], a[60:63], v[200:203]\n" \
  "s_waitcnt vmcnt(24)\n" \
  "v_mfma_f32_16x16x32_bf16 v[204:207], v[44:47], a[60:63], v[204:207]\n" \
  "s_waitcnt vmcnt(23)\n" \
  "v_mfma_f32_16x16x32_bf16 v[192:195], v[48:51], a[8:11], v[192:195]\n" \
  "v_mfma_f32_16x16x32_bf16 v[200:203], v[48:51], a[64:67], v[200:203]\n" \
  "s_waitcnt vmcnt(22)\n" \
  "v_mfma_f32_16x16x32_bf16 v[204:207], v[52:55], a[64:67], v[204:207]\n" \
  "s_waitcnt vmcnt(21)\n" \
  "v_mfma_f32_16x16x32_bf16 v[192:195], v[56:59], a[12:15], v[192:195]\n" \
  "v_mfma_f32_16x16x32_bf16 v[200:203], v[56:59], a[68:71], v[200:203]\n" \
  "s_waitcnt vmcnt(20)\n" \
  "v_mfma_f32_16x16x32_bf16 v[204:207], v[60:63], a[68:71], v[204:207]\n" \
  "s_waitcnt vmcnt(19)\n" \
  "v_mfma_f32_16x16x32_bf16 v[192:195], v[64:67], a[16:19], v[192:195]\n" \
  "v_mfma_f32_16x16x32_bf16 v[200:203], v[64:67], a[72:75], v[200:203]\n" \
  "s_waitcnt vmcnt(18)\n" \
  "v_mfma_f32_16x16x32_bf16 v[204:207], v[68:71], a[72:75], v[204:207]\n" \
  "s_waitcnt vmcnt(17)\n" \
  "v_mfma_f32_16x16x32_bf16 v[192:195], v[72:75], a[20:23], v[192:195]\n" \
  "v_mfma_f32_16x16x32_bf16 v[200:203], v[72:75], a[76:79], v[200:203]\n" \
  "s_waitcnt vmcnt(16)\n" \
  "v_mfma_f32_16x16x32_bf16 v[204:207], v[76:79], a[76:79], v[204:207]\n" \
  "s_waitcnt vmcnt(15)\n" \
  "v_mfma_f32_16x16x32_bf16 v[192:195], v[80:83], a[24:27], v[192:195]\n" \
  "v_mfma_f32_16x16x32_bf16 v[200:203], v[80:83], a[80:83], v[200:203]\n" \
  "s_waitcnt vmcnt(14)\n" \
  "v_mfma_f32_16x16x32_bf16 v[204:207], v[84:87], a[80:83], v[204:207]\n" \
  "s_waitcnt vmcnt(13)\n" \
  "v_mfma_f32_16x16x32_bf16 v[192:195], v[88:91], a[28:31], v[192:195]\n" \
  "v_mfma_f32_16x16x32_bf16 v[200:203], v[88:91], a[84:87], v[200:203]\n" \
  "s_waitcnt vmcnt(12)\n" \
  "v_mfma_f32_16x16x32_bf16 v[204:207], v[92:95], a[84:87], v[204:207]\n" \
  "s_waitcnt vmcnt(11)\n" \
  "v_mfma_f32_16x16x32_bf16 v[192:195], v[96:99], a[32:35], v[192:195]\n" \
  "v_mfma_f32_16x16x32_bf16 v[200:203], v[96:99], a[88:91], v[200:203]\n" \
  "s_waitcnt vmcnt(10)\n" \
  "v_mfma_f32_16x16x32_bf16 v[204:207], v[100:103], a[88:91], v[204:207]\n" \
  "s_waitcnt vmcnt(9)\n" \
  "v_mfma_f32_16x16x32_bf16 v[192:195], v[104:107], a[36:39], v[192:195]\n" \
  "v_mfma_f32_16x16x32_bf16 v[200:203], v[104:107], a[92:95], v[200:203]\n" \
  "s_waitcnt vmcnt(8)\n" \
  "v_mfma_f32_16x16x32_bf16 v[204:207], v[108:111], a[92:95], v[204:207]\n" \
  "s_waitcnt vmcnt(7)\n" \
  "v_mfma_f32_16x16x32_bf16 v[192:195], v[112:115], a[40:43], v[192:195]\n" \
  "v_mfma_f32_16x16x32_bf16 v[200:203], v[112:115], a[96:99], v[200:203]\n" \
  "s_waitcnt vmcnt(6)\n" \
  "v_mfma_f32_16x16x32_bf16 v[204:207], v[116:119], a[96:99], v[204:207]\n" \
  "s_waitcnt vmcnt(5)\n" \
  "v_mfma_f32_16x16x32_bf16 v[192:195], v[120:123], a[44:47], v[192:195]\n" \
  "v_mfma_f32_16x16x32_bf16 v[200:203], v[120:123], a[100:103], v[200:203]\n" \
  "s_waitcnt vmcnt(4)\n" \
  "v_mfma_f32_16x16x32_bf16 v[204:207], v[124:127], a[100:103], v[204:207]\n" \
  "s_waitcnt vmcnt(3)\n" \
  "v_mfma_f32_16x16x32_bf16 v[192:195], v[128:131], a[48:51], v[192:195]\n" \
  "v_mfma_f32_16x16x32_bf16 v[200:203], v[128:131], a[104:107], v[200:203]\n" \
  "s_waitcnt vmcnt(2)\n" \
  "v_mfma_f32_16x16x32_bf16 v[204:207], v[132:135], a[104:107], v[204:207]\n" \
  "s_waitcnt vmcnt(1)\n" \
  "v_mfma_f32_16x16x32_bf16 v[192:195], v[136:139], a[52:55], v[192:195]\n" \
  "v_mfma_f32_16x16x32_bf16 v[200:203], v[136:139], a[108:111], v[200:203]\n" \
  "s_waitcnt vmcnt(0)\n" \
  "v_mfma_f32_16x16x32_bf16 v[204:207], v[140:143], a[108:111], v[204:207]\n" \
  "v_mov_b32 v236, s56\n" \
  "v_mov_b32 v196, s56\n" \
  "v_mov_b32 v197, s56\n" \
  "v_mov_b32 v198, s56\n" \
  "v_mov_b32 v199, s56\n" \
  "v_cndmask_b32 v192, v192, v236, s[58:59]\n" \
  "v_cndmask_b32 v193, v193, v236, s[60:61]\n" \
  "v_cndmask_b32 v194, v194, v236, s[62:63]\n" \
  "v_cndmask_b32 v195, v195, v236, s[64:65]\n" \
  "v_max3_f32 v234, v192, v193, v194\n" \
  "v_max3_f32 v234, v234, v195, v196\n" \
  "v_max3_f32 v234, v234, v197, v198\n" \
  "v_max_f32 v234, v234, v199\n" \
  "v_mov_b32 v235, v234\n" \
  "s_nop 1\n" \
  "v_permlane16_swap_b32 v235, v234\n" \
  "v_max_f32 v234, v234, v235\n" \
  "v_mov_b32 v235, v234\n" \
  "s_nop 1\n" \
  "v_permlane32_swap_b32 v235, v234\n" \
  "v_max_f32 v234, v234, v235\n" \
  "v_sub_f32 v192, v192, v234\n" \
  "v_sub_f32 v193, v193, v234\n" \
  "v_sub_f32 v194, v194, v234\n" \
  "v_sub_f32 v195, v195, v234\n" \
  "v_sub_f32 v196, v196, v234\n" \
  "v_sub_f32 v197, v197, v234\n" \
  "v_sub_f32 v198, v198, v234\n" \
  "v_sub_f32 v199, v199, v234\n" \
  "v_exp_f32 v192, v192\n" \
  "v_exp_f32 v193, v193\n" \
  "v_exp_f32 v194, v194\n" \
  "v_exp_f32 v195, v195\n" \
  "v_exp_f32 v196, v196\n" \
  "v_exp_f32 v197, v197\n" \
  "v_exp_f32 v198, v198\n" \
  "v_exp_f32 v199, v199\n" \
  "v_add_f32 v237, v192, v193\n" \
  "v_add_f32 v237, v237, v194\n" \
  "v_add_f32 v237, v237, v195\n" \
  "v_add_f32 v237, v237, v196\n" \
  "v_add_f32 v237, v237, v197\n" \
  "v_add_f32 v237, v237, v198\n" \
  "v_add_f32 v237, v237, v199\n" \
  "v_mov_b32 v235, v237\n" \
  "s_nop 1\n" \
  "v_permlane16_swap_b32 v235, v237\n" \
  "v_add_f32 v237, v237, v235\n" \
  "v_mov_b32 v235, v237\n" \
  "s_nop 1\n" \
  "v_permlane32_swap_b32 v235, v237\n" \
  "v_add_f32 v237, v237, v235\n" \
  "s_mov_b32 exec_lo, 0\n" \
  "v_mov_b32 v226, v234\n" \
  "v_mov_b32 v230, v237\n" \
  "s_mov_b32 exec_lo, -1\n" \
  "v_cndmask_b32 v204, v204, v236, s[58:59]\n" \
  "v_cndmask_b32 v205, v205, v236, s[60:61]\n" \
  "v_cndmask_b32 v206, v206, v236, s[62:63]\n" \
  "v_cndmask_b32 v207, v207, v236, s[64:65]\n" \
  "v_max3_f32 v234, v200, v201, v202\n" \
  "v_max3_f32 v234, v234, v203, v204\n" \
  "v_max3_f32 v234, v234, v205, v206\n" \
  "v_max_f32 v234, v234, v207\n" \
  "v_mov_b32 v235, v234\n" \
  "s_nop 1\n" \
  "v_permlane16_swap_b32 v235, v234\n" \
  "v_max_f32 v234, v234, v235\n" \
  "v_mov_b32 v235, v234\n" \
  "s_nop 1\n" \
  "v_permlane32_swap_b32 v235, v234\n" \
  "v_max_f32 v234, v234, v235\n" \
  "v_sub_f32 v200, v200, v234\n" \
  "v_sub_f32 v201, v201, v234\n" \
  "v_sub_f32 v202, v202, v234\n" \
  "v_sub_f32 v203, v203, v234\n" \
  "v_sub_f32 v204, v204, v234\n" \
  "v_sub_f32 v205, v205, v234\n" \
  "v_sub_f32 v206, v206, v234\n" \
  "v_sub_f32 v207, v207, v234\n" \
  "v_exp_f32 v200, v200\n" \
  "v_exp_f32 v201, v201\n" \
  "v_exp_f32 v202, v202\n" \
  "v_exp_f32 v203, v203\n" \
  "v_exp_f32 v204, v204\n" \
  "v_exp_f32 v205, v205\n" \
  "v_exp_f32 v206, v206\n" \
  "v_exp_f32 v207, v207\n" \
  "v_add_f32 v237, v200, v201\n" \
  "v_add_f32 v237, v237, v202\n" \
  "v_add_f32 v237, v237, v203\n" \
  "v_add_f32 v237, v237, v204\n" \
  "v_add_f32 v237, v237, v205\n" \
  "v_add_f32 v237, v237, v206\n" \
  "v_add_f32 v237, v237, v207\n" \
  "v_mov_b32 v235, v237\n" \
  "s_nop 1\n" \
  "v_permlane16_swap_b32 v235, v237\n" \
  "v_add_f32 v237, v237, v235\n" \
  "v_mov_b32 v235, v237\n" \
  "s_nop 1\n" \
  "v_permlane32_swap_b32 v235, v237\n" \
  "v_add_f32 v237, v237, v235\n" \
  "s_mov_b32 exec_lo, 0\n" \
  "v_mov_b32 v227, v234\n" \
  "v_mov_b32 v231, v237\n" \
  "s_mov_b32 exec_lo, -1\n" \
  "s_mov_b32 s44, 262144\n" \
  "s_mov_b32 exec_hi, 0\n" \
  "s_mov_b32 exec_lo, 0xffffffff\n" \
  "s_branch .LF_wv_in%=\n" \
  ".LF_wv%=:\n" \
  "s_sleep 8\n" \
  "s_sub_u32 s44, s44, 1\n" \
  "s_cbranch_scc1 .LF_abort%=\n" \
  ".LF_wv_in%=:\n" \
  "buffer_load_dword v234, %[pVS], %[rs], 0 offen sc1\n" \
  "s_waitcnt vmcnt(0)\n" \
  "v_cmp_gt_i32 vcc, s53, v234\n" \
  "s_cbranch_vccnz .LF_wv%=\n" \
  "s_mov_b64 exec, -1\n" \
  "s_lshl_b32 s49, s52, 12\n" \
  "s_and_b32 s49, s49, 8192\n" \
  "s_xor_b32 s49, s49, s88\n" \
  "s_cmp_lt_u32 s52, 2\n" \
  "s_cselect_b32 s49, 0, s49\n" \
  "s_lshl_b32 s54, s52, 20\n" \
  "s_add_u32 s54, s54, %[sVX]\n" \
  "s_xor_b32 s54, s54, s49\n" \
  "s_xor_b32 s46, s54, 0\n" \
  "buffer_load_dwordx4 a[112:115], %[l16], %[rs], s46 offen sc1\n" \
  "buffer_load_dwordx4 a[116:119], %[l16], %[rs], s46 offen offset:1024 sc1\n" \
  "buffer_load_dwordx4 a[120:123], %[l16], %[rs], s46 offen offset:2048 sc1\n" \
  "buffer_load_dwordx4 a[124:127], %[l16], %[rs], s46 offen offset:3072 sc1\n" \
  "s_xor_b32 s46, s54, 4096\n" \
  "buffer_load_dwordx4 a[128:131], %[l16], %[rs], s46 offen sc1\n" \
  "buffer_load_dwordx4 a[132:135], %[l16], %[rs], s46 offen offset:1024 sc1\n" \
  "buffer_load_dwordx4 a[136:139], %[l16], %[rs], s46 offen offset:2048 sc1\n" \
  "buffer_load_dwordx4 a[140:143], %[l16], %[rs], s46 offen offset:3072 sc1\n" \
  "s_xor_b32 s46, s54, 16384\n" \
  "buffer_load_dwordx4 a[144:147], %[l16], %[rs], s46 offen sc1\n" \
  "buffer_load_dwordx4 a[148:151], %[l16], %[rs], s46 offen offset:1024 sc1\n" \
  "buffer_load_dwordx4 a[152:155], %[l16], %[rs], s46 offen offset:2048 sc1\n" \
  "buffer_load_dwordx4 a[156:159], %[l16], %[rs], s46 offen offset:3072 sc1\n" \
  "s_xor_b32 s46, s54, 20480\n" \
  "buffer_load_dwordx4 a[160:163], %[l16], %[rs], s46 offen sc1\n" \
  "buffer_load_dwordx4 a[164:167], %[l16], %[rs], s46 offen offset:1024 sc1\n" \
  "buffer_load_dwordx4 a[168:171], %[l16], %[rs], s46 offen offset:2048 sc1\n" \
  "buffer_load_dwordx4 a[172:175], %[l16], %[rs], s46 offen offset:3072 sc1\n" \
  "s_add_u32 s54, s54, 32768\n" \
  "s_xor_b32 s46, s54, 0\n" \
  "buffer_load_dwordx4 a[176:179], %[l16b], %[rs], s46 offen sc1\n" \
  "buffer_load_dwordx4 a[180:183], %[l16b], %[rs], s46 offen offset:1024 sc1\n" \
  "buffer_load_dwordx4 a[184:187], %[l16b], %[rs], s46 offen offset:2048 sc1\n" \
  "buffer_load_dwordx4 a[188:191], %[l16b], %[rs], s46 offen offset:3072 sc1\n" \
  "s_xor_b32 s46, s54, 4096\n" \
  "buffer_load_dwordx4 a[192:195], %[l16b], %[rs], s46 offen sc1\n" \
  "buffer_load_dwordx4 a[196:199], %[l16b], %[rs], s46 offen offset:1024 sc1\n" \
  "buffer_load_dwordx4 a[200:203], %[l16b], %[rs], s46 offen offset:2048 sc1\n" \
  "buffer_load_dwordx4 a[204:207], %[l16b], %[rs], s46 offen offset:3072 sc1\n" \
  "s_xor_b32 s46, s54, 16384\n" \
  "buffer_load_dwordx4 a[208:211], %[l16b], %[rs], s46 offen sc1\n" \
  "buffer_load_dwordx4 a[212:215], %[l16b], %[rs], s46 offen offset:1024 sc1\n" \
  "buffer_load_dwordx4 a[216:219], %[l16b], %[rs], s46 offen offset:2048 sc1\n" \
  "buffer_load_dwordx4 a[220:223], %[l16b], %[rs], s46 offen offset:3072 sc1\n" \
  "s_xor_b32 s46, s54, 20480\n" \
  "buffer_load_dwordx4 a[224:227], %[l16b], %[rs], s46 offen sc1\n" \
  "buffer_load_dwordx4 a[228:231], %[l16b], %[rs], s46 offen offset:1024 sc1\n" \
  "buffer_load_dwordx4 a[232:235], %[l16b], %[rs], s46 offen offset:2048 sc1\n" \
  "buffer_load_dwordx4 a[236:239], %[l16b], %[rs], s46 offen offset:3072 sc1\n" \
  "s_lshr_b32 s46, s52, 2\n" \
  "s_lshl_b32 s46, s46, 8\n" \
  "s_add_u32 s46, s46, 0x2912000\n" \
  "v_mov_b32 v235, s46\n" \
  "s_mov_b32 s44, 262144\n" \
  "s_mov_b32 exec_hi, 0\n" \
  "s_mov_b32 exec_lo, 0x1\n" \
  "s_branch .LF_wl_in%=\n" \
  ".LF_wl%=:\n" \
  "s_sleep 8\n" \
  "s_sub_u32 s44, s44, 1\n" \
  "s_cbranch_scc1 .LF_abort%=\n" \
  ".LF_wl_in%=:\n" \
  "buffer_load_dword v234, v235, %[rs], 0 offen sc1\n" \
  "s_waitcnt vmcnt(0)\n" \
  "v_cmp_gt_i32 vcc, 1, v234\n" \
  "s_cbranch_vccnz .LF_wl%=\n" \
  "s_mov_b64 exec, -1\n" \
  "s_lshl_b32 s54, s52, 15\n" \
  "s_add_u32 s54, s54, 0x2500000\n" \
  "s_add_u32 s46, s54, 0\n" \
  "s_add_u32 s47, s46, 16384\n" \
  "buffer_load_dwordx4 v[32:35], %[l16], %[rs], s46 offen sc1\n" \
  "buffer_load_dwordx4 v[36:39], %[l16], %[rs], s47 offen sc1\n" \
  "s_add_u32 s46, %[sWo], 0\n" \
  "s_add_u32 s47, s46, 16384\n" \
  "buffer_load_dwordx4 v[40:43], %[l16], %[rs], s46 offen sc1\n" \
  "buffer_load_dwordx4 v[44:47], %[l16b], %[rs], s47 offen sc1\n" \
  "s_add_u32 s46, s54, 1024\n" \
  "s_add_u32 s47, s46, 16384\n" \
  "buffer_load_dwordx4 v[48:51], %[l16], %[rs], s46 offen sc1\n" \
  "buffer_load_dwordx4 v[52:55], %[l16], %[rs], s47 offen sc1\n" \
  "s_add_u32 s46, %[sWo], 1024\n" \
  "s_add_u32 s47, s46, 16384\n" \
  "buffer_load_dwordx4 v[56:59], %[l16], %[rs], s46 offen sc1\n" \
  "buffer_load_dwordx4 v[60:63], %[l16b], %[rs], s47 offen sc1\n" \
  "s_add_u32 s46, s54, 2048\n" \
  "s_add_u32 s47, s46, 16384\n" \
  "buffer_load_dwordx4 v[64:67], %[l16], %[rs], s46 offen sc1\n" \
  "buffer_load_dwordx4 v[68:71], %[l16], %[rs], s47 offen sc1\n" \
  "s_add_u32 s46, %[sWo], 2048\n" \
  "s_add_u32 s47, s46, 16384\n" \
  "buffer_load_dwordx4 v[72:75], %[l16], %[rs], s46 offen sc1\n" \
  "buffer_load_dwordx4 v[76:79], %[l16b], %[rs], s47 offen sc1\n" \
  "s_add_u32 s46, s54, 3072\n" \
  "s_add_u32 s47, s46, 16384\n" \
  "buffer_load_dwordx4 v[80:83], %[l16], %[rs], s46 offen sc1\n" \
  "buffer_load_dwordx4 v[84:87], %[l16], %[rs], s47 offen sc1\n" \
  "s_add_u32 s46, %[sWo], 3072\n" \
  "s_add_u32 s47, s46, 16384\n" \
  "buffer_load_dwordx4 v[88:91], %[l16], %[rs], s46 offen sc1\n" \
  "buffer_load_dwordx4 v[92:95], %[l16b], %[rs], s47 offen sc1\n" \
  "s_add_u32 s46, s54, 4096\n" \
  "s_add_u32 s47, s46, 16384\n" \
  "buffer_load_dwordx4 v[96:99], %[l16], %[rs], s46 offen sc1\n" \
  "buffer_load_dwordx4 v[100:103], %[l16], %[rs], s47 offen sc1\n" \
  "s_add_u32 s46, %[sWo], 4096\n" \
  "s_add_u32 s47, s46, 16384\n" \
  "buffer_load_dwordx4 v[104:107], %[l16], %[rs], s46 offen sc1\n" \
  "buffer_load_dwordx4 v[108:111], %[l16b], %[rs], s47 offen sc1\n" \
  "s_add_u32 s46, s54, 5120\n" \
  "s_add_u32 s47, s46, 16384\n" \
  "buffer_load_dwordx4 v[112:115], %[l16], %[rs], s46 offen sc1\n" \
  "buffer_load_dwordx4 v[116:119], %[l16], %[rs], s47 offen sc1\n" \
  "s_add_u32 s46, %[sWo], 5120\n" \
  "s_add_u32 s47, s46, 16384\n" \
  "buffer_load_dwordx4 v[120:123], %[l16], %[rs], s46 offen sc1\n" \
  "buffer_load_dwordx4 v[124:127], %[l16b], %[rs], s47 offen sc1\n" \
  "s_add_u32 s46, s54, 6144\n" \
  "s_add_u32 s47, s46, 16384\n" \
  "buffer_load_dwordx4 v[128:131], %[l16], %[rs], s46 offen sc1\n" \
  "buffer_load_dwordx4 v[132:135], %[l16], %[rs], s47 offen sc1\n" \
  "s_add_u32 s46, %[sWo], 6144\n" \
  "s_add_u32 s47, s46, 16384\n" \
  "buffer_load_dwordx4 v[136:139], %[l16], %[rs], s46 offen sc1\n" \
  "buffer_load_dwordx4 v[140:143], %[l16b], %[rs], s47 offen sc1\n" \
  "s_waitcnt vmcnt(25)\n" \
  "v_mfma_f32_16x16x32_bf16 v[240:243], v[40:43], v[32:35], 0\n" \
  "v_mfma_f32_16x16x32_bf16 v[244:247], v[40:43], v[36:39], 0\n" \
  "s_waitcnt vmcnt(24)\n" \
  "v_mfma_f32_16x16x32_bf16 v[248:251], v[44:47], v[32:35], 0\n" \
  "v_mfma_f32_16x16x32_bf16 v[252:255], v[44:47], v[36:39], 0\n" \
  "s_add_u32 s46, s54, 7168\n" \
  "s_add_u32 s47, s46, 16384\n" \
  "buffer_load_dwordx4 v[32:35], %[l16], %[rs], s46 offen sc1\n" \
  "buffer_load_dwordx4 v[36:39], %[l16], %[rs], s47 offen sc1\n" \
  "s_add_u32 s46, %[sWo], 7168\n" \
  "s_add_u32 s47, s46, 16384\n" \
  "buffer_load_dwordx4 v[40:43], %[l16], %[rs], s46 offen sc1\n" \
  "buffer_load_dwordx4 v[44:47], %[l16b], %[rs], s47 offen sc1\n" \
  "s_waitcnt vmcnt(25)\n" \
  "v_mfma_f32_16x16x32_bf16 v[240:243], v[56:59], v[48:51], v[240:243]\n" \
  "v_mfma_f32_16x16x32_bf16 v[244:247], v[56:59], v[52:55], v[244:247]\n" \
  "s_waitcnt vmcnt(24)\n" \
  "v_mfma_f32_16x16x32_bf16 v[248:251], v[60:63], v[48:51], v[248:251]\n" \
  "v_mfma_f32_16x16x32_bf16 v[252:255], v[60:63], v[52:55], v[252:255]\n" \
  "s_add_u32 s46, s54, 8192\n" \
  "s_add_u32 s47, s46, 16384\n" \
  "buffer_load_dwordx4 v[48:51], %[l16], %[rs], s46 offen sc1\n" \
  "buffer_load_dwordx4 v[52:55], %[l16], %[rs], s47 offen sc1\n" \
  "s_add_u32 s46, %[sWo], 8192\n" \
  "s_add_u32 s47, s46, 16384\n" \
  "buffer_load_dwordx4 v[56:59], %[l16], %[rs], s46 offen sc1\n" \
  "buffer_load_dwordx4 v[60:63], %[l16b], %[rs], s47 offen sc1\n" \
  "s_waitcnt vmcnt(25)\n" \
  "v_mfma_f32_16x16x32_bf16 v[240:243], v[72:75], v[64:67], v[240:243]\n" \
  "v_mfma_f32_16x16x32_bf16 v[244:247], v[72:75], v[68:71], v[244:247]\n" \
  "s_waitcnt vmcnt(24)\n" \
  "v_mfma_f32_16x16x32_bf16 v[248:251], v[76:79], v[64:67], v[248:251]\n" \
  "v_mfma_f32_16x16x32_bf16 v[252:255], v[76:79], v[68:71], v[252:255]\n" \
  "s_add_u32 s46, s54, 9216\n" \
  "s_add_u32 s47, s46, 16384\n" \
  "buffer_load_dwordx4 v[64:67], %[l16], %[rs], s46 offen sc1\n" \
  "buffer_load_dwordx4 v[68:71], %[l16], %[rs], s47 offen sc1\n" \
  "s_add_u32 s46, %[sWo], 9216\n" \
  "s_add_u32 s47, s46, 16384\n" \
  "buffer_load_dwordx4 v[72:75], %[l16], %[rs], s46 offen sc1\n" \
  "buffer_load_dwordx4 v[76:79], %[l16b], %[rs], s47 offen sc1\n" \
  "s_waitcnt vmcnt(25)\n" \
  "v_mfma_f32_16x16x32_bf16 v[240:243], v[88:91], v[80:83], v[240:243]\n" \
  "v_mfma_f32_16x16x32_bf16 v[244:247], v[88:91], v[84:87], v[244:247]\n" \
  "s_waitcnt vmcnt(24)\n" \
  "v_mfma_f32_16x16x32_bf16 v[248:251], v[92:95], v[80:83], v[248:251]\n" \
  "v_mfma_f32_16x16x32_bf16 v[252:255], v[92:95], v[84:87], v[252:255]\n" \
  "s_add_u32 s46, s54, 10240\n" \
  "s_add_u32 s47, s46, 16384\n" \
  "buffer_load_dwordx4 v[80:83], %[l16], %[rs], s46 offen sc1\n" \
  "buffer_load_dwordx4 v[84:87], %[l16], %[rs], s47 offen sc1\n" \
  "s_add_u32 s46, %[sWo], 10240\n" \
  "s_add_u32 s47, s46, 16384\n" \
  "buffer_load_dwordx4 v[88:91], %[l16], %[rs], s46 offen sc1\n" \
  "buffer_load_dwordx4 v[92:95], %[l16b], %[rs], s47 offen sc1\n" \
  "s_waitcnt vmcnt(25)\n" \
  "v_mfma_f32_16x16x32_bf16 v[240:243], v[104:107], v[96:99], v[240:243]\n" \
  "v_mfma_f32_16x16x32_bf16 v[244:247], v[104:107], v[100:103], v[244:247]\n" \
  "s_waitcnt vmcnt(24)\n" \
  "v_mfma_f32_16x16x32_bf16 v[248:251], v[108:111], v[96:99], v[248:251]\n" \
  "v_mfma_f32_16x16x32_bf16 v[252:255], v[108:111], v[100:103], v[252:255]\n" \
  "s_add_u32 s46, s54, 11264\n" \
  "s_add_u32 s47, s46, 16384\n" \
  "buffer_load_dwordx4 v[96:99], %[l16], %[rs], s46 offen sc1\n" \
  "buffer_load_dwordx4 v[100:103], %[l16], %[rs], s47 offen sc1\n" \
  "s_add_u32 s46, %[sWo], 11264\n" \
  "s_add_u32 s47, s46, 16384\n" \
  "buffer_load_dwordx4 v[104:107], %[l16], %[rs], s46 offen sc1\n" \
  "buffer_load_dwordx4 v[108:111], %[l16b], %[rs], s47 offen sc1\n" \
  "s_waitcnt vmcnt(25)\n" \
  "v_mfma_f32_16x16x32_bf16 v[240:243], v[120:123], v[112:115], v[240:243]\n" \
  "v_mfma_f32_16x16x32_bf16 v[244:247], v[120:123], v[116:119], v[244:247]\n" \
  "s_waitcnt vmcnt(24)\n" \
  "v_mfma_f32_16x16x32_bf16 v[248:251], v[124:127], v[112:115], v[248:251]\n" \
  "v_mfma_f32_16x16x32_bf16 v[252:255], v[124:127], v[116:119], v[252:255]\n" \
  "s_add_u32 s46, s54, 12288\n" \
  "s_add_u32 s47, s46, 16384\n" \
  "buffer_load_dwordx4 v[112:115], %[l16], %[rs], s46 offen sc1\n" \
  "buffer_load_dwordx4 v[116:119], %[l16], %[rs], s47 offen sc1\n" \
  "s_add_u32 s46, %[sWo], 12288\n" \
  "s_add_u32 s47, s46, 16384\n" \
  "buffer_load_dwordx4 v[120:123], %[l16], %[rs], s46 offen sc1\n" \
  "buffer_load_dwordx4 v[124:127], %[l16b], %[rs], s47 offen sc1\n" \
  "s_waitcnt vmcnt(25)\n" \
  "v_mfma_f32_16x16x32_bf16 v[240:243], v[136:139], v[128:131], v[240:243]\n" \
  "v_mfma_f32_16x16x32_bf16 v[244:247], v[136:139], v[132:135], v[244:247]\n" \
  "s_waitcnt vmcnt(24)\n" \
  "v_mfma_f32_16x16x32_bf16 v[248:251], v[140:143], v[128:131], v[248:251]\n" \
  "v_mfma_f32_16x16x32_bf16 v[252:255], v[140:143], v[132:135], v[252:255]\n" \
  "s_add_u32 s46, s54, 13312\n" \
  "s_add_u32 s47, s46, 16384\n" \
  "buffer_load_dwordx4 v[128:131], %[l16], %[rs], s46 offen sc1\n" \
  "buffer_load_dwordx4 v[132:135], %[l16], %[rs], s47 offen sc1\n" \
  "s_add_u32 s46, %[sWo], 13312\n" \
  "s_add_u32 s47, s46, 16384\n" \
  "buffer_load_dwordx4 v[136:139], %[l16], %[rs], s46 offen sc1\n" \
  "buffer_load_dwordx4 v[140:143], %[l16b], %[rs], s47 offen sc1\n" \
  "s_waitcnt vmcnt(25)\n" \
  "v_mfma_f32_16x16x32_bf16 v[240:243], v[40:43], v[32:35], v[240:243]\n" \
  "v_mfma_f32_16x16x32_bf16 v[244:247], v[40:43], v[36:39], v[244:247]\n" \
  "s_waitcnt vmcnt(24)\n" \
  "v_mfma_f32_16x16x32_bf16 v[248:251], v[44:47], v[32:35], v[248:251]\n" \
  "v_mfma_f32_16x16x32_bf16 v[252:255], v[44:47], v[36:39], v[252:255]\n" \
  "s_add_u32 s46, s54, 14336\n" \
  "s_add_u32 s47, s46, 16384\n" \
  "buffer_load_dwordx4 v[32:35], %[l16], %[rs], s46 offen sc1\n" \
  "buffer_load_dwordx4 v[36:39], %[l16], %[rs], s47 offen sc1\n" \
  "s_add_u32 s46, %[sWo], 14336\n" \
  "s_add_u32 s47, s46, 16384\n" \
  "buffer_load_dwordx4 v[40:43], %[l16], %[rs], s46 offen sc1\n" \
  "buffer_load_dwordx4 v[44:47], %[l16b], %[rs], s47 offen sc1\n" \
  "s_waitcnt vmcnt(25)\n" \
  "v_mfma_f32_16x16x32_bf16 v[240:243], v[56:59], v[48:51], v[240:243]\n" \
  "v_mfma_f32_16x16x32_bf16 v[244:247], v[56:59], v[52:55], v[244:247]\n" \
  "s_waitcnt vmcnt(24)\n" \
  "v_mfma_f32_16x16x32_bf16 v[248:251], v[60:63], v[48:51], v[248:251]\n" \
  "v_mfma_f32_16x16x32_bf16 v[252:255], v[60:63], v[52:55], v[252:255]\n" \
  "s_add_u32 s46, s54, 15360\n" \
  "s_add_u32 s47, s46, 16384\n" \
  "buffer_load_dwordx4 v[48:51], %[l16], %[rs], s46 offen sc1\n" \
  "buffer_load_dwordx4 v[52:55], %[l16], %[rs], s47 offen sc1\n" \
  "s_add_u32 s46, %[sWo], 15360\n" \
  "s_add_u32 s47, s46, 16384\n" \
  "buffer_load_dwordx4 v[56:59], %[l16], %[rs], s46 offen sc1\n" \
  "buffer_load_dwordx4 v[60:63], %[l16b], %[rs], s47 offen sc1\n" \
  "s_waitcnt vmcnt(25)\n" \
  "v_mfma_f32_16x16x32_bf16 v[240:243], v[72:75], v[64:67], v[240:243]\n" \
  "v_mfma_f32_16x16x32_bf16 v[244:247], v[72:75], v[68:71], v[244:247]\n" \
  "s_waitcnt vmcnt(24)\n" \
  "v_mfma_f32_16x16x32_bf16 v[248:251], v[76:79], v[64:67], v[248:251]\n" \
  "v_mfma_f32_16x16x32_bf16 v[252:255], v[76:79], v[68:71], v[252:255]\n" \
  "s_waitcnt vmcnt(21)\n" \
  "v_mfma_f32_16x16x32_bf16 v[240:243], v[88:91], v[80:83], v[240:243]\n" \
  "v_mfma_f32_16x16x32_bf16 v[244:247], v[88:91], v[84:87], v[244:247]\n" \
  "s_waitcnt vmcnt(20)\n" \
  "v_mfma_f32_16x16x32_bf16 v[248:251], v[92:95], v[80:83], v[248:251]\n" \
  "v_mfma_f32_16x16x32_bf16 v[252:255], v[92:95], v[84:87], v[252:255]\n" \
  "s_waitcnt vmcnt(17)\n" \
  "v_mfma_f32_16x16x32_bf16 v[240:243], v[104:107], v[96:99], v[240:243]\n" \
  "v_mfma_f32_16x16x32_bf16 v[244:247], v[104:107], v[100:103], v[244:247]\n" \
  "s_waitcnt vmcnt(16)\n" \
  "v_mfma_f32_16x16x32_bf16 v[248:251], v[108:111], v[96:99], v[248:251]\n" \
  "v_mfma_f32_16x16x32_bf16 v[252:255], v[108:111], v[100:103], v[252:255]\n" \
  "s_waitcnt vmcnt(13)\n" \
  "v_mfma_f32_16x16x32_bf16 v[240:243], v[120:123], v[112:115], v[240:243]\n" \
  "v_mfma_f32_16x16x32_bf16 v[244:247], v[120:123], v[116:119], v[244:247]\n" \
  "s_waitcnt vmcnt(12)\n" \
  "v_mfma_f32_16x16x32_bf16 v[248:251], v[124:127], v[112:115], v[248:251]\n" \
  "v_mfma_f32_16x16x32_bf16 v[252:255], v[124:127], v[116:119], v[252:255]\n" \
  "s_waitcnt vmcnt(9)\n" \
  "v_mfma_f32_16x16x32_bf16 v[240:243], v[136:139], v[128:131], v[240:243]\n" \
  "v_mfma_f32_16x16x32_bf16 v[244:247], v[136:139], v[132:135], v[244:247]\n" \
  "s_waitcnt vmcnt(8)\n" \
  "v_mfma_f32_16x16x32_bf16 v[248:251], v[140:143], v[128:131], v[248:251]\n" \
  "v_mfma_f32_16x16x32_bf16 v[252:255], v[140:143], v[132:135], v[252:255]\n" \
  "s_waitcnt vmcnt(5)\n" \
  "v_mfma_f32_16x16x32_bf16 v[240:243], v[40:43], v[32:35], v[240:243]\n" \
  "v_mfma_f32_16x16x32_bf16 v[244:247], v[40:43], v[36:39], v[244:247]\n" \
  "s_waitcnt vmcnt(4)\n" \
  "v_mfma_f32_16x16x32_bf16 v[248:251], v[44:47], v[32:35], v[248:251]\n" \
  "v_mfma_f32_16x16x32_bf16 v[252:255], v[44:47], v[36:39], v[252:255]\n" \
  "s_waitcnt vmcnt(1)\n" \
  "v_mfma_f32_16x16x32_bf16 v[240:243], v[56:59], v[48:51], v[240:243]\n" \
  "v_mfma_f32_16x16x32_bf16 v[244:247], v[56:59], v[52:55], v[244:247]\n" \
  "s_waitcnt vmcnt(0)\n" \
  "v_mfma_f32_16x16x32_bf16 v[248:251], v[60:63], v[48:51], v[248:251]\n" \
  "v_mfma_f32_16x16x32_bf16 v[252:255], v[60:63], v[52:55], v[252:255]\n" \
  "s_nop 3\n" \
  "v_mul_f32 v240, v240, v232\n" \
  "v_mul_f32 v241, v241, v232\n" \
  "v_mul_f32 v242, v242, v232\n" \
  "v_mul_f32 v243, v243, v232\n" \
  "v_mul_f32 v248, v248, v232\n" \
  "v_mul_f32 v249, v249, v232\n" \
  "v_mul_f32 v250, v250, v232\n" \
  "v_mul_f32 v251, v251, v232\n" \
  "v_sub_f32 v232, 1.0, v232\n" \
  "v_mul_f32 v244, v244, v233\n" \
  "v_mul_f32 v245, v245, v233\n" \
  "v_mul_f32 v246, v246, v233\n" \
  "v_mul_f32 v247, v247, v233\n" \
  "v_mul_f32 v252, v252, v233\n" \
  "v_mul_f32 v253, v253, v233\n" \
  "v_mul_f32 v254, v254, v233\n" \
  "v_mul_f32 v255, v255, v233\n" \
  "v_sub_f32 v233, 1.0, v233\n" \
  "s_mul_i32 s54, s52, 114688\n" \
  "s_add_u32 s48, s54, %[sU]\n" \
  "s_add_u32 s54, s54, %[sKM]\n" \
  "s_sub_u32 s67, s52, 1\n" \
  "s_mov_b32 s44, 262144\n" \
  "s_mov_b32 exec_hi, 0\n" \
  "s_mov_b32 exec_lo, 0x1\n" \
  "s_branch .LF_wa0_in%=\n" \
  ".LF_wa0%=:\n" \
  "s_sleep 16\n" \
  "s_sub_u32 s44, s44, 1\n" \
  "s_cbranch_scc1 .LF_abort%=\n" \
  ".LF_wa0_in%=:\n" \
  "buffer_load_dword v234, %[pA], %[rs], 0 offen sc1\n" \
  "s_waitcnt vmcnt(0)\n" \
  "v_cmp_gt_i32 vcc, s67, v234\n" \
  "s_cbranch_vccnz .LF_wa0%=\n" \
  "s_mov_b64 exec, -1\n" \
  "s_min_u32 s67, s53, 63\n" \
  "s_cmp_eq_u32 s67, s53\n" \
  "s_cselect_b32 s66, 1, 0\n" \
  "s_mov_b32 s44, 262144\n" \
  "s_mov_b32 exec_hi, 0\n" \
  "s_mov_b32 exec_lo, 0x3ffff\n" \
  "s_branch .LF_wa_in%=\n" \
  ".LF_wa%=:\n" \
  "s_sleep 1\n" \
  "s_sub_u32 s44, s44, 1\n" \
  "s_cbranch_scc1 .LF_abort%=\n" \
  ".LF_wa_in%=:\n" \
  "buffer_load_dword v234, %[pA], %[rs], 0 offen sc1\n" \
  "s_waitcnt vmcnt(0)\n" \
  "v_cmp_gt_i32 vcc, s67, v234\n" \
  "s_cbranch_vccnz .LF_wa%=\n" \
  "s_mov_b64 exec, -1\n" \
  "s_add_u32 s46, s54, 0\n" \
  "buffer_load_dwordx4 v[32:35], %[l16], %[rs], s46 offen sc1\n" \
  "buffer_load_dwordx4 v[36:39], %[l16], %[rs], s46 offen offset:1024 sc1\n" \
  "buffer_load_dwordx4 v[40:43], %[l16], %[rs], s46 offen offset:2048 sc1\n" \
  "buffer_load_dwordx4 v[44:47], %[l16], %[rs], s46 offen offset:3072 sc1\n" \
  "s_add_u32 s46, s54, 4096\n" \
  "buffer_load_dwordx4 v[48:51], %[l16], %[rs], s46 offen sc1\n" \
  "buffer_load_dwordx4 v[52:55], %[l16], %[rs], s46 offen offset:1024 sc1\n" \
  "buffer_load_dwordx4 v[56:59], %[l16], %[rs], s46 offen offset:2048 sc1\n" \
  "buffer_load_dwordx4 v[60:63], %[l16], %[rs], s46 offen offset:3072 sc1\n" \
  "s_add_u32 s46, s54, 8192\n" \
  "buffer_load_dwordx4 v[64:67], %[l16], %[rs], s46 offen sc1\n" \
  "buffer_load_dwordx4 v[68:71], %[l16], %[rs], s46 offen offset:1024 sc1\n" \
  "buffer_load_dwordx4 v[72:75], %[l16], %[rs], s46 offen offset:2048 sc1\n" \
  "buffer_load_dwordx4 v[76:79], %[l16], %[rs], s46 offen offset:3072 sc1\n" \
  "s_add_u32 s46, s54, 12288\n" \
  "buffer_load_dwordx4 v[80:83], %[l16], %[rs], s46 offen sc1\n" \
  "buffer_load_dwordx4 v[84:87], %[l16], %[rs], s46 offen offset:1024 sc1\n" \
  "buffer_load_dwordx4 v[88:91], %[l16], %[rs], s46 offen offset:2048 sc1\n" \
  "buffer_load_dwordx4 v[92:95], %[l16], %[rs], s46 offen offset:3072 sc1\n" \
  "s_add_u32 s46, s54, 16384\n" \
  "buffer_load_dwordx4 v[96:99], %[l16], %[rs], s46 offen sc1\n" \
  "buffer_load_dwordx4 v[100:103], %[l16], %[rs], s46 offen offset:1024 sc1\n" \
  "buffer_load_dwordx4 v[104:107], %[l16], %[rs], s46 offen offset:2048 sc1\n" \
  "buffer_load_dwordx4 v[108:111], %[l16], %[rs], s46 offen offset:3072 sc1\n" \
  "s_add_u32 s46, s54, 20480\n" \
  "buffer_load_dwordx4 v[112:115], %[l16], %[rs], s46 offen sc1\n" \
  "buffer_load_dwordx4 v[116:119], %[l16], %[rs], s46 offen offset:1024 sc1\n" \
  "buffer_load_dwordx4 v[120:123], %[l16], %[rs], s46 offen offset:2048 sc1\n" \
  "buffer_load_dwordx4 v[124:127], %[l16], %[rs], s46 offen offset:3072 sc1\n" \
  "s_add_u32 s46, s54, 24576\n" \
  "buffer_load_dwordx4 v[128:131], %[l16], %[rs], s46 offen sc1\n" \
  "buffer_load_dwordx4 v[132:135], %[l16], %[rs], s46 offen offset:1024 sc1\n" \
  "buffer_load_dwordx4 v[136:139], %[l16], %[rs], s46 offen offset:2048 sc1\n" \
  "buffer_load_dwordx4 v[140:143], %[l16], %[rs], s46 offen offset:3072 sc1\n" \
  "s_waitcnt vmcnt(27)\n" \
  "v_mfma_f32_16x16x32_bf16 v[208:211], v[32:35], a[0:3], 0\n" \
  "v_mfma_f32_16x16x32_bf16 v[212:215], v[32:35], a[56:59], 0\n" \
  "s_waitcnt vmcnt(26)\n" \
  "v_mfma_f32_16x16x32_bf16 v[208:211], v[36:39], a[4:7], v[208:211]\n" \
  "v_mfma_f32_16x16x32_bf16 v[212:215], v[36:39], a[60:63], v[212:215]\n" \
  "s_waitcnt vmcnt(25)\n" \
  "v_mfma_f32_16x16x32_bf16 v[208:211], v[40:43], a[8:11], v[208:211]\n" \
  "v_mfma_f32_16x16x32_bf16 v[212:215], v[40:43], a[64:67], v[212:215]\n" \
  "s_waitcnt vmcnt(24)\n" \
  "v_mfma_f32_16x16x32_bf16 v[208:211], v[44:47], a[12:15], v[208:211]\n" \
  "v_mfma_f32_16x16x32_bf16 v[212:215], v[44:47], a[68:71], v[212:215]\n" \
  "s_waitcnt vmcnt(23)\n" \
  "v_mfma_f32_16x16x32_bf16 v[208:211], v[48:51], a[16:19], v[208:211]\n" \
  "v_mfma_f32_16x16x32_bf16 v[212:215], v[48:51], a[72:75], v[212:215]\n" \
  "s_waitcnt vmcnt(22)\n" \
  "v_mfma_f32_16x16x32_bf16 v[208:211], v[52:55], a[20:23], v[208:211]\n" \
  "v_mfma_f32_16x16x32_bf16 v[212:215], v[52:55], a[76:79], v[212:215]\n" \
  "s_waitcnt vmcnt(21)\n" \
  "v_mfma_f32_16x16x32_bf16 v[208:211], v[56:59], a[24:27], v[208:211]\n" \
  "v_mfma_f32_16x16x32_bf16 v[212:215], v[56:59], a[80:83], v[212:215]\n" \
  "s_waitcnt vmcnt(20)\n" \
  "v_mfma_f32_16x16x32_bf16 v[208:211], v[60:63], a[28:31], v[208:211]\n" \
  "v_mfma_f32_16x16x32_bf16 v[212:215], v[60:63], a[84:87], v[212:215]\n" \
  "s_waitcnt vmcnt(19)\n" \
  "v_mfma_f32_16x16x32_bf16 v[208:211], v[64:67], a[32:35], v[208:211]\n" \
  "v_mfma_f32_16x16x32_bf16 v[212:215], v[64:67], a[88:91], v[212:215]\n" \
  "s_waitcnt vmcnt(18)\n" \
  "v_mfma_f32_16x16x32_bf16 v[208:211], v[68:71], a[36:39], v[208:211]\n" \
  "v_mfma_f32_16x16x32_bf16 v[212:215], v[68:71], a[92:95], v[212:215]\n" \
  "s_waitcnt vmcnt(17)\n" \
  "v_mfma_f32_16x16x32_bf16 v[208:211], v[72:75], a[40:43], v[208:211]\n" \
  "v_mfma_f32_16x16x32_bf16 v[212:215], v[72:75], a[96:99], v[212:215]\n" \
  "s_waitcnt vmcnt(16)\n" \
  "v_mfma_f32_16x16x32_bf16 v[208:211], v[76:79], a[44:47], v[208:211]\n" \
  "v_mfma_f32_16x16x32_bf16 v[212:215], v[76:79], a[100:103], v[212:215]\n" \
  "s_waitcnt vmcnt(15)\n" \
  "v_mfma_f32_16x16x32_bf16 v[208:211], v[80:83], a[48:51], v[208:211]\n" \
  "v_mfma_f32_16x16x32_bf16 v[212:215], v[80:83], a[104:107], v[212:215]\n" \
  "s_waitcnt vmcnt(14)\n" \
  "v_mfma_f32_16x16x32_bf16 v[208:211], v[84:87], a[52:55], v[208:211]\n" \
  "v_mfma_f32_16x16x32_bf16 v[212:215], v[84:87], a[108:111], v[212:215]\n" \
  "s_waitcnt vmcnt(13)\n" \
  "v_mfma_f32_16x16x32_bf16 v[216:219], v[88:91], a[0:3], 0\n" \
  "v_mfma_f32_16x16x32_bf16 v[220:223], v[88:91], a[56:59], 0\n" \
  "s_waitcnt vmcnt(12)\n" \
  "v_mfma_f32_16x16x32_bf16 v[216:219], v[92:95], a[4:7], v[216:219]\n" \
  "v_mfma_f32_16x16x32_bf16 v[220:223], v[92:95], a[60:63], v[220:223]\n" \
  "s_cmp_eq_u32 s66, 0\n" \
  "s_cbranch_scc1 .LF_v0%=\n" \
  ".LF_v0b%=:\n" \
  "s_add_u32 s46, s48, 4096\n" \
  "buffer_load_dwordx4 v[32:35], %[l16], %[rs], s48 offen sc1\n" \
  "buffer_load_dwordx4 v[36:39], %[l16], %[rs], s48 offen offset:1024 sc1\n" \
  "buffer_load_dwordx4 v[40:43], %[l16], %[rs], s48 offen offset:2048 sc1\n" \
  "buffer_load_dwordx4 v[44:47], %[l16], %[rs], s48 offen offset:3072 sc1\n" \
  "buffer_load_dwordx4 v[48:51], %[l16b], %[rs], s46 offen sc1\n" \
  "buffer_load_dwordx4 v[52:55], %[l16b], %[rs], s46 offen offset:1024 sc1\n" \
  "buffer_load_dwordx4 v[56:59], %[l16b], %[rs], s46 offen offset:2048 sc1\n" \
  "buffer_load_dwordx4 v[60:63], %[l16b], %[rs], s46 offen offset:3072 sc1\n" \
  "s_waitcnt vmcnt(19)\n" \
  "v_mfma_f32_16x16x32_bf16 v[216:219], v[96:99], a[8:11], v[216:219]\n" \
  "v_max3_f32 v64, v208, v209, v210\n" \
  "v_max3_f32 v68, v212, v213, v214\n" \
  "v_max3_f32 v64, v64, v211, v224\n" \
  "v_max3_f32 v68, v68, v215, v225\n" \
  "v_mov_b32 v65, v64\n" \
  "v_mfma_f32_16x16x32_bf16 v[220:223], v[96:99], a[64:67], v[220:223]\n" \
  "v_mov_b32 v69, v68\n" \
  "v_permlane16_swap_b32 v65, v64\n" \
  "s_nop 0\n" \
  "v_permlane16_swap_b32 v69, v68\n" \
  "v_max_f32 v64, v64, v65\n" \
  "v_max_f32 v68, v68, v69\n" \
  "s_waitcnt vmcnt(18)\n" \
  "v_mfma_f32_16x16x32_bf16 v[216:219], v[100:103], a[12:15], v[216:219]\n" \
  "v_sub_f32 v208, v208, v64\n" \
  "v_sub_f32 v212, v212, v68\n" \
  "v_sub_f32 v209, v209, v64\n" \
  "v_sub_f32 v213, v213, v68\n" \
  "v_sub_f32 v210, v210, v64\n" \
  "v_mfma_f32_16x16x32_bf16 v[220:223], v[100:103], a[68:71], v[220:223]\n" \
  "v_sub_f32 v214, v214, v68\n" \
  "v_sub_f32 v211, v211, v64\n" \
  "v_sub_f32 v215, v215, v68\n" \
  "v_sub_f32 v224, v224, v64\n" \
  "v_sub_f32 v225, v225, v68\n" \
  "s_waitcnt vmcnt(17)\n" \
  "v_mfma_f32_16x16x32_bf16 v[216:219], v[104:107], a[16:19], v[216:219]\n" \
  "v_exp_f32 v208, v208\n" \
  "v_exp_f32 v212, v212\n" \
  "v_exp_f32 v209, v209\n" \
  "v_exp_f32 v213, v213\n" \
  "v_exp_f32 v210, v210\n" \
  "v_mfma_f32_16x16x32_bf16 v[220:223], v[104:107], a[72:75], v[220:223]\n" \
  "v_exp_f32 v214, v214\n" \
  "v_exp_f32 v211, v211\n" \
  "v_exp_f32 v215, v215\n" \
  "v_exp_f32 v224, v224\n" \
  "v_exp_f32 v225, v225\n" \
  "s_waitcnt vmcnt(16)\n" \
  "v_mfma_f32_16x16x32_bf16 v[216:219], v[108:111], a[20:23], v[216:219]\n" \
  "v_add_f32 v66, v208, v210\n" \
  "v_add_f32 v70, v212, v214\n" \
  "v_add_f32 v67, v209, v211\n" \
  "v_add_f32 v71, v213, v215\n" \
  "v_add_f32 v66, v66, v67\n" \
  "v_mfma_f32_16x16x32_bf16 v[220:223], v[108:111], a[76:79], v[220:223]\n" \
  "v_add_f32 v70, v70, v71\n" \
  "v_mov_b32 v65, v66\n" \
  "v_mov_b32 v69, v70\n" \
  "s_nop 0\n" \
  "v_permlane16_swap_b32 v65, v66\n" \
  "v_permlane16_swap_b32 v69, v70\n" \
  "s_waitcnt vmcnt(15)\n" \
  "v_mfma_f32_16x16x32_bf16 v[216:219], v[112:115], a[24:27], v[216:219]\n" \
  "v_add_f32 v66, v66, v65\n" \
  "v_add_f32 v70, v70, v69\n" \
  "v_fmac_f32 v66, v224, v228\n" \
  "v_fmac_f32 v70, v225, v229\n" \
  "v_rcp_f32 v66, v66\n" \
  "v_mfma_f32_16x16x32_bf16 v[220:223], v[112:115], a[80:83], v[220:223]\n" \
  "v_rcp_f32 v70, v70\n" \
  "v_mul_f32 v66, v66, v232\n" \
  "v_mul_f32 v70, v70, v233\n" \
  "v_mul_f32 v224, v224, v66\n" \
  "v_mul_f32 v225, v225, v70\n" \
  "s_waitcnt vmcnt(14)\n" \
  "v_mfma_f32_16x16x32_bf16 v[216:219], v[116:119], a[28:31], v[216:219]\n" \
  "v_mov_b32 v228, v224\n" \
  "v_mov_b32 v229, v225\n" \
  "v_mul_f32 v208, v208, v66\n" \
  "v_mul_f32 v212, v212, v70\n" \
  "v_mul_f32 v209, v209, v66\n" \
  "v_mfma_f32_16x16x32_bf16 v[220:223], v[116:119], a[84:87], v[220:223]\n" \
  "v_mul_f32 v213, v213, v70\n" \
  "v_mul_f32 v210, v210, v66\n" \
  "v_mul_f32 v214, v214, v70\n" \
  "v_mul_f32 v211, v211, v66\n" \
  "v_mul_f32 v215, v215, v70\n" \
  "s_waitcnt vmcnt(13)\n" \
  "v_mfma_f32_16x16x32_bf16 v[216:219], v[120:123], a[32:35], v[216:219]\n" \
  "v_permlane32_swap_b32 v224, v228\n" \
  "v_permlane32_swap_b32 v225, v229\n" \
  "v_cvt_pk_bf16_f32 v72, v208, v209\n" \
  "v_cvt_pk_bf16_f32 v76, v212, v213\n" \
  "v_cvt_pk_bf16_f32 v73, v210, v211\n" \
  "v_mfma_f32_16x16x32_bf16 v[220:223], v[120:123], a[88:91], v[220:223]\n" \
  "v_cvt_pk_bf16_f32 v77, v214, v215\n" \
  "v_mul_f32 v144, v144, v224\n" \
  "v_mul_f32 v152, v152, v225\n" \
  "v_mul_f32 v145, v145, v224\n" \
  "v_mul_f32 v153, v153, v225\n" \
  "s_waitcnt vmcnt(12)\n" \
  "v_mfma_f32_16x16x32_bf16 v[216:219], v[124:127], a[36:39], v[216:219]\n" \
  "v_mul_f32 v146, v146, v224\n" \
  "v_mul_f32 v154, v154, v225\n" \
  "v_mul_f32 v147, v147, v224\n" \
  "v_mul_f32 v155, v155, v225\n" \
  "v_mul_f32 v148, v148, v224\n" \
  "v_mfma_f32_16x16x32_bf16 v[220:223], v[124:127], a[92:95], v[220:223]\n" \
  "v_mul_f32 v156, v156, v225\n" \
  "v_mul_f32 v149, v149, v224\n" \
  "v_mul_f32 v157, v157, v225\n" \
  "v_mul_f32 v150, v150, v224\n" \
  "v_mul_f32 v158, v158, v225\n" \
  "s_waitcnt vmcnt(11)\n" \
  "v_mfma_f32_16x16x32_bf16 v[216:219], v[128:131], a[40:43], v[216:219]\n" \
  "v_mul_f32 v151, v151, v224\n" \
  "v_mul_f32 v159, v159, v225\n" \
  "v_cvt_pk_bf16_f32 v144, v144, v145\n" \
  "v_cvt_pk_bf16_f32 v152, v152, v153\n" \
  "v_cvt_pk_bf16_f32 v145, v146, v147\n" \
  "v_mfma_f32_16x16x32_bf16 v[220:223], v[128:131], a[96:99], v[220:223]\n" \
  "v_cvt_pk_bf16_f32 v153, v154, v155\n" \
  "v_cvt_pk_bf16_f32 v146, v148, v149\n" \
  "v_cvt_pk_bf16_f32 v154, v156, v157\n" \
  "v_cvt_pk_bf16_f32 v147, v150, v151\n" \
  "v_cvt_pk_bf16_f32 v155, v158, v159\n" \
  "s_waitcnt vmcnt(10)\n" \
  "v_mfma_f32_16x16x32_bf16 v[216:219], v[132:135], a[44:47], v[216:219]\n" \
  "ds_write_b128 %[wrP], v[144:147]\n" \
  "ds_write_b128 %[wrP], v[152:155] offset:1024\n" \
  "v_mul_f32 v176, v176, v228\n" \
  "v_mul_f32 v184, v184, v229\n" \
  "v_mul_f32 v177, v177, v228\n" \
  "v_mfma_f32_16x16x32_bf16 v[220:223], v[132:135], a[100:103], v[220:223]\n" \
  "v_mul_f32 v185, v185, v229\n" \
  "v_mul_f32 v178, v178, v228\n" \
  "v_mul_f32 v186, v186, v229\n" \
  "v_mul_f32 v179, v179, v228\n" \
  "v_mul_f32 v187, v187, v229\n" \
  "s_waitcnt vmcnt(9)\n" \
  "v_mfma_f32_16x16x32_bf16 v[216:219], v[136:139], a[48:51], v[216:219]\n" \
  "v_mul_f32 v180, v180, v228\n" \
  "v_mul_f32 v188, v188, v229\n" \
  "v_mul_f32 v181, v181, v228\n" \
  "v_mul_f32 v189, v189, v229\n" \
  "v_mul_f32 v182, v182, v228\n" \
  "v_mfma_f32_16x16x32_bf16 v[220:223], v[136:139], a[104:107], v[220:223]\n" \
  "v_mul_f32 v190, v190, v229\n" \
  "v_mul_f32 v183, v183, v228\n" \
  "v_mul_f32 v191, v191, v229\n" \
  "v_cvt_pk_bf16_f32 v176, v176, v177\n" \
  "v_cvt_pk_bf16_f32 v184, v184, v185\n" \
  "s_waitcnt vmcnt(8)\n" \
  "v_mfma_f32_16x16x32_bf16 v[216:219], v[140:143], a[52:55], v[216:219]\n" \
  "v_cvt_pk_bf16_f32 v177, v178, v179\n" \
  "v_cvt_pk_bf16_f32 v185, v186, v187\n" \
  "v_cvt_pk_bf16_f32 v178, v180, v181\n" \
  "v_cvt_pk_bf16_f32 v186, v188, v189\n" \
  "v_cvt_pk_bf16_f32 v179, v182, v183\n" \
  "v_mfma_f32_16x16x32_bf16 v[220:223], v[140:143], a[108:111], v[220:223]\n" \
  "v_cvt_pk_bf16_f32 v187, v190, v191\n" \
  "ds_write_b128 %[wrP], v[176:179] offset:4096\n" \
  "ds_write_b128 %[wrP], v[184:187] offset:5120\n" \
  "s_cmp_eq_u32 s66, 0\n" \
  "s_cbranch_scc1 .LF_v1%=\n" \
  ".LF_v1b%=:\n" \
  "s_nop 5\n" \
  "v_max3_f32 v64, v216, v217, v218\n" \
  "v_max3_f32 v68, v220, v221, v222\n" \
  "v_max3_f32 v64, v64, v219, v226\n" \
  "v_max3_f32 v68, v68, v223, v227\n" \
  "v_mov_b32 v65, v64\n" \
  "v_mov_b32 v69, v68\n" \
  "s_nop 0\n" \
  "v_permlane16_swap_b32 v65, v64\n" \
  "v_permlane16_swap_b32 v69, v68\n" \
  "v_max_f32 v64, v64, v65\n" \
  "v_max_f32 v68, v68, v69\n" \
  "v_sub_f32 v216, v216, v64\n" \
  "v_sub_f32 v220, v220, v68\n" \
  "v_sub_f32 v217, v217, v64\n" \
  "v_sub_f32 v221, v221, v68\n" \
  "v_sub_f32 v218, v218, v64\n" \
  "v_sub_f32 v222, v222, v68\n" \
  "v_sub_f32 v219, v219, v64\n" \
  "v_sub_f32 v223, v223, v68\n" \
  "v_sub_f32 v226, v226, v64\n" \
  "v_sub_f32 v227, v227, v68\n" \
  "v_exp_f32 v216, v216\n" \
  "v_exp_f32 v220, v220\n" \
  "v_exp_f32 v217, v217\n" \
  "v_exp_f32 v221, v221\n" \
  "v_exp_f32 v218, v218\n" \
  "v_exp_f32 v222, v222\n" \
  "v_exp_f32 v219, v219\n" \
  "v_exp_f32 v223, v223\n" \
  "v_exp_f32 v226, v226\n" \
  "v_exp_f32 v227, v227\n" \
  "v_add_f32 v66, v216, v218\n" \
  "v_add_f32 v70, v220, v222\n" \
  "v_add_f32 v67, v217, v219\n" \
  "v_add_f32 v71, v221, v223\n" \
  "v_add_f32 v66, v66, v67\n" \
  "v_add_f32 v70, v70, v71\n" \
  "v_mov_b32 v65, v66\n" \
  "v_mov_b32 v69, v70\n" \
  "s_nop 0\n" \
  "v_permlane16_swap_b32 v65, v66\n" \
  "v_permlane16_swap_b32 v69, v70\n" \
  "v_add_f32 v66, v66, v65\n" \
  "v_add_f32 v70, v70, v69\n" \
  "v_fmac_f32 v66, v226, v230\n" \
  "v_fmac_f32 v70, v227, v231\n" \
  "v_rcp_f32 v66, v66\n" \
  "v_rcp_f32 v70, v70\n" \
  "v_mul_f32 v66, v66, v232\n" \
  "v_mul_f32 v70, v70, v233\n" \
  "v_mul_f32 v226, v226, v66\n" \
  "v_mul_f32 v227, v227, v70\n" \
  "v_mov_b32 v230, v226\n" \
  "v_mov_b32 v231, v227\n" \
  "v_mul_f32 v216, v216, v66\n" \
  "v_mul_f32 v220, v220, v70\n" \
  "v_mul_f32 v217, v217, v66\n" \
  "v_mul_f32 v221, v221, v70\n" \
  "v_mul_f32 v218, v218, v66\n" \
  "v_mul_f32 v222, v222, v70\n" \
  "v_mul_f32 v219, v219, v66\n" \
  "v_mul_f32 v223, v223, v70\n" \
  "v_permlane32_swap_b32 v226, v230\n" \
  "v_permlane32_swap_b32 v227, v231\n" \
  "v_cvt_pk_bf16_f32 v74, v216, v217\n" \
  "v_cvt_pk_bf16_f32 v78, v220, v221\n" \
  "v_cvt_pk_bf16_f32 v75, v218, v219\n" \
  "v_cvt_pk_bf16_f32 v79, v222, v223\n" \
  "v_mul_f32 v160, v160, v226\n" \
  "v_mul_f32 v168, v168, v227\n" \
  "v_mul_f32 v161, v161, v226\n" \
  "v_mul_f32 v169, v169, v227\n" \
  "v_mul_f32 v162, v162, v226\n" \
  "v_mul_f32 v170, v170, v227\n" \
  "v_mul_f32 v163, v163, v226\n" \
  "v_mul_f32 v171, v171, v227\n" \
  "v_mul_f32 v164, v164, v226\n" \
  "v_mul_f32 v172, v172, v227\n" \
  "v_mul_f32 v165, v165, v226\n" \
  "v_mul_f32 v173, v173, v227\n" \
  "v_mul_f32 v166, v166, v226\n" \
  "v_mul_f32 v174, v174, v227\n" \
  "v_mul_f32 v167, v167, v226\n" \
  "v_mul_f32 v175, v175, v227\n" \
  "v_cvt_pk_bf16_f32 v160, v160, v161\n" \
  "v_cvt_pk_bf16_f32 v168, v168, v169\n" \
  "v_cvt_pk_bf16_f32 v161, v162, v163\n" \
  "v_cvt_pk_bf16_f32 v169, v170, v171\n" \
  "v_cvt_pk_bf16_f32 v162, v164, v165\n" \
  "v_cvt_pk_bf16_f32 v170, v172, v173\n" \
  "v_cvt_pk_bf16_f32 v163, v166, v167\n" \
  "v_cvt_pk_bf16_f32 v171, v174, v175\n" \
  "ds_write_b128 %[wrP], v[160:163] offset:2048\n" \
  "ds_write_b128 %[wrP], v[168:171] offset:3072\n" \
  "v_mul_f32 v192, v192, v230\n" \
  "v_mul_f32 v200, v200, v231\n" \
  "v_mul_f32 v193, v193, v230\n" \
  "v_mul_f32 v201, v201, v231\n" \
  "v_mul_f32 v194, v194, v230\n" \
  "v_mul_f32 v202, v202, v231\n" \
  "v_mul_f32 v195, v195, v230\n" \
  "v_mul_f32 v203, v203, v231\n" \
  "v_mul_f32 v196, v196, v230\n" \
  "v_mul_f32 v204, v204, v231\n" \
  "v_mul_f32 v197, v197, v230\n" \
  "v_mul_f32 v205, v205, v231\n" \
  "v_mul_f32 v198, v198, v230\n" \
  "v_mul_f32 v206, v206, v231\n" \
  "v_mul_f32 v199, v199, v230\n" \
  "v_mul_f32 v207, v207, v231\n" \
  "v_cvt_pk_bf16_f32 v192, v192, v193\n" \
  "v_cvt_pk_bf16_f32 v200, v200, v201\n" \
  "v_cvt_pk_bf16_f32 v193, v194, v195\n" \
  "v_cvt_pk_bf16_f32 v201, v202, v203\n" \
  "v_cvt_pk_bf16_f32 v194, v196, v197\n" \
  "v_cvt_pk_bf16_f32 v202, v204, v205\n" \
  "v_cvt_pk_bf16_f32 v195, v198, v199\n" \
  "v_cvt_pk_bf16_f32 v203, v206, v207\n" \
  "ds_write_b128 %[wrP], v[192:195] offset:6144\n" \
  "ds_write_b128 %[wrP], v[200:203] offset:7168\n" \
  "ds_write_b128 %[wrM], v[72:75]\n" \
  "ds_write_b128 %[wrM], v[76:79] offset:1024\n" \
  "s_cmp_eq_u32 s66, 0\n" \
  "s_cbranch_scc1 .LF_v2%=\n" \
  ".LF_v2b%=:\n" \
  "s_waitcnt lgkmcnt(0)\n" \
  "s_barrier\n" \
  "ds_read_b128 v[80:83], %[lr]\n" \
  "ds_read_b128 v[84:87], %[lr] offset:1024\n" \
  "ds_read_b128 v[88:91], %[lr] offset:2048\n" \
  "ds_read_b128 v[92:95], %[lr] offset:3072\n" \
  "ds_read_b128 v[96:99], %[lr] offset:4096\n" \
  "ds_read_b128 v[100:103], %[lr] offset:5120\n" \
  "ds_read_b128 v[104:107], %[lr] offset:6144\n" \
  "ds_read_b128 v[108:111], %[lr] offset:7168\n" \
  "ds_read_b128 v[112:115], %[lr] offset:8192\n" \
  "ds_read_b128 v[116:119], %[lr] offset:9216\n" \
  "s_waitcnt lgkmcnt(9)\n" \
  "v_mfma_f32_16x16x32_bf16 v[240:243], a[112:115], v[80:83], v[240:243]\n" \
  "v_mfma_f32_16x16x32_bf16 v[248:251], a[176:179], v[80:83], v[248:251]\n" \
  "ds_read_b128 v[80:83], %[lr] offset:10240\n" \
  "s_waitcnt lgkmcnt(9)\n" \
  "v_mfma_f32_16x16x32_bf16 v[244:247], a[112:115], v[84:87], v[244:247]\n" \
  "v_mfma_f32_16x16x32_bf16 v[252:255], a[176:179], v[84:87], v[252:255]\n" \
  "ds_read_b128 v[84:87], %[lr] offset:11264\n" \
  "s_waitcnt lgkmcnt(9)\n" \
  "v_mfma_f32_16x16x32_bf16 v[240:243], a[116:119], v[88:91], v[240:243]\n" \
  "v_mfma_f32_16x16x32_bf16 v[248:251], a[180:183], v[88:91], v[248:251]\n" \
  "ds_read_b128 v[88:91], %[lr] offset:12288\n" \
  "s_waitcnt lgkmcnt(9)\n" \
  "v_mfma_f32_16x16x32_bf16 v[244:247], a[116:119], v[92:95], v[244:247]\n" \
  "v_mfma_f32_16x16x32_bf16 v[252:255], a[180:183], v[92:95], v[252:255]\n" \
  "ds_read_b128 v[92:95], %[lr] offset:13312\n" \
  "s_waitcnt lgkmcnt(9)\n" \
  "v_mfma_f32_16x16x32_bf16 v[240:243], a[120:123], v[96:99], v[240:243]\n" \
  "v_mfma_f32_16x16x32_bf16 v[248:251], a[184:187], v[96:99], v[248:251]\n" \
  "ds_read_b128 v[96:99], %[lr] offset:14336\n" \
  "s_waitcnt lgkmcnt(9)\n" \
  "v_mfma_f32_16x16x32_bf16 v[244:247], a[120:123], v[100:103], v[244:247]\n" \
  "v_mfma_f32_16x16x32_bf16 v[252:255], a[184:187], v[100:103], v[252:255]\n" \
  "ds_read_b128 v[100:103], %[lr] offset:15360\n" \
  "s_waitcnt lgkmcnt(9)\n" \
  "v_mfma_f32_16x16x32_bf16 v[240:243], a[124:127], v[104:107], v[240:243]\n" \
  "v_mfma_f32_16x16x32_bf16 v[248:251], a[188:191], v[104:107], v[248:251]\n" \
  "ds_read_b128 v[104:107], %[lr] offset:16384\n" \
  "s_waitcnt lgkmcnt(9)\n" \
  "v_mfma_f32_16x16x32_bf16 v[244:247], a[124:127], v[108:111], v[244:247]\n" \
  "v_mfma_f32_16x16x32_bf16 v[252:255], a[188:191], v[108:111], v[252:255]\n" \
  "ds_read_b128 v[108:111], %[lr] offset:17408\n" \
  "s_waitcnt lgkmcnt(9)\n" \
  "v_mfma_f32_16x16x32_bf16 v[240:243], a[128:131], v[112:115], v[240:243]\n" \
  "v_mfma_f32_16x16x32_bf16 v[248:251], a[192:195], v[112:115], v[248:251]\n" \
  "ds_read_b128 v[112:115], %[lr] offset:18432\n" \
  "s_waitcnt lgkmcnt(9)\n" \
  "v_mfma_f32_16x16x32_bf16 v[244:247], a[128:131], v[116:119], v[244:247]\n" \
  "v_mfma_f32_16x16x32_bf16 v[252:255], a[192:195], v[116:119], v[252:255]\n" \
  "ds_read_b128 v[116:119], %[lr] offset:19456\n" \
  "s_waitcnt lgkmcnt(9)\n" \
  "v_mfma_f32_16x16x32_bf16 v[240:243], a[132:135], v[80:83], v[240:243]\n" \
  "v_mfma_f32_16x16x32_bf16 v[248:251], a[196:199], v[80:83], v[248:251]\n" \
  "ds_read_b128 v[80:83], %[lr] offset:20480\n" \
  "s_waitcnt lgkmcnt(9)\n" \
  "v_mfma_f32_16x16x32_bf16 v[244:247], a[132:135], v[84:87], v[244:247]\n" \
  "v_mfma_f32_16x16x32_bf16 v[252:255], a[196:199], v[84:87], v[252:255]\n" \
  "ds_read_b128 v[84:87], %[lr] offset:21504\n" \
  "s_waitcnt lgkmcnt(9)\n" \
  "v_mfma_f32_16x16x32_bf16 v[240:243], a[136:139], v[88:91], v[240:243]\n" \
  "v_mfma_f32_16x16x32_bf16 v[248:251], a[200:203], v[88:91], v[248:251]\n" \
  "ds_read_b128 v[88:91], %[lr] offset:22528\n" \
  "s_waitcnt lgkmcnt(9)\n" \
  "v_mfma_f32_16x16x32_bf16 v[244:247], a[136:139], v[92:95], v[244:247]\n" \
  "v_mfma_f32_16x16x32_bf16 v[252:255], a[200:203], v[92:95], v[252:255]\n" \
  "ds_read_b128 v[92:95], %[lr] offset:23552\n" \
  "s_waitcnt lgkmcnt(9)\n" \
  "v_mfma_f32_16x16x32_bf16 v[240:243], a[140:143], v[96:99], v[240:243]\n" \
  "v_mfma_f32_16x16x32_bf16 v[248:251], a[204:207], v[96:99], v[248:251]\n" \
  "ds_read_b128 v[96:99], %[lr] offset:24576\n" \
  "s_waitcnt lgkmcnt(9)\n" \
  "v_mfma_f32_16x16x32_bf16 v[244:247], a[140:143], v[100:103], v[244:247]\n" \
  "v_mfma_f32_16x16x32_bf16 v[252:255], a[204:207], v[100:103], v[252:255]\n" \
  "ds_read_b128 v[100:103], %[lr] offset:25600\n" \
  "s_waitcnt lgkmcnt(9)\n" \
  "v_mfma_f32_16x16x32_bf16 v[240:243], a[144:147], v[104:107], v[240:243]\n" \
  "v_mfma_f32_16x16x32_bf16 v[248:251], a[208:211], v[104:107], v[248:251]\n" \
  "ds_read_b128 v[104:107], %[lr] offset:26624\n" \
  "s_waitcnt lgkmcnt(9)\n" \
  "v_mfma_f32_16x16x32_bf16 v[244:247], a[144:147], v[108:111], v[244:247]\n" \
  "v_mfma_f32_16x16x32_bf16 v[252:255], a[208:211], v[108:111], v[252:255]\n" \
  "ds_read_b128 v[108:111], %[lr] offset:27648\n" \
  "s_waitcnt lgkmcnt(9)\n" \
  "v_mfma_f32_16x16x32_bf16 v[240:243], a[148:151], v[112:115], v[240:243]\n" \
  "v_mfma_f32_16x16x32_bf16 v[248:251], a[212:215], v[112:115], v[248:251]\n" \
  "ds_read_b128 v[112:115], %[lr] offset:28672\n" \
  "s_waitcnt lgkmcnt(9)\n" \
  "v_mfma_f32_16x16x32_bf16 v[244:247], a[148:151], v[116:119], v[244:247]\n" \
  "v_mfma_f32_16x16x32_bf16 v[252:255], a[212:215], v[116:119], v[252:255]\n" \
  "ds_read_b128 v[116:119], %[lr] offset:29696\n" \
  "s_waitcnt lgkmcnt(9)\n" \
  "v_mfma_f32_16x16x32_bf16 v[240:243], a[152:155], v[80:83], v[240:243]\n" \
  "v_mfma_f32_16x16x32_bf16 v[248:251], a[216:219], v[80:83], v[248:251]\n" \
  "ds_read_b128 v[80:83], %[lr] offset:30720\n" \
  "s_waitcnt lgkmcnt(9)\n" \
  "v_mfma_f32_16x16x32_bf16 v[244:247], a[152:155], v[84:87], v[244:247]\n" \
  "v_mfma_f32_16x16x32_bf16 v[252:255], a[216:219], v[84:87], v[252:255]\n" \
  "ds_read_b128 v[84:87], %[lr] offset:31744\n" \
  "s_waitcnt lgkmcnt(9)\n" \
  "v_mfma_f32_16x16x32_bf16 v[240:243], a[156:159], v[88:91], v[240:243]\n" \
  "v_mfma_f32_16x16x32_bf16 v[248:251], a[220:223], v[88:91], v[248:251]\n" \
  "ds_read_b128 v[88:91], %[lr] offset:32768\n" \
  "s_waitcnt lgkmcnt(9)\n" \
  "v_mfma_f32_16x16x32_bf16 v[244:247], a[156:159], v[92:95], v[244:247]\n" \
  "v_mfma_f32_16x16x32_bf16 v[252:255], a[220:223], v[92:95], v[252:255]\n" \
  "ds_read_b128 v[92:95], %[lr] offset:33792\n" \
  "s_waitcnt lgkmcnt(9)\n" \
  "v_mfma_f32_16x16x32_bf16 v[240:243], a[160:163], v[96:99], v[240:243]\n" \
  "v_mfma_f32_16x16x32_bf16 v[248:251], a[224:227], v[96:99], v[248:251]\n" \
  "ds_read_b128 v[96:99], %[lr] offset:34816\n" \
  "s_waitcnt lgkmcnt(9)\n" \
  "v_mfma_f32_16x16x32_bf16 v[244:247], a[160:163], v[100:103], v[244:247]\n" \
  "v_mfma_f32_16x16x32_bf16 v[252:255], a[224:227], v[100:103], v[252:255]\n" \
  "ds_read_b128 v[100:103], %[lr] offset:35840\n" \
  "s_waitcnt lgkmcnt(9)\n" \
  "v_mfma_f32_16x16x32_bf16 v[240:243], a[164:167], v[104:107], v[240:243]\n" \
  "v_mfma_f32_16x16x32_bf16 v[248:251], a[228:231], v[104:107], v[248:251]\n" \
  "ds_read_b128 v[104:107], %[lr] offset:36864\n" \
  "s_waitcnt lgkmcnt(9)\n" \
  "v_mfma_f32_16x16x32_bf16 v[244:247], a[164:167], v[108:111], v[244:247]\n" \
  "v_mfma_f32_16x16x32_bf16 v[252:255], a[228:231], v[108:111], v[252:255]\n" \
  "ds_read_b128 v[108:111], %[lr] offset:37888\n" \
  "s_waitcnt lgkmcnt(9)\n" \
  "v_mfma_f32_16x16x32_bf16 v[240:243], a[168:171], v[112:115], v[240:243]\n" \
  "v_mfma_f32_16x16x32_bf16 v[248:251], a[232:235], v[112:115], v[248:251]\n" \
  "ds_read_b128 v[112:115], %[lr] offset:38912\n" \
  "s_waitcnt lgkmcnt(9)\n" \
  "v_mfma_f32_16x16x32_bf16 v[244:247], a[168:171], v[116:119], v[244:247]\n" \
  "v_mfma_f32_16x16x32_bf16 v[252:255], a[232:235], v[116:119], v[252:255]\n" \
  "ds_read_b128 v[116:119], %[lr] offset:39936\n" \
  "s_waitcnt lgkmcnt(9)\n" \
  "v_mfma_f32_16x16x32_bf16 v[240:243], a[172:175], v[80:83], v[240:243]\n" \
  "v_mfma_f32_16x16x32_bf16 v[248:251], a[236:239], v[80:83], v[248:251]\n" \
  "s_waitcnt lgkmcnt(8)\n" \
  "v_mfma_f32_16x16x32_bf16 v[244:247], a[172:175], v[84:87], v[244:247]\n" \
  "v_mfma_f32_16x16x32_bf16 v[252:255], a[236:239], v[84:87], v[252:255]\n" \
  "s_waitcnt vmcnt(7) lgkmcnt(7)\n" \
  "v_mfma_f32_16x16x32_bf16 v[240:243], v[32:35], v[88:91], v[240:243]\n" \
  "s_waitcnt vmcnt(3)\n" \
  "v_mfma_f32_16x16x32_bf16 v[248:251], v[48:51], v[88:91], v[248:251]\n" \
  "s_waitcnt lgkmcnt(6)\n" \
  "v_mfma_f32_16x16x32_bf16 v[244:247], v[32:35], v[92:95], v[244:247]\n" \
  "v_mfma_f32_16x16x32_bf16 v[252:255], v[48:51], v[92:95], v[252:255]\n" \
  "s_waitcnt lgkmcnt(5)\n" \
  "v_mfma_f32_16x16x32_bf16 v[240:243], v[36:39], v[96:99], v[240:243]\n" \
  "s_waitcnt vmcnt(2)\n" \
  "v_mfma_f32_16x16x32_bf16 v[248:251], v[52:55], v[96:99], v[248:251]\n" \
  "s_waitcnt lgkmcnt(4)\n" \
  "v_mfma_f32_16x16x32_bf16 v[244:247], v[36:39], v[100:103], v[244:247]\n" \
  "v_mfma_f32_16x16x32_bf16 v[252:255], v[52:55], v[100:103], v[252:255]\n" \
  "s_waitcnt lgkmcnt(3)\n" \
  "v_mfma_f32_16x16x32_bf16 v[240:243], v[40:43], v[104:107], v[240:243]\n" \
  "s_waitcnt vmcnt(1)\n" \
  "v_mfma_f32_16x16x32_bf16 v[248:251], v[56:59], v[104:107], v[248:251]\n" \
  "s_waitcnt lgkmcnt(2)\n" \
  "v_mfma_f32_16x16x32_bf16 v[244:247], v[40:43], v[108:111], v[244:247]\n" \
  "v_mfma_f32_16x16x32_bf16 v[252:255], v[56:59], v[108:111], v[252:255]\n" \
  "s_waitcnt lgkmcnt(1)\n" \
  "v_mfma_f32_16x16x32_bf16 v[240:243], v[44:47], v[112:115], v[240:243]\n" \
  "s_waitcnt vmcnt(0)\n" \
  "v_mfma_f32_16x16x32_bf16 v[248:251], v[60:63], v[112:115], v[248:251]\n" \
  "s_waitcnt lgkmcnt(0)\n" \
  "v_mfma_f32_16x16x32_bf16 v[244:247], v[44:47], v[116:119], v[244:247]\n" \
  "v_mfma_f32_16x16x32_bf16 v[252:255], v[60:63], v[116:119], v[252:255]\n" \
  "s_mul_i32 s54, s52, 28672\n" \
  "s_add_u32 s46, s54, 14336\n" \
  "s_nop 0\n" \
  "v_cvt_pk_bf16_f32 v240, v240, v241\n" \
  "v_cvt_pk_bf16_f32 v241, v242, v243\n" \
  "buffer_store_dwordx2 v[240:241], %[oO], %[ro], s54 offen\n" \
  "v_cvt_pk_bf16_f32 v248, v248, v249\n" \
  "v_cvt_pk_bf16_f32 v249, v250, v251\n" \
  "buffer_store_dwordx2 v[248:249], %[oOb], %[ro], s54 offen\n" \
  "v_cvt_pk_bf16_f32 v244, v244, v245\n" \
  "v_cvt_pk_bf16_f32 v245, v246, v247\n" \
  "buffer_store_dwordx2 v[244:245], %[oO], %[ro], s46 offen\n" \
  "v_cvt_pk_bf16_f32 v252, v252, v253\n" \
  "v_cvt_pk_bf16_f32 v253, v254, v255\n" \
  "buffer_store_dwordx2 v[252:253], %[oOb], %[ro], s46 offen\n" \
  "s_barrier\n" \
  "s_add_u32 s52, s52, 32\n" \
  "s_add_u32 s55, s55, 1\n" \
  "s_cmp_lt_u32 s55, 2\n" \
  "s_cbranch_scc1 .LF_job%=\n" \
  "s_branch .LF_end%=\n" \
  ".LF_v0%=:\n" \
  "v_add_f32 v234, v208, v209\n" \
  "v_add_f32 v234, v234, v210\n" \
  "v_cmp_u_f32 vcc, v234, v211\n" \
  "s_cbranch_vccz .LF_v0b%=\n" \
  "s_mov_b32 s44, 262144\n" \
  "s_mov_b32 exec_hi, 0\n" \
  "s_mov_b32 exec_lo, 0x3ffff\n" \
  "s_branch .LF_v0w_in%=\n" \
  ".LF_v0w%=:\n" \
  "s_sleep 1\n" \
  "s_sub_u32 s44, s44, 1\n" \
  "s_cbranch_scc1 .LF_abort%=\n" \
  ".LF_v0w_in%=:\n" \
  "buffer_load_dword v234, %[pA], %[rs], 0 offen sc1\n" \
  "s_waitcnt vmcnt(0)\n" \
  "v_cmp_gt_i32 vcc, s53, v234\n" \
  "s_cbranch_vccnz .LF_v0w%=\n" \
  "s_mov_b64 exec, -1\n" \
  "s_add_u32 s46, s54, 0\n" \
  "buffer_load_dwordx4 v[32:35], %[l16], %[rs], s46 offen sc1\n" \
  "buffer_load_dwordx4 v[36:39], %[l16], %[rs], s46 offen offset:1024 sc1\n" \
  "buffer_load_dwordx4 v[40:43], %[l16], %[rs], s46 offen offset:2048 sc1\n" \
  "buffer_load_dwordx4 v[44:47], %[l16], %[rs], s46 offen offset:3072 sc1\n" \
  "s_add_u32 s46, s54, 4096\n" \
  "buffer_load_dwordx4 v[48:51], %[l16], %[rs], s46 offen sc1\n" \
  "buffer_load_dwordx4 v[52:55], %[l16], %[rs], s46 offen offset:1024 sc1\n" \
  "buffer_load_dwordx4 v[56:59], %[l16], %[rs], s46 offen offset:2048 sc1\n" \
  "buffer_load_dwordx4 v[60:63], %[l16], %[rs], s46 offen offset:3072 sc1\n" \
  "s_add_u32 s46, s54, 8192\n" \
  "buffer_load_dwordx4 v[64:67], %[l16], %[rs], s46 offen sc1\n" \
  "buffer_load_dwordx4 v[68:71], %[l16], %[rs], s46 offen offset:1024 sc1\n" \
  "buffer_load_dwordx4 v[72:75], %[l16], %[rs], s46 offen offset:2048 sc1\n" \
  "buffer_load_dwordx4 v[76:79], %[l16], %[rs], s46 offen offset:3072 sc1\n" \
  "s_add_u32 s46, s54, 12288\n" \
  "buffer_load_dwordx4 v[80:83], %[l16], %[rs], s46 offen sc1\n" \
  "buffer_load_dwordx4 v[84:87], %[l16], %[rs], s46 offen offset:1024 sc1\n" \
  "s_waitcnt vmcnt(13)\n" \
  "v_mfma_f32_16x16x32_bf16 v[208:211], v[32:35], a[0:3], 0\n" \
  "v_mfma_f32_16x16x32_bf16 v[212:215], v[32:35], a[56:59], 0\n" \
  "s_waitcnt vmcnt(12)\n" \
  "v_mfma_f32_16x16x32_bf16 v[208:211], v[36:39], a[4:7], v[208:211]\n" \
  "v_mfma_f32_16x16x32_bf16 v[212:215], v[36:39], a[60:63], v[212:215]\n" \
  "s_waitcnt vmcnt(11)\n" \
  "v_mfma_f32_16x16x32_bf16 v[208:211], v[40:43], a[8:11], v[208:211]\n" \
  "v_mfma_f32_16x16x32_bf16 v[212:215], v[40:43], a[64:67], v[212:215]\n" \
  "s_waitcnt vmcnt(10)\n" \
  "v_mfma_f32_16x16x32_bf16 v[208:211], v[44:47], a[12:15], v[208:211]\n" \
  "v_mfma_f32_16x16x32_bf16 v[212:215], v[44:47], a[68:71], v[212:215]\n" \
  "s_waitcnt vmcnt(9)\n" \
  "v_mfma_f32_16x16x32_bf16 v[208:211], v[48:51], a[16:19], v[208:211]\n" \
  "v_mfma_f32_16x16x32_bf16 v[212:215], v[48:51], a[72:75], v[212:215]\n" \
  "s_waitcnt vmcnt(8)\n" \
  "v_mfma_f32_16x16x32_bf16 v[208:211], v[52:55], a[20:23], v[208:211]\n" \
  "v_mfma_f32_16x16x32_bf16 v[212:215], v[52:55], a[76:79], v[212:215]\n" \
  "s_waitcnt vmcnt(7)\n" \
  "v_mfma_f32_16x16x32_bf16 v[208:211], v[56:59], a[24:27], v[208:211]\n" \
  "v_mfma_f32_16x16x32_bf16 v[212:215], v[56:59], a[80:83], v[212:215]\n" \
  "s_waitcnt vmcnt(6)\n" \
  "v_mfma_f32_16x16x32_bf16 v[208:211], v[60:63], a[28:31], v[208:211]\n" \
  "v_mfma_f32_16x16x32_bf16 v[212:215], v[60:63], a[84:87], v[212:215]\n" \
  "s_waitcnt vmcnt(5)\n" \
  "v_mfma_f32_16x16x32_bf16 v[208:211], v[64:67], a[32:35], v[208:211]\n" \
  "v_mfma_f32_16x16x32_bf16 v[212:215], v[64:67], a[88:91], v[212:215]\n" \
  "s_waitcnt vmcnt(4)\n" \
  "v_mfma_f32_16x16x32_bf16 v[208:211], v[68:71], a[36:39], v[208:211]\n" \
  "v_mfma_f32_16x16x32_bf16 v[212:215], v[68:71], a[92:95], v[212:215]\n" \
  "s_waitcnt vmcnt(3)\n" \
  "v_mfma_f32_16x16x32_bf16 v[208:211], v[72:75], a[40:43], v[208:211]\n" \
  "v_mfma_f32_16x16x32_bf16 v[212:215], v[72:75], a[96:99], v[212:215]\n" \
  "s_waitcnt vmcnt(2)\n" \
  "v_mfma_f32_16x16x32_bf16 v[208:211], v[76:79], a[44:47], v[208:211]\n" \
  "v_mfma_f32_16x16x32_bf16 v[212:215], v[76:79], a[100:103], v[212:215]\n" \
  "s_waitcnt vmcnt(1)\n" \
  "v_mfma_f32_16x16x32_bf16 v[208:211], v[80:83], a[48:51], v[208:211]\n" \
  "v_mfma_f32_16x16x32_bf16 v[212:215], v[80:83], a[104:107], v[212:215]\n" \
  "s_waitcnt vmcnt(0)\n" \
  "v_mfma_f32_16x16x32_bf16 v[208:211], v[84:87], a[52:55], v[208:211]\n" \
  "v_mfma_f32_16x16x32_bf16 v[212:215], v[84:87], a[108:111], v[212:215]\n" \
  "s_branch .LF_v0b%=\n" \
  ".LF_v1%=:\n" \
  "v_add_f32 v234, v216, v217\n" \
  "v_add_f32 v234, v234, v218\n" \
  "v_cmp_u_f32 vcc, v234, v219\n" \
  "s_cbranch_vccz .LF_v1b%=\n" \
  "s_mov_b32 s44, 262144\n" \
  "s_mov_b32 exec_hi, 0\n" \
  "s_mov_b32 exec_lo, 0x3ffff\n" \
  "s_branch .LF_v1w_in%=\n" \
  ".LF_v1w%=:\n" \
  "s_sleep 1\n" \
  "s_sub_u32 s44, s44, 1\n" \
  "s_cbranch_scc1 .LF_abort%=\n" \
  ".LF_v1w_in%=:\n" \
  "buffer_load_dword v234, %[pA], %[rs], 0 offen sc1\n" \
  "s_waitcnt vmcnt(0)\n" \
  "v_cmp_gt_i32 vcc, s53, v234\n" \
  "s_cbranch_vccnz .LF_v1w%=\n" \
  "s_mov_b64 exec, -1\n" \
  "s_add_u32 s46, s54, 14336\n" \
  "buffer_load_dwordx4 v[88:91], %[l16], %[rs], s46 offen sc1\n" \
  "buffer_load_dwordx4 v[92:95], %[l16], %[rs], s46 offen offset:1024 sc1\n" \
  "buffer_load_dwordx4 v[96:99], %[l16], %[rs], s46 offen offset:2048 sc1\n" \
  "buffer_load_dwordx4 v[100:103], %[l16], %[rs], s46 offen offset:3072 sc1\n" \
  "s_add_u32 s46, s54, 18432\n" \
  "buffer_load_dwordx4 v[104:107], %[l16], %[rs], s46 offen sc1\n" \
  "buffer_load_dwordx4 v[108:111], %[l16], %[rs], s46 offen offset:1024 sc1\n" \
  "buffer_load_dwordx4 v[112:115], %[l16], %[rs], s46 offen offset:2048 sc1\n" \
  "buffer_load_dwordx4 v[116:119], %[l16], %[rs], s46 offen offset:3072 sc1\n" \
  "s_add_u32 s46, s54, 22528\n" \
  "buffer_load_dwordx4 v[120:123], %[l16], %[rs], s46 offen sc1\n" \
  "buffer_load_dwordx4 v[124:127], %[l16], %[rs], s46 offen offset:1024 sc1\n" \
  "buffer_load_dwordx4 v[128:131], %[l16], %[rs], s46 offen offset:2048 sc1\n" \
  "buffer_load_dwordx4 v[132:135], %[l16], %[rs], s46 offen offset:3072 sc1\n" \
  "s_add_u32 s46, s54, 26624\n" \
  "buffer_load_dwordx4 v[136:139], %[l16], %[rs], s46 offen sc1\n" \
  "buffer_load_dwordx4 v[140:143], %[l16], %[rs], s46 offen offset:1024 sc1\n" \
  "s_waitcnt vmcnt(13)\n" \
  "v_mfma_f32_16x16x32_bf16 v[216:219], v[88:91], a[0:3], 0\n" \
  "v_mfma_f32_16x16x32_bf16 v[220:223], v[88:91], a[56:59], 0\n" \
  "s_waitcnt vmcnt(12)\n" \
  "v_mfma_f32_16x16x32_bf16 v[216:219], v[92:95], a[4:7], v[216:219]\n" \
  "v_mfma_f32_16x16x32_bf16 v[220:223], v[92:95], a[60:63], v[220:223]\n" \
  "s_waitcnt vmcnt(11)\n" \
  "v_mfma_f32_16x16x32_bf16 v[216:219], v[96:99], a[8:11], v[216:219]\n" \
  "v_mfma_f32_16x16x32_bf16 v[220:223], v[96:99], a[64:67], v[220:223]\n" \
  "s_waitcnt vmcnt(10)\n" \
  "v_mfma_f32_16x16x32_bf16 v[216:219], v[100:103], a[12:15], v[216:219]\n" \
  "v_mfma_f32_16x16x32_bf16 v[220:223], v[100:103], a[68:71], v[220:223]\n" \
  "s_waitcnt vmcnt(9)\n" \
  "v_mfma_f32_16x16x32_bf16 v[216:219], v[104:107], a[16:19], v[216:219]\n" \
  "v_mfma_f32_16x16x32_bf16 v[220:223], v[104:107], a[72:75], v[220:223]\n" \
  "s_waitcnt vmcnt(8)\n" \
  "v_mfma_f32_16x16x32_bf16 v[216:219], v[108:111], a[20:23], v[216:219]\n" \
  "v_mfma_f32_16x16x32_bf16 v[220:223], v[108:111], a[76:79], v[220:223]\n" \
  "s_waitcnt vmcnt(7)\n" \
  "v_mfma_f32_16x16x32_bf16 v[216:219], v[112:115], a[24:27], v[216:219]\n" \
  "v_mfma_f32_16x16x32_bf16 v[220:223], v[112:115], a[80:83], v[220:223]\n" \
  "s_waitcnt vmcnt(6)\n" \
  "v_mfma_f32_16x16x32_bf16 v[216:219], v[116:119], a[28:31], v[216:219]\n" \
  "v_mfma_f32_16x16x32_bf16 v[220:223], v[116:119], a[84:87], v[220:223]\n" \
  "s_waitcnt vmcnt(5)\n" \
  "v_mfma_f32_16x16x32_bf16 v[216:219], v[120:123], a[32:35], v[216:219]\n" \
  "v_mfma_f32_16x16x32_bf16 v[220:223], v[120:123], a[88:91], v[220:223]\n" \
  "s_waitcnt vmcnt(4)\n" \
  "v_mfma_f32_16x16x32_bf16 v[216:219], v[124:127], a[36:39], v[216:219]\n" \
  "v_mfma_f32_16x16x32_bf16 v[220:223], v[124:127], a[92:95], v[220:223]\n" \
  "s_waitcnt vmcnt(3)\n" \
  "v_mfma_f32_16x16x32_bf16 v[216:219], v[128:131], a[40:43], v[216:219]\n" \
  "v_mfma_f32_16x16x32_bf16 v[220:223], v[128:131], a[96:99], v[220:223]\n" \
  "s_waitcnt vmcnt(2)\n" \
  "v_mfma_f32_16x16x32_bf16 v[216:219], v[132:135], a[44:47], v[216:219]\n" \
  "v_mfma_f32_16x16x32_bf16 v[220:223], v[132:135], a[100:103], v[220:223]\n" \
  "s_waitcnt vmcnt(1)\n" \
  "v_mfma_f32_16x16x32_bf16 v[216:219], v[136:139], a[48:51], v[216:219]\n" \
  "v_mfma_f32_16x16x32_bf16 v[220:223], v[136:139], a[104:107], v[220:223]\n" \
  "s_waitcnt vmcnt(0)\n" \
  "v_mfma_f32_16x16x32_bf16 v[216:219], v[140:143], a[52:55], v[216:219]\n" \
  "v_mfma_f32_16x16x32_bf16 v[220:223], v[140:143], a[108:111], v[220:223]\n" \
  "s_branch .LF_v1b%=\n" \
  ".LF_v2%=:\n" \
  "s_waitcnt vmcnt(7)\n" \
  "v_max_u32 v234, v32, v33\n" \
  "v_max3_u32 v234, v234, v34, v35\n" \
  "s_waitcnt vmcnt(6)\n" \
  "v_max3_u32 v234, v234, v36, v37\n" \
  "v_max3_u32 v234, v234, v38, v39\n" \
  "s_waitcnt vmcnt(5)\n" \
  "v_max3_u32 v234, v234, v40, v41\n" \
  "v_max3_u32 v234, v234, v42, v43\n" \
  "s_waitcnt vmcnt(4)\n" \
  "v_max3_u32 v234, v234, v44, v45\n" \
  "v_max3_u32 v234, v234, v46, v47\n" \
  "s_waitcnt vmcnt(3)\n" \
  "v_max3_u32 v234, v234, v48, v49\n" \
  "v_max3_u32 v234, v234, v50, v51\n" \
  "s_waitcnt vmcnt(2)\n" \
  "v_max3_u32 v234, v234, v52, v53\n" \
  "v_max3_u32 v234, v234, v54, v55\n" \
  "s_waitcnt vmcnt(1)\n" \
  "v_max3_u32 v234, v234, v56, v57\n" \
  "v_max3_u32 v234, v234, v58, v59\n" \
  "s_waitcnt vmcnt(0)\n" \
  "v_max3_u32 v234, v234, v60, v61\n" \
  "v_max3_u32 v234, v234, v62, v63\n" \
  "v_cmp_eq_u32 vcc, -1, v234\n" \
  "s_cbranch_vccz .LF_v2b%=\n" \
  "s_mov_b32 s44, 262144\n" \
  "s_mov_b32 exec_hi, 0\n" \
  "s_mov_b32 exec_lo, 0x3ffff\n" \
  "s_branch .LF_v2w_in%=\n" \
  ".LF_v2w%=:\n" \
  "s_sleep 1\n" \
  "s_sub_u32 s44, s44, 1\n" \
  "s_cbranch_scc1 .LF_abort%=\n" \
  ".LF_v2w_in%=:\n" \
  "buffer_load_dword v234, %[pA], %[rs], 0 offen sc1\n" \
  "s_waitcnt vmcnt(0)\n" \
  "v_cmp_gt_i32 vcc, s53, v234\n" \
  "s_cbranch_vccnz .LF_v2w%=\n" \
  "s_mov_b64 exec, -1\n" \
  "s_add_u32 s46, s48, 4096\n" \
  "buffer_load_dwordx4 v[32:35], %[l16], %[rs], s48 offen sc1\n" \
  "buffer_load_dwordx4 v[36:39], %[l16], %[rs], s48 offen offset:1024 sc1\n" \
  "buffer_load_dwordx4 v[40:43], %[l16], %[rs], s48 offen offset:2048 sc1\n" \
  "buffer_load_dwordx4 v[44:47], %[l16], %[rs], s48 offen offset:3072 sc1\n" \
  "buffer_load_dwordx4 v[48:51], %[l16b], %[rs], s46 offen sc1\n" \
  "buffer_load_dwordx4 v[52:55], %[l16b], %[rs], s46 offen offset:1024 sc1\n" \
  "buffer_load_dwordx4 v[56:59], %[l16b], %[rs], s46 offen offset:2048 sc1\n" \
  "buffer_load_dwordx4 v[60:63], %[l16b], %[rs], s46 offen offset:3072 sc1\n" \
  "s_branch .LF_v2b%=\n" \
  ".LF_abort%=:\n" \
  "s_waitcnt vmcnt(0) lgkmcnt(0)\n" \
  "s_mov_b64 exec, -1\n" \
  "v_mov_b32 v234, 1\n" \
  "v_mov_b32 v235, 0x2913000\n" \
  "buffer_store_dword v234, v235, %[rs], 0 offen sc1\n" \
  ".LF_end%=:\n" \
  "s_waitcnt vmcnt(0) lgkmcnt(0)\n" \
  ".LF_zend%=:\n" \
  ""
#define ASM_F_CLOB "memory", "scc", "vcc", "a0", "a1", "a2", "a3", "a4", "a5", "a6", "a7", "a8", "a9", "a10", "a11", "a12", "a13", "a14", "a15", "a16", "a17", "a18", "a19", "a20", "a21", "a22", "a23", "a24", "a25", "a26", "a27", "a28", "a29", "a30", "a31", "a32", "a33", "a34", "a35", "a36", "a37", "a38", "a39", "a40", "a41", "a42", "a43", "a44", "a45", "a46", "a47", "a48", "a49", "a50", "a51", "a52", "a53", "a54", "a55", "a56", "a57", "a58", "a59", "a60", "a61", "a62", "a63", "a64", "a65", "a66", "a67", "a68", "a69", "a70", "a71", "a72", "a73", "a74", "a75", "a76", "a77", "a78", "a79", "a80", "a81", "a82", "a83", "a84", "a85", "a86", "a87", "a88", "a89", "a90", "a91", "a92", "a93", "a94", "a95", "a96", "a97", "a98", "a99", "a100", "a101", "a102", "a103", "a104", "a105", "a106", "a107", "a108", "a109", "a110", "a111", "a112", "a113", "a114", "a115", "a116", "a117", "a118", "a119", "a120", "a121", "a122", "a123", "a124", "a125", "a126", "a127", "a128", "a129", "a130", "a131", "a132", "a133", "a134", "a135", "a136", "a137", "a138", "a139", "a140", "a141", "a142", "a143", "a144", "a145", "a146", "a147", "a148", "a149", "a150", "a151", "a152", "a153", "a154", "a155", "a156", "a157", "a158", "a159", "a160", "a161", "a162", "a163", "a164", "a165", "a166", "a167", "a168", "a169", "a170", "a171", "a172", "a173", "a174", "a175", "a176", "a177", "a178", "a179", "a180", "a181", "a182", "a183", "a184", "a185", "a186", "a187", "a188", "a189", "a190", "a191", "a192", "a193", "a194", "a195", "a196", "a197", "a198", "a199", "a200", "a201", "a202", "a203", "a204", "a205", "a206", "a207", "a208", "a209", "a210", "a211", "a212", "a213", "a214", "a215", "a216", "a217", "a218", "a219", "a220", "a221", "a222", "a223", "a224", "a225", "a226", "a227", "a228", "a229", "a230", "a231", "a232", "a233", "a234", "a235", "a236", "a237", "a238", "a239", "s44", "s46", "s47", "s48", "s49", "s52", "s53", "s54", "s55", "s56", "s58", "s59", "s60", "s61", "s62", "s63", "s64", "s65", "s66", "s67", "s88", "v30", "v32", "v33", "v34", "v35", "v36", "v37", "v38", "v39", "v40", "v41", "v42", "v43", "v44", "v45", "v46", "v47", "v48", "v49", "v50", "v51", "v52", "v53", "v54", "v55", "v56", "v57", "v58", "v59", "v60", "v61", "v62", "v63", "v64", "v65", "v66", "v67", "v68", "v69", "v70", "v71", "v72", "v73", "v74", "v75", "v76", "v77", "v78", "v79", "v80", "v81", "v82", "v83", "v84", "v85", "v86", "v87", "v88", "v89", "v90", "v91", "v92", "v93", "v94", "v95", "v96", "v97", "v98", "v99", "v100", "v101", "v102", "v103", "v104", "v105", "v106", "v107", "v108", "v109", "v110", "v111", "v112", "v113", "v114", "v115", "v116", "v117", "v118", "v119", "v120", "v121", "v122", "v123", "v124", "v125", "v126", "v127", "v128", "v129", "v130", "v131", "v132", "v133", "v134", "v135", "v136", "v137", "v138", "v139", "v140", "v141", "v142", "v143", "v144", "v145", "v146", "v147", "v148", "v149", "v150", "v151", "v152", "v153", "v154", "v155", "v156", "v157", "v158", "v159", "v160", "v161", "v162", "v163", "v164", "v165", "v166", "v167", "v168", "v169", "v170", "v171", "v172", "v173", "v174", "v175", "v176", "v177", "v178", "v179", "v180", "v181", "v182", "v183", "v184", "v185", "v186", "v187", "v188", "v189", "v190", "v191", "v192", "v193", "v194", "v195", "v196", "v197", "v198", "v199", "v200", "v201", "v202", "v203", "v204", "v205", "v206", "v207", "v208", "v209", "v210", "v211", "v212", "v213", "v214", "v215", "v216", "v217", "v218", "v219", "v220", "v221", "v222", "v223", "v224", "v225", "v226", "v227", "v228", "v229", "v230", "v231", "v232", "v233", "v234", "v235", "v236", "v237", "v240", "v241", "v242", "v243", "v244", "v245", "v246", "v247", "v248", "v249", "v250", "v251", "v252", "v253", "v254", "v255"
#define ASM_PA \
  "s_nop 4\n" \
  "v_xor_b32 v192, 0, %[oA]\n" \
  "v_xor_b32 v193, 32, %[oA]\n" \
  "v_xor_b32 v194, 64, %[oA]\n" \
  "v_xor_b32 v195, 96, %[oA]\n" \
  "v_xor_b32 v196, 128, %[oA]\n" \
  "v_xor_b32 v197, 160, %[oA]\n" \
  "v_xor_b32 v198, 192, %[oA]\n" \
  "v_xor_b32 v199, 224, %[oA]\n" \
  "s_lshl_b32 m0, %[w], 13\n" \
  "s_add_u32 s46, %[sB], 0\n" \
  "buffer_load_dwordx4 v192, %[rb], s46 offen nt lds\n" \
  "s_add_u32 m0, m0, 1024\n" \
  "s_add_u32 s46, %[sB], 16384\n" \
  "buffer_load_dwordx4 v193, %[rb], s46 offen nt lds\n" \
  "s_add_u32 m0, m0, 1024\n" \
  "s_add_u32 s46, %[sB], 65536\n" \
  "buffer_load_dwordx4 v194, %[rb], s46 offen nt lds\n" \
  "s_add_u32 m0, m0, 1024\n" \
  "s_add_u32 s46, %[sB], 81920\n" \
  "buffer_load_dwordx4 v195, %[rb], s46 offen nt lds\n" \
  "s_add_u32 m0, m0, 1024\n" \
  "s_add_u32 s46, %[sB], 131072\n" \
  "buffer_load_dwordx4 v196, %[rb], s46 offen nt lds\n" \
  "s_add_u32 m0, m0, 1024\n" \
  "s_add_u32 s46, %[sB], 147456\n" \
  "buffer_load_dwordx4 v197, %[rb], s46 offen nt lds\n" \
  "s_add_u32 m0, m0, 1024\n" \
  "s_add_u32 s46, %[sB], 196608\n" \
  "buffer_load_dwordx4 v198, %[rb], s46 offen nt lds\n" \
  "s_add_u32 m0, m0, 1024\n" \
  "s_add_u32 s46, %[sB], 212992\n" \
  "buffer_load_dwordx4 v199, %[rb], s46 offen nt lds\n" \
  "s_add_u32 m0, m0, 1024\n" \
  "s_add_u32 s52, %[k0], 0\n" \
  "s_sub_u32 s46, s52, 14\n" \
  "s_cmp_ge_u32 s52, 14\n" \
  "s_cselect_b32 s52, s46, s52\n" \
  "s_add_u32 s53, %[k0], 4\n" \
  "s_sub_u32 s46, s53, 14\n" \
  "s_cmp_ge_u32 s53, 14\n" \
  "s_cselect_b32 s53, s46, s53\n" \
  "s_add_u32 s54, %[k0], 8\n" \
  "s_sub_u32 s46, s54, 14\n" \
  "s_cmp_ge_u32 s54, 14\n" \
  "s_cselect_b32 s54, s46, s54\n" \
  "s_add_u32 s55, %[k0], 12\n" \
  "s_sub_u32 s46, s55, 14\n" \
  "s_cmp_ge_u32 s55, 14\n" \
  "s_cselect_b32 s55, s46, s55\n" \
  "s_add_u32 m0, %[ma], 0\n" \
  "s_lshl_b32 s48, s52, 18\n" \
  "s_add_u32 s46, s48, 0\n" \
  "buffer_load_dwordx4 v192, %[ra], s46 offen lds\n" \
  "s_add_u32 m0, m0, 1024\n" \
  "s_add_u32 s46, s48, 16384\n" \
  "buffer_load_dwordx4 v193, %[ra], s46 offen lds\n" \
  "s_add_u32 m0, m0, 1024\n" \
  "s_add_u32 s46, s48, 65536\n" \
  "buffer_load_dwordx4 v194, %[ra], s46 offen lds\n" \
  "s_add_u32 m0, m0, 1024\n" \
  "s_add_u32 s46, s48, 81920\n" \
  "buffer_load_dwordx4 v195, %[ra], s46 offen lds\n" \
  "s_add_u32 m0, m0, 1024\n" \
  "s_add_u32 s46, s48, 131072\n" \
  "buffer_load_dwordx4 v196, %[ra], s46 offen lds\n" \
  "s_add_u32 m0, m0, 1024\n" \
  "s_add_u32 s46, s48, 147456\n" \
  "buffer_load_dwordx4 v197, %[ra], s46 offen lds\n" \
  "s_add_u32 m0, m0, 1024\n" \
  "s_add_u32 s46, s48, 196608\n" \
  "buffer_load_dwordx4 v198, %[ra], s46 offen lds\n" \
  "s_add_u32 m0, m0, 1024\n" \
  "s_add_u32 s46, s48, 212992\n" \
  "buffer_load_dwordx4 v199, %[ra], s46 offen lds\n" \
  "s_add_u32 m0, m0, 1024\n" \
  "s_add_u32 m0, %[ma], 8192\n" \
  "s_lshl_b32 s48, s52, 18\n" \
  "s_add_u32 s46, s48, 32768\n" \
  "buffer_load_dwordx4 v192, %[ra], s46 offen lds\n" \
  "s_add_u32 m0, m0, 1024\n" \
  "s_add_u32 s46, s48, 49152\n" \
  "buffer_load_dwordx4 v193, %[ra], s46 offen lds\n" \
  "s_add_u32 m0, m0, 1024\n" \
  "s_add_u32 s46, s48, 98304\n" \
  "buffer_load_dwordx4 v194, %[ra], s46 offen lds\n" \
  "s_add_u32 m0, m0, 1024\n" \
  "s_add_u32 s46, s48, 114688\n" \
  "buffer_load_dwordx4 v195, %[ra], s46 offen lds\n" \
  "s_add_u32 m0, m0, 1024\n" \
  "s_add_u32 s46, s48, 163840\n" \
  "buffer_load_dwordx4 v196, %[ra], s46 offen lds\n" \
  "s_add_u32 m0, m0, 1024\n" \
  "s_add_u32 s46, s48, 180224\n" \
  "buffer_load_dwordx4 v197, %[ra], s46 offen lds\n" \
  "s_add_u32 m0, m0, 1024\n" \
  "s_add_u32 s46, s48, 229376\n" \
  "buffer_load_dwordx4 v198, %[ra], s46 offen lds\n" \
  "s_add_u32 m0, m0, 1024\n" \
  "s_add_u32 s46, s48, 245760\n" \
  "buffer_load_dwordx4 v199, %[ra], s46 offen lds\n" \
  "s_add_u32 m0, m0, 1024\n" \
  "v_xor_b32 v204, 0, %[rA]\n" \
  "v_add_u32 v200, %[ma], v204\n" \
  "v_xor_b32 v205, 64, %[rA]\n" \
  "v_add_u32 v201, %[ma], v205\n" \
  "v_xor_b32 v206, 128, %[rA]\n" \
  "v_add_u32 v202, %[ma], v206\n" \
  "v_xor_b32 v207, 192, %[rA]\n" \
  "v_add_u32 v203, %[ma], v207\n" \
  "s_mov_b32 s56, 0x3db8aa3b\n" \
  "s_waitcnt vmcnt(16)\n" \
  "s_barrier\n" \
  "s_waitcnt vmcnt(8)\n" \
  "ds_read_b128 v[32:35], v200\n" \
  "ds_read_b128 a[0:3], v204\n" \
  "ds_read_b128 a[32:35], v204 offset:8192\n" \
  "ds_read_b128 a[64:67], v204 offset:16384\n" \
  "ds_read_b128 a[96:99], v204 offset:24576\n" \
  "s_add_u32 m0, %[ma], 16384\n" \
  "s_lshl_b32 s48, s53, 18\n" \
  "s_add_u32 s46, s48, 0\n" \
  "buffer_load_dwordx4 v192, %[ra], s46 offen lds\n" \
  "s_add_u32 m0, m0, 1024\n" \
  "ds_read_b128 v[36:39], v201\n" \
  "ds_read_b128 a[4:7], v205\n" \
  "ds_read_b128 a[36:39], v205 offset:8192\n" \
  "ds_read_b128 a[68:71], v205 offset:16384\n" \
  "ds_read_b128 a[100:103], v205 offset:24576\n" \
  "s_add_u32 s46, s48, 16384\n" \
  "buffer_load_dwordx4 v193, %[ra], s46 offen lds\n" \
  "s_add_u32 m0, m0, 1024\n" \
  "ds_read_b128 v[40:43], v202\n" \
  "ds_read_b128 a[8:11], v206\n" \
  "ds_read_b128 a[40:43], v206 offset:8192\n" \
  "ds_read_b128 a[72:75], v206 offset:16384\n" \
  "ds_read_b128 a[104:107], v206 offset:24576\n" \
  "s_add_u32 s46, s48, 65536\n" \
  "buffer_load_dwordx4 v194, %[ra], s46 offen lds\n" \
  "s_add_u32 m0, m0, 1024\n" \
  "ds_read_b128 v[44:47], v203\n" \
  "ds_read_b128 a[12:15], v207\n" \
  "ds_read_b128 a[44:47], v207 offset:8192\n" \
  "ds_read_b128 a[76:79], v207 offset:16384\n" \
  "ds_read_b128 a[108:111], v207 offset:24576\n" \
  "s_add_u32 s46, s48, 81920\n" \
  "buffer_load_dwordx4 v195, %[ra], s46 offen lds\n" \
  "s_add_u32 m0, m0, 1024\n" \
  "ds_read_b128 v[48:51], v200 offset:256\n" \
  "ds_read_b128 a[16:19], v204 offset:256\n" \
  "ds_read_b128 a[48:51], v204 offset:8448\n" \
  "ds_read_b128 a[80:83], v204 offset:16640\n" \
  "ds_read_b128 a[112:115], v204 offset:24832\n" \
  "s_add_u32 s46, s48, 131072\n" \
  "buffer_load_dwordx4 v196, %[ra], s46 offen lds\n" \
  "s_add_u32 m0, m0, 1024\n" \
  "ds_read_b128 v[52:55], v201 offset:256\n" \
  "ds_read_b128 a[20:23], v205 offset:256\n" \
  "ds_read_b128 a[52:55], v205 offset:8448\n" \
  "ds_read_b128 a[84:87], v205 offset:16640\n" \
  "ds_read_b128 a[116:119], v205 offset:24832\n" \
  "s_add_u32 s46, s48, 147456\n" \
  "buffer_load_dwordx4 v197, %[ra], s46 offen lds\n" \
  "s_add_u32 m0, m0, 1024\n" \
  "ds_read_b128 v[56:59], v202 offset:256\n" \
  "ds_read_b128 a[24:27], v206 offset:256\n" \
  "ds_read_b128 a[56:59], v206 offset:8448\n" \
  "ds_read_b128 a[88:91], v206 offset:16640\n" \
  "ds_read_b128 a[120:123], v206 offset:24832\n" \
  "s_add_u32 s46, s48, 196608\n" \
  "buffer_load_dwordx4 v198, %[ra], s46 offen lds\n" \
  "s_add_u32 m0, m0, 1024\n" \
  "ds_read_b128 v[60:63], v203 offset:256\n" \
  "ds_read_b128 a[28:31], v207 offset:256\n" \
  "ds_read_b128 a[60:63], v207 offset:8448\n" \
  "ds_read_b128 a[92:95], v207 offset:16640\n" \
  "ds_read_b128 a[124:127], v207 offset:24832\n" \
  "s_add_u32 s46, s48, 212992\n" \
  "buffer_load_dwordx4 v199, %[ra], s46 offen lds\n" \
  "s_add_u32 m0, m0, 1024\n" \
  "s_waitcnt lgkmcnt(15)\n" \
  "v_mfma_f32_16x16x32_bf16 v[96:99], v[32:35], a[0:3], 0\n" \
  "v_mfma_f32_16x16x32_bf16 v[100:103], v[32:35], a[32:35], 0\n" \
  "v_mfma_f32_16x16x32_bf16 v[104:107], v[32:35], a[64:67], 0\n" \
  "v_mfma_f32_16x16x32_bf16 v[108:111], v[32:35], a[96:99], 0\n" \
  "s_add_u32 m0, %[ma], 24576\n" \
  "s_lshl_b32 s48, s53, 18\n" \
  "s_add_u32 s46, s48, 32768\n" \
  "buffer_load_dwordx4 v192, %[ra], s46 offen lds\n" \
  "s_add_u32 m0, m0, 1024\n" \
  "v_mfma_f32_16x16x32_bf16 v[96:99], v[36:39], a[4:7], v[96:99]\n" \
  "v_mfma_f32_16x16x32_bf16 v[100:103], v[36:39], a[36:39], v[100:103]\n" \
  "s_add_u32 s46, s48, 49152\n" \
  "buffer_load_dwordx4 v193, %[ra], s46 offen lds\n" \
  "s_add_u32 m0, m0, 1024\n" \
  "v_mfma_f32_16x16x32_bf16 v[104:107], v[36:39], a[68:71], v[104:107]\n" \
  "v_mfma_f32_16x16x32_bf16 v[108:111], v[36:39], a[100:103], v[108:111]\n" \
  "s_add_u32 s46, s48, 98304\n" \
  "buffer_load_dwordx4 v194, %[ra], s46 offen lds\n" \
  "s_add_u32 m0, m0, 1024\n" \
  "v_mfma_f32_16x16x32_bf16 v[96:99], v[40:43], a[8:11], v[96:99]\n" \
  "s_waitcnt vmcnt(11)\n" \
  "v_mfma_f32_16x16x32_bf16 v[100:103], v[40:43], a[40:43], v[100:103]\n" \
  "s_add_u32 s46, s48, 114688\n" \
  "buffer_load_dwordx4 v195, %[ra], s46 offen lds\n" \
  "s_add_u32 m0, m0, 1024\n" \
  "v_mfma_f32_16x16x32_bf16 v[104:107], v[40:43], a[72:75], v[104:107]\n" \
  "ds_read_b128 v[64:67], v200 offset:8192\n" \
  "v_mfma_f32_16x16x32_bf16 v[108:111], v[40:43], a[104:107], v[108:111]\n" \
  "s_add_u32 s46, s48, 163840\n" \
  "buffer_load_dwordx4 v196, %[ra], s46 offen lds\n" \
  "s_add_u32 m0, m0, 1024\n" \
  "v_mfma_f32_16x16x32_bf16 v[96:99], v[44:47], a[12:15], v[96:99]\n" \
  "ds_read_b128 v[68:71], v201 offset:8192\n" \
  "v_mfma_f32_16x16x32_bf16 v[100:103], v[44:47], a[44:47], v[100:103]\n" \
  "s_add_u32 s46, s48, 180224\n" \
  "buffer_load_dwordx4 v197, %[ra], s46 offen lds\n" \
  "s_add_u32 m0, m0, 1024\n" \
  "v_mfma_f32_16x16x32_bf16 v[104:107], v[44:47], a[76:79], v[104:107]\n" \
  "ds_read_b128 v[72:75], v202 offset:8192\n" \
  "v_mfma_f32_16x16x32_bf16 v[108:111], v[44:47], a[108:111], v[108:111]\n" \
  "s_add_u32 s46, s48, 229376\n" \
  "buffer_load_dwordx4 v198, %[ra], s46 offen lds\n" \
  "s_add_u32 m0, m0, 1024\n" \
  "v_mfma_f32_16x16x32_bf16 v[96:99], v[48:51], a[16:19], v[96:99]\n" \
  "ds_read_b128 v[76:79], v203 offset:8192\n" \
  "v_mfma_f32_16x16x32_bf16 v[100:103], v[48:51], a[48:51], v[100:103]\n" \
  "s_add_u32 s46, s48, 245760\n" \
  "buffer_load_dwordx4 v199, %[ra], s46 offen lds\n" \
  "s_add_u32 m0, m0, 1024\n" \
  "v_mfma_f32_16x16x32_bf16 v[104:107], v[48:51], a[80:83], v[104:107]\n" \
  "ds_read_b128 v[80:83], v200 offset:8448\n" \
  "v_mfma_f32_16x16x32_bf16 v[108:111], v[48:51], a[112:115], v[108:111]\n" \
  "s_waitcnt lgkmcnt(15)\n" \
  "v_mfma_f32_16x16x32_bf16 v[96:99], v[52:55], a[20:23], v[96:99]\n" \
  "ds_read_b128 v[84:87], v201 offset:8448\n" \
  "v_mfma_f32_16x16x32_bf16 v[100:103], v[52:55], a[52:55], v[100:103]\n" \
  "v_mfma_f32_16x16x32_bf16 v[104:107], v[52:55], a[84:87], v[104:107]\n" \
  "ds_read_b128 v[88:91], v202 offset:8448\n" \
  "v_mfma_f32_16x16x32_bf16 v[108:111], v[52:55], a[116:119], v[108:111]\n" \
  "s_waitcnt lgkmcnt(15)\n" \
  "v_mfma_f32_16x16x32_bf16 v[96:99], v[56:59], a[24:27], v[96:99]\n" \
  "ds_read_b128 v[92:95], v203 offset:8448\n" \
  "s_waitcnt lgkmcnt(15)\n" \
  "v_mfma_f32_16x16x32_bf16 v[100:103], v[56:59], a[56:59], v[100:103]\n" \
  "s_waitcnt lgkmcnt(14)\n" \
  "v_mfma_f32_16x16x32_bf16 v[104:107], v[56:59], a[88:91], v[104:107]\n" \
  "s_waitcnt lgkmcnt(13)\n" \
  "v_mfma_f32_16x16x32_bf16 v[108:111], v[56:59], a[120:123], v[108:111]\n" \
  "s_waitcnt lgkmcnt(11)\n" \
  "v_mfma_f32_16x16x32_bf16 v[96:99], v[60:63], a[28:31], v[96:99]\n" \
  "s_waitcnt lgkmcnt(10)\n" \
  "v_mfma_f32_16x16x32_bf16 v[100:103], v[60:63], a[60:63], v[100:103]\n" \
  "s_waitcnt lgkmcnt(9)\n" \
  "v_mfma_f32_16x16x32_bf16 v[104:107], v[60:63], a[92:95], v[104:107]\n" \
  "s_waitcnt lgkmcnt(8)\n" \
  "v_mfma_f32_16x16x32_bf16 v[108:111], v[60:63], a[124:127], v[108:111]\n" \
  "s_waitcnt lgkmcnt(7)\n" \
  "v_mfma_f32_16x16x32_bf16 v[112:115], v[64:67], a[0:3], 0\n" \
  "v_mfma_f32_16x16x32_bf16 v[116:119], v[64:67], a[32:35], 0\n" \
  "v_mfma_f32_16x16x32_bf16 v[120:123], v[64:67], a[64:67], 0\n" \
  "v_mfma_f32_16x16x32_bf16 v[124:127], v[64:67], a[96:99], 0\n" \
  "s_add_u32 m0, %[ma], 0\n" \
  "s_lshl_b32 s48, s54, 18\n" \
  "s_add_u32 s46, s48, 0\n" \
  "buffer_load_dwordx4 v192, %[ra], s46 offen lds\n" \
  "s_add_u32 m0, m0, 1024\n" \
  "s_waitcnt lgkmcnt(6)\n" \
  "v_mfma_f32_16x16x32_bf16 v[112:115], v[68:71], a[4:7], v[112:115]\n" \
  "v_mfma_f32_16x16x32_bf16 v[116:119], v[68:71], a[36:39], v[116:119]\n" \
  "s_add_u32 s46, s48, 16384\n" \
  "buffer_load_dwordx4 v193, %[ra], s46 offen lds\n" \
  "s_add_u32 m0, m0, 1024\n" \
  "v_mfma_f32_16x16x32_bf16 v[120:123], v[68:71], a[68:71], v[120:123]\n" \
  "v_mul_f32 v96, s56, v96\n" \
  "v_mul_f32 v97, s56, v97\n" \
  "v_cvt_pk_bf16_f32 v128, v96, v97\n" \
  "v_mfma_f32_16x16x32_bf16 v[124:127], v[68:71], a[100:103], v[124:127]\n" \
  "s_add_u32 s46, s48, 65536\n" \
  "buffer_load_dwordx4 v194, %[ra], s46 offen lds\n" \
  "s_add_u32 m0, m0, 1024\n" \
  "s_waitcnt lgkmcnt(5)\n" \
  "v_mfma_f32_16x16x32_bf16 v[112:115], v[72:75], a[8:11], v[112:115]\n" \
  "s_waitcnt vmcnt(11)\n" \
  "v_mul_f32 v98, s56, v98\n" \
  "v_mul_f32 v99, s56, v99\n" \
  "v_cvt_pk_bf16_f32 v129, v98, v99\n" \
  "v_mfma_f32_16x16x32_bf16 v[116:119], v[72:75], a[40:43], v[116:119]\n" \
  "s_add_u32 s46, s48, 81920\n" \
  "buffer_load_dwordx4 v195, %[ra], s46 offen lds\n" \
  "s_add_u32 m0, m0, 1024\n" \
  "v_mfma_f32_16x16x32_bf16 v[120:123], v[72:75], a[72:75], v[120:123]\n" \
  "ds_read_b128 v[32:35], v200 offset:16384\n" \
  "v_mul_f32 v100, s56, v100\n" \
  "v_mul_f32 v101, s56, v101\n" \
  "v_cvt_pk_bf16_f32 v132, v100, v101\n" \
  "v_mfma_f32_16x16x32_bf16 v[124:127], v[72:75], a[104:107], v[124:127]\n" \
  "s_add_u32 s46, s48, 131072\n" \
  "buffer_load_dwordx4 v196, %[ra], s46 offen lds\n" \
  "s_add_u32 m0, m0, 1024\n" \
  "s_waitcnt lgkmcnt(5)\n" \
  "v_mfma_f32_16x16x32_bf16 v[112:115], v[76:79], a[12:15], v[112:115]\n" \
  "ds_read_b128 v[36:39], v201 offset:16384\n" \
  "v_mul_f32 v102, s56, v102\n" \
  "v_mul_f32 v103, s56, v103\n" \
  "v_cvt_pk_bf16_f32 v133, v102, v103\n" \
  "v_mfma_f32_16x16x32_bf16 v[116:119], v[76:79], a[44:47], v[116:119]\n" \
  "s_add_u32 s46, s48, 147456\n" \
  "buffer_load_dwordx4 v197, %[ra], s46 offen lds\n" \
  "s_add_u32 m0, m0, 1024\n" \
  "v_mfma_f32_16x16x32_bf16 v[120:123], v[76:79], a[76:79], v[120:123]\n" \
  "ds_read_b128 v[40:43], v202 offset:16384\n" \
  "v_mul_f32 v104, s56, v104\n" \
  "v_mul_f32 v105, s56, v105\n" \
  "v_cvt_pk_bf16_f32 v136, v104, v105\n" \
  "v_mfma_f32_16x16x32_bf16 v[124:127], v[76:79], a[108:111], v[124:127]\n" \
  "s_add_u32 s46, s48, 196608\n" \
  "buffer_load_dwordx4 v198, %[ra], s46 offen lds\n" \
  "s_add_u32 m0, m0, 1024\n" \
  "s_waitcnt lgkmcnt(6)\n" \
  "v_mfma_f32_16x16x32_bf16 v[112:115], v[80:83], a[16:19], v[112:115]\n" \
  "ds_read_b128 v[44:47], v203 offset:16384\n" \
  "v_mul_f32 v106, s56, v106\n" \
  "v_mul_f32 v107, s56, v107\n" \
  "v_cvt_pk_bf16_f32 v137, v106, v107\n" \
  "v_mfma_f32_16x16x32_bf16 v[116:119], v[80:83], a[48:51], v[116:119]\n" \
  "s_add_u32 s46, s48, 212992\n" \
  "buffer_load_dwordx4 v199, %[ra], s46 offen lds\n" \
  "s_add_u32 m0, m0, 1024\n" \
  "v_mfma_f32_16x16x32_bf16 v[120:123], v[80:83], a[80:83], v[120:123]\n" \
  "ds_read_b128 v[48:51], v200 offset:16640\n" \
  "v_mul_f32 v108, s56, v108\n" \
  "v_mul_f32 v109, s56, v109\n" \
  "v_cvt_pk_bf16_f32 v140, v108, v109\n" \
  "v_mfma_f32_16x16x32_bf16 v[124:127], v[80:83], a[112:115], v[124:127]\n" \
  "s_waitcnt lgkmcnt(7)\n" \
  "v_mfma_f32_16x16x32_bf16 v[112:115], v[84:87], a[20:23], v[112:115]\n" \
  "ds_read_b128 v[52:55], v201 offset:16640\n" \
  "v_mul_f32 v110, s56, v110\n" \
  "v_mul_f32 v111, s56, v111\n" \
  "v_cvt_pk_bf16_f32 v141, v110, v111\n" \
  "v_mfma_f32_16x16x32_bf16 v[116:119], v[84:87], a[52:55], v[116:119]\n" \
  "v_mfma_f32_16x16x32_bf16 v[120:123], v[84:87], a[84:87], v[120:123]\n" \
  "ds_read_b128 v[56:59], v202 offset:16640\n" \
  "v_mfma_f32_16x16x32_bf16 v[124:127], v[84:87], a[116:119], v[124:127]\n" \
  "s_waitcnt lgkmcnt(8)\n" \
  "v_mfma_f32_16x16x32_bf16 v[112:115], v[88:91], a[24:27], v[112:115]\n" \
  "ds_read_b128 v[60:63], v203 offset:16640\n" \
  "v_mfma_f32_16x16x32_bf16 v[116:119], v[88:91], a[56:59], v[116:119]\n" \
  "v_mfma_f32_16x16x32_bf16 v[120:123], v[88:91], a[88:91], v[120:123]\n" \
  "v_mfma_f32_16x16x32_bf16 v[124:127], v[88:91], a[120:123], v[124:127]\n" \
  "s_waitcnt lgkmcnt(8)\n" \
  "v_mfma_f32_16x16x32_bf16 v[112:115], v[92:95], a[28:31], v[112:115]\n" \
  "v_mfma_f32_16x16x32_bf16 v[116:119], v[92:95], a[60:63], v[116:119]\n" \
  "v_mfma_f32_16x16x32_bf16 v[120:123], v[92:95], a[92:95], v[120:123]\n" \
  "v_mfma_f32_16x16x32_bf16 v[124:127], v[92:95], a[124:127], v[124:127]\n" \
  "s_waitcnt lgkmcnt(7)\n" \
  "v_mfma_f32_16x16x32_bf16 v[96:99], v[32:35], a[0:3], 0\n" \
  "v_mfma_f32_16x16x32_bf16 v[100:103], v[32:35], a[32:35], 0\n" \
  "v_mfma_f32_16x16x32_bf16 v[104:107], v[32:35], a[64:67], 0\n" \
  "v_mfma_f32_16x16x32_bf16 v[108:111], v[32:35], a[96:99], 0\n" \
  "s_add_u32 m0, %[ma], 8192\n" \
  "s_lshl_b32 s48, s54, 18\n" \
  "s_add_u32 s46, s48, 32768\n" \
  "buffer_load_dwordx4 v192, %[ra], s46 offen lds\n" \
  "s_add_u32 m0, m0, 1024\n" \
  "s_waitcnt lgkmcnt(6)\n" \
  "v_mfma_f32_16x16x32_bf16 v[96:99], v[36:39], a[4:7], v[96:99]\n" \
  "v_mfma_f32_16x16x32_bf16 v[100:103], v[36:39], a[36:39], v[100:103]\n" \
  "s_add_u32 s46, s48, 49152\n" \
  "buffer_load_dwordx4 v193, %[ra], s46 offen lds\n" \
  "s_add_u32 m0, m0, 1024\n" \
  "v_mfma_f32_16x16x32_bf16 v[104:107], v[36:39], a[68:71], v[104:107]\n" \
  "s_lshl_b32 s57, s52, 10\n" \
  "s_add_u32 s57, s57, %[sO]\n" \
  "v_mfma_f32_16x16x32_bf16 v[108:111], v[36:39], a[100:103], v[108:111]\n" \
  "s_add_u32 s46, s48, 98304\n" \
  "buffer_load_dwordx4 v194, %[ra], s46 offen lds\n" \
  "s_add_u32 m0, m0, 1024\n" \
  "s_waitcnt lgkmcnt(5)\n" \
  "v_mfma_f32_16x16x32_bf16 v[96:99], v[40:43], a[8:11], v[96:99]\n" \
  "s_waitcnt vmcnt(11)\n" \
  "v_mul_f32 v112, s56, v112\n" \
  "v_mul_f32 v113, s56, v113\n" \
  "v_cvt_pk_bf16_f32 v130, v112, v113\n" \
  "v_mfma_f32_16x16x32_bf16 v[100:103], v[40:43], a[40:43], v[100:103]\n" \
  "s_add_u32 s46, s48, 114688\n" \
  "buffer_load_dwordx4 v195, %[ra], s46 offen lds\n" \
  "s_add_u32 m0, m0, 1024\n" \
  "v_mfma_f32_16x16x32_bf16 v[104:107], v[40:43], a[72:75], v[104:107]\n" \
  "ds_read_b128 v[64:67], v200 offset:24576\n" \
  "v_mul_f32 v114, s56, v114\n" \
  "v_mul_f32 v115, s56, v115\n" \
  "v_cvt_pk_bf16_f32 v131, v114, v115\n" \
  "v_mfma_f32_16x16x32_bf16 v[108:111], v[40:43], a[104:107], v[108:111]\n" \
  "s_add_u32 s46, s48, 163840\n" \
  "buffer_load_dwordx4 v196, %[ra], s46 offen lds\n" \
  "s_add_u32 m0, m0, 1024\n" \
  "s_waitcnt lgkmcnt(5)\n" \
  "v_mfma_f32_16x16x32_bf16 v[96:99], v[44:47], a[12:15], v[96:99]\n" \
  "ds_read_b128 v[68:71], v201 offset:24576\n" \
  "buffer_store_dwordx4 v[128:131], %[l16], %[rs], s57 offen sc1\n" \
  "s_add_u32 s57, s57, 14336\n" \
  "v_mfma_f32_16x16x32_bf16 v[100:103], v[44:47], a[44:47], v[100:103]\n" \
  "s_add_u32 s46, s48, 180224\n" \
  "buffer_load_dwordx4 v197, %[ra], s46 offen lds\n" \
  "s_add_u32 m0, m0, 1024\n" \
  "v_mfma_f32_16x16x32_bf16 v[104:107], v[44:47], a[76:79], v[104:107]\n" \
  "ds_read_b128 v[72:75], v202 offset:24576\n" \
  "v_mul_f32 v116, s56, v116\n" \
  "v_mul_f32 v117, s56, v117\n" \
  "v_cvt_pk_bf16_f32 v134, v116, v117\n" \
  "v_mfma_f32_16x16x32_bf16 v[108:111], v[44:47], a[108:111], v[108:111]\n" \
  "s_add_u32 s46, s48, 229376\n" \
  "buffer_load_dwordx4 v198, %[ra], s46 offen lds\n" \
  "s_add_u32 m0, m0, 1024\n" \
  "s_waitcnt lgkmcnt(6)\n" \
  "v_mfma_f32_16x16x32_bf16 v[96:99], v[48:51], a[16:19], v[96:99]\n" \
  "ds_read_b128 v[76:79], v203 offset:24576\n" \
  "v_mul_f32 v118, s56, v118\n" \
  "v_mul_f32 v119, s56, v119\n" \
  "v_cvt_pk_bf16_f32 v135, v118, v119\n" \
  "v_mfma_f32_16x16x32_bf16 v[100:103], v[48:51], a[48:51], v[100:103]\n" \
  "s_add_u32 s46, s48, 245760\n" \
  "buffer_load_dwordx4 v199, %[ra], s46 offen lds\n" \
  "s_add_u32 m0, m0, 1024\n" \
  "v_mfma_f32_16x16x32_bf16 v[104:107], v[48:51], a[80:83], v[104:107]\n" \
  "ds_read_b128 v[80:83], v200 offset:24832\n" \
  "buffer_store_dwordx4 v[132:135], %[l16], %[rs], s57 offen sc1\n" \
  "s_add_u32 s57, s57, 14336\n" \
  "v_mfma_f32_16x16x32_bf16 v[108:111], v[48:51], a[112:115], v[108:111]\n" \
  "s_waitcnt lgkmcnt(7)\n" \
  "v_mfma_f32_16x16x32_bf16 v[96:99], v[52:55], a[20:23], v[96:99]\n" \
  "ds_read_b128 v[84:87], v201 offset:24832\n" \
  "v_mul_f32 v120, s56, v120\n" \
  "v_mul_f32 v121, s56, v121\n" \
  "v_cvt_pk_bf16_f32 v138, v120, v121\n" \
  "v_mfma_f32_16x16x32_bf16 v[100:103], v[52:55], a[52:55], v[100:103]\n" \
  "v_mfma_f32_16x16x32_bf16 v[104:107], v[52:55], a[84:87], v[104:107]\n" \
  "ds_read_b128 v[88:91], v202 offset:24832\n" \
  "v_mul_f32 v122, s56, v122\n" \
  "v_mul_f32 v123, s56, v123\n" \
  "v_cvt_pk_bf16_f32 v139, v122, v123\n" \
  "v_mfma_f32_16x16x32_bf16 v[108:111], v[52:55], a[116:119], v[108:111]\n" \
  "s_waitcnt lgkmcnt(8)\n" \
  "v_mfma_f32_16x16x32_bf16 v[96:99], v[56:59], a[24:27], v[96:99]\n" \
  "ds_read_b128 v[92:95], v203 offset:24832\n" \
  "buffer_store_dwordx4 v[136:139], %[l16], %[rs], s57 offen sc1\n" \
  "s_add_u32 s57, s57, 14336\n" \
  "v_mfma_f32_16x16x32_bf16 v[100:103], v[56:59], a[56:59], v[100:103]\n" \
  "v_mfma_f32_16x16x32_bf16 v[104:107], v[56:59], a[88:91], v[104:107]\n" \
  "v_mul_f32 v124, s56, v124\n" \
  "v_mul_f32 v125, s56, v125\n" \
  "v_cvt_pk_bf16_f32 v142, v124, v125\n" \
  "v_mfma_f32_16x16x32_bf16 v[108:111], v[56:59], a[120:123], v[108:111]\n" \
  "s_waitcnt lgkmcnt(8)\n" \
  "v_mfma_f32_16x16x32_bf16 v[96:99], v[60:63], a[28:31], v[96:99]\n" \
  "v_mul_f32 v126, s56, v126\n" \
  "v_mul_f32 v127, s56, v127\n" \
  "v_cvt_pk_bf16_f32 v143, v126, v127\n" \
  "v_mfma_f32_16x16x32_bf16 v[100:103], v[60:63], a[60:63], v[100:103]\n" \
  "v_mfma_f32_16x16x32_bf16 v[104:107], v[60:63], a[92:95], v[104:107]\n" \
  "buffer_store_dwordx4 v[140:143], %[l16], %[rs], s57 offen sc1\n" \
  "s_add_u32 s57, s57, 14336\n" \
  "v_mfma_f32_16x16x32_bf16 v[108:111], v[60:63], a[124:127], v[108:111]\n" \
  "s_cmp_lt_u32 %[w], 2\n" \
  "s_cbranch_scc0 .LP_three%=\n" \
  "s_waitcnt lgkmcnt(7)\n" \
  "v_mfma_f32_16x16x32_bf16 v[112:115], v[64:67], a[0:3], 0\n" \
  "v_mfma_f32_16x16x32_bf16 v[116:119], v[64:67], a[32:35], 0\n" \
  "v_mfma_f32_16x16x32_bf16 v[120:123], v[64:67], a[64:67], 0\n" \
  "v_mfma_f32_16x16x32_bf16 v[124:127], v[64:67], a[96:99], 0\n" \
  "s_add_u32 m0, %[ma], 16384\n" \
  "s_lshl_b32 s48, s55, 18\n" \
  "s_add_u32 s46, s48, 0\n" \
  "buffer_load_dwordx4 v192, %[ra], s46 offen lds\n" \
  "s_add_u32 m0, m0, 1024\n" \
  "s_waitcnt lgkmcnt(6)\n" \
  "v_mfma_f32_16x16x32_bf16 v[112:115], v[68:71], a[4:7], v[112:115]\n" \
  "v_mfma_f32_16x16x32_bf16 v[116:119], v[68:71], a[36:39], v[116:119]\n" \
  "s_add_u32 s46, s48, 16384\n" \
  "buffer_load_dwordx4 v193, %[ra], s46 offen lds\n" \
  "s_add_u32 m0, m0, 1024\n" \
  "v_mfma_f32_16x16x32_bf16 v[120:123], v[68:71], a[68:71], v[120:123]\n" \
  "v_mul_f32 v96, s56, v96\n" \
  "v_mul_f32 v97, s56, v97\n" \
  "v_cvt_pk_bf16_f32 v144, v96, v97\n" \
  "v_mfma_f32_16x16x32_bf16 v[124:127], v[68:71], a[100:103], v[124:127]\n" \
  "s_add_u32 s46, s48, 65536\n" \
  "buffer_load_dwordx4 v194, %[ra], s46 offen lds\n" \
  "s_add_u32 m0, m0, 1024\n" \
  "s_waitcnt lgkmcnt(5)\n" \
  "v_mfma_f32_16x16x32_bf16 v[112:115], v[72:75], a[8:11], v[112:115]\n" \
  "s_waitcnt vmcnt(15)\n" \
  "v_mul_f32 v98, s56, v98\n" \
  "v_mul_f32 v99, s56, v99\n" \
  "v_cvt_pk_bf16_f32 v145, v98, v99\n" \
  "v_mfma_f32_16x16x32_bf16 v[116:119], v[72:75], a[40:43], v[116:119]\n" \
  "s_add_u32 s46, s48, 81920\n" \
  "buffer_load_dwordx4 v195, %[ra], s46 offen lds\n" \
  "s_add_u32 m0, m0, 1024\n" \
  "v_mfma_f32_16x16x32_bf16 v[120:123], v[72:75], a[72:75], v[120:123]\n" \
  "ds_read_b128 v[32:35], v200\n" \
  "v_mul_f32 v100, s56, v100\n" \
  "v_mul_f32 v101, s56, v101\n" \
  "v_cvt_pk_bf16_f32 v148, v100, v101\n" \
  "v_mfma_f32_16x16x32_bf16 v[124:127], v[72:75], a[104:107], v[124:127]\n" \
  "s_add_u32 s46, s48, 131072\n" \
  "buffer_load_dwordx4 v196, %[ra], s46 offen lds\n" \
  "s_add_u32 m0, m0, 1024\n" \
  "s_waitcnt lgkmcnt(5)\n" \
  "v_mfma_f32_16x16x32_bf16 v[112:115], v[76:79], a[12:15], v[112:115]\n" \
  "ds_read_b128 v[36:39], v201\n" \
  "v_mul_f32 v102, s56, v102\n" \
  "v_mul_f32 v103, s56, v103\n" \
  "v_cvt_pk_bf16_f32 v149, v102, v103\n" \
  "v_mfma_f32_16x16x32_bf16 v[116:119], v[76:79], a[44:47], v[116:119]\n" \
  "s_add_u32 s46, s48, 147456\n" \
  "buffer_load_dwordx4 v197, %[ra], s46 offen lds\n" \
  "s_add_u32 m0, m0, 1024\n" \
  "v_mfma_f32_16x16x32_bf16 v[120:123], v[76:79], a[76:79], v[120:123]\n" \
  "ds_read_b128 v[40:43], v202\n" \
  "v_mul_f32 v104, s56, v104\n" \
  "v_mul_f32 v105, s56, v105\n" \
  "v_cvt_pk_bf16_f32 v152, v104, v105\n" \
  "v_mfma_f32_16x16x32_bf16 v[124:127], v[76:79], a[108:111], v[124:127]\n" \
  "s_add_u32 s46, s48, 196608\n" \
  "buffer_load_dwordx4 v198, %[ra], s46 offen lds\n" \
  "s_add_u32 m0, m0, 1024\n" \
  "s_waitcnt lgkmcnt(6)\n" \
  "v_mfma_f32_16x16x32_bf16 v[112:115], v[80:83], a[16:19], v[112:115]\n" \
  "ds_read_b128 v[44:47], v203\n" \
  "v_mul_f32 v106, s56, v106\n" \
  "v_mul_f32 v107, s56, v107\n" \
  "v_cvt_pk_bf16_f32 v153, v106, v107\n" \
  "v_mfma_f32_16x16x32_bf16 v[116:119], v[80:83], a[48:51], v[116:119]\n" \
  "s_add_u32 s46, s48, 212992\n" \
  "buffer_load_dwordx4 v199, %[ra], s46 offen lds\n" \
  "s_add_u32 m0, m0, 1024\n" \
  "v_mfma_f32_16x16x32_bf16 v[120:123], v[80:83], a[80:83], v[120:123]\n" \
  "ds_read_b128 v[48:51], v200 offset:256\n" \
  "v_mul_f32 v108, s56, v108\n" \
  "v_mul_f32 v109, s56, v109\n" \
  "v_cvt_pk_bf16_f32 v156, v108, v109\n" \
  "v_mfma_f32_16x16x32_bf16 v[124:127], v[80:83], a[112:115], v[124:127]\n" \
  "s_waitcnt lgkmcnt(7)\n" \
  "v_mfma_f32_16x16x32_bf16 v[112:115], v[84:87], a[20:23], v[112:115]\n" \
  "ds_read_b128 v[52:55], v201 offset:256\n" \
  "v_mul_f32 v110, s56, v110\n" \
  "v_mul_f32 v111, s56, v111\n" \
  "v_cvt_pk_bf16_f32 v157, v110, v111\n" \
  "v_mfma_f32_16x16x32_bf16 v[116:119], v[84:87], a[52:55], v[116:119]\n" \
  "v_mfma_f32_16x16x32_bf16 v[120:123], v[84:87], a[84:87], v[120:123]\n" \
  "ds_read_b128 v[56:59], v202 offset:256\n" \
  "v_mfma_f32_16x16x32_bf16 v[124:127], v[84:87], a[116:119], v[124:127]\n" \
  "s_waitcnt lgkmcnt(8)\n" \
  "v_mfma_f32_16x16x32_bf16 v[112:115], v[88:91], a[24:27], v[112:115]\n" \
  "ds_read_b128 v[60:63], v203 offset:256\n" \
  "v_mfma_f32_16x16x32_bf16 v[116:119], v[88:91], a[56:59], v[116:119]\n" \
  "v_mfma_f32_16x16x32_bf16 v[120:123], v[88:91], a[88:91], v[120:123]\n" \
  "v_mfma_f32_16x16x32_bf16 v[124:127], v[88:91], a[120:123], v[124:127]\n" \
  "s_waitcnt lgkmcnt(8)\n" \
  "v_mfma_f32_16x16x32_bf16 v[112:115], v[92:95], a[28:31], v[112:115]\n" \
  "v_mfma_f32_16x16x32_bf16 v[116:119], v[92:95], a[60:63], v[116:119]\n" \
  "v_mfma_f32_16x16x32_bf16 v[120:123], v[92:95], a[92:95], v[120:123]\n" \
  "v_mfma_f32_16x16x32_bf16 v[124:127], v[92:95], a[124:127], v[124:127]\n" \
  "s_waitcnt lgkmcnt(7)\n" \
  "v_mfma_f32_16x16x32_bf16 v[96:99], v[32:35], a[0:3], 0\n" \
  "v_mfma_f32_16x16x32_bf16 v[100:103], v[32:35], a[32:35], 0\n" \
  "v_mfma_f32_16x16x32_bf16 v[104:107], v[32:35], a[64:67], 0\n" \
  "v_mfma_f32_16x16x32_bf16 v[108:111], v[32:35], a[96:99], 0\n" \
  "s_add_u32 m0, %[ma], 24576\n" \
  "s_lshl_b32 s48, s55, 18\n" \
  "s_add_u32 s46, s48, 32768\n" \
  "buffer_load_dwordx4 v192, %[ra], s46 offen lds\n" \
  "s_add_u32 m0, m0, 1024\n" \
  "s_waitcnt lgkmcnt(6)\n" \
  "v_mfma_f32_16x16x32_bf16 v[96:99], v[36:39], a[4:7], v[96:99]\n" \
  "v_mfma_f32_16x16x32_bf16 v[100:103], v[36:39], a[36:39], v[100:103]\n" \
  "s_add_u32 s46, s48, 49152\n" \
  "buffer_load_dwordx4 v193, %[ra], s46 offen lds\n" \
  "s_add_u32 m0, m0, 1024\n" \
  "v_mfma_f32_16x16x32_bf16 v[104:107], v[36:39], a[68:71], v[104:107]\n" \
  "s_lshl_b32 s57, s53, 10\n" \
  "s_add_u32 s57, s57, %[sO]\n" \
  "v_mfma_f32_16x16x32_bf16 v[108:111], v[36:39], a[100:103], v[108:111]\n" \
  "s_add_u32 s46, s48, 98304\n" \
  "buffer_load_dwordx4 v194, %[ra], s46 offen lds\n" \
  "s_add_u32 m0, m0, 1024\n" \
  "s_waitcnt lgkmcnt(5)\n" \
  "v_mfma_f32_16x16x32_bf16 v[96:99], v[40:43], a[8:11], v[96:99]\n" \
  "s_waitcnt vmcnt(14)\n" \
  "v_mul_f32 v112, s56, v112\n" \
  "v_mul_f32 v113, s56, v113\n" \
  "v_cvt_pk_bf16_f32 v146, v112, v113\n" \
  "v_mfma_f32_16x16x32_bf16 v[100:103], v[40:43], a[40:43], v[100:103]\n" \
  "s_add_u32 s46, s48, 114688\n" \
  "buffer_load_dwordx4 v195, %[ra], s46 offen lds\n" \
  "s_add_u32 m0, m0, 1024\n" \
  "v_mfma_f32_16x16x32_bf16 v[104:107], v[40:43], a[72:75], v[104:107]\n" \
  "ds_read_b128 v[64:67], v200 offset:8192\n" \
  "v_mul_f32 v114, s56, v114\n" \
  "v_mul_f32 v115, s56, v115\n" \
  "v_cvt_pk_bf16_f32 v147, v114, v115\n" \
  "v_mfma_f32_16x16x32_bf16 v[108:111], v[40:43], a[104:107], v[108:111]\n" \
  "s_add_u32 s46, s48, 163840\n" \
  "buffer_load_dwordx4 v196, %[ra], s46 offen lds\n" \
  "s_add_u32 m0, m0, 1024\n" \
  "s_waitcnt lgkmcnt(5)\n" \
  "v_mfma_f32_16x16x32_bf16 v[96:99], v[44:47], a[12:15], v[96:99]\n" \
  "ds_read_b128 v[68:71], v201 offset:8192\n" \
  "buffer_store_dwordx4 v[144:147], %[l16], %[rs], s57 offen sc1\n" \
  "s_add_u32 s57, s57, 14336\n" \
  "v_mfma_f32_16x16x32_bf16 v[100:103], v[44:47], a[44:47], v[100:103]\n" \
  "s_add_u32 s46, s48, 180224\n" \
  "buffer_load_dwordx4 v197, %[ra], s46 offen lds\n" \
  "s_add_u32 m0, m0, 1024\n" \
  "v_mfma_f32_16x16x32_bf16 v[104:107], v[44:47], a[76:79], v[104:107]\n" \
  "ds_read_b128 v[72:75], v202 offset:8192\n" \
  "v_mul_f32 v116, s56, v116\n" \
  "v_mul_f32 v117, s56, v117\n" \
  "v_cvt_pk_bf16_f32 v150, v116, v117\n" \
  "v_mfma_f32_16x16x32_bf16 v[108:111], v[44:47], a[108:111], v[108:111]\n" \
  "s_add_u32 s46, s48, 229376\n" \
  "buffer_load_dwordx4 v198, %[ra], s46 offen lds\n" \
  "s_add_u32 m0, m0, 1024\n" \
  "s_waitcnt lgkmcnt(6)\n" \
  "v_mfma_f32_16x16x32_bf16 v[96:99], v[48:51], a[16:19], v[96:99]\n" \
  "ds_read_b128 v[76:79], v203 offset:8192\n" \
  "v_mul_f32 v118, s56, v118\n" \
  "v_mul_f32 v119, s56, v119\n" \
  "v_cvt_pk_bf16_f32 v151, v118, v119\n" \
  "v_mfma_f32_16x16x32_bf16 v[100:103], v[48:51], a[48:51], v[100:103]\n" \
  "s_add_u32 s46, s48, 245760\n" \
  "buffer_load_dwordx4 v199, %[ra], s46 offen lds\n" \
  "s_add_u32 m0, m0, 1024\n" \
  "v_mfma_f32_16x16x32_bf16 v[104:107], v[48:51], a[80:83], v[104:107]\n" \
  "ds_read_b128 v[80:83], v200 offset:8448\n" \
  "buffer_store_dwordx4 v[148:151], %[l16], %[rs], s57 offen sc1\n" \
  "s_add_u32 s57, s57, 14336\n" \
  "v_mfma_f32_16x16x32_bf16 v[108:111], v[48:51], a[112:115], v[108:111]\n" \
  "s_waitcnt lgkmcnt(7)\n" \
  "v_mfma_f32_16x16x32_bf16 v[96:99], v[52:55], a[20:23], v[96:99]\n" \
  "ds_read_b128 v[84:87], v201 offset:8448\n" \
  "v_mul_f32 v120, s56, v120\n" \
  "v_mul_f32 v121, s56, v121\n" \
  "v_cvt_pk_bf16_f32 v154, v120, v121\n" \
  "v_mfma_f32_16x16x32_bf16 v[100:103], v[52:55], a[52:55], v[100:103]\n" \
  "v_mfma_f32_16x16x32_bf16 v[104:107], v[52:55], a[84:87], v[104:107]\n" \
  "ds_read_b128 v[88:91], v202 offset:8448\n" \
  "v_mul_f32 v122, s56, v122\n" \
  "v_mul_f32 v123, s56, v123\n" \
  "v_cvt_pk_bf16_f32 v155, v122, v123\n" \
  "v_mfma_f32_16x16x32_bf16 v[108:111], v[52:55], a[116:119], v[108:111]\n" \
  "s_waitcnt lgkmcnt(8)\n" \
  "v_mfma_f32_16x16x32_bf16 v[96:99], v[56:59], a[24:27], v[96:99]\n" \
  "ds_read_b128 v[92:95], v203 offset:8448\n" \
  "buffer_store_dwordx4 v[152:155], %[l16], %[rs], s57 offen sc1\n" \
  "s_add_u32 s57, s57, 14336\n" \
  "v_mfma_f32_16x16x32_bf16 v[100:103], v[56:59], a[56:59], v[100:103]\n" \
  "v_mfma_f32_16x16x32_bf16 v[104:107], v[56:59], a[88:91], v[104:107]\n" \
  "v_mul_f32 v124, s56, v124\n" \
  "v_mul_f32 v125, s56, v125\n" \
  "v_cvt_pk_bf16_f32 v158, v124, v125\n" \
  "v_mfma_f32_16x16x32_bf16 v[108:111], v[56:59], a[120:123], v[108:111]\n" \
  "s_waitcnt lgkmcnt(8)\n" \
  "v_mfma_f32_16x16x32_bf16 v[96:99], v[60:63], a[28:31], v[96:99]\n" \
  "v_mul_f32 v126, s56, v126\n" \
  "v_mul_f32 v127, s56, v127\n" \
  "v_cvt_pk_bf16_f32 v159, v126, v127\n" \
  "v_mfma_f32_16x16x32_bf16 v[100:103], v[60:63], a[60:63], v[100:103]\n" \
  "v_mfma_f32_16x16x32_bf16 v[104:107], v[60:63], a[92:95], v[104:107]\n" \
  "buffer_store_dwordx4 v[156:159], %[l16], %[rs], s57 offen sc1\n" \
  "s_add_u32 s57, s57, 14336\n" \
  "v_mfma_f32_16x16x32_bf16 v[108:111], v[60:63], a[124:127], v[108:111]\n" \
  "s_waitcnt lgkmcnt(7)\n" \
  "v_mfma_f32_16x16x32_bf16 v[112:115], v[64:67], a[0:3], 0\n" \
  "v_mfma_f32_16x16x32_bf16 v[116:119], v[64:67], a[32:35], 0\n" \
  "v_mfma_f32_16x16x32_bf16 v[120:123], v[64:67], a[64:67], 0\n" \
  "v_mfma_f32_16x16x32_bf16 v[124:127], v[64:67], a[96:99], 0\n" \
  "s_waitcnt lgkmcnt(6)\n" \
  "v_mfma_f32_16x16x32_bf16 v[112:115], v[68:71], a[4:7], v[112:115]\n" \
  "v_mfma_f32_16x16x32_bf16 v[116:119], v[68:71], a[36:39], v[116:119]\n" \
  "v_mfma_f32_16x16x32_bf16 v[120:123], v[68:71], a[68:71], v[120:123]\n" \
  "v_mul_f32 v96, s56, v96\n" \
  "v_mul_f32 v97, s56, v97\n" \
  "v_cvt_pk_bf16_f32 v160, v96, v97\n" \
  "v_mfma_f32_16x16x32_bf16 v[124:127], v[68:71], a[100:103], v[124:127]\n" \
  "s_waitcnt lgkmcnt(5)\n" \
  "v_mfma_f32_16x16x32_bf16 v[112:115], v[72:75], a[8:11], v[112:115]\n" \
  "s_waitcnt vmcnt(12)\n" \
  "v_mul_f32 v98, s56, v98\n" \
  "v_mul_f32 v99, s56, v99\n" \
  "v_cvt_pk_bf16_f32 v161, v98, v99\n" \
  "v_mfma_f32_16x16x32_bf16 v[116:119], v[72:75], a[40:43], v[116:119]\n" \
  "v_mfma_f32_16x16x32_bf16 v[120:123], v[72:75], a[72:75], v[120:123]\n" \
  "ds_read_b128 v[32:35], v200 offset:16384\n" \
  "v_mul_f32 v100, s56, v100\n" \
  "v_mul_f32 v101, s56, v101\n" \
  "v_cvt_pk_bf16_f32 v164, v100, v101\n" \
  "v_mfma_f32_16x16x32_bf16 v[124:127], v[72:75], a[104:107], v[124:127]\n" \
  "s_waitcnt lgkmcnt(5)\n" \
  "v_mfma_f32_16x16x32_bf16 v[112:115], v[76:79], a[12:15], v[112:115]\n" \
  "ds_read_b128 v[36:39], v201 offset:16384\n" \
  "v_mul_f32 v102, s56, v102\n" \
  "v_mul_f32 v103, s56, v103\n" \
  "v_cvt_pk_bf16_f32 v165, v102, v103\n" \
  "v_mfma_f32_16x16x32_bf16 v[116:119], v[76:79], a[44:47], v[116:119]\n" \
  "v_mfma_f32_16x16x32_bf16 v[120:123], v[76:79], a[76:79], v[120:123]\n" \
  "ds_read_b128 v[40:43], v202 offset:16384\n" \
  "v_mul_f32 v104, s56, v104\n" \
  "v_mul_f32 v105, s56, v105\n" \
  "v_cvt_pk_bf16_f32 v168, v104, v105\n" \
  "v_mfma_f32_16x16x32_bf16 v[124:127], v[76:79], a[108:111], v[124:127]\n" \
  "s_waitcnt lgkmcnt(6)\n" \
  "v_mfma_f32_16x16x32_bf16 v[112:115], v[80:83], a[16:19], v[112:115]\n" \
  "ds_read_b128 v[44:47], v203 offset:16384\n" \
  "v_mul_f32 v106, s56, v106\n" \
  "v_mul_f32 v107, s56, v107\n" \
  "v_cvt_pk_bf16_f32 v169, v106, v107\n" \
  "v_mfma_f32_16x16x32_bf16 v[116:119], v[80:83], a[48:51], v[116:119]\n" \
  "v_mfma_f32_16x16x32_bf16 v[120:123], v[80:83], a[80:83], v[120:123]\n" \
  "ds_read_b128 v[48:51], v200 offset:16640\n" \
  "v_mul_f32 v108, s56, v108\n" \
  "v_mul_f32 v109, s56, v109\n" \
  "v_cvt_pk_bf16_f32 v172, v108, v109\n" \
  "v_mfma_f32_16x16x32_bf16 v[124:127], v[80:83], a[112:115], v[124:127]\n" \
  "s_waitcnt lgkmcnt(7)\n" \
  "v_mfma_f32_16x16x32_bf16 v[112:115], v[84:87], a[20:23], v[112:115]\n" \
  "ds_read_b128 v[52:55], v201 offset:16640\n" \
  "v_mul_f32 v110, s56, v110\n" \
  "v_mul_f32 v111, s56, v111\n" \
  "v_cvt_pk_bf16_f32 v173, v110, v111\n" \
  "v_mfma_f32_16x16x32_bf16 v[116:119], v[84:87], a[52:55], v[116:119]\n" \
  "v_mfma_f32_16x16x32_bf16 v[120:123], v[84:87], a[84:87], v[120:123]\n" \
  "ds_read_b128 v[56:59], v202 offset:16640\n" \
  "v_mfma_f32_16x16x32_bf16 v[124:127], v[84:87], a[116:119], v[124:127]\n" \
  "s_waitcnt lgkmcnt(8)\n" \
  "v_mfma_f32_16x16x32_bf16 v[112:115], v[88:91], a[24:27], v[112:115]\n" \
  "ds_read_b128 v[60:63], v203 offset:16640\n" \
  "v_mfma_f32_16x16x32_bf16 v[116:119], v[88:91], a[56:59], v[116:119]\n" \
  "v_mfma_f32_16x16x32_bf16 v[120:123], v[88:91], a[88:91], v[120:123]\n" \
  "v_mfma_f32_16x16x32_bf16 v[124:127], v[88:91], a[120:123], v[124:127]\n" \
  "s_waitcnt lgkmcnt(8)\n" \
  "v_mfma_f32_16x16x32_bf16 v[112:115], v[92:95], a[28:31], v[112:115]\n" \
  "v_mfma_f32_16x16x32_bf16 v[116:119], v[92:95], a[60:63], v[116:119]\n" \
  "v_mfma_f32_16x16x32_bf16 v[120:123], v[92:95], a[92:95], v[120:123]\n" \
  "v_mfma_f32_16x16x32_bf16 v[124:127], v[92:95], a[124:127], v[124:127]\n" \
  "s_waitcnt lgkmcnt(7)\n" \
  "v_mfma_f32_16x16x32_bf16 v[96:99], v[32:35], a[0:3], 0\n" \
  "v_mfma_f32_16x16x32_bf16 v[100:103], v[32:35], a[32:35], 0\n" \
  "v_mfma_f32_16x16x32_bf16 v[104:107], v[32:35], a[64:67], 0\n" \
  "v_mfma_f32_16x16x32_bf16 v[108:111], v[32:35], a[96:99], 0\n" \
  "s_waitcnt lgkmcnt(6)\n" \
  "v_mfma_f32_16x16x32_bf16 v[96:99], v[36:39], a[4:7], v[96:99]\n" \
  "v_mfma_f32_16x16x32_bf16 v[100:103], v[36:39], a[36:39], v[100:103]\n" \
  "v_mfma_f32_16x16x32_bf16 v[104:107], v[36:39], a[68:71], v[104:107]\n" \
  "s_lshl_b32 s57, s54, 10\n" \
  "s_add_u32 s57, s57, %[sO]\n" \
  "v_mfma_f32_16x16x32_bf16 v[108:111], v[36:39], a[100:103], v[108:111]\n" \
  "s_waitcnt lgkmcnt(5)\n" \
  "v_mfma_f32_16x16x32_bf16 v[96:99], v[40:43], a[8:11], v[96:99]\n" \
  "s_waitcnt vmcnt(3)\n" \
  "v_mul_f32 v112, s56, v112\n" \
  "v_mul_f32 v113, s56, v113\n" \
  "v_cvt_pk_bf16_f32 v162, v112, v113\n" \
  "v_mfma_f32_16x16x32_bf16 v[100:103], v[40:43], a[40:43], v[100:103]\n" \
  "v_mfma_f32_16x16x32_bf16 v[104:107], v[40:43], a[72:75], v[104:107]\n" \
  "ds_read_b128 v[64:67], v200 offset:24576\n" \
  "v_mul_f32 v114, s56, v114\n" \
  "v_mul_f32 v115, s56, v115\n" \
  "v_cvt_pk_bf16_f32 v163, v114, v115\n" \
  "v_mfma_f32_16x16x32_bf16 v[108:111], v[40:43], a[104:107], v[108:111]\n" \
  "s_waitcnt lgkmcnt(5)\n" \
  "v_mfma_f32_16x16x32_bf16 v[96:99], v[44:47], a[12:15], v[96:99]\n" \
  "ds_read_b128 v[68:71], v201 offset:24576\n" \
  "buffer_store_dwordx4 v[160:163], %[l16], %[rs], s57 offen sc1\n" \
  "s_add_u32 s57, s57, 14336\n" \
  "v_mfma_f32_16x16x32_bf16 v[100:103], v[44:47], a[44:47], v[100:103]\n" \
  "v_mfma_f32_16x16x32_bf16 v[104:107], v[44:47], a[76:79], v[104:107]\n" \
  "ds_read_b128 v[72:75], v202 offset:24576\n" \
  "v_mul_f32 v116, s56, v116\n" \
  "v_mul_f32 v117, s56, v117\n" \
  "v_cvt_pk_bf16_f32 v166, v116, v117\n" \
  "v_mfma_f32_16x16x32_bf16 v[108:111], v[44:47], a[108:111], v[108:111]\n" \
  "s_waitcnt lgkmcnt(6)\n" \
  "v_mfma_f32_16x16x32_bf16 v[96:99], v[48:51], a[16:19], v[96:99]\n" \
  "ds_read_b128 v[76:79], v203 offset:24576\n" \
  "v_mul_f32 v118, s56, v118\n" \
  "v_mul_f32 v119, s56, v119\n" \
  "v_cvt_pk_bf16_f32 v167, v118, v119\n" \
  "v_mfma_f32_16x16x32_bf16 v[100:103], v[48:51], a[48:51], v[100:103]\n" \
  "v_mfma_f32_16x16x32_bf16 v[104:107], v[48:51], a[80:83], v[104:107]\n" \
  "ds_read_b128 v[80:83], v200 offset:24832\n" \
  "buffer_store_dwordx4 v[164:167], %[l16], %[rs], s57 offen sc1\n" \
  "s_add_u32 s57, s57, 14336\n" \
  "v_mfma_f32_16x16x32_bf16 v[108:111], v[48:51], a[112:115], v[108:111]\n" \
  "s_waitcnt lgkmcnt(7)\n" \
  "v_mfma_f32_16x16x32_bf16 v[96:99], v[52:55], a[20:23], v[96:99]\n" \
  "ds_read_b128 v[84:87], v201 offset:24832\n" \
  "v_mul_f32 v120, s56, v120\n" \
  "v_mul_f32 v121, s56, v121\n" \
  "v_cvt_pk_bf16_f32 v170, v120, v121\n" \
  "v_mfma_f32_16x16x32_bf16 v[100:103], v[52:55], a[52:55], v[100:103]\n" \
  "v_mfma_f32_16x16x32_bf16 v[104:107], v[52:55], a[84:87], v[104:107]\n" \
  "ds_read_b128 v[88:91], v202 offset:24832\n" \
  "v_mul_f32 v122, s56, v122\n" \
  "v_mul_f32 v123, s56, v123\n" \
  "v_cvt_pk_bf16_f32 v171, v122, v123\n" \
  "v_mfma_f32_16x16x32_bf16 v[108:111], v[52:55], a[116:119], v[108:111]\n" \
  "s_waitcnt lgkmcnt(8)\n" \
  "v_mfma_f32_16x16x32_bf16 v[96:99], v[56:59], a[24:27], v[96:99]\n" \
  "ds_read_b128 v[92:95], v203 offset:24832\n" \
  "buffer_store_dwordx4 v[168:171], %[l16], %[rs], s57 offen sc1\n" \
  "s_add_u32 s57, s57, 14336\n" \
  "v_mfma_f32_16x16x32_bf16 v[100:103], v[56:59], a[56:59], v[100:103]\n" \
  "v_mfma_f32_16x16x32_bf16 v[104:107], v[56:59], a[88:91], v[104:107]\n" \
  "v_mul_f32 v124, s56, v124\n" \
  "v_mul_f32 v125, s56, v125\n" \
  "v_cvt_pk_bf16_f32 v174, v124, v125\n" \
  "v_mfma_f32_16x16x32_bf16 v[108:111], v[56:59], a[120:123], v[108:111]\n" \
  "s_waitcnt lgkmcnt(8)\n" \
  "v_mfma_f32_16x16x32_bf16 v[96:99], v[60:63], a[28:31], v[96:99]\n" \
  "v_mul_f32 v126, s56, v126\n" \
  "v_mul_f32 v127, s56, v127\n" \
  "v_cvt_pk_bf16_f32 v175, v126, v127\n" \
  "v_mfma_f32_16x16x32_bf16 v[100:103], v[60:63], a[60:63], v[100:103]\n" \
  "v_mfma_f32_16x16x32_bf16 v[104:107], v[60:63], a[92:95], v[104:107]\n" \
  "buffer_store_dwordx4 v[172:175], %[l16], %[rs], s57 offen sc1\n" \
  "s_add_u32 s57, s57, 14336\n" \
  "v_mfma_f32_16x16x32_bf16 v[108:111], v[60:63], a[124:127], v[108:111]\n" \
  "s_waitcnt lgkmcnt(0)\n" \
  "s_barrier\n" \
  "v_mfma_f32_16x16x32_bf16 v[112:115], v[64:67], a[0:3], 0\n" \
  "v_mfma_f32_16x16x32_bf16 v[116:119], v[64:67], a[32:35], 0\n" \
  "v_mfma_f32_16x16x32_bf16 v[120:123], v[64:67], a[64:67], 0\n" \
  "v_mfma_f32_16x16x32_bf16 v[124:127], v[64:67], a[96:99], 0\n" \
  "s_lshl_b32 s47, s52, 10\n" \
  "v_add_u32 v232, s47, %[l16]\n" \
  "ds_write_b128 v232, v[128:131]\n" \
  "v_mfma_f32_16x16x32_bf16 v[112:115], v[68:71], a[4:7], v[112:115]\n" \
  "v_mfma_f32_16x16x32_bf16 v[116:119], v[68:71], a[36:39], v[116:119]\n" \
  "ds_write_b128 v232, v[132:135] offset:14336\n" \
  "ds_write_b128 v232, v[136:139] offset:28672\n" \
  "v_mfma_f32_16x16x32_bf16 v[120:123], v[68:71], a[68:71], v[120:123]\n" \
  "v_mul_f32 v96, s56, v96\n" \
  "v_mul_f32 v97, s56, v97\n" \
  "v_cvt_pk_bf16_f32 v176, v96, v97\n" \
  "v_mfma_f32_16x16x32_bf16 v[124:127], v[68:71], a[100:103], v[124:127]\n" \
  "ds_write_b128 v232, v[140:143] offset:43008\n" \
  "s_lshl_b32 s47, s53, 10\n" \
  "v_add_u32 v232, s47, %[l16]\n" \
  "v_mfma_f32_16x16x32_bf16 v[112:115], v[72:75], a[8:11], v[112:115]\n" \
  "v_mul_f32 v98, s56, v98\n" \
  "v_mul_f32 v99, s56, v99\n" \
  "v_cvt_pk_bf16_f32 v177, v98, v99\n" \
  "v_mfma_f32_16x16x32_bf16 v[116:119], v[72:75], a[40:43], v[116:119]\n" \
  "ds_write_b128 v232, v[144:147]\n" \
  "ds_write_b128 v232, v[148:151] offset:14336\n" \
  "v_mfma_f32_16x16x32_bf16 v[120:123], v[72:75], a[72:75], v[120:123]\n" \
  "v_mul_f32 v100, s56, v100\n" \
  "v_mul_f32 v101, s56, v101\n" \
  "v_cvt_pk_bf16_f32 v180, v100, v101\n" \
  "v_mfma_f32_16x16x32_bf16 v[124:127], v[72:75], a[104:107], v[124:127]\n" \
  "ds_write_b128 v232, v[152:155] offset:28672\n" \
  "ds_write_b128 v232, v[156:159] offset:43008\n" \
  "v_mfma_f32_16x16x32_bf16 v[112:115], v[76:79], a[12:15], v[112:115]\n" \
  "v_mul_f32 v102, s56, v102\n" \
  "v_mul_f32 v103, s56, v103\n" \
  "v_cvt_pk_bf16_f32 v181, v102, v103\n" \
  "v_mfma_f32_16x16x32_bf16 v[116:119], v[76:79], a[44:47], v[116:119]\n" \
  "s_lshl_b32 s47, s54, 10\n" \
  "v_add_u32 v232, s47, %[l16]\n" \
  "ds_write_b128 v232, v[160:163]\n" \
  "v_mfma_f32_16x16x32_bf16 v[120:123], v[76:79], a[76:79], v[120:123]\n" \
  "v_mul_f32 v104, s56, v104\n" \
  "v_mul_f32 v105, s56, v105\n" \
  "v_cvt_pk_bf16_f32 v184, v104, v105\n" \
  "v_mfma_f32_16x16x32_bf16 v[124:127], v[76:79], a[108:111], v[124:127]\n" \
  "ds_write_b128 v232, v[164:167] offset:14336\n" \
  "ds_write_b128 v232, v[168:171] offset:28672\n" \
  "v_mfma_f32_16x16x32_bf16 v[112:115], v[80:83], a[16:19], v[112:115]\n" \
  "v_mul_f32 v106, s56, v106\n" \
  "v_mul_f32 v107, s56, v107\n" \
  "v_cvt_pk_bf16_f32 v185, v106, v107\n" \
  "v_mfma_f32_16x16x32_bf16 v[116:119], v[80:83], a[48:51], v[116:119]\n" \
  "ds_write_b128 v232, v[172:175] offset:43008\n" \
  "v_mfma_f32_16x16x32_bf16 v[120:123], v[80:83], a[80:83], v[120:123]\n" \
  "v_mul_f32 v108, s56, v108\n" \
  "v_mul_f32 v109, s56, v109\n" \
  "v_cvt_pk_bf16_f32 v188, v108, v109\n" \
  "v_mfma_f32_16x16x32_bf16 v[124:127], v[80:83], a[112:115], v[124:127]\n" \
  "v_mfma_f32_16x16x32_bf16 v[112:115], v[84:87], a[20:23], v[112:115]\n" \
  "v_mul_f32 v110, s56, v110\n" \
  "v_mul_f32 v111, s56, v111\n" \
  "v_cvt_pk_bf16_f32 v189, v110, v111\n" \
  "v_mfma_f32_16x16x32_bf16 v[116:119], v[84:87], a[52:55], v[116:119]\n" \
  "v_mfma_f32_16x16x32_bf16 v[120:123], v[84:87], a[84:87], v[120:123]\n" \
  "v_mfma_f32_16x16x32_bf16 v[124:127], v[84:87], a[116:119], v[124:127]\n" \
  "v_mfma_f32_16x16x32_bf16 v[112:115], v[88:91], a[24:27], v[112:115]\n" \
  "v_mfma_f32_16x16x32_bf16 v[116:119], v[88:91], a[56:59], v[116:119]\n" \
  "v_mfma_f32_16x16x32_bf16 v[120:123], v[88:91], a[88:91], v[120:123]\n" \
  "v_mfma_f32_16x16x32_bf16 v[124:127], v[88:91], a[120:123], v[124:127]\n" \
  "v_mfma_f32_16x16x32_bf16 v[112:115], v[92:95], a[28:31], v[112:115]\n" \
  "v_mfma_f32_16x16x32_bf16 v[116:119], v[92:95], a[60:63], v[116:119]\n" \
  "v_mfma_f32_16x16x32_bf16 v[120:123], v[92:95], a[92:95], v[120:123]\n" \
  "v_mfma_f32_16x16x32_bf16 v[124:127], v[92:95], a[124:127], v[124:127]\n" \
  "s_lshl_b32 s57, s55, 10\n" \
  "s_add_u32 s57, s57, %[sO]\n" \
  "s_nop 2\n" \
  "v_mul_f32 v112, s56, v112\n" \
  "v_mul_f32 v113, s56, v113\n" \
  "v_cvt_pk_bf16_f32 v178, v112, v113\n" \
  "v_mul_f32 v114, s56, v114\n" \
  "v_mul_f32 v115, s56, v115\n" \
  "v_cvt_pk_bf16_f32 v179, v114, v115\n" \
  "buffer_store_dwordx4 v[176:179], %[l16], %[rs], s57 offen sc1\n" \
  "s_add_u32 s57, s57, 14336\n" \
  "v_mul_f32 v116, s56, v116\n" \
  "v_mul_f32 v117, s56, v117\n" \
  "v_cvt_pk_bf16_f32 v182, v116, v117\n" \
  "v_mul_f32 v118, s56, v118\n" \
  "v_mul_f32 v119, s56, v119\n" \
  "v_cvt_pk_bf16_f32 v183, v118, v119\n" \
  "buffer_store_dwordx4 v[180:183], %[l16], %[rs], s57 offen sc1\n" \
  "s_add_u32 s57, s57, 14336\n" \
  "v_mul_f32 v120, s56, v120\n" \
  "v_mul_f32 v121, s56, v121\n" \
  "v_cvt_pk_bf16_f32 v186, v120, v121\n" \
  "v_mul_f32 v122, s56, v122\n" \
  "v_mul_f32 v123, s56, v123\n" \
  "v_cvt_pk_bf16_f32 v187, v122, v123\n" \
  "buffer_store_dwordx4 v[184:187], %[l16], %[rs], s57 offen sc1\n" \
  "s_add_u32 s57, s57, 14336\n" \
  "v_mul_f32 v124, s56, v124\n" \
  "v_mul_f32 v125, s56, v125\n" \
  "v_cvt_pk_bf16_f32 v190, v124, v125\n" \
  "v_mul_f32 v126, s56, v126\n" \
  "v_mul_f32 v127, s56, v127\n" \
  "v_cvt_pk_bf16_f32 v191, v126, v127\n" \
  "buffer_store_dwordx4 v[188:191], %[l16], %[rs], s57 offen sc1\n" \
  "s_add_u32 s57, s57, 14336\n" \
  "s_lshl_b32 s47, s55, 10\n" \
  "v_add_u32 v232, s47, %[l16]\n" \
  "ds_write_b128 v232, v[176:179]\n" \
  "ds_write_b128 v232, v[180:183] offset:14336\n" \
  "ds_write_b128 v232, v[184:187] offset:28672\n" \
  "ds_write_b128 v232, v[188:191] offset:43008\n" \
  "s_branch .LP_fin%=\n" \
  ".LP_three%=:\n" \
  "s_waitcnt lgkmcnt(7)\n" \
  "v_mfma_f32_16x16x32_bf16 v[112:115], v[64:67], a[0:3], 0\n" \
  "v_mfma_f32_16x16x32_bf16 v[116:119], v[64:67], a[32:35], 0\n" \
  "v_mfma_f32_16x16x32_bf16 v[120:123], v[64:67], a[64:67], 0\n" \
  "v_mfma_f32_16x16x32_bf16 v[124:127], v[64:67], a[96:99], 0\n" \
  "s_waitcnt lgkmcnt(6)\n" \
  "v_mfma_f32_16x16x32_bf16 v[112:115], v[68:71], a[4:7], v[112:115]\n" \
  "v_mfma_f32_16x16x32_bf16 v[116:119], v[68:71], a[36:39], v[116:119]\n" \
  "v_mfma_f32_16x16x32_bf16 v[120:123], v[68:71], a[68:71], v[120:123]\n" \
  "v_mul_f32 v96, s56, v96\n" \
  "v_mul_f32 v97, s56, v97\n" \
  "v_cvt_pk_bf16_f32 v144, v96, v97\n" \
  "v_mfma_f32_16x16x32_bf16 v[124:127], v[68:71], a[100:103], v[124:127]\n" \
  "s_waitcnt lgkmcnt(5)\n" \
  "v_mfma_f32_16x16x32_bf16 v[112:115], v[72:75], a[8:11], v[112:115]\n" \
  "s_waitcnt vmcnt(12)\n" \
  "v_mul_f32 v98, s56, v98\n" \
  "v_mul_f32 v99, s56, v99\n" \
  "v_cvt_pk_bf16_f32 v145, v98, v99\n" \
  "v_mfma_f32_16x16x32_bf16 v[116:119], v[72:75], a[40:43], v[116:119]\n" \
  "v_mfma_f32_16x16x32_bf16 v[120:123], v[72:75], a[72:75], v[120:123]\n" \
  "ds_read_b128 v[32:35], v200\n" \
  "v_mul_f32 v100, s56, v100\n" \
  "v_mul_f32 v101, s56, v101\n" \
  "v_cvt_pk_bf16_f32 v148, v100, v101\n" \
  "v_mfma_f32_16x16x32_bf16 v[124:127], v[72:75], a[104:107], v[124:127]\n" \
  "s_waitcnt lgkmcnt(5)\n" \
  "v_mfma_f32_16x16x32_bf16 v[112:115], v[76:79], a[12:15], v[112:115]\n" \
  "ds_read_b128 v[36:39], v201\n" \
  "v_mul_f32 v102, s56, v102\n" \
  "v_mul_f32 v103, s56, v103\n" \
  "v_cvt_pk_bf16_f32 v149, v102, v103\n" \
  "v_mfma_f32_16x16x32_bf16 v[116:119], v[76:79], a[44:47], v[116:119]\n" \
  "v_mfma_f32_16x16x32_bf16 v[120:123], v[76:79], a[76:79], v[120:123]\n" \
  "ds_read_b128 v[40:43], v202\n" \
  "v_mul_f32 v104, s56, v104\n" \
  "v_mul_f32 v105, s56, v105\n" \
  "v_cvt_pk_bf16_f32 v152, v104, v105\n" \
  "v_mfma_f32_16x16x32_bf16 v[124:127], v[76:79], a[108:111], v[124:127]\n" \
  "s_waitcnt lgkmcnt(6)\n" \
  "v_mfma_f32_16x16x32_bf16 v[112:115], v[80:83], a[16:19], v[112:115]\n" \
  "ds_read_b128 v[44:47], v203\n" \
  "v_mul_f32 v106, s56, v106\n" \
  "v_mul_f32 v107, s56, v107\n" \
  "v_cvt_pk_bf16_f32 v153, v106, v107\n" \
  "v_mfma_f32_16x16x32_bf16 v[116:119], v[80:83], a[48:51], v[116:119]\n" \
  "v_mfma_f32_16x16x32_bf16 v[120:123], v[80:83], a[80:83], v[120:123]\n" \
  "ds_read_b128 v[48:51], v200 offset:256\n" \
  "v_mul_f32 v108, s56, v108\n" \
  "v_mul_f32 v109, s56, v109\n" \
  "v_cvt_pk_bf16_f32 v156, v108, v109\n" \
  "v_mfma_f32_16x16x32_bf16 v[124:127], v[80:83], a[112:115], v[124:127]\n" \
  "s_waitcnt lgkmcnt(7)\n" \
  "v_mfma_f32_16x16x32_bf16 v[112:115], v[84:87], a[20:23], v[112:115]\n" \
  "ds_read_b128 v[52:55], v201 offset:256\n" \
  "v_mul_f32 v110, s56, v110\n" \
  "v_mul_f32 v111, s56, v111\n" \
  "v_cvt_pk_bf16_f32 v157, v110, v111\n" \
  "v_mfma_f32_16x16x32_bf16 v[116:119], v[84:87], a[52:55], v[116:119]\n" \
  "v_mfma_f32_16x16x32_bf16 v[120:123], v[84:87], a[84:87], v[120:123]\n" \
  "ds_read_b128 v[56:59], v202 offset:256\n" \
  "v_mfma_f32_16x16x32_bf16 v[124:127], v[84:87], a[116:119], v[124:127]\n" \
  "s_waitcnt lgkmcnt(8)\n" \
  "v_mfma_f32_16x16x32_bf16 v[112:115], v[88:91], a[24:27], v[112:115]\n" \
  "ds_read_b128 v[60:63], v203 offset:256\n" \
  "v_mfma_f32_16x16x32_bf16 v[116:119], v[88:91], a[56:59], v[116:119]\n" \
  "v_mfma_f32_16x16x32_bf16 v[120:123], v[88:91], a[88:91], v[120:123]\n" \
  "v_mfma_f32_16x16x32_bf16 v[124:127], v[88:91], a[120:123], v[124:127]\n" \
  "s_waitcnt lgkmcnt(8)\n" \
  "v_mfma_f32_16x16x32_bf16 v[112:115], v[92:95], a[28:31], v[112:115]\n" \
  "v_mfma_f32_16x16x32_bf16 v[116:119], v[92:95], a[60:63], v[116:119]\n" \
  "v_mfma_f32_16x16x32_bf16 v[120:123], v[92:95], a[92:95], v[120:123]\n" \
  "v_mfma_f32_16x16x32_bf16 v[124:127], v[92:95], a[124:127], v[124:127]\n" \
  "s_waitcnt lgkmcnt(7)\n" \
  "v_mfma_f32_16x16x32_bf16 v[96:99], v[32:35], a[0:3], 0\n" \
  "v_mfma_f32_16x16x32_bf16 v[100:103], v[32:35], a[32:35], 0\n" \
  "v_mfma_f32_16x16x32_bf16 v[104:107], v[32:35], a[64:67], 0\n" \
  "v_mfma_f32_16x16x32_bf16 v[108:111], v[32:35], a[96:99], 0\n" \
  "v_add_u32 v192, 0, %[l16]\n" \
  "v_lshrrev_b32 v199, 8, v192\n" \
  "v_mul_u32_u24 v199, 9363, v199\n" \
  "v_lshrrev_b32 v199, 12, v199\n" \
  "v_and_b32 v199, 0x70, v199\n" \
  "v_xor_b32 v192, v192, v199\n" \
  "s_waitcnt lgkmcnt(6)\n" \
  "v_mfma_f32_16x16x32_bf16 v[96:99], v[36:39], a[4:7], v[96:99]\n" \
  "v_mfma_f32_16x16x32_bf16 v[100:103], v[36:39], a[36:39], v[100:103]\n" \
  "v_add_u32 v193, 1024, %[l16]\n" \
  "v_lshrrev_b32 v199, 8, v193\n" \
  "v_mul_u32_u24 v199, 9363, v199\n" \
  "v_lshrrev_b32 v199, 12, v199\n" \
  "v_and_b32 v199, 0x70, v199\n" \
  "v_xor_b32 v193, v193, v199\n" \
  "v_mfma_f32_16x16x32_bf16 v[104:107], v[36:39], a[68:71], v[104:107]\n" \
  "s_lshl_b32 s57, s53, 10\n" \
  "s_add_u32 s57, s57, %[sO]\n" \
  "v_mfma_f32_16x16x32_bf16 v[108:111], v[36:39], a[100:103], v[108:111]\n" \
  "v_add_u32 v194, 2048, %[l16]\n" \
  "v_lshrrev_b32 v199, 8, v194\n" \
  "v_mul_u32_u24 v199, 9363, v199\n" \
  "v_lshrrev_b32 v199, 12, v199\n" \
  "v_and_b32 v199, 0x70, v199\n" \
  "v_xor_b32 v194, v194, v199\n" \
  "s_waitcnt lgkmcnt(5)\n" \
  "v_mfma_f32_16x16x32_bf16 v[96:99], v[40:43], a[8:11], v[96:99]\n" \
  "s_waitcnt vmcnt(3)\n" \
  "v_mul_f32 v112, s56, v112\n" \
  "v_mul_f32 v113, s56, v113\n" \
  "v_cvt_pk_bf16_f32 v146, v112, v113\n" \
  "v_mfma_f32_16x16x32_bf16 v[100:103], v[40:43], a[40:43], v[100:103]\n" \
  "v_add_u32 v195, 3072, %[l16]\n" \
  "v_lshrrev_b32 v199, 8, v195\n" \
  "v_mul_u32_u24 v199, 9363, v199\n" \
  "v_lshrrev_b32 v199, 12, v199\n" \
  "v_and_b32 v199, 0x70, v199\n" \
  "v_xor_b32 v195, v195, v199\n" \
  "v_mfma_f32_16x16x32_bf16 v[104:107], v[40:43], a[72:75], v[104:107]\n" \
  "ds_read_b128 v[64:67], v200 offset:8192\n" \
  "v_mul_f32 v114, s56, v114\n" \
  "v_mul_f32 v115, s56, v115\n" \
  "v_cvt_pk_bf16_f32 v147, v114, v115\n" \
  "v_mfma_f32_16x16x32_bf16 v[108:111], v[40:43], a[104:107], v[108:111]\n" \
  "v_add_u32 v196, 4096, %[l16]\n" \
  "v_lshrrev_b32 v199, 8, v196\n" \
  "v_mul_u32_u24 v199, 9363, v199\n" \
  "v_lshrrev_b32 v199, 12, v199\n" \
  "v_and_b32 v199, 0x70, v199\n" \
  "v_xor_b32 v196, v196, v199\n" \
  "s_waitcnt lgkmcnt(5)\n" \
  "v_mfma_f32_16x16x32_bf16 v[96:99], v[44:47], a[12:15], v[96:99]\n" \
  "ds_read_b128 v[68:71], v201 offset:8192\n" \
  "buffer_store_dwordx4 v[144:147], %[l16], %[rs], s57 offen sc1\n" \
  "s_add_u32 s57, s57, 14336\n" \
  "v_mfma_f32_16x16x32_bf16 v[100:103], v[44:47], a[44:47], v[100:103]\n" \
  "v_add_u32 v197, 5120, %[l16]\n" \
  "v_lshrrev_b32 v199, 8, v197\n" \
  "v_mul_u32_u24 v199, 9363, v199\n" \
  "v_lshrrev_b32 v199, 12, v199\n" \
  "v_and_b32 v199, 0x70, v199\n" \
  "v_xor_b32 v197, v197, v199\n" \
  "v_mfma_f32_16x16x32_bf16 v[104:107], v[44:47], a[76:79], v[104:107]\n" \
  "ds_read_b128 v[72:75], v202 offset:8192\n" \
  "v_mul_f32 v116, s56, v116\n" \
  "v_mul_f32 v117, s56, v117\n" \
  "v_cvt_pk_bf16_f32 v150, v116, v117\n" \
  "v_mfma_f32_16x16x32_bf16 v[108:111], v[44:47], a[108:111], v[108:111]\n" \
  "v_add_u32 v198, 6144, %[l16]\n" \
  "v_lshrrev_b32 v199, 8, v198\n" \
  "v_mul_u32_u24 v199, 9363, v199\n" \
  "v_lshrrev_b32 v199, 12, v199\n" \
  "v_and_b32 v199, 0x70, v199\n" \
  "v_xor_b32 v198, v198, v199\n" \
  "s_waitcnt lgkmcnt(6)\n" \
  "v_mfma_f32_16x16x32_bf16 v[96:99], v[48:51], a[16:19], v[96:99]\n" \
  "ds_read_b128 v[76:79], v203 offset:8192\n" \
  "v_mul_f32 v118, s56, v118\n" \
  "v_mul_f32 v119, s56, v119\n" \
  "v_cvt_pk_bf16_f32 v151, v118, v119\n" \
  "v_mfma_f32_16x16x32_bf16 v[100:103], v[48:51], a[48:51], v[100:103]\n" \
  "v_mfma_f32_16x16x32_bf16 v[104:107], v[48:51], a[80:83], v[104:107]\n" \
  "ds_read_b128 v[80:83], v200 offset:8448\n" \
  "buffer_store_dwordx4 v[148:151], %[l16], %[rs], s57 offen sc1\n" \
  "s_add_u32 s57, s57, 14336\n" \
  "v_mfma_f32_16x16x32_bf16 v[108:111], v[48:51], a[112:115], v[108:111]\n" \
  "s_waitcnt lgkmcnt(7)\n" \
  "v_mfma_f32_16x16x32_bf16 v[96:99], v[52:55], a[20:23], v[96:99]\n" \
  "ds_read_b128 v[84:87], v201 offset:8448\n" \
  "v_mul_f32 v120, s56, v120\n" \
  "v_mul_f32 v121, s56, v121\n" \
  "v_cvt_pk_bf16_f32 v154, v120, v121\n" \
  "v_mfma_f32_16x16x32_bf16 v[100:103], v[52:55], a[52:55], v[100:103]\n" \
  "v_mfma_f32_16x16x32_bf16 v[104:107], v[52:55], a[84:87], v[104:107]\n" \
  "ds_read_b128 v[88:91], v202 offset:8448\n" \
  "v_mul_f32 v122, s56, v122\n" \
  "v_mul_f32 v123, s56, v123\n" \
  "v_cvt_pk_bf16_f32 v155, v122, v123\n" \
  "v_mfma_f32_16x16x32_bf16 v[108:111], v[52:55], a[116:119], v[108:111]\n" \
  "s_waitcnt lgkmcnt(8)\n" \
  "v_mfma_f32_16x16x32_bf16 v[96:99], v[56:59], a[24:27], v[96:99]\n" \
  "ds_read_b128 v[92:95], v203 offset:8448\n" \
  "buffer_store_dwordx4 v[152:155], %[l16], %[rs], s57 offen sc1\n" \
  "s_add_u32 s57, s57, 14336\n" \
  "v_mfma_f32_16x16x32_bf16 v[100:103], v[56:59], a[56:59], v[100:103]\n" \
  "v_mfma_f32_16x16x32_bf16 v[104:107], v[56:59], a[88:91], v[104:107]\n" \
  "v_mul_f32 v124, s56, v124\n" \
  "v_mul_f32 v125, s56, v125\n" \
  "v_cvt_pk_bf16_f32 v158, v124, v125\n" \
  "v_mfma_f32_16x16x32_bf16 v[108:111], v[56:59], a[120:123], v[108:111]\n" \
  "s_waitcnt lgkmcnt(8)\n" \
  "v_mfma_f32_16x16x32_bf16 v[96:99], v[60:63], a[28:31], v[96:99]\n" \
  "v_mul_f32 v126, s56, v126\n" \
  "v_mul_f32 v127, s56, v127\n" \
  "v_cvt_pk_bf16_f32 v159, v126, v127\n" \
  "v_mfma_f32_16x16x32_bf16 v[100:103], v[60:63], a[60:63], v[100:103]\n" \
  "v_mfma_f32_16x16x32_bf16 v[104:107], v[60:63], a[92:95], v[104:107]\n" \
  "buffer_store_dwordx4 v[156:159], %[l16], %[rs], s57 offen sc1\n" \
  "s_add_u32 s57, s57, 14336\n" \
  "v_mfma_f32_16x16x32_bf16 v[108:111], v[60:63], a[124:127], v[108:111]\n" \
  "s_waitcnt lgkmcnt(7)\n" \
  "v_mfma_f32_16x16x32_bf16 v[112:115], v[64:67], a[0:3], 0\n" \
  "v_mfma_f32_16x16x32_bf16 v[116:119], v[64:67], a[32:35], 0\n" \
  "v_mfma_f32_16x16x32_bf16 v[120:123], v[64:67], a[64:67], 0\n" \
  "v_mfma_f32_16x16x32_bf16 v[124:127], v[64:67], a[96:99], 0\n" \
  "s_waitcnt lgkmcnt(0)\n" \
  "s_mov_b32 m0, %[ma]\n" \
  "s_sub_u32 s48, %[w], 2\n" \
  "s_mul_i32 s48, s48, 28672\n" \
  "buffer_load_dwordx4 v192, %[rx], s48 offen lds\n" \
  "s_add_u32 m0, m0, 1024\n" \
  "v_xor_b32 v192, 64, v192\n" \
  "buffer_load_dwordx4 v193, %[rx], s48 offen lds\n" \
  "s_add_u32 m0, m0, 1024\n" \
  "v_xor_b32 v193, 64, v193\n" \
  "v_mfma_f32_16x16x32_bf16 v[112:115], v[68:71], a[4:7], v[112:115]\n" \
  "v_mfma_f32_16x16x32_bf16 v[116:119], v[68:71], a[36:39], v[116:119]\n" \
  "buffer_load_dwordx4 v194, %[rx], s48 offen lds\n" \
  "s_add_u32 m0, m0, 1024\n" \
  "v_xor_b32 v194, 64, v194\n" \
  "buffer_load_dwordx4 v195, %[rx], s48 offen lds\n" \
  "s_add_u32 m0, m0, 1024\n" \
  "v_xor_b32 v195, 64, v195\n" \
  "v_mfma_f32_16x16x32_bf16 v[120:123], v[68:71], a[68:71], v[120:123]\n" \
  "v_mul_f32 v96, s56, v96\n" \
  "v_mul_f32 v97, s56, v97\n" \
  "v_cvt_pk_bf16_f32 v160, v96, v97\n" \
  "v_mfma_f32_16x16x32_bf16 v[124:127], v[68:71], a[100:103], v[124:127]\n" \
  "buffer_load_dwordx4 v196, %[rx], s48 offen lds\n" \
  "s_add_u32 m0, m0, 1024\n" \
  "v_xor_b32 v196, 64, v196\n" \
  "buffer_load_dwordx4 v197, %[rx], s48 offen lds\n" \
  "s_add_u32 m0, m0, 1024\n" \
  "v_xor_b32 v197, 64, v197\n" \
  "v_mfma_f32_16x16x32_bf16 v[112:115], v[72:75], a[8:11], v[112:115]\n" \
  "v_mul_f32 v98, s56, v98\n" \
  "v_mul_f32 v99, s56, v99\n" \
  "v_cvt_pk_bf16_f32 v161, v98, v99\n" \
  "v_mfma_f32_16x16x32_bf16 v[116:119], v[72:75], a[40:43], v[116:119]\n" \
  "buffer_load_dwordx4 v198, %[rx], s48 offen lds\n" \
  "s_add_u32 m0, m0, 1024\n" \
  "v_xor_b32 v198, 64, v198\n" \
  "s_add_u32 s48, s48, 7168\n" \
  "buffer_load_dwordx4 v192, %[rx], s48 offen lds\n" \
  "s_add_u32 m0, m0, 1024\n" \
  "v_xor_b32 v192, 64, v192\n" \
  "v_mfma_f32_16x16x32_bf16 v[120:123], v[72:75], a[72:75], v[120:123]\n" \
  "v_mul_f32 v100, s56, v100\n" \
  "v_mul_f32 v101, s56, v101\n" \
  "v_cvt_pk_bf16_f32 v164, v100, v101\n" \
  "v_mfma_f32_16x16x32_bf16 v[124:127], v[72:75], a[104:107], v[124:127]\n" \
  "buffer_load_dwordx4 v193, %[rx], s48 offen lds\n" \
  "s_add_u32 m0, m0, 1024\n" \
  "v_xor_b32 v193, 64, v193\n" \
  "buffer_load_dwordx4 v194, %[rx], s48 offen lds\n" \
  "s_add_u32 m0, m0, 1024\n" \
  "v_xor_b32 v194, 64, v194\n" \
  "v_mfma_f32_16x16x32_bf16 v[112:115], v[76:79], a[12:15], v[112:115]\n" \
  "v_mul_f32 v102, s56, v102\n" \
  "v_mul_f32 v103, s56, v103\n" \
  "v_cvt_pk_bf16_f32 v165, v102, v103\n" \
  "v_mfma_f32_16x16x32_bf16 v[116:119], v[76:79], a[44:47], v[116:119]\n" \
  "buffer_load_dwordx4 v195, %[rx], s48 offen lds\n" \
  "s_add_u32 m0, m0, 1024\n" \
  "v_xor_b32 v195, 64, v195\n" \
  "buffer_load_dwordx4 v196, %[rx], s48 offen lds\n" \
  "s_add_u32 m0, m0, 1024\n" \
  "v_xor_b32 v196, 64, v196\n" \
  "v_mfma_f32_16x16x32_bf16 v[120:123], v[76:79], a[76:79], v[120:123]\n" \
  "v_mul_f32 v104, s56, v104\n" \
  "v_mul_f32 v105, s56, v105\n" \
  "v_cvt_pk_bf16_f32 v168, v104, v105\n" \
  "v_mfma_f32_16x16x32_bf16 v[124:127], v[76:79], a[108:111], v[124:127]\n" \
  "buffer_load_dwordx4 v197, %[rx], s48 offen lds\n" \
  "s_add_u32 m0, m0, 1024\n" \
  "v_xor_b32 v197, 64, v197\n" \
  "buffer_load_dwordx4 v198, %[rx], s48 offen lds\n" \
  "s_add_u32 m0, m0, 1024\n" \
  "v_xor_b32 v198, 64, v198\n" \
  "s_add_u32 s48, s48, 7168\n" \
  "v_mfma_f32_16x16x32_bf16 v[112:115], v[80:83], a[16:19], v[112:115]\n" \
  "v_mul_f32 v106, s56, v106\n" \
  "v_mul_f32 v107, s56, v107\n" \
  "v_cvt_pk_bf16_f32 v169, v106, v107\n" \
  "v_mfma_f32_16x16x32_bf16 v[116:119], v[80:83], a[48:51], v[116:119]\n" \
  "buffer_load_dwordx4 v192, %[rx], s48 offen lds\n" \
  "s_add_u32 m0, m0, 1024\n" \
  "v_xor_b32 v192, 64, v192\n" \
  "buffer_load_dwordx4 v193, %[rx], s48 offen lds\n" \
  "s_add_u32 m0, m0, 1024\n" \
  "v_xor_b32 v193, 64, v193\n" \
  "v_mfma_f32_16x16x32_bf16 v[120:123], v[80:83], a[80:83], v[120:123]\n" \
  "v_mul_f32 v108, s56, v108\n" \
  "v_mul_f32 v109, s56, v109\n" \
  "v_cvt_pk_bf16_f32 v172, v108, v109\n" \
  "v_mfma_f32_16x16x32_bf16 v[124:127], v[80:83], a[112:115], v[124:127]\n" \
  "buffer_load_dwordx4 v194, %[rx], s48 offen lds\n" \
  "s_add_u32 m0, m0, 1024\n" \
  "v_xor_b32 v194, 64, v194\n" \
  "buffer_load_dwordx4 v195, %[rx], s48 offen lds\n" \
  "s_add_u32 m0, m0, 1024\n" \
  "v_xor_b32 v195, 64, v195\n" \
  "v_mfma_f32_16x16x32_bf16 v[112:115], v[84:87], a[20:23], v[112:115]\n" \
  "v_mul_f32 v110, s56, v110\n" \
  "v_mul_f32 v111, s56, v111\n" \
  "v_cvt_pk_bf16_f32 v173, v110, v111\n" \
  "v_mfma_f32_16x16x32_bf16 v[116:119], v[84:87], a[52:55], v[116:119]\n" \
  "buffer_load_dwordx4 v196, %[rx], s48 offen lds\n" \
  "s_add_u32 m0, m0, 1024\n" \
  "v_xor_b32 v196, 64, v196\n" \
  "buffer_load_dwordx4 v197, %[rx], s48 offen lds\n" \
  "s_add_u32 m0, m0, 1024\n" \
  "v_xor_b32 v197, 64, v197\n" \
  "v_mfma_f32_16x16x32_bf16 v[120:123], v[84:87], a[84:87], v[120:123]\n" \
  "v_mfma_f32_16x16x32_bf16 v[124:127], v[84:87], a[116:119], v[124:127]\n" \
  "buffer_load_dwordx4 v198, %[rx], s48 offen lds\n" \
  "s_add_u32 m0, m0, 1024\n" \
  "v_xor_b32 v198, 64, v198\n" \
  "s_add_u32 s48, s48, 7168\n" \
  "buffer_load_dwordx4 v192, %[rx], s48 offen lds\n" \
  "s_add_u32 m0, m0, 1024\n" \
  "v_xor_b32 v192, 64, v192\n" \
  "v_mfma_f32_16x16x32_bf16 v[112:115], v[88:91], a[24:27], v[112:115]\n" \
  "v_mfma_f32_16x16x32_bf16 v[116:119], v[88:91], a[56:59], v[116:119]\n" \
  "buffer_load_dwordx4 v193, %[rx], s48 offen lds\n" \
  "s_add_u32 m0, m0, 1024\n" \
  "v_xor_b32 v193, 64, v193\n" \
  "buffer_load_dwordx4 v194, %[rx], s48 offen lds\n" \
  "s_add_u32 m0, m0, 1024\n" \
  "v_xor_b32 v194, 64, v194\n" \
  "v_mfma_f32_16x16x32_bf16 v[120:123], v[88:91], a[88:91], v[120:123]\n" \
  "v_mfma_f32_16x16x32_bf16 v[124:127], v[88:91], a[120:123], v[124:127]\n" \
  "buffer_load_dwordx4 v195, %[rx], s48 offen lds\n" \
  "s_add_u32 m0, m0, 1024\n" \
  "v_xor_b32 v195, 64, v195\n" \
  "buffer_load_dwordx4 v196, %[rx], s48 offen lds\n" \
  "s_add_u32 m0, m0, 1024\n" \
  "v_xor_b32 v196, 64, v196\n" \
  "v_mfma_f32_16x16x32_bf16 v[112:115], v[92:95], a[28:31], v[112:115]\n" \
  "v_mfma_f32_16x16x32_bf16 v[116:119], v[92:95], a[60:63], v[116:119]\n" \
  "buffer_load_dwordx4 v197, %[rx], s48 offen lds\n" \
  "s_add_u32 m0, m0, 1024\n" \
  "v_xor_b32 v197, 64, v197\n" \
  "buffer_load_dwordx4 v198, %[rx], s48 offen lds\n" \
  "s_add_u32 m0, m0, 1024\n" \
  "v_xor_b32 v198, 64, v198\n" \
  "s_add_u32 s48, s48, 7168\n" \
  "v_mfma_f32_16x16x32_bf16 v[120:123], v[92:95], a[92:95], v[120:123]\n" \
  "v_mfma_f32_16x16x32_bf16 v[124:127], v[92:95], a[124:127], v[124:127]\n" \
  "s_lshl_b32 s57, s54, 10\n" \
  "s_add_u32 s57, s57, %[sO]\n" \
  "v_mul_f32 v112, s56, v112\n" \
  "v_mul_f32 v113, s56, v113\n" \
  "v_cvt_pk_bf16_f32 v162, v112, v113\n" \
  "v_mul_f32 v114, s56, v114\n" \
  "v_mul_f32 v115, s56, v115\n" \
  "v_cvt_pk_bf16_f32 v163, v114, v115\n" \
  "buffer_store_dwordx4 v[160:163], %[l16], %[rs], s57 offen sc1\n" \
  "s_add_u32 s57, s57, 14336\n" \
  "v_mul_f32 v116, s56, v116\n" \
  "v_mul_f32 v117, s56, v117\n" \
  "v_cvt_pk_bf16_f32 v166, v116, v117\n" \
  "v_mul_f32 v118, s56, v118\n" \
  "v_mul_f32 v119, s56, v119\n" \
  "v_cvt_pk_bf16_f32 v167, v118, v119\n" \
  "buffer_store_dwordx4 v[164:167], %[l16], %[rs], s57 offen sc1\n" \
  "s_add_u32 s57, s57, 14336\n" \
  "v_mul_f32 v120, s56, v120\n" \
  "v_mul_f32 v121, s56, v121\n" \
  "v_cvt_pk_bf16_f32 v170, v120, v121\n" \
  "v_mul_f32 v122, s56, v122\n" \
  "v_mul_f32 v123, s56, v123\n" \
  "v_cvt_pk_bf16_f32 v171, v122, v123\n" \
  "buffer_store_dwordx4 v[168:171], %[l16], %[rs], s57 offen sc1\n" \
  "s_add_u32 s57, s57, 14336\n" \
  "v_mul_f32 v124, s56, v124\n" \
  "v_mul_f32 v125, s56, v125\n" \
  "v_cvt_pk_bf16_f32 v174, v124, v125\n" \
  "v_mul_f32 v126, s56, v126\n" \
  "v_mul_f32 v127, s56, v127\n" \
  "v_cvt_pk_bf16_f32 v175, v126, v127\n" \
  "buffer_store_dwordx4 v[172:175], %[l16], %[rs], s57 offen sc1\n" \
  "s_add_u32 s57, s57, 14336\n" \
  "s_barrier\n" \
  "s_lshl_b32 s47, s52, 10\n" \
  "v_add_u32 v232, s47, %[l16]\n" \
  "ds_write_b128 v232, v[128:131]\n" \
  "ds_write_b128 v232, v[132:135] offset:14336\n" \
  "ds_write_b128 v232, v[136:139] offset:28672\n" \
  "ds_write_b128 v232, v[140:143] offset:43008\n" \
  "s_lshl_b32 s47, s53, 10\n" \
  "v_add_u32 v232, s47, %[l16]\n" \
  "ds_write_b128 v232, v[144:147]\n" \
  "ds_write_b128 v232, v[148:151] offset:14336\n" \
  "ds_write_b128 v232, v[152:155] offset:28672\n" \
  "ds_write_b128 v232, v[156:159] offset:43008\n" \
  "s_lshl_b32 s47, s54, 10\n" \
  "v_add_u32 v232, s47, %[l16]\n" \
  "ds_write_b128 v232, v[160:163]\n" \
  "ds_write_b128 v232, v[164:167] offset:14336\n" \
  "ds_write_b128 v232, v[168:171] offset:28672\n" \
  "ds_write_b128 v232, v[172:175] offset:43008\n" \
  "s_waitcnt vmcnt(4)\n" \
  ".LP_fin%=:\n" \
  "s_and_b32 s46, %[w], 1\n" \
  "s_mul_i32 s46, s46, 28672\n" \
  "v_add_u32 v232, s46, %[l16]\n" \
  "v_xor_b32 v233, 64, %[rX]\n" \
  "s_waitcnt lgkmcnt(0)\n" \
  "s_barrier\n" \
  "ds_read_b128 v[32:35], v232\n" \
  "ds_read_b128 v[36:39], v232 offset:14336\n" \
  "ds_read_b128 v[40:43], %[rX]\n" \
  "ds_read_b128 v[44:47], %[rX] offset:14336\n" \
  "ds_read_b128 v[48:51], v232 offset:1024\n" \
  "ds_read_b128 v[52:55], v232 offset:15360\n" \
  "ds_read_b128 v[56:59], v233\n" \
  "ds_read_b128 v[60:63], v233 offset:14336\n" \
  "ds_read_b128 v[64:67], v232 offset:2048\n" \
  "ds_read_b128 v[68:71], v232 offset:16384\n" \
  "ds_read_b128 v[72:75], %[rX] offset:128\n" \
  "ds_read_b128 v[76:79], %[rX] offset:14464\n" \
  "ds_read_b128 v[80:83], v232 offset:3072\n" \
  "ds_read_b128 v[84:87], v232 offset:17408\n" \
  "ds_read_b128 v[88:91], v233 offset:128\n" \
  "ds_read_b128 v[92:95], v233 offset:14464\n" \
  "s_waitcnt lgkmcnt(13)\n" \
  "v_mfma_f32_16x16x32_bf16 v[96:99], v[32:35], v[40:43], 0\n" \
  "s_waitcnt lgkmcnt(12)\n" \
  "v_mfma_f32_16x16x32_bf16 v[100:103], v[32:35], v[44:47], 0\n" \
  "v_mfma_f32_16x16x32_bf16 v[104:107], v[36:39], v[40:43], 0\n" \
  "v_mfma_f32_16x16x32_bf16 v[108:111], v[36:39], v[44:47], 0\n" \
  "ds_read_b128 v[32:35], v232 offset:4096\n" \
  "ds_read_b128 v[36:39], v232 offset:18432\n" \
  "ds_read_b128 v[40:43], %[rX] offset:256\n" \
  "ds_read_b128 v[44:47], %[rX] offset:14592\n" \
  "s_waitcnt lgkmcnt(13)\n" \
  "v_mfma_f32_16x16x32_bf16 v[96:99], v[48:51], v[56:59], v[96:99]\n" \
  "s_waitcnt lgkmcnt(12)\n" \
  "v_mfma_f32_16x16x32_bf16 v[100:103], v[48:51], v[60:63], v[100:103]\n" \
  "v_mfma_f32_16x16x32_bf16 v[104:107], v[52:55], v[56:59], v[104:107]\n" \
  "v_mfma_f32_16x16x32_bf16 v[108:111], v[52:55], v[60:63], v[108:111]\n" \
  "ds_read_b128 v[48:51], v232 offset:5120\n" \
  "ds_read_b128 v[52:55], v232 offset:19456\n" \
  "ds_read_b128 v[56:59], v233 offset:256\n" \
  "ds_read_b128 v[60:63], v233 offset:14592\n" \
  "s_waitcnt lgkmcnt(13)\n" \
  "v_mfma_f32_16x16x32_bf16 v[96:99], v[64:67], v[72:75], v[96:99]\n" \
  "s_waitcnt lgkmcnt(12)\n" \
  "v_mfma_f32_16x16x32_bf16 v[100:103], v[64:67], v[76:79], v[100:103]\n" \
  "v_mfma_f32_16x16x32_bf16 v[104:107], v[68:71], v[72:75], v[104:107]\n" \
  "v_mfma_f32_16x16x32_bf16 v[108:111], v[68:71], v[76:79], v[108:111]\n" \
  "ds_read_b128 v[64:67], v232 offset:6144\n" \
  "ds_read_b128 v[68:71], v232 offset:20480\n" \
  "ds_read_b128 v[72:75], %[rX] offset:384\n" \
  "ds_read_b128 v[76:79], %[rX] offset:14720\n" \
  "s_waitcnt lgkmcnt(13)\n" \
  "v_mfma_f32_16x16x32_bf16 v[96:99], v[80:83], v[88:91], v[96:99]\n" \
  "s_waitcnt lgkmcnt(12)\n" \
  "v_mfma_f32_16x16x32_bf16 v[100:103], v[80:83], v[92:95], v[100:103]\n" \
  "v_mfma_f32_16x16x32_bf16 v[104:107], v[84:87], v[88:91], v[104:107]\n" \
  "v_mfma_f32_16x16x32_bf16 v[108:111], v[84:87], v[92:95], v[108:111]\n" \
  "ds_read_b128 v[80:83], v232 offset:7168\n" \
  "ds_read_b128 v[84:87], v232 offset:21504\n" \
  "ds_read_b128 v[88:91], v233 offset:384\n" \
  "ds_read_b128 v[92:95], v233 offset:14720\n" \
  "s_waitcnt lgkmcnt(13)\n" \
  "v_mfma_f32_16x16x32_bf16 v[96:99], v[32:35], v[40:43], v[96:99]\n" \
  "s_waitcnt lgkmcnt(12)\n" \
  "v_mfma_f32_16x16x32_bf16 v[100:103], v[32:35], v[44:47], v[100:103]\n" \
  "v_mfma_f32_16x16x32_bf16 v[104:107], v[36:39], v[40:43], v[104:107]\n" \
  "v_mfma_f32_16x16x32_bf16 v[108:111], v[36:39], v[44:47], v[108:111]\n" \
  "ds_read_b128 v[32:35], v232 offset:8192\n" \
  "ds_read_b128 v[36:39], v232 offset:22528\n" \
  "ds_read_b128 v[40:43], %[rX] offset:512\n" \
  "ds_read_b128 v[44:47], %[rX] offset:14848\n" \
  "s_waitcnt lgkmcnt(13)\n" \
  "v_mfma_f32_16x16x32_bf16 v[96:99], v[48:51], v[56:59], v[96:99]\n" \
  "s_waitcnt lgkmcnt(12)\n" \
  "v_mfma_f32_16x16x32_bf16 v[100:103], v[48:51], v[60:63], v[100:103]\n" \
  "v_mfma_f32_16x16x32_bf16 v[104:107], v[52:55], v[56:59], v[104:107]\n" \
  "v_mfma_f32_16x16x32_bf16 v[108:111], v[52:55], v[60:63], v[108:111]\n" \
  "ds_read_b128 v[48:51], v232 offset:9216\n" \
  "ds_read_b128 v[52:55], v232 offset:23552\n" \
  "ds_read_b128 v[56:59], v233 offset:512\n" \
  "ds_read_b128 v[60:63], v233 offset:14848\n" \
  "s_waitcnt lgkmcnt(13)\n" \
  "v_mfma_f32_16x16x32_bf16 v[96:99], v[64:67], v[72:75], v[96:99]\n" \
  "s_waitcnt lgkmcnt(12)\n" \
  "v_mfma_f32_16x16x32_bf16 v[100:103], v[64:67], v[76:79], v[100:103]\n" \
  "v_mfma_f32_16x16x32_bf16 v[104:107], v[68:71], v[72:75], v[104:107]\n" \
  "v_mfma_f32_16x16x32_bf16 v[108:111], v[68:71], v[76:79], v[108:111]\n" \
  "ds_read_b128 v[64:67], v232 offset:10240\n" \
  "ds_read_b128 v[68:71], v232 offset:24576\n" \
  "ds_read_b128 v[72:75], %[rX] offset:640\n" \
  "ds_read_b128 v[76:79], %[rX] offset:14976\n" \
  "s_waitcnt lgkmcnt(13)\n" \
  "v_mfma_f32_16x16x32_bf16 v[96:99], v[80:83], v[88:91], v[96:99]\n" \
  "s_waitcnt lgkmcnt(12)\n" \
  "v_mfma_f32_16x16x32_bf16 v[100:103], v[80:83], v[92:95], v[100:103]\n" \
  "v_mfma_f32_16x16x32_bf16 v[104:107], v[84:87], v[88:91], v[104:107]\n" \
  "v_mfma_f32_16x16x32_bf16 v[108:111], v[84:87], v[92:95], v[108:111]\n" \
  "ds_read_b128 v[80:83], v232 offset:11264\n" \
  "ds_read_b128 v[84:87], v232 offset:25600\n" \
  "ds_read_b128 v[88:91], v233 offset:640\n" \
  "ds_read_b128 v[92:95], v233 offset:14976\n" \
  "s_waitcnt lgkmcnt(13)\n" \
  "v_mfma_f32_16x16x32_bf16 v[96:99], v[32:35], v[40:43], v[96:99]\n" \
  "s_waitcnt lgkmcnt(12)\n" \
  "v_mfma_f32_16x16x32_bf16 v[100:103], v[32:35], v[44:47], v[100:103]\n" \
  "v_mfma_f32_16x16x32_bf16 v[104:107], v[36:39], v[40:43], v[104:107]\n" \
  "v_mfma_f32_16x16x32_bf16 v[108:111], v[36:39], v[44:47], v[108:111]\n" \
  "ds_read_b128 v[32:35], v232 offset:12288\n" \
  "ds_read_b128 v[36:39], v232 offset:26624\n" \
  "ds_read_b128 v[40:43], %[rX] offset:768\n" \
  "ds_read_b128 v[44:47], %[rX] offset:15104\n" \
  "s_waitcnt lgkmcnt(13)\n" \
  "v_mfma_f32_16x16x32_bf16 v[96:99], v[48:51], v[56:59], v[96:99]\n" \
  "s_waitcnt lgkmcnt(12)\n" \
  "v_mfma_f32_16x16x32_bf16 v[100:103], v[48:51], v[60:63], v[100:103]\n" \
  "v_mfma_f32_16x16x32_bf16 v[104:107], v[52:55], v[56:59], v[104:107]\n" \
  "v_mfma_f32_16x16x32_bf16 v[108:111], v[52:55], v[60:63], v[108:111]\n" \
  "ds_read_b128 v[48:51], v232 offset:13312\n" \
  "ds_read_b128 v[52:55], v232 offset:27648\n" \
  "ds_read_b128 v[56:59], v233 offset:768\n" \
  "ds_read_b128 v[60:63], v233 offset:15104\n" \
  "s_waitcnt lgkmcnt(13)\n" \
  "v_mfma_f32_16x16x32_bf16 v[96:99], v[64:67], v[72:75], v[96:99]\n" \
  "s_waitcnt lgkmcnt(12)\n" \
  "v_mfma_f32_16x16x32_bf16 v[100:103], v[64:67], v[76:79], v[100:103]\n" \
  "v_mfma_f32_16x16x32_bf16 v[104:107], v[68:71], v[72:75], v[104:107]\n" \
  "v_mfma_f32_16x16x32_bf16 v[108:111], v[68:71], v[76:79], v[108:111]\n" \
  "s_waitcnt lgkmcnt(9)\n" \
  "v_mfma_f32_16x16x32_bf16 v[96:99], v[80:83], v[88:91], v[96:99]\n" \
  "s_waitcnt lgkmcnt(8)\n" \
  "v_mfma_f32_16x16x32_bf16 v[100:103], v[80:83], v[92:95], v[100:103]\n" \
  "v_mfma_f32_16x16x32_bf16 v[104:107], v[84:87], v[88:91], v[104:107]\n" \
  "v_mfma_f32_16x16x32_bf16 v[108:111], v[84:87], v[92:95], v[108:111]\n" \
  "s_waitcnt lgkmcnt(5)\n" \
  "v_mfma_f32_16x16x32_bf16 v[96:99], v[32:35], v[40:43], v[96:99]\n" \
  "s_waitcnt lgkmcnt(4)\n" \
  "v_mfma_f32_16x16x32_bf16 v[100:103], v[32:35], v[44:47], v[100:103]\n" \
  "v_mfma_f32_16x16x32_bf16 v[104:107], v[36:39], v[40:43], v[104:107]\n" \
  "v_mfma_f32_16x16x32_bf16 v[108:111], v[36:39], v[44:47], v[108:111]\n" \
  "s_waitcnt lgkmcnt(1)\n" \
  "v_mfma_f32_16x16x32_bf16 v[96:99], v[48:51], v[56:59], v[96:99]\n" \
  "s_waitcnt lgkmcnt(0)\n" \
  "v_mfma_f32_16x16x32_bf16 v[100:103], v[48:51], v[60:63], v[100:103]\n" \
  "v_mfma_f32_16x16x32_bf16 v[104:107], v[52:55], v[56:59], v[104:107]\n" \
  "v_mfma_f32_16x16x32_bf16 v[108:111], v[52:55], v[60:63], v[108:111]\n" \
  "s_nop 3\n" \
  "v_cvt_pk_bf16_f32 v128, v96, v97\n" \
  "v_cvt_pk_bf16_f32 v129, v98, v99\n" \
  "v_cvt_pk_bf16_f32 v132, v100, v101\n" \
  "v_cvt_pk_bf16_f32 v133, v102, v103\n" \
  "v_cvt_pk_bf16_f32 v130, v104, v105\n" \
  "v_cvt_pk_bf16_f32 v131, v106, v107\n" \
  "v_cvt_pk_bf16_f32 v134, v108, v109\n" \
  "v_cvt_pk_bf16_f32 v135, v110, v111\n" \
  "s_add_u32 s46, %[sF], 0\n" \
  "buffer_store_dwordx4 v[128:131], %[l16], %[rs], s46 offen sc1\n" \
  "s_add_u32 s46, %[sF], 1024\n" \
  "buffer_store_dwordx4 v[132:135], %[l16], %[rs], s46 offen sc1\n" \
  "s_waitcnt vmcnt(0) lgkmcnt(0)\n" \
  ".LP_zend%=:\n" \
  ""
#define ASM_PA_CLOB "memory", "scc", "vcc", "a0", "a1", "a2", "a3", "a4", "a5", "a6", "a7", "a8", "a9", "a10", "a11", "a12", "a13", "a14", "a15", "a16", "a17", "a18", "a19", "a20", "a21", "a22", "a23", "a24", "a25", "a26", "a27", "a28", "a29", "a30", "a31", "a32", "a33", "a34", "a35", "a36", "a37", "a38", "a39", "a40", "a41", "a42", "a43", "a44", "a45", "a46", "a47", "a48", "a49", "a50", "a51", "a52", "a53", "a54", "a55", "a56", "a57", "a58", "a59", "a60", "a61", "a62", "a63", "a64", "a65", "a66", "a67", "a68", "a69", "a70", "a71", "a72", "a73", "a74", "a75", "a76", "a77", "a78", "a79", "a80", "a81", "a82", "a83", "a84", "a85", "a86", "a87", "a88", "a89", "a90", "a91", "a92", "a93", "a94", "a95", "a96", "a97", "a98", "a99", "a100", "a101", "a102", "a103", "a104", "a105", "a106", "a107", "a108", "a109", "a110", "a111", "a112", "a113", "a114", "a115", "a116", "a117", "a118", "a119", "a120", "a121", "a122", "a123", "a124", "a125", "a126", "a127", "s46", "s47", "s48", "s52", "s53", "s54", "s55", "s56", "s57", "v32", "v33", "v34", "v35", "v36", "v37", "v38", "v39", "v40", "v41", "v42", "v43", "v44", "v45", "v46", "v47", "v48", "v49", "v50", "v51", "v52", "v53", "v54", "v55", "v56", "v57", "v58", "v59", "v60", "v61", "v62", "v63", "v64", "v65", "v66", "v67", "v68", "v69", "v70", "v71", "v72", "v73", "v74", "v75", "v76", "v77", "v78", "v79", "v80", "v81", "v82", "v83", "v84", "v85", "v86", "v87", "v88", "v89", "v90", "v91", "v92", "v93", "v94", "v95", "v96", "v97", "v98", "v99", "v100", "v101", "v102", "v103", "v104", "v105", "v106", "v107", "v108", "v109", "v110", "v111", "v112", "v113", "v114", "v115", "v116", "v117", "v118", "v119", "v120", "v121", "v122", "v123", "v124", "v125", "v126", "v127", "v128", "v129", "v130", "v131", "v132", "v133", "v134", "v135", "v136", "v137", "v138", "v139", "v140", "v141", "v142", "v143", "v144", "v145", "v146", "v147", "v148", "v149", "v150", "v151", "v152", "v153", "v154", "v155", "v156", "v157", "v158", "v159", "v160", "v161", "v162", "v163", "v164", "v165", "v166", "v167", "v168", "v169", "v170", "v171", "v172", "v173", "v174", "v175", "v176", "v177", "v178", "v179", "v180", "v181", "v182", "v183", "v184", "v185", "v186", "v187", "v188", "v189", "v190", "v191", "v192", "v193", "v194", "v195", "v196", "v197", "v198", "v199", "v200", "v201", "v202", "v203", "v204", "v205", "v206", "v207", "v232", "v233"
#define ASM_PB \
  "s_nop 4\n" \
  "v_xor_b32 v192, 0, %[oA]\n" \
  "v_xor_b32 v193, 32, %[oA]\n" \
  "v_xor_b32 v194, 64, %[oA]\n" \
  "v_xor_b32 v195, 96, %[oA]\n" \
  "v_xor_b32 v196, 128, %[oA]\n" \
  "v_xor_b32 v197, 160, %[oA]\n" \
  "v_xor_b32 v198, 192, %[oA]\n" \
  "v_xor_b32 v199, 224, %[oA]\n" \
  "s_lshl_b32 m0, %[w], 13\n" \
  "s_mov_b32 s46, 0\n" \
  "buffer_load_dwordx4 %[oB], %[rb], s46 offen nt lds\n" \
  "s_add_u32 m0, m0, 1024\n" \
  "s_mov_b32 s46, 7168\n" \
  "buffer_load_dwordx4 %[oB1], %[rb], s46 offen nt lds\n" \
  "s_add_u32 m0, m0, 1024\n" \
  "s_mov_b32 s46, 14336\n" \
  "buffer_load_dwordx4 %[oB], %[rb], s46 offen nt lds\n" \
  "s_add_u32 m0, m0, 1024\n" \
  "s_mov_b32 s46, 21504\n" \
  "buffer_load_dwordx4 %[oB1], %[rb], s46 offen nt lds\n" \
  "s_add_u32 m0, m0, 1024\n" \
  "s_mov_b32 s46, 28672\n" \
  "buffer_load_dwordx4 %[oB], %[rb], s46 offen nt lds\n" \
  "s_add_u32 m0, m0, 1024\n" \
  "s_mov_b32 s46, 35840\n" \
  "buffer_load_dwordx4 %[oB1], %[rb], s46 offen nt lds\n" \
  "s_add_u32 m0, m0, 1024\n" \
  "s_mov_b32 s46, 43008\n" \
  "buffer_load_dwordx4 %[oB], %[rb], s46 offen nt lds\n" \
  "s_add_u32 m0, m0, 1024\n" \
  "s_mov_b32 s46, 50176\n" \
  "buffer_load_dwordx4 %[oB1], %[rb], s46 offen nt lds\n" \
  "s_add_u32 m0, m0, 1024\n" \
  "s_add_u32 s52, %[k0], 0\n" \
  "s_sub_u32 s46, s52, 14\n" \
  "s_cmp_ge_u32 s52, 14\n" \
  "s_cselect_b32 s52, s46, s52\n" \
  "s_add_u32 s53, %[k0], 4\n" \
  "s_sub_u32 s46, s53, 14\n" \
  "s_cmp_ge_u32 s53, 14\n" \
  "s_cselect_b32 s53, s46, s53\n" \
  "s_add_u32 s54, %[k0], 8\n" \
  "s_sub_u32 s46, s54, 14\n" \
  "s_cmp_ge_u32 s54, 14\n" \
  "s_cselect_b32 s54, s46, s54\n" \
  "s_add_u32 s55, %[k0], 12\n" \
  "s_sub_u32 s46, s55, 14\n" \
  "s_cmp_ge_u32 s55, 14\n" \
  "s_cselect_b32 s55, s46, s55\n" \
  "s_add_u32 m0, %[ma], 0\n" \
  "s_lshl_b32 s48, s52, 18\n" \
  "s_add_u32 s46, s48, 0\n" \
  "buffer_load_dwordx4 v192, %[ra], s46 offen lds\n" \
  "s_add_u32 m0, m0, 1024\n" \
  "s_add_u32 s46, s48, 16384\n" \
  "buffer_load_dwordx4 v193, %[ra], s46 offen lds\n" \
  "s_add_u32 m0, m0, 1024\n" \
  "s_add_u32 s46, s48, 65536\n" \
  "buffer_load_dwordx4 v194, %[ra], s46 offen lds\n" \
  "s_add_u32 m0, m0, 1024\n" \
  "s_add_u32 s46, s48, 81920\n" \
  "buffer_load_dwordx4 v195, %[ra], s46 offen lds\n" \
  "s_add_u32 m0, m0, 1024\n" \
  "s_add_u32 s46, s48, 131072\n" \
  "buffer_load_dwordx4 v196, %[ra], s46 offen lds\n" \
  "s_add_u32 m0, m0, 1024\n" \
  "s_add_u32 s46, s48, 147456\n" \
  "buffer_load_dwordx4 v197, %[ra], s46 offen lds\n" \
  "s_add_u32 m0, m0, 1024\n" \
  "s_add_u32 s46, s48, 196608\n" \
  "buffer_load_dwordx4 v198, %[ra], s46 offen lds\n" \
  "s_add_u32 m0, m0, 1024\n" \
  "s_add_u32 s46, s48, 212992\n" \
  "buffer_load_dwordx4 v199, %[ra], s46 offen lds\n" \
  "s_add_u32 m0, m0, 1024\n" \
  "s_add_u32 m0, %[ma], 8192\n" \
  "s_lshl_b32 s48, s52, 18\n" \
  "s_add_u32 s46, s48, 32768\n" \
  "buffer_load_dwordx4 v192, %[ra], s46 offen lds\n" \
  "s_add_u32 m0, m0, 1024\n" \
  "s_add_u32 s46, s48, 49152\n" \
  "buffer_load_dwordx4 v193, %[ra], s46 offen lds\n" \
  "s_add_u32 m0, m0, 1024\n" \
  "s_add_u32 s46, s48, 98304\n" \
  "buffer_load_dwordx4 v194, %[ra], s46 offen lds\n" \
  "s_add_u32 m0, m0, 1024\n" \
  "s_add_u32 s46, s48, 114688\n" \
  "buffer_load_dwordx4 v195, %[ra], s46 offen lds\n" \
  "s_add_u32 m0, m0, 1024\n" \
  "s_add_u32 s46, s48, 163840\n" \
  "buffer_load_dwordx4 v196, %[ra], s46 offen lds\n" \
  "s_add_u32 m0, m0, 1024\n" \
  "s_add_u32 s46, s48, 180224\n" \
  "buffer_load_dwordx4 v197, %[ra], s46 offen lds\n" \
  "s_add_u32 m0, m0, 1024\n" \
  "s_add_u32 s46, s48, 229376\n" \
  "buffer_load_dwordx4 v198, %[ra], s46 offen lds\n" \
  "s_add_u32 m0, m0, 1024\n" \
  "s_add_u32 s46, s48, 245760\n" \
  "buffer_load_dwordx4 v199, %[ra], s46 offen lds\n" \
  "s_add_u32 m0, m0, 1024\n" \
  "v_xor_b32 v204, 0, %[rA]\n" \
  "v_add_u32 v200, %[ma], v204\n" \
  "v_xor_b32 v205, 64, %[rA]\n" \
  "v_add_u32 v201, %[ma], v205\n" \
  "v_xor_b32 v206, 128, %[rA]\n" \
  "v_add_u32 v202, %[ma], v206\n" \
  "v_xor_b32 v207, 192, %[rA]\n" \
  "v_add_u32 v203, %[ma], v207\n" \
  "s_mov_b32 s56, 0x3db8aa3b\n" \
  "s_waitcnt vmcnt(16)\n" \
  "s_barrier\n" \
  "s_waitcnt vmcnt(8)\n" \
  "ds_read_b128 v[32:35], v200\n" \
  "ds_read_b64_tr_b16 a[0:1], %[rT0]\n" \
  "ds_read_b64_tr_b16 a[2:3], %[rT0] offset:512\n" \
  "ds_read_b64_tr_b16 a[32:33], %[rT1]\n" \
  "ds_read_b64_tr_b16 a[34:35], %[rT1] offset:512\n" \
  "ds_read_b64_tr_b16 a[64:65], %[rT2]\n" \
  "ds_read_b64_tr_b16 a[66:67], %[rT2] offset:512\n" \
  "ds_read_b64_tr_b16 a[96:97], %[rT3]\n" \
  "ds_read_b64_tr_b16 a[98:99], %[rT3] offset:512\n" \
  "s_add_u32 m0, %[ma], 16384\n" \
  "s_lshl_b32 s48, s53, 18\n" \
  "s_add_u32 s46, s48, 0\n" \
  "buffer_load_dwordx4 v192, %[ra], s46 offen lds\n" \
  "s_add_u32 m0, m0, 1024\n" \
  "ds_read_b128 v[36:39], v201\n" \
  "ds_read_b64_tr_b16 a[4:5], %[rT0] offset:4096\n" \
  "ds_read_b64_tr_b16 a[6:7], %[rT0] offset:4608\n" \
  "ds_read_b64_tr_b16 a[36:37], %[rT1] offset:4096\n" \
  "ds_read_b64_tr_b16 a[38:39], %[rT1] offset:4608\n" \
  "ds_read_b64_tr_b16 a[68:69], %[rT2] offset:4096\n" \
  "ds_read_b64_tr_b16 a[70:71], %[rT2] offset:4608\n" \
  "ds_read_b64_tr_b16 a[100:101], %[rT3] offset:4096\n" \
  "ds_read_b64_tr_b16 a[102:103], %[rT3] offset:4608\n" \
  "s_add_u32 s46, s48, 16384\n" \
  "buffer_load_dwordx4 v193, %[ra], s46 offen lds\n" \
  "s_add_u32 m0, m0, 1024\n" \
  "ds_read_b128 v[40:43], v202\n" \
  "ds_read_b64_tr_b16 a[8:9], %[rT0] offset:8192\n" \
  "ds_read_b64_tr_b16 a[10:11], %[rT0] offset:8704\n" \
  "ds_read_b64_tr_b16 a[40:41], %[rT1] offset:8192\n" \
  "ds_read_b64_tr_b16 a[42:43], %[rT1] offset:8704\n" \
  "ds_read_b64_tr_b16 a[72:73], %[rT2] offset:8192\n" \
  "ds_read_b64_tr_b16 a[74:75], %[rT2] offset:8704\n" \
  "ds_read_b64_tr_b16 a[104:105], %[rT3] offset:8192\n" \
  "ds_read_b64_tr_b16 a[106:107], %[rT3] offset:8704\n" \
  "s_add_u32 s46, s48, 65536\n" \
  "buffer_load_dwordx4 v194, %[ra], s46 offen lds\n" \
  "s_add_u32 m0, m0, 1024\n" \
  "ds_read_b128 v[44:47], v203\n" \
  "ds_read_b64_tr_b16 a[12:13], %[rT0] offset:12288\n" \
  "ds_read_b64_tr_b16 a[14:15], %[rT0] offset:12800\n" \
  "ds_read_b64_tr_b16 a[44:45], %[rT1] offset:12288\n" \
  "ds_read_b64_tr_b16 a[46:47], %[rT1] offset:12800\n" \
  "ds_read_b64_tr_b16 a[76:77], %[rT2] offset:12288\n" \
  "ds_read_b64_tr_b16 a[78:79], %[rT2] offset:12800\n" \
  "ds_read_b64_tr_b16 a[108:109], %[rT3] offset:12288\n" \
  "ds_read_b64_tr_b16 a[110:111], %[rT3] offset:12800\n" \
  "s_add_u32 s46, s48, 81920\n" \
  "buffer_load_dwordx4 v195, %[ra], s46 offen lds\n" \
  "s_add_u32 m0, m0, 1024\n" \
  "ds_read_b128 v[48:51], v200 offset:256\n" \
  "ds_read_b64_tr_b16 a[16:17], %[rT0] offset:16384\n" \
  "ds_read_b64_tr_b16 a[18:19], %[rT0] offset:16896\n" \
  "ds_read_b64_tr_b16 a[48:49], %[rT1] offset:16384\n" \
  "ds_read_b64_tr_b16 a[50:51], %[rT1] offset:16896\n" \
  "ds_read_b64_tr_b16 a[80:81], %[rT2] offset:16384\n" \
  "ds_read_b64_tr_b16 a[82:83], %[rT2] offset:16896\n" \
  "ds_read_b64_tr_b16 a[112:113], %[rT3] offset:16384\n" \
  "ds_read_b64_tr_b16 a[114:115], %[rT3] offset:16896\n" \
  "s_add_u32 s46, s48, 131072\n" \
  "buffer_load_dwordx4 v196, %[ra], s46 offen lds\n" \
  "s_add_u32 m0, m0, 1024\n" \
  "ds_read_b128 v[52:55], v201 offset:256\n" \
  "ds_read_b64_tr_b16 a[20:21], %[rT0] offset:20480\n" \
  "ds_read_b64_tr_b16 a[22:23], %[rT0] offset:20992\n" \
  "ds_read_b64_tr_b16 a[52:53], %[rT1] offset:20480\n" \
  "ds_read_b64_tr_b16 a[54:55], %[rT1] offset:20992\n" \
  "ds_read_b64_tr_b16 a[84:85], %[rT2] offset:20480\n" \
  "ds_read_b64_tr_b16 a[86:87], %[rT2] offset:20992\n" \
  "ds_read_b64_tr_b16 a[116:117], %[rT3] offset:20480\n" \
  "ds_read_b64_tr_b16 a[118:119], %[rT3] offset:20992\n" \
  "s_add_u32 s46, s48, 147456\n" \
  "buffer_load_dwordx4 v197, %[ra], s46 offen lds\n" \
  "s_add_u32 m0, m0, 1024\n" \
  "ds_read_b128 v[56:59], v202 offset:256\n" \
  "ds_read_b64_tr_b16 a[24:25], %[rT0] offset:24576\n" \
  "ds_read_b64_tr_b16 a[26:27], %[rT0] offset:25088\n" \
  "ds_read_b64_tr_b16 a[56:57], %[rT1] offset:24576\n" \
  "ds_read_b64_tr_b16 a[58:59], %[rT1] offset:25088\n" \
  "ds_read_b64_tr_b16 a[88:89], %[rT2] offset:24576\n" \
  "ds_read_b64_tr_b16 a[90:91], %[rT2] offset:25088\n" \
  "ds_read_b64_tr_b16 a[120:121], %[rT3] offset:24576\n" \
  "ds_read_b64_tr_b16 a[122:123], %[rT3] offset:25088\n" \
  "s_add_u32 s46, s48, 196608\n" \
  "buffer_load_dwordx4 v198, %[ra], s46 offen lds\n" \
  "s_add_u32 m0, m0, 1024\n" \
  "ds_read_b128 v[60:63], v203 offset:256\n" \
  "ds_read_b64_tr_b16 a[28:29], %[rT0] offset:28672\n" \
  "ds_read_b64_tr_b16 a[30:31], %[rT0] offset:29184\n" \
  "ds_read_b64_tr_b16 a[60:61], %[rT1] offset:28672\n" \
  "ds_read_b64_tr_b16 a[62:63], %[rT1] offset:29184\n" \
  "ds_read_b64_tr_b16 a[92:93], %[rT2] offset:28672\n" \
  "ds_read_b64_tr_b16 a[94:95], %[rT2] offset:29184\n" \
  "ds_read_b64_tr_b16 a[124:125], %[rT3] offset:28672\n" \
  "ds_read_b64_tr_b16 a[126:127], %[rT3] offset:29184\n" \
  "s_add_u32 s46, s48, 212992\n" \
  "buffer_load_dwordx4 v199, %[ra], s46 offen lds\n" \
  "s_add_u32 m0, m0, 1024\n" \
  "s_waitcnt lgkmcnt(15)\n" \
  "v_mfma_f32_16x16x32_bf16 v[96:99], v[32:35], a[0:3], 0\n" \
  "v_mfma_f32_16x16x32_bf16 v[100:103], v[32:35], a[32:35], 0\n" \
  "v_mfma_f32_16x16x32_bf16 v[104:107], v[32:35], a[64:67], 0\n" \
  "v_mfma_f32_16x16x32_bf16 v[108:111], v[32:35], a[96:99], 0\n" \
  "s_add_u32 m0, %[ma], 24576\n" \
  "s_lshl_b32 s48, s53, 18\n" \
  "s_add_u32 s46, s48, 32768\n" \
  "buffer_load_dwordx4 v192, %[ra], s46 offen lds\n" \
  "s_add_u32 m0, m0, 1024\n" \
  "v_mfma_f32_16x16x32_bf16 v[96:99], v[36:39], a[4:7], v[96:99]\n" \
  "v_mfma_f32_16x16x32_bf16 v[100:103], v[36:39], a[36:39], v[100:103]\n" \
  "s_add_u32 s46, s48, 49152\n" \
  "buffer_load_dwordx4 v193, %[ra], s46 offen lds\n" \
  "s_add_u32 m0, m0, 1024\n" \
  "v_mfma_f32_16x16x32_bf16 v[104:107], v[36:39], a[68:71], v[104:107]\n" \
  "v_mfma_f32_16x16x32_bf16 v[108:111], v[36:39], a[100:103], v[108:111]\n" \
  "s_add_u32 s46, s48, 98304\n" \
  "buffer_load_dwordx4 v194, %[ra], s46 offen lds\n" \
  "s_add_u32 m0, m0, 1024\n" \
  "v_mfma_f32_16x16x32_bf16 v[96:99], v[40:43], a[8:11], v[96:99]\n" \
  "s_waitcnt vmcnt(11)\n" \
  "v_mfma_f32_16x16x32_bf16 v[100:103], v[40:43], a[40:43], v[100:103]\n" \
  "s_add_u32 s46, s48, 114688\n" \
  "buffer_load_dwordx4 v195, %[ra], s46 offen lds\n" \
  "s_add_u32 m0, m0, 1024\n" \
  "v_mfma_f32_16x16x32_bf16 v[104:107], v[40:43], a[72:75], v[104:107]\n" \
  "ds_read_b128 v[64:67], v200 offset:8192\n" \
  "v_mfma_f32_16x16x32_bf16 v[108:111], v[40:43], a[104:107], v[108:111]\n" \
  "s_add_u32 s46, s48, 163840\n" \
  "buffer_load_dwordx4 v196, %[ra], s46 offen lds\n" \
  "s_add_u32 m0, m0, 1024\n" \
  "v_mfma_f32_16x16x32_bf16 v[96:99], v[44:47], a[12:15], v[96:99]\n" \
  "ds_read_b128 v[68:71], v201 offset:8192\n" \
  "v_mfma_f32_16x16x32_bf16 v[100:103], v[44:47], a[44:47], v[100:103]\n" \
  "s_add_u32 s46, s48, 180224\n" \
  "buffer_load_dwordx4 v197, %[ra], s46 offen lds\n" \
  "s_add_u32 m0, m0, 1024\n" \
  "v_mfma_f32_16x16x32_bf16 v[104:107], v[44:47], a[76:79], v[104:107]\n" \
  "ds_read_b128 v[72:75], v202 offset:8192\n" \
  "v_mfma_f32_16x16x32_bf16 v[108:111], v[44:47], a[108:111], v[108:111]\n" \
  "s_add_u32 s46, s48, 229376\n" \
  "buffer_load_dwordx4 v198, %[ra], s46 offen lds\n" \
  "s_add_u32 m0, m0, 1024\n" \
  "v_mfma_f32_16x16x32_bf16 v[96:99], v[48:51], a[16:19], v[96:99]\n" \
  "ds_read_b128 v[76:79], v203 offset:8192\n" \
  "v_mfma_f32_16x16x32_bf16 v[100:103], v[48:51], a[48:51], v[100:103]\n" \
  "s_add_u32 s46, s48, 245760\n" \
  "buffer_load_dwordx4 v199, %[ra], s46 offen lds\n" \
  "s_add_u32 m0, m0, 1024\n" \
  "v_mfma_f32_16x16x32_bf16 v[104:107], v[48:51], a[80:83], v[104:107]\n" \
  "ds_read_b128 v[80:83], v200 offset:8448\n" \
  "v_mfma_f32_16x16x32_bf16 v[108:111], v[48:51], a[112:115], v[108:111]\n" \
  "v_mfma_f32_16x16x32_bf16 v[96:99], v[52:55], a[20:23], v[96:99]\n" \
  "ds_read_b128 v[84:87], v201 offset:8448\n" \
  "v_mfma_f32_16x16x32_bf16 v[100:103], v[52:55], a[52:55], v[100:103]\n" \
  "v_mfma_f32_16x16x32_bf16 v[104:107], v[52:55], a[84:87], v[104:107]\n" \
  "ds_read_b128 v[88:91], v202 offset:8448\n" \
  "v_mfma_f32_16x16x32_bf16 v[108:111], v[52:55], a[116:119], v[108:111]\n" \
  "v_mfma_f32_16x16x32_bf16 v[96:99], v[56:59], a[24:27], v[96:99]\n" \
  "ds_read_b128 v[92:95], v203 offset:8448\n" \
  "s_waitcnt lgkmcnt(15)\n" \
  "v_mfma_f32_16x16x32_bf16 v[100:103], v[56:59], a[56:59], v[100:103]\n" \
  "v_mfma_f32_16x16x32_bf16 v[104:107], v[56:59], a[88:91], v[104:107]\n" \
  "v_mfma_f32_16x16x32_bf16 v[108:111], v[56:59], a[120:123], v[108:111]\n" \
  "s_waitcnt lgkmcnt(14)\n" \
  "v_mfma_f32_16x16x32_bf16 v[96:99], v[60:63], a[28:31], v[96:99]\n" \
  "s_waitcnt lgkmcnt(12)\n" \
  "v_mfma_f32_16x16x32_bf16 v[100:103], v[60:63], a[60:63], v[100:103]\n" \
  "s_waitcnt lgkmcnt(10)\n" \
  "v_mfma_f32_16x16x32_bf16 v[104:107], v[60:63], a[92:95], v[104:107]\n" \
  "s_waitcnt lgkmcnt(8)\n" \
  "v_mfma_f32_16x16x32_bf16 v[108:111], v[60:63], a[124:127], v[108:111]\n" \
  "s_waitcnt lgkmcnt(7)\n" \
  "v_mfma_f32_16x16x32_bf16 v[112:115], v[64:67], a[0:3], 0\n" \
  "v_mfma_f32_16x16x32_bf16 v[116:119], v[64:67], a[32:35], 0\n" \
  "v_mfma_f32_16x16x32_bf16 v[120:123], v[64:67], a[64:67], 0\n" \
  "v_mfma_f32_16x16x32_bf16 v[124:127], v[64:67], a[96:99], 0\n" \
  "s_add_u32 m0, %[ma], 0\n" \
  "s_lshl_b32 s48, s54, 18\n" \
  "s_add_u32 s46, s48, 0\n" \
  "buffer_load_dwordx4 v192, %[ra], s46 offen lds\n" \
  "s_add_u32 m0, m0, 1024\n" \
  "s_waitcnt lgkmcnt(6)\n" \
  "v_mfma_f32_16x16x32_bf16 v[112:115], v[68:71], a[4:7], v[112:115]\n" \
  "v_mfma_f32_16x16x32_bf16 v[116:119], v[68:71], a[36:39], v[116:119]\n" \
  "s_add_u32 s46, s48, 16384\n" \
  "buffer_load_dwordx4 v193, %[ra], s46 offen lds\n" \
  "s_add_u32 m0, m0, 1024\n" \
  "v_mfma_f32_16x16x32_bf16 v[120:123], v[68:71], a[68:71], v[120:123]\n" \
  "v_cvt_pk_bf16_f32 v128, v96, v97\n" \
  "v_mfma_f32_16x16x32_bf16 v[124:127], v[68:71], a[100:103], v[124:127]\n" \
  "s_add_u32 s46, s48, 65536\n" \
  "buffer_load_dwordx4 v194, %[ra], s46 offen lds\n" \
  "s_add_u32 m0, m0, 1024\n" \
  "s_waitcnt lgkmcnt(5)\n" \
  "v_mfma_f32_16x16x32_bf16 v[112:115], v[72:75], a[8:11], v[112:115]\n" \
  "s_waitcnt vmcnt(11)\n" \
  "v_cvt_pk_bf16_f32 v129, v98, v99\n" \
  "v_mfma_f32_16x16x32_bf16 v[116:119], v[72:75], a[40:43], v[116:119]\n" \
  "s_add_u32 s46, s48, 81920\n" \
  "buffer_load_dwordx4 v195, %[ra], s46 offen lds\n" \
  "s_add_u32 m0, m0, 1024\n" \
  "v_mfma_f32_16x16x32_bf16 v[120:123], v[72:75], a[72:75], v[120:123]\n" \
  "ds_read_b128 v[32:35], v200 offset:16384\n" \
  "v_cvt_pk_bf16_f32 v132, v100, v101\n" \
  "v_mfma_f32_16x16x32_bf16 v[124:127], v[72:75], a[104:107], v[124:127]\n" \
  "s_add_u32 s46, s48, 131072\n" \
  "buffer_load_dwordx4 v196, %[ra], s46 offen lds\n" \
  "s_add_u32 m0, m0, 1024\n" \
  "s_waitcnt lgkmcnt(5)\n" \
  "v_mfma_f32_16x16x32_bf16 v[112:115], v[76:79], a[12:15], v[112:115]\n" \
  "ds_read_b128 v[36:39], v201 offset:16384\n" \
  "v_cvt_pk_bf16_f32 v133, v102, v103\n" \
  "v_mfma_f32_16x16x32_bf16 v[116:119], v[76:79], a[44:47], v[116:119]\n" \
  "s_add_u32 s46, s48, 147456\n" \
  "buffer_load_dwordx4 v197, %[ra], s46 offen lds\n" \
  "s_add_u32 m0, m0, 1024\n" \
  "v_mfma_f32_16x16x32_bf16 v[120:123], v[76:79], a[76:79], v[120:123]\n" \
  "ds_read_b128 v[40:43], v202 offset:16384\n" \
  "v_cvt_pk_bf16_f32 v136, v104, v105\n" \
  "v_mfma_f32_16x16x32_bf16 v[124:127], v[76:79], a[108:111], v[124:127]\n" \
  "s_add_u32 s46, s48, 196608\n" \
  "buffer_load_dwordx4 v198, %[ra], s46 offen lds\n" \
  "s_add_u32 m0, m0, 1024\n" \
  "s_waitcnt lgkmcnt(6)\n" \
  "v_mfma_f32_16x16x32_bf16 v[112:115], v[80:83], a[16:19], v[112:115]\n" \
  "ds_read_b128 v[44:47], v203 offset:16384\n" \
  "v_cvt_pk_bf16_f32 v137, v106, v107\n" \
  "v_mfma_f32_16x16x32_bf16 v[116:119], v[80:83], a[48:51], v[116:119]\n" \
  "s_add_u32 s46, s48, 212992\n" \
  "buffer_load_dwordx4 v199, %[ra], s46 offen lds\n" \
  "s_add_u32 m0, m0, 1024\n" \
  "v_mfma_f32_16x16x32_bf16 v[120:123], v[80:83], a[80:83], v[120:123]\n" \
  "ds_read_b128 v[48:51], v200 offset:16640\n" \
  "v_cvt_pk_bf16_f32 v140, v108, v109\n" \
  "v_mfma_f32_16x16x32_bf16 v[124:127], v[80:83], a[112:115], v[124:127]\n" \
  "s_waitcnt lgkmcnt(7)\n" \
  "v_mfma_f32_16x16x32_bf16 v[112:115], v[84:87], a[20:23], v[112:115]\n" \
  "ds_read_b128 v[52:55], v201 offset:16640\n" \
  "v_cvt_pk_bf16_f32 v141, v110, v111\n" \
  "v_mfma_f32_16x16x32_bf16 v[116:119], v[84:87], a[52:55], v[116:119]\n" \
  "v_mfma_f32_16x16x32_bf16 v[120:123], v[84:87], a[84:87], v[120:123]\n" \
  "ds_read_b128 v[56:59], v202 offset:16640\n" \
  "v_mfma_f32_16x16x32_bf16 v[124:127], v[84:87], a[116:119], v[124:127]\n" \
  "s_waitcnt lgkmcnt(8)\n" \
  "v_mfma_f32_16x16x32_bf16 v[112:115], v[88:91], a[24:27], v[112:115]\n" \
  "ds_read_b128 v[60:63], v203 offset:16640\n" \
  "v_mfma_f32_16x16x32_bf16 v[116:119], v[88:91], a[56:59], v[116:119]\n" \
  "v_mfma_f32_16x16x32_bf16 v[120:123], v[88:91], a[88:91], v[120:123]\n" \
  "v_mfma_f32_16x16x32_bf16 v[124:127], v[88:91], a[120:123], v[124:127]\n" \
  "s_waitcnt lgkmcnt(8)\n" \
  "v_mfma_f32_16x16x32_bf16 v[112:115], v[92:95], a[28:31], v[112:115]\n" \
  "v_mfma_f32_16x16x32_bf16 v[116:119], v[92:95], a[60:63], v[116:119]\n" \
  "v_mfma_f32_16x16x32_bf16 v[120:123], v[92:95], a[92:95], v[120:123]\n" \
  "v_mfma_f32_16x16x32_bf16 v[124:127], v[92:95], a[124:127], v[124:127]\n" \
  "s_waitcnt lgkmcnt(7)\n" \
  "v_mfma_f32_16x16x32_bf16 v[96:99], v[32:35], a[0:3], 0\n" \
  "v_mfma_f32_16x16x32_bf16 v[100:103], v[32:35], a[32:35], 0\n" \
  "v_mfma_f32_16x16x32_bf16 v[104:107], v[32:35], a[64:67], 0\n" \
  "v_mfma_f32_16x16x32_bf16 v[108:111], v[32:35], a[96:99], 0\n" \
  "s_add_u32 m0, %[ma], 8192\n" \
  "s_lshl_b32 s48, s54, 18\n" \
  "s_add_u32 s46, s48, 32768\n" \
  "buffer_load_dwordx4 v192, %[ra], s46 offen lds\n" \
  "s_add_u32 m0, m0, 1024\n" \
  "s_waitcnt lgkmcnt(6)\n" \
  "v_mfma_f32_16x16x32_bf16 v[96:99], v[36:39], a[4:7], v[96:99]\n" \
  "v_mfma_f32_16x16x32_bf16 v[100:103], v[36:39], a[36:39], v[100:103]\n" \
  "s_add_u32 s46, s48, 49152\n" \
  "buffer_load_dwordx4 v193, %[ra], s46 offen lds\n" \
  "s_add_u32 m0, m0, 1024\n" \
  "v_mfma_f32_16x16x32_bf16 v[104:107], v[36:39], a[68:71], v[104:107]\n" \
  "s_lshl_b32 s57, s52, 10\n" \
  "s_add_u32 s57, s57, %[sO]\n" \
  "v_mfma_f32_16x16x32_bf16 v[108:111], v[36:39], a[100:103], v[108:111]\n" \
  "s_add_u32 s46, s48, 98304\n" \
  "buffer_load_dwordx4 v194, %[ra], s46 offen lds\n" \
  "s_add_u32 m0, m0, 1024\n" \
  "s_waitcnt lgkmcnt(5)\n" \
  "v_mfma_f32_16x16x32_bf16 v[96:99], v[40:43], a[8:11], v[96:99]\n" \
  "s_waitcnt vmcnt(11)\n" \
  "v_cvt_pk_bf16_f32 v130, v112, v113\n" \
  "v_mfma_f32_16x16x32_bf16 v[100:103], v[40:43], a[40:43], v[100:103]\n" \
  "s_add_u32 s46, s48, 114688\n" \
  "buffer_load_dwordx4 v195, %[ra], s46 offen lds\n" \
  "s_add_u32 m0, m0, 1024\n" \
  "v_mfma_f32_16x16x32_bf16 v[104:107], v[40:43], a[72:75], v[104:107]\n" \
  "ds_read_b128 v[64:67], v200 offset:24576\n" \
  "v_cvt_pk_bf16_f32 v131, v114, v115\n" \
  "v_mfma_f32_16x16x32_bf16 v[108:111], v[40:43], a[104:107], v[108:111]\n" \
  "s_add_u32 s46, s48, 163840\n" \
  "buffer_load_dwordx4 v196, %[ra], s46 offen lds\n" \
  "s_add_u32 m0, m0, 1024\n" \
  "s_waitcnt lgkmcnt(5)\n" \
  "v_mfma_f32_16x16x32_bf16 v[96:99], v[44:47], a[12:15], v[96:99]\n" \
  "ds_read_b128 v[68:71], v201 offset:24576\n" \
  "buffer_store_dwordx4 v[128:131], %[l16], %[rs], s57 offen sc1\n" \
  "s_add_u32 s57, s57, 14336\n" \
  "v_mfma_f32_16x16x32_bf16 v[100:103], v[44:47], a[44:47], v[100:103]\n" \
  "s_add_u32 s46, s48, 180224\n" \
  "buffer_load_dwordx4 v197, %[ra], s46 offen lds\n" \
  "s_add_u32 m0, m0, 1024\n" \
  "v_mfma_f32_16x16x32_bf16 v[104:107], v[44:47], a[76:79], v[104:107]\n" \
  "ds_read_b128 v[72:75], v202 offset:24576\n" \
  "v_cvt_pk_bf16_f32 v134, v116, v117\n" \
  "v_mfma_f32_16x16x32_bf16 v[108:111], v[44:47], a[108:111], v[108:111]\n" \
  "s_add_u32 s46, s48, 229376\n" \
  "buffer_load_dwordx4 v198, %[ra], s46 offen lds\n" \
  "s_add_u32 m0, m0, 1024\n" \
  "s_waitcnt lgkmcnt(6)\n" \
  "v_mfma_f32_16x16x32_bf16 v[96:99], v[48:51], a[16:19], v[96:99]\n" \
  "ds_read_b128 v[76:79], v203 offset:24576\n" \
  "v_cvt_pk_bf16_f32 v135, v118, v119\n" \
  "v_mfma_f32_16x16x32_bf16 v[100:103], v[48:51], a[48:51], v[100:103]\n" \
  "s_add_u32 s46, s48, 245760\n" \
  "buffer_load_dwordx4 v199, %[ra], s46 offen lds\n" \
  "s_add_u32 m0, m0, 1024\n" \
  "v_mfma_f32_16x16x32_bf16 v[104:107], v[48:51], a[80:83], v[104:107]\n" \
  "ds_read_b128 v[80:83], v200 offset:24832\n" \
  "buffer_store_dwordx4 v[132:135], %[l16], %[rs], s57 offen sc1\n" \
  "s_add_u32 s57, s57, 14336\n" \
  "v_mfma_f32_16x16x32_bf16 v[108:111], v[48:51], a[112:115], v[108:111]\n" \
  "s_waitcnt lgkmcnt(7)\n" \
  "v_mfma_f32_16x16x32_bf16 v[96:99], v[52:55], a[20:23], v[96:99]\n" \
  "ds_read_b128 v[84:87], v201 offset:24832\n" \
  "v_cvt_pk_bf16_f32 v138, v120, v121\n" \
  "v_mfma_f32_16x16x32_bf16 v[100:103], v[52:55], a[52:55], v[100:103]\n" \
  "v_mfma_f32_16x16x32_bf16 v[104:107], v[52:55], a[84:87], v[104:107]\n" \
  "ds_read_b128 v[88:91], v202 offset:24832\n" \
  "v_cvt_pk_bf16_f32 v139, v122, v123\n" \
  "v_mfma_f32_16x16x32_bf16 v[108:111], v[52:55], a[116:119], v[108:111]\n" \
  "s_waitcnt lgkmcnt(8)\n" \
  "v_mfma_f32_16x16x32_bf16 v[96:99], v[56:59], a[24:27], v[96:99]\n" \
  "ds_read_b128 v[92:95], v203 offset:24832\n" \
  "buffer_store_dwordx4 v[136:139], %[l16], %[rs], s57 offen sc1\n" \
  "s_add_u32 s57, s57, 14336\n" \
  "v_mfma_f32_16x16x32_bf16 v[100:103], v[56:59], a[56:59], v[100:103]\n" \
  "v_mfma_f32_16x16x32_bf16 v[104:107], v[56:59], a[88:91], v[104:107]\n" \
  "v_cvt_pk_bf16_f32 v142, v124, v125\n" \
  "v_mfma_f32_16x16x32_bf16 v[108:111], v[56:59], a[120:123], v[108:111]\n" \
  "s_waitcnt lgkmcnt(8)\n" \
  "v_mfma_f32_16x16x32_bf16 v[96:99], v[60:63], a[28:31], v[96:99]\n" \
  "v_cvt_pk_bf16_f32 v143, v126, v127\n" \
  "v_mfma_f32_16x16x32_bf16 v[100:103], v[60:63], a[60:63], v[100:103]\n" \
  "v_mfma_f32_16x16x32_bf16 v[104:107], v[60:63], a[92:95], v[104:107]\n" \
  "buffer_store_dwordx4 v[140:143], %[l16], %[rs], s57 offen sc1\n" \
  "s_add_u32 s57, s57, 14336\n" \
  "v_mfma_f32_16x16x32_bf16 v[108:111], v[60:63], a[124:127], v[108:111]\n" \
  "s_cmp_lt_u32 %[w], 2\n" \
  "s_cbranch_scc0 .LQ_three%=\n" \
  "s_waitcnt lgkmcnt(7)\n" \
  "v_mfma_f32_16x16x32_bf16 v[112:115], v[64:67], a[0:3], 0\n" \
  "v_mfma_f32_16x16x32_bf16 v[116:119], v[64:67], a[32:35], 0\n" \
  "v_mfma_f32_16x16x32_bf16 v[120:123], v[64:67], a[64:67], 0\n" \
  "v_mfma_f32_16x16x32_bf16 v[124:127], v[64:67], a[96:99], 0\n" \
  "s_add_u32 m0, %[ma], 16384\n" \
  "s_lshl_b32 s48, s55, 18\n" \
  "s_add_u32 s46, s48, 0\n" \
  "buffer_load_dwordx4 v192, %[ra], s46 offen lds\n" \
  "s_add_u32 m0, m0, 1024\n" \
  "s_waitcnt lgkmcnt(6)\n" \
  "v_mfma_f32_16x16x32_bf16 v[112:115], v[68:71], a[4:7], v[112:115]\n" \
  "v_mfma_f32_16x16x32_bf16 v[116:119], v[68:71], a[36:39], v[116:119]\n" \
  "s_add_u32 s46, s48, 16384\n" \
  "buffer_load_dwordx4 v193, %[ra], s46 offen lds\n" \
  "s_add_u32 m0, m0, 1024\n" \
  "v_mfma_f32_16x16x32_bf16 v[120:123], v[68:71], a[68:71], v[120:123]\n" \
  "v_cvt_pk_bf16_f32 v144, v96, v97\n" \
  "v_mfma_f32_16x16x32_bf16 v[124:127], v[68:71], a[100:103], v[124:127]\n" \
  "s_add_u32 s46, s48, 65536\n" \
  "buffer_load_dwordx4 v194, %[ra], s46 offen lds\n" \
  "s_add_u32 m0, m0, 1024\n" \
  "s_waitcnt lgkmcnt(5)\n" \
  "v_mfma_f32_16x16x32_bf16 v[112:115], v[72:75], a[8:11], v[112:115]\n" \
  "s_waitcnt vmcnt(15)\n" \
  "v_cvt_pk_bf16_f32 v145, v98, v99\n" \
  "v_mfma_f32_16x16x32_bf16 v[116:119], v[72:75], a[40:43], v[116:119]\n" \
  "s_add_u32 s46, s48, 81920\n" \
  "buffer_load_dwordx4 v195, %[ra], s46 offen lds\n" \
  "s_add_u32 m0, m0, 1024\n" \
  "v_mfma_f32_16x16x32_bf16 v[120:123], v[72:75], a[72:75], v[120:123]\n" \
  "ds_read_b128 v[32:35], v200\n" \
  "v_cvt_pk_bf16_f32 v148, v100, v101\n" \
  "v_mfma_f32_16x16x32_bf16 v[124:127], v[72:75], a[104:107], v[124:127]\n" \
  "s_add_u32 s46, s48, 131072\n" \
  "buffer_load_dwordx4 v196, %[ra], s46 offen lds\n" \
  "s_add_u32 m0, m0, 1024\n" \
  "s_waitcnt lgkmcnt(5)\n" \
  "v_mfma_f32_16x16x32_bf16 v[112:115], v[76:79], a[12:15], v[112:115]\n" \
  "ds_read_b128 v[36:39], v201\n" \
  "v_cvt_pk_bf16_f32 v149, v102, v103\n" \
  "v_mfma_f32_16x16x32_bf16 v[116:119], v[76:79], a[44:47], v[116:119]\n" \
  "s_add_u32 s46, s48, 147456\n" \
  "buffer_load_dwordx4 v197, %[ra], s46 offen lds\n" \
  "s_add_u32 m0, m0, 1024\n" \
  "v_mfma_f32_16x16x32_bf16 v[120:123], v[76:79], a[76:79], v[120:123]\n" \
  "ds_read_b128 v[40:43], v202\n" \
  "v_cvt_pk_bf16_f32 v152, v104, v105\n" \
  "v_mfma_f32_16x16x32_bf16 v[124:127], v[76:79], a[108:111], v[124:127]\n" \
  "s_add_u32 s46, s48, 196608\n" \
  "buffer_load_dwordx4 v198, %[ra], s46 offen lds\n" \
  "s_add_u32 m0, m0, 1024\n" \
  "s_waitcnt lgkmcnt(6)\n" \
  "v_mfma_f32_16x16x32_bf16 v[112:115], v[80:83], a[16:19], v[112:115]\n" \
  "ds_read_b128 v[44:47], v203\n" \
  "v_cvt_pk_bf16_f32 v153, v106, v107\n" \
  "v_mfma_f32_16x16x32_bf16 v[116:119], v[80:83], a[48:51], v[116:119]\n" \
  "s_add_u32 s46, s48, 212992\n" \
  "buffer_load_dwordx4 v199, %[ra], s46 offen lds\n" \
  "s_add_u32 m0, m0, 1024\n" \
  "v_mfma_f32_16x16x32_bf16 v[120:123], v[80:83], a[80:83], v[120:123]\n" \
  "ds_read_b128 v[48:51], v200 offset:256\n" \
  "v_cvt_pk_bf16_f32 v156, v108, v109\n" \
  "v_mfma_f32_16x16x32_bf16 v[124:127], v[80:83], a[112:115], v[124:127]\n" \
  "s_waitcnt lgkmcnt(7)\n" \
  "v_mfma_f32_16x16x32_bf16 v[112:115], v[84:87], a[20:23], v[112:115]\n" \
  "ds_read_b128 v[52:55], v201 offset:256\n" \
  "v_cvt_pk_bf16_f32 v157, v110, v111\n" \
  "v_mfma_f32_16x16x32_bf16 v[116:119], v[84:87], a[52:55], v[116:119]\n" \
  "v_mfma_f32_16x16x32_bf16 v[120:123], v[84:87], a[84:87], v[120:123]\n" \
  "ds_read_b128 v[56:59], v202 offset:256\n" \
  "v_mfma_f32_16x16x32_bf16 v[124:127], v[84:87], a[116:119], v[124:127]\n" \
  "s_waitcnt lgkmcnt(8)\n" \
  "v_mfma_f32_16x16x32_bf16 v[112:115], v[88:91], a[24:27], v[112:115]\n" \
  "ds_read_b128 v[60:63], v203 offset:256\n" \
  "v_mfma_f32_16x16x32_bf16 v[116:119], v[88:91], a[56:59], v[116:119]\n" \
  "v_mfma_f32_16x16x32_bf16 v[120:123], v[88:91], a[88:91], v[120:123]\n" \
  "v_mfma_f32_16x16x32_bf16 v[124:127], v[88:91], a[120:123], v[124:127]\n" \
  "s_waitcnt lgkmcnt(8)\n" \
  "v_mfma_f32_16x16x32_bf16 v[112:115], v[92:95], a[28:31], v[112:115]\n" \
  "v_mfma_f32_16x16x32_bf16 v[116:119], v[92:95], a[60:63], v[116:119]\n" \
  "v_mfma_f32_16x16x32_bf16 v[120:123], v[92:95], a[92:95], v[120:123]\n" \
  "v_mfma_f32_16x16x32_bf16 v[124:127], v[92:95], a[124:127], v[124:127]\n" \
  "s_waitcnt lgkmcnt(7)\n" \
  "v_mfma_f32_16x16x32_bf16 v[96:99], v[32:35], a[0:3], 0\n" \
  "v_mfma_f32_16x16x32_bf16 v[100:103], v[32:35], a[32:35], 0\n" \
  "v_mfma_f32_16x16x32_bf16 v[104:107], v[32:35], a[64:67], 0\n" \
  "v_mfma_f32_16x16x32_bf16 v[108:111], v[32:35], a[96:99], 0\n" \
  "s_add_u32 m0, %[ma], 24576\n" \
  "s_lshl_b32 s48, s55, 18\n" \
  "s_add_u32 s46, s48, 32768\n" \
  "buffer_load_dwordx4 v192, %[ra], s46 offen lds\n" \
  "s_add_u32 m0, m0, 1024\n" \
  "s_waitcnt lgkmcnt(6)\n" \
  "v_mfma_f32_16x16x32_bf16 v[96:99], v[36:39], a[4:7], v[96:99]\n" \
  "v_mfma_f32_16x16x32_bf16 v[100:103], v[36:39], a[36:39], v[100:103]\n" \
  "s_add_u32 s46, s48, 49152\n" \
  "buffer_load_dwordx4 v193, %[ra], s46 offen lds\n" \
  "s_add_u32 m0, m0, 1024\n" \
  "v_mfma_f32_16x16x32_bf16 v[104:107], v[36:39], a[68:71], v[104:107]\n" \
  "s_lshl_b32 s57, s53, 10\n" \
  "s_add_u32 s57, s57, %[sO]\n" \
  "v_mfma_f32_16x16x32_bf16 v[108:111], v[36:39], a[100:103], v[108:111]\n" \
  "s_add_u32 s46, s48, 98304\n" \
  "buffer_load_dwordx4 v194, %[ra], s46 offen lds\n" \
  "s_add_u32 m0, m0, 1024\n" \
  "s_waitcnt lgkmcnt(5)\n" \
  "v_mfma_f32_16x16x32_bf16 v[96:99], v[40:43], a[8:11], v[96:99]\n" \
  "s_waitcnt vmcnt(14)\n" \
  "v_cvt_pk_bf16_f32 v146, v112, v113\n" \
  "v_mfma_f32_16x16x32_bf16 v[100:103], v[40:43], a[40:43], v[100:103]\n" \
  "s_add_u32 s46, s48, 114688\n" \
  "buffer_load_dwordx4 v195, %[ra], s46 offen lds\n" \
  "s_add_u32 m0, m0, 1024\n" \
  "v_mfma_f32_16x16x32_bf16 v[104:107], v[40:43], a[72:75], v[104:107]\n" \
  "ds_read_b128 v[64:67], v200 offset:8192\n" \
  "v_cvt_pk_bf16_f32 v147, v114, v115\n" \
  "v_mfma_f32_16x16x32_bf16 v[108:111], v[40:43], a[104:107], v[108:111]\n" \
  "s_add_u32 s46, s48, 163840\n" \
  "buffer_load_dwordx4 v196, %[ra], s46 offen lds\n" \
  "s_add_u32 m0, m0, 1024\n" \
  "s_waitcnt lgkmcnt(5)\n" \
  "v_mfma_f32_16x16x32_bf16 v[96:99], v[44:47], a[12:15], v[96:99]\n" \
  "ds_read_b128 v[68:71], v201 offset:8192\n" \
  "buffer_store_dwordx4 v[144:147], %[l16], %[rs], s57 offen sc1\n" \
  "s_add_u32 s57, s57, 14336\n" \
  "v_mfma_f32_16x16x32_bf16 v[100:103], v[44:47], a[44:47], v[100:103]\n" \
  "s_add_u32 s46, s48, 180224\n" \
  "buffer_load_dwordx4 v197, %[ra], s46 offen lds\n" \
  "s_add_u32 m0, m0, 1024\n" \
  "v_mfma_f32_16x16x32_bf16 v[104:107], v[44:47], a[76:79], v[104:107]\n" \
  "ds_read_b128 v[72:75], v202 offset:8192\n" \
  "v_cvt_pk_bf16_f32 v150, v116, v117\n" \
  "v_mfma_f32_16x16x32_bf16 v[108:111], v[44:47], a[108:111], v[108:111]\n" \
  "s_add_u32 s46, s48, 229376\n" \
  "buffer_load_dwordx4 v198, %[ra], s46 offen lds\n" \
  "s_add_u32 m0, m0, 1024\n" \
  "s_waitcnt lgkmcnt(6)\n" \
  "v_mfma_f32_16x16x32_bf16 v[96:99], v[48:51], a[16:19], v[96:99]\n" \
  "ds_read_b128 v[76:79], v203 offset:8192\n" \
  "v_cvt_pk_bf16_f32 v151, v118, v119\n" \
  "v_mfma_f32_16x16x32_bf16 v[100:103], v[48:51], a[48:51], v[100:103]\n" \
  "s_add_u32 s46, s48, 245760\n" \
  "buffer_load_dwordx4 v199, %[ra], s46 offen lds\n" \
  "s_add_u32 m0, m0, 1024\n" \
  "v_mfma_f32_16x16x32_bf16 v[104:107], v[48:51], a[80:83], v[104:107]\n" \
  "ds_read_b128 v[80:83], v200 offset:8448\n" \
  "buffer_store_dwordx4 v[148:151], %[l16], %[rs], s57 offen sc1\n" \
  "s_add_u32 s57, s57, 14336\n" \
  "v_mfma_f32_16x16x32_bf16 v[108:111], v[48:51], a[112:115], v[108:111]\n" \
  "s_waitcnt lgkmcnt(7)\n" \
  "v_mfma_f32_16x16x32_bf16 v[96:99], v[52:55], a[20:23], v[96:99]\n" \
  "ds_read_b128 v[84:87], v201 offset:8448\n" \
  "v_cvt_pk_bf16_f32 v154, v120, v121\n" \
  "v_mfma_f32_16x16x32_bf16 v[100:103], v[52:55], a[52:55], v[100:103]\n" \
  "v_mfma_f32_16x16x32_bf16 v[104:107], v[52:55], a[84:87], v[104:107]\n" \
  "ds_read_b128 v[88:91], v202 offset:8448\n" \
  "v_cvt_pk_bf16_f32 v155, v122, v123\n" \
  "v_mfma_f32_16x16x32_bf16 v[108:111], v[52:55], a[116:119], v[108:111]\n" \
  "s_waitcnt lgkmcnt(8)\n" \
  "v_mfma_f32_16x16x32_bf16 v[96:99], v[56:59], a[24:27], v[96:99]\n" \
  "ds_read_b128 v[92:95], v203 offset:8448\n" \
  "buffer_store_dwordx4 v[152:155], %[l16], %[rs], s57 offen sc1\n" \
  "s_add_u32 s57, s57, 14336\n" \
  "v_mfma_f32_16x16x32_bf16 v[100:103], v[56:59], a[56:59], v[100:103]\n" \
  "v_mfma_f32_16x16x32_bf16 v[104:107], v[56:59], a[88:91], v[104:107]\n" \
  "v_cvt_pk_bf16_f32 v158, v124, v125\n" \
  "v_mfma_f32_16x16x32_bf16 v[108:111], v[56:59], a[120:123], v[108:111]\n" \
  "s_waitcnt lgkmcnt(8)\n" \
  "v_mfma_f32_16x16x32_bf16 v[96:99], v[60:63], a[28:31], v[96:99]\n" \
  "v_cvt_pk_bf16_f32 v159, v126, v127\n" \
  "v_mfma_f32_16x16x32_bf16 v[100:103], v[60:63], a[60:63], v[100:103]\n" \
  "v_mfma_f32_16x16x32_bf16 v[104:107], v[60:63], a[92:95], v[104:107]\n" \
  "buffer_store_dwordx4 v[156:159], %[l16], %[rs], s57 offen sc1\n" \
  "s_add_u32 s57, s57, 14336\n" \
  "v_mfma_f32_16x16x32_bf16 v[108:111], v[60:63], a[124:127], v[108:111]\n" \
  "s_waitcnt lgkmcnt(7)\n" \
  "v_mfma_f32_16x16x32_bf16 v[112:115], v[64:67], a[0:3], 0\n" \
  "v_mfma_f32_16x16x32_bf16 v[116:119], v[64:67], a[32:35], 0\n" \
  "v_mfma_f32_16x16x32_bf16 v[120:123], v[64:67], a[64:67], 0\n" \
  "v_mfma_f32_16x16x32_bf16 v[124:127], v[64:67], a[96:99], 0\n" \
  "s_waitcnt lgkmcnt(6)\n" \
  "v_mfma_f32_16x16x32_bf16 v[112:115], v[68:71], a[4:7], v[112:115]\n" \
  "v_mfma_f32_16x16x32_bf16 v[116:119], v[68:71], a[36:39], v[116:119]\n" \
  "v_mfma_f32_16x16x32_bf16 v[120:123], v[68:71], a[68:71], v[120:123]\n" \
  "v_cvt_pk_bf16_f32 v160, v96, v97\n" \
  "v_mfma_f32_16x16x32_bf16 v[124:127], v[68:71], a[100:103], v[124:127]\n" \
  "s_waitcnt lgkmcnt(5)\n" \
  "v_mfma_f32_16x16x32_bf16 v[112:115], v[72:75], a[8:11], v[112:115]\n" \
  "s_waitcnt vmcnt(12)\n" \
  "v_cvt_pk_bf16_f32 v161, v98, v99\n" \
  "v_mfma_f32_16x16x32_bf16 v[116:119], v[72:75], a[40:43], v[116:119]\n" \
  "v_mfma_f32_16x16x32_bf16 v[120:123], v[72:75], a[72:75], v[120:123]\n" \
  "ds_read_b128 v[32:35], v200 offset:16384\n" \
  "v_cvt_pk_bf16_f32 v164, v100, v101\n" \
  "v_mfma_f32_16x16x32_bf16 v[124:127], v[72:75], a[104:107], v[124:127]\n" \
  "s_waitcnt lgkmcnt(5)\n" \
  "v_mfma_f32_16x16x32_bf16 v[112:115], v[76:79], a[12:15], v[112:115]\n" \
  "ds_read_b128 v[36:39], v201 offset:16384\n" \
  "v_cvt_pk_bf16_f32 v165, v102, v103\n" \
  "v_mfma_f32_16x16x32_bf16 v[116:119], v[76:79], a[44:47], v[116:119]\n" \
  "v_mfma_f32_16x16x32_bf16 v[120:123], v[76:79], a[76:79], v[120:123]\n" \
  "ds_read_b128 v[40:43], v202 offset:16384\n" \
  "v_cvt_pk_bf16_f32 v168, v104, v105\n" \
  "v_mfma_f32_16x16x32_bf16 v[124:127], v[76:79], a[108:111], v[124:127]\n" \
  "s_waitcnt lgkmcnt(6)\n" \
  "v_mfma_f32_16x16x32_bf16 v[112:115], v[80:83], a[16:19], v[112:115]\n" \
  "ds_read_b128 v[44:47], v203 offset:16384\n" \
  "v_cvt_pk_bf16_f32 v169, v106, v107\n" \
  "v_mfma_f32_16x16x32_bf16 v[116:119], v[80:83], a[48:51], v[116:119]\n" \
  "v_mfma_f32_16x16x32_bf16 v[120:123], v[80:83], a[80:83], v[120:123]\n" \
  "ds_read_b128 v[48:51], v200 offset:16640\n" \
  "v_cvt_pk_bf16_f32 v172, v108, v109\n" \
  "v_mfma_f32_16x16x32_bf16 v[124:127], v[80:83], a[112:115], v[124:127]\n" \
  "s_waitcnt lgkmcnt(7)\n" \
  "v_mfma_f32_16x16x32_bf16 v[112:115], v[84:87], a[20:23], v[112:115]\n" \
  "ds_read_b128 v[52:55], v201 offset:16640\n" \
  "v_cvt_pk_bf16_f32 v173, v110, v111\n" \
  "v_mfma_f32_16x16x32_bf16 v[116:119], v[84:87], a[52:55], v[116:119]\n" \
  "v_mfma_f32_16x16x32_bf16 v[120:123], v[84:87], a[84:87], v[120:123]\n" \
  "ds_read_b128 v[56:59], v202 offset:16640\n" \
  "v_mfma_f32_16x16x32_bf16 v[124:127], v[84:87], a[116:119], v[124:127]\n" \
  "s_waitcnt lgkmcnt(8)\n" \
  "v_mfma_f32_16x16x32_bf16 v[112:115], v[88:91], a[24:27], v[112:115]\n" \
  "ds_read_b128 v[60:63], v203 offset:16640\n" \
  "v_mfma_f32_16x16x32_bf16 v[116:119], v[88:91], a[56:59], v[116:119]\n" \
  "v_mfma_f32_16x16x32_bf16 v[120:123], v[88:91], a[88:91], v[120:123]\n" \
  "v_mfma_f32_16x16x32_bf16 v[124:127], v[88:91], a[120:123], v[124:127]\n" \
  "s_waitcnt lgkmcnt(8)\n" \
  "v_mfma_f32_16x16x32_bf16 v[112:115], v[92:95], a[28:31], v[112:115]\n" \
  "v_mfma_f32_16x16x32_bf16 v[116:119], v[92:95], a[60:63], v[116:119]\n" \
  "v_mfma_f32_16x16x32_bf16 v[120:123], v[92:95], a[92:95], v[120:123]\n" \
  "v_mfma_f32_16x16x32_bf16 v[124:127], v[92:95], a[124:127], v[124:127]\n" \
  "s_waitcnt lgkmcnt(7)\n" \
  "v_mfma_f32_16x16x32_bf16 v[96:99], v[32:35], a[0:3], 0\n" \
  "v_mfma_f32_16x16x32_bf16 v[100:103], v[32:35], a[32:35], 0\n" \
  "v_mfma_f32_16x16x32_bf16 v[104:107], v[32:35], a[64:67], 0\n" \
  "v_mfma_f32_16x16x32_bf16 v[108:111], v[32:35], a[96:99], 0\n" \
  "s_waitcnt lgkmcnt(6)\n" \
  "v_mfma_f32_16x16x32_bf16 v[96:99], v[36:39], a[4:7], v[96:99]\n" \
  "v_mfma_f32_16x16x32_bf16 v[100:103], v[36:39], a[36:39], v[100:103]\n" \
  "v_mfma_f32_16x16x32_bf16 v[104:107], v[36:39], a[68:71], v[104:107]\n" \
  "s_lshl_b32 s57, s54, 10\n" \
  "s_add_u32 s57, s57, %[sO]\n" \
  "v_mfma_f32_16x16x32_bf16 v[108:111], v[36:39], a[100:103], v[108:111]\n" \
  "s_waitcnt lgkmcnt(5)\n" \
  "v_mfma_f32_16x16x32_bf16 v[96:99], v[40:43], a[8:11], v[96:99]\n" \
  "s_waitcnt vmcnt(3)\n" \
  "v_cvt_pk_bf16_f32 v162, v112, v113\n" \
  "v_mfma_f32_16x16x32_bf16 v[100:103], v[40:43], a[40:43], v[100:103]\n" \
  "v_mfma_f32_16x16x32_bf16 v[104:107], v[40:43], a[72:75], v[104:107]\n" \
  "ds_read_b128 v[64:67], v200 offset:24576\n" \
  "v_cvt_pk_bf16_f32 v163, v114, v115\n" \
  "v_mfma_f32_16x16x32_bf16 v[108:111], v[40:43], a[104:107], v[108:111]\n" \
  "s_waitcnt lgkmcnt(5)\n" \
  "v_mfma_f32_16x16x32_bf16 v[96:99], v[44:47], a[12:15], v[96:99]\n" \
  "ds_read_b128 v[68:71], v201 offset:24576\n" \
  "buffer_store_dwordx4 v[160:163], %[l16], %[rs], s57 offen sc1\n" \
  "s_add_u32 s57, s57, 14336\n" \
  "v_mfma_f32_16x16x32_bf16 v[100:103], v[44:47], a[44:47], v[100:103]\n" \
  "v_mfma_f32_16x16x32_bf16 v[104:107], v[44:47], a[76:79], v[104:107]\n" \
  "ds_read_b128 v[72:75], v202 offset:24576\n" \
  "v_cvt_pk_bf16_f32 v166, v116, v117\n" \
  "v_mfma_f32_16x16x32_bf16 v[108:111], v[44:47], a[108:111], v[108:111]\n" \
  "s_waitcnt lgkmcnt(6)\n" \
  "v_mfma_f32_16x16x32_bf16 v[96:99], v[48:51], a[16:19], v[96:99]\n" \
  "ds_read_b128 v[76:79], v203 offset:24576\n" \
  "v_cvt_pk_bf16_f32 v167, v118, v119\n" \
  "v_mfma_f32_16x16x32_bf16 v[100:103], v[48:51], a[48:51], v[100:103]\n" \
  "v_mfma_f32_16x16x32_bf16 v[104:107], v[48:51], a[80:83], v[104:107]\n" \
  "ds_read_b128 v[80:83], v200 offset:24832\n" \
  "buffer_store_dwordx4 v[164:167], %[l16], %[rs], s57 offen sc1\n" \
  "s_add_u32 s57, s57, 14336\n" \
  "v_mfma_f32_16x16x32_bf16 v[108:111], v[48:51], a[112:115], v[108:111]\n" \
  "s_waitcnt lgkmcnt(7)\n" \
  "v_mfma_f32_16x16x32_bf16 v[96:99], v[52:55], a[20:23], v[96:99]\n" \
  "ds_read_b128 v[84:87], v201 offset:24832\n" \
  "v_cvt_pk_bf16_f32 v170, v120, v121\n" \
  "v_mfma_f32_16x16x32_bf16 v[100:103], v[52:55], a[52:55], v[100:103]\n" \
  "v_mfma_f32_16x16x32_bf16 v[104:107], v[52:55], a[84:87], v[104:107]\n" \
  "ds_read_b128 v[88:91], v202 offset:24832\n" \
  "v_cvt_pk_bf16_f32 v171, v122, v123\n" \
  "v_mfma_f32_16x16x32_bf16 v[108:111], v[52:55], a[116:119], v[108:111]\n" \
  "s_waitcnt lgkmcnt(8)\n" \
  "v_mfma_f32_16x16x32_bf16 v[96:99], v[56:59], a[24:27], v[96:99]\n" \
  "ds_read_b128 v[92:95], v203 offset:24832\n" \
  "buffer_store_dwordx4 v[168:171], %[l16], %[rs], s57 offen sc1\n" \
  "s_add_u32 s57, s57, 14336\n" \
  "v_mfma_f32_16x16x32_bf16 v[100:103], v[56:59], a[56:59], v[100:103]\n" \
  "v_mfma_f32_16x16x32_bf16 v[104:107], v[56:59], a[88:91], v[104:107]\n" \
  "v_cvt_pk_bf16_f32 v174, v124, v125\n" \
  "v_mfma_f32_16x16x32_bf16 v[108:111], v[56:59], a[120:123], v[108:111]\n" \
  "s_waitcnt lgkmcnt(8)\n" \
  "v_mfma_f32_16x16x32_bf16 v[96:99], v[60:63], a[28:31], v[96:99]\n" \
  "v_cvt_pk_bf16_f32 v175, v126, v127\n" \
  "v_mfma_f32_16x16x32_bf16 v[100:103], v[60:63], a[60:63], v[100:103]\n" \
  "v_mfma_f32_16x16x32_bf16 v[104:107], v[60:63], a[92:95], v[104:107]\n" \
  "buffer_store_dwordx4 v[172:175], %[l16], %[rs], s57 offen sc1\n" \
  "s_add_u32 s57, s57, 14336\n" \
  "v_mfma_f32_16x16x32_bf16 v[108:111], v[60:63], a[124:127], v[108:111]\n" \
  "s_waitcnt lgkmcnt(0)\n" \
  "s_barrier\n" \
  "v_mfma_f32_16x16x32_bf16 v[112:115], v[64:67], a[0:3], 0\n" \
  "v_mfma_f32_16x16x32_bf16 v[116:119], v[64:67], a[32:35], 0\n" \
  "v_mfma_f32_16x16x32_bf16 v[120:123], v[64:67], a[64:67], 0\n" \
  "v_mfma_f32_16x16x32_bf16 v[124:127], v[64:67], a[96:99], 0\n" \
  "s_lshl_b32 s47, s52, 10\n" \
  "v_add_u32 v232, s47, %[l16]\n" \
  "ds_write_b128 v232, v[128:131]\n" \
  "v_mfma_f32_16x16x32_bf16 v[112:115], v[68:71], a[4:7], v[112:115]\n" \
  "v_mfma_f32_16x16x32_bf16 v[116:119], v[68:71], a[36:39], v[116:119]\n" \
  "ds_write_b128 v232, v[132:135] offset:14336\n" \
  "ds_write_b128 v232, v[136:139] offset:28672\n" \
  "v_mfma_f32_16x16x32_bf16 v[120:123], v[68:71], a[68:71], v[120:123]\n" \
  "v_cvt_pk_bf16_f32 v176, v96, v97\n" \
  "v_mfma_f32_16x16x32_bf16 v[124:127], v[68:71], a[100:103], v[124:127]\n" \
  "ds_write_b128 v232, v[140:143] offset:43008\n" \
  "s_lshl_b32 s47, s53, 10\n" \
  "v_add_u32 v232, s47, %[l16]\n" \
  "v_mfma_f32_16x16x32_bf16 v[112:115], v[72:75], a[8:11], v[112:115]\n" \
  "v_cvt_pk_bf16_f32 v177, v98, v99\n" \
  "v_mfma_f32_16x16x32_bf16 v[116:119], v[72:75], a[40:43], v[116:119]\n" \
  "ds_write_b128 v232, v[144:147]\n" \
  "ds_write_b128 v232, v[148:151] offset:14336\n" \
  "v_mfma_f32_16x16x32_bf16 v[120:123], v[72:75], a[72:75], v[120:123]\n" \
  "v_cvt_pk_bf16_f32 v180, v100, v101\n" \
  "v_mfma_f32_16x16x32_bf16 v[124:127], v[72:75], a[104:107], v[124:127]\n" \
  "ds_write_b128 v232, v[152:155] offset:28672\n" \
  "ds_write_b128 v232, v[156:159] offset:43008\n" \
  "v_mfma_f32_16x16x32_bf16 v[112:115], v[76:79], a[12:15], v[112:115]\n" \
  "v_cvt_pk_bf16_f32 v181, v102, v103\n" \
  "v_mfma_f32_16x16x32_bf16 v[116:119], v[76:79], a[44:47], v[116:119]\n" \
  "s_lshl_b32 s47, s54, 10\n" \
  "v_add_u32 v232, s47, %[l16]\n" \
  "ds_write_b128 v232, v[160:163]\n" \
  "v_mfma_f32_16x16x32_bf16 v[120:123], v[76:79], a[76:79], v[120:123]\n" \
  "v_cvt_pk_bf16_f32 v184, v104, v105\n" \
  "v_mfma_f32_16x16x32_bf16 v[124:127], v[76:79], a[108:111], v[124:127]\n" \
  "ds_write_b128 v232, v[164:167] offset:14336\n" \
  "ds_write_b128 v232, v[168:171] offset:28672\n" \
  "v_mfma_f32_16x16x32_bf16 v[112:115], v[80:83], a[16:19], v[112:115]\n" \
  "v_cvt_pk_bf16_f32 v185, v106, v107\n" \
  "v_mfma_f32_16x16x32_bf16 v[116:119], v[80:83], a[48:51], v[116:119]\n" \
  "ds_write_b128 v232, v[172:175] offset:43008\n" \
  "v_mfma_f32_16x16x32_bf16 v[120:123], v[80:83], a[80:83], v[120:123]\n" \
  "v_cvt_pk_bf16_f32 v188, v108, v109\n" \
  "v_mfma_f32_16x16x32_bf16 v[124:127], v[80:83], a[112:115], v[124:127]\n" \
  "v_mfma_f32_16x16x32_bf16 v[112:115], v[84:87], a[20:23], v[112:115]\n" \
  "v_cvt_pk_bf16_f32 v189, v110, v111\n" \
  "v_mfma_f32_16x16x32_bf16 v[116:119], v[84:87], a[52:55], v[116:119]\n" \
  "v_mfma_f32_16x16x32_bf16 v[120:123], v[84:87], a[84:87], v[120:123]\n" \
  "v_mfma_f32_16x16x32_bf16 v[124:127], v[84:87], a[116:119], v[124:127]\n" \
  "v_mfma_f32_16x16x32_bf16 v[112:115], v[88:91], a[24:27], v[112:115]\n" \
  "v_mfma_f32_16x16x32_bf16 v[116:119], v[88:91], a[56:59], v[116:119]\n" \
  "v_mfma_f32_16x16x32_bf16 v[120:123], v[88:91], a[88:91], v[120:123]\n" \
  "v_mfma_f32_16x16x32_bf16 v[124:127], v[88:91], a[120:123], v[124:127]\n" \
  "v_mfma_f32_16x16x32_bf16 v[112:115], v[92:95], a[28:31], v[112:115]\n" \
  "v_mfma_f32_16x16x32_bf16 v[116:119], v[92:95], a[60:63], v[116:119]\n" \
  "v_mfma_f32_16x16x32_bf16 v[120:123], v[92:95], a[92:95], v[120:123]\n" \
  "v_mfma_f32_16x16x32_bf16 v[124:127], v[92:95], a[124:127], v[124:127]\n" \
  "s_lshl_b32 s57, s55, 10\n" \
  "s_add_u32 s57, s57, %[sO]\n" \
  "s_nop 2\n" \
  "v_cvt_pk_bf16_f32 v178, v112, v113\n" \
  "v_cvt_pk_bf16_f32 v179, v114, v115\n" \
  "buffer_store_dwordx4 v[176:179], %[l16], %[rs], s57 offen sc1\n" \
  "s_add_u32 s57, s57, 14336\n" \
  "v_cvt_pk_bf16_f32 v182, v116, v117\n" \
  "v_cvt_pk_bf16_f32 v183, v118, v119\n" \
  "buffer_store_dwordx4 v[180:183], %[l16], %[rs], s57 offen sc1\n" \
  "s_add_u32 s57, s57, 14336\n" \
  "v_cvt_pk_bf16_f32 v186, v120, v121\n" \
  "v_cvt_pk_bf16_f32 v187, v122, v123\n" \
  "buffer_store_dwordx4 v[184:187], %[l16], %[rs], s57 offen sc1\n" \
  "s_add_u32 s57, s57, 14336\n" \
  "v_cvt_pk_bf16_f32 v190, v124, v125\n" \
  "v_cvt_pk_bf16_f32 v191, v126, v127\n" \
  "buffer_store_dwordx4 v[188:191], %[l16], %[rs], s57 offen sc1\n" \
  "s_add_u32 s57, s57, 14336\n" \
  "s_lshl_b32 s47, s55, 10\n" \
  "v_add_u32 v232, s47, %[l16]\n" \
  "ds_write_b128 v232, v[176:179]\n" \
  "ds_write_b128 v232, v[180:183] offset:14336\n" \
  "ds_write_b128 v232, v[184:187] offset:28672\n" \
  "ds_write_b128 v232, v[188:191] offset:43008\n" \
  "s_branch .LQ_fin%=\n" \
  ".LQ_three%=:\n" \
  "s_waitcnt lgkmcnt(7)\n" \
  "v_mfma_f32_16x16x32_bf16 v[112:115], v[64:67], a[0:3], 0\n" \
  "v_mfma_f32_16x16x32_bf16 v[116:119], v[64:67], a[32:35], 0\n" \
  "v_mfma_f32_16x16x32_bf16 v[120:123], v[64:67], a[64:67], 0\n" \
  "v_mfma_f32_16x16x32_bf16 v[124:127], v[64:67], a[96:99], 0\n" \
  "s_waitcnt lgkmcnt(6)\n" \
  "v_mfma_f32_16x16x32_bf16 v[112:115], v[68:71], a[4:7], v[112:115]\n" \
  "v_mfma_f32_16x16x32_bf16 v[116:119], v[68:71], a[36:39], v[116:119]\n" \
  "v_mfma_f32_16x16x32_bf16 v[120:123], v[68:71], a[68:71], v[120:123]\n" \
  "v_cvt_pk_bf16_f32 v144, v96, v97\n" \
  "v_mfma_f32_16x16x32_bf16 v[124:127], v[68:71], a[100:103], v[124:127]\n" \
  "s_waitcnt lgkmcnt(5)\n" \
  "v_mfma_f32_16x16x32_bf16 v[112:115], v[72:75], a[8:11], v[112:115]\n" \
  "s_waitcnt vmcnt(12)\n" \
  "v_cvt_pk_bf16_f32 v145, v98, v99\n" \
  "v_mfma_f32_16x16x32_bf16 v[116:119], v[72:75], a[40:43], v[116:119]\n" \
  "v_mfma_f32_16x16x32_bf16 v[120:123], v[72:75], a[72:75], v[120:123]\n" \
  "ds_read_b128 v[32:35], v200\n" \
  "v_cvt_pk_bf16_f32 v148, v100, v101\n" \
  "v_mfma_f32_16x16x32_bf16 v[124:127], v[72:75], a[104:107], v[124:127]\n" \
  "s_waitcnt lgkmcnt(5)\n" \
  "v_mfma_f32_16x16x32_bf16 v[112:115], v[76:79], a[12:15], v[112:115]\n" \
  "ds_read_b128 v[36:39], v201\n" \
  "v_cvt_pk_bf16_f32 v149, v102, v103\n" \
  "v_mfma_f32_16x16x32_bf16 v[116:119], v[76:79], a[44:47], v[116:119]\n" \
  "v_mfma_f32_16x16x32_bf16 v[120:123], v[76:79], a[76:79], v[120:123]\n" \
  "ds_read_b128 v[40:43], v202\n" \
  "v_cvt_pk_bf16_f32 v152, v104, v105\n" \
  "v_mfma_f32_16x16x32_bf16 v[124:127], v[76:79], a[108:111], v[124:127]\n" \
  "s_waitcnt lgkmcnt(6)\n" \
  "v_mfma_f32_16x16x32_bf16 v[112:115], v[80:83], a[16:19], v[112:115]\n" \
  "ds_read_b128 v[44:47], v203\n" \
  "v_cvt_pk_bf16_f32 v153, v106, v107\n" \
  "v_mfma_f32_16x16x32_bf16 v[116:119], v[80:83], a[48:51], v[116:119]\n" \
  "v_mfma_f32_16x16x32_bf16 v[120:123], v[80:83], a[80:83], v[120:123]\n" \
  "ds_read_b128 v[48:51], v200 offset:256\n" \
  "v_cvt_pk_bf16_f32 v156, v108, v109\n" \
  "v_mfma_f32_16x16x32_bf16 v[124:127], v[80:83], a[112:115], v[124:127]\n" \
  "s_waitcnt lgkmcnt(7)\n" \
  "v_mfma_f32_16x16x32_bf16 v[112:115], v[84:87], a[20:23], v[112:115]\n" \
  "ds_read_b128 v[52:55], v201 offset:256\n" \
  "v_cvt_pk_bf16_f32 v157, v110, v111\n" \
  "v_mfma_f32_16x16x32_bf16 v[116:119], v[84:87], a[52:55], v[116:119]\n" \
  "v_mfma_f32_16x16x32_bf16 v[120:123], v[84:87], a[84:87], v[120:123]\n" \
  "ds_read_b128 v[56:59], v202 offset:256\n" \
  "v_mfma_f32_16x16x32_bf16 v[124:127], v[84:87], a[116:119], v[124:127]\n" \
  "s_waitcnt lgkmcnt(8)\n" \
  "v_mfma_f32_16x16x32_bf16 v[112:115], v[88:91], a[24:27], v[112:115]\n" \
  "ds_read_b128 v[60:63], v203 offset:256\n" \
  "v_mfma_f32_16x16x32_bf16 v[116:119], v[88:91], a[56:59], v[116:119]\n" \
  "v_mfma_f32_16x16x32_bf16 v[120:123], v[88:91], a[88:91], v[120:123]\n" \
  "v_mfma_f32_16x16x32_bf16 v[124:127], v[88:91], a[120:123], v[124:127]\n" \
  "s_waitcnt lgkmcnt(8)\n" \
  "v_mfma_f32_16x16x32_bf16 v[112:115], v[92:95], a[28:31], v[112:115]\n" \
  "v_mfma_f32_16x16x32_bf16 v[116:119], v[92:95], a[60:63], v[116:119]\n" \
  "v_mfma_f32_16x16x32_bf16 v[120:123], v[92:95], a[92:95], v[120:123]\n" \
  "v_mfma_f32_16x16x32_bf16 v[124:127], v[92:95], a[124:127], v[124:127]\n" \
  "s_waitcnt lgkmcnt(7)\n" \
  "v_mfma_f32_16x16x32_bf16 v[96:99], v[32:35], a[0:3], 0\n" \
  "v_mfma_f32_16x16x32_bf16 v[100:103], v[32:35], a[32:35], 0\n" \
  "v_mfma_f32_16x16x32_bf16 v[104:107], v[32:35], a[64:67], 0\n" \
  "v_mfma_f32_16x16x32_bf16 v[108:111], v[32:35], a[96:99], 0\n" \
  "v_add_u32 v192, 0, %[l16]\n" \
  "v_lshrrev_b32 v199, 8, v192\n" \
  "v_mul_u32_u24 v199, 9363, v199\n" \
  "v_lshrrev_b32 v199, 12, v199\n" \
  "v_and_b32 v199, 0x70, v199\n" \
  "v_xor_b32 v192, v192, v199\n" \
  "s_waitcnt lgkmcnt(6)\n" \
  "v_mfma_f32_16x16x32_bf16 v[96:99], v[36:39], a[4:7], v[96:99]\n" \
  "v_mfma_f32_16x16x32_bf16 v[100:103], v[36:39], a[36:39], v[100:103]\n" \
  "v_add_u32 v193, 1024, %[l16]\n" \
  "v_lshrrev_b32 v199, 8, v193\n" \
  "v_mul_u32_u24 v199, 9363, v199\n" \
  "v_lshrrev_b32 v199, 12, v199\n" \
  "v_and_b32 v199, 0x70, v199\n" \
  "v_xor_b32 v193, v193, v199\n" \
  "v_mfma_f32_16x16x32_bf16 v[104:107], v[36:39], a[68:71], v[104:107]\n" \
  "s_lshl_b32 s57, s53, 10\n" \
  "s_add_u32 s57, s57, %[sO]\n" \
  "v_mfma_f32_16x16x32_bf16 v[108:111], v[36:39], a[100:103], v[108:111]\n" \
  "v_add_u32 v194, 2048, %[l16]\n" \
  "v_lshrrev_b32 v199, 8, v194\n" \
  "v_mul_u32_u24 v199, 9363, v199\n" \
  "v_lshrrev_b32 v199, 12, v199\n" \
  "v_and_b32 v199, 0x70, v199\n" \
  "v_xor_b32 v194, v194, v199\n" \
  "s_waitcnt lgkmcnt(5)\n" \
  "v_mfma_f32_16x16x32_bf16 v[96:99], v[40:43], a[8:11], v[96:99]\n" \
  "s_waitcnt vmcnt(3)\n" \
  "v_cvt_pk_bf16_f32 v146, v112, v113\n" \
  "v_mfma_f32_16x16x32_bf16 v[100:103], v[40:43], a[40:43], v[100:103]\n" \
  "v_add_u32 v195, 3072, %[l16]\n" \
  "v_lshrrev_b32 v199, 8, v195\n" \
  "v_mul_u32_u24 v199, 9363, v199\n" \
  "v_lshrrev_b32 v199, 12, v199\n" \
  "v_and_b32 v199, 0x70, v199\n" \
  "v_xor_b32 v195, v195, v199\n" \
  "v_mfma_f32_16x16x32_bf16 v[104:107], v[40:43], a[72:75], v[104:107]\n" \
  "ds_read_b128 v[64:67], v200 offset:8192\n" \
  "v_cvt_pk_bf16_f32 v147, v114, v115\n" \
  "v_mfma_f32_16x16x32_bf16 v[108:111], v[40:43], a[104:107], v[108:111]\n" \
  "v_add_u32 v196, 4096, %[l16]\n" \
  "v_lshrrev_b32 v199, 8, v196\n" \
  "v_mul_u32_u24 v199, 9363, v199\n" \
  "v_lshrrev_b32 v199, 12, v199\n" \
  "v_and_b32 v199, 0x70, v199\n" \
  "v_xor_b32 v196, v196, v199\n" \
  "s_waitcnt lgkmcnt(5)\n" \
  "v_mfma_f32_16x16x32_bf16 v[96:99], v[44:47], a[12:15], v[96:99]\n" \
  "ds_read_b128 v[68:71], v201 offset:8192\n" \
  "buffer_store_dwordx4 v[144:147], %[l16], %[rs], s57 offen sc1\n" \
  "s_add_u32 s57, s57, 14336\n" \
  "v_mfma_f32_16x16x32_bf16 v[100:103], v[44:47], a[44:47], v[100:103]\n" \
  "v_add_u32 v197, 5120, %[l16]\n" \
  "v_lshrrev_b32 v199, 8, v197\n" \
  "v_mul_u32_u24 v199, 9363, v199\n" \
  "v_lshrrev_b32 v199, 12, v199\n" \
  "v_and_b32 v199, 0x70, v199\n" \
  "v_xor_b32 v197, v197, v199\n" \
  "v_mfma_f32_16x16x32_bf16 v[104:107], v[44:47], a[76:79], v[104:107]\n" \
  "ds_read_b128 v[72:75], v202 offset:8192\n" \
  "v_cvt_pk_bf16_f32 v150, v116, v117\n" \
  "v_mfma_f32_16x16x32_bf16 v[108:111], v[44:47], a[108:111], v[108:111]\n" \
  "v_add_u32 v198, 6144, %[l16]\n" \
  "v_lshrrev_b32 v199, 8, v198\n" \
  "v_mul_u32_u24 v199, 9363, v199\n" \
  "v_lshrrev_b32 v199, 12, v199\n" \
  "v_and_b32 v199, 0x70, v199\n" \
  "v_xor_b32 v198, v198, v199\n" \
  "s_waitcnt lgkmcnt(6)\n" \
  "v_mfma_f32_16x16x32_bf16 v[96:99], v[48:51], a[16:19], v[96:99]\n" \
  "ds_read_b128 v[76:79], v203 offset:8192\n" \
  "v_cvt_pk_bf16_f32 v151, v118, v119\n" \
  "v_mfma_f32_16x16x32_bf16 v[100:103], v[48:51], a[48:51], v[100:103]\n" \
  "v_mfma_f32_16x16x32_bf16 v[104:107], v[48:51], a[80:83], v[104:107]\n" \
  "ds_read_b128 v[80:83], v200 offset:8448\n" \
  "buffer_store_dwordx4 v[148:151], %[l16], %[rs], s57 offen sc1\n" \
  "s_add_u32 s57, s57, 14336\n" \
  "v_mfma_f32_16x16x32_bf16 v[108:111], v[48:51], a[112:115], v[108:111]\n" \
  "s_waitcnt lgkmcnt(7)\n" \
  "v_mfma_f32_16x16x32_bf16 v[96:99], v[52:55], a[20:23], v[96:99]\n" \
  "ds_read_b128 v[84:87], v201 offset:8448\n" \
  "v_cvt_pk_bf16_f32 v154, v120, v121\n" \
  "v_mfma_f32_16x16x32_bf16 v[100:103], v[52:55], a[52:55], v[100:103]\n" \
  "v_mfma_f32_16x16x32_bf16 v[104:107], v[52:55], a[84:87], v[104:107]\n" \
  "ds_read_b128 v[88:91], v202 offset:8448\n" \
  "v_cvt_pk_bf16_f32 v155, v122, v123\n" \
  "v_mfma_f32_16x16x32_bf16 v[108:111], v[52:55], a[116:119], v[108:111]\n" \
  "s_waitcnt lgkmcnt(8)\n" \
  "v_mfma_f32_16x16x32_bf16 v[96:99], v[56:59], a[24:27], v[96:99]\n" \
  "ds_read_b128 v[92:95], v203 offset:8448\n" \
  "buffer_store_dwordx4 v[152:155], %[l16], %[rs], s57 offen sc1\n" \
  "s_add_u32 s57, s57, 14336\n" \
  "v_mfma_f32_16x16x32_bf16 v[100:103], v[56:59], a[56:59], v[100:103]\n" \
  "v_mfma_f32_16x16x32_bf16 v[104:107], v[56:59], a[88:91], v[104:107]\n" \
  "v_cvt_pk_bf16_f32 v158, v124, v125\n" \
  "v_mfma_f32_16x16x32_bf16 v[108:111], v[56:59], a[120:123], v[108:111]\n" \
  "s_waitcnt lgkmcnt(8)\n" \
  "v_mfma_f32_16x16x32_bf16 v[96:99], v[60:63], a[28:31], v[96:99]\n" \
  "v_cvt_pk_bf16_f32 v159, v126, v127\n" \
  "v_mfma_f32_16x16x32_bf16 v[100:103], v[60:63], a[60:63], v[100:103]\n" \
  "v_mfma_f32_16x16x32_bf16 v[104:107], v[60:63], a[92:95], v[104:107]\n" \
  "buffer_store_dwordx4 v[156:159], %[l16], %[rs], s57 offen sc1\n" \
  "s_add_u32 s57, s57, 14336\n" \
  "v_mfma_f32_16x16x32_bf16 v[108:111], v[60:63], a[124:127], v[108:111]\n" \
  "s_waitcnt lgkmcnt(7)\n" \
  "v_mfma_f32_16x16x32_bf16 v[112:115], v[64:67], a[0:3], 0\n" \
  "v_mfma_f32_16x16x32_bf16 v[116:119], v[64:67], a[32:35], 0\n" \
  "v_mfma_f32_16x16x32_bf16 v[120:123], v[64:67], a[64:67], 0\n" \
  "v_mfma_f32_16x16x32_bf16 v[124:127], v[64:67], a[96:99], 0\n" \
  "s_waitcnt lgkmcnt(0)\n" \
  "s_mov_b32 m0, %[ma]\n" \
  "s_sub_u32 s48, %[w], 2\n" \
  "s_mul_i32 s48, s48, 28672\n" \
  "buffer_load_dwordx4 v192, %[rx], s48 offen lds\n" \
  "s_add_u32 m0, m0, 1024\n" \
  "v_xor_b32 v192, 64, v192\n" \
  "buffer_load_dwordx4 v193, %[rx], s48 offen lds\n" \
  "s_add_u32 m0, m0, 1024\n" \
  "v_xor_b32 v193, 64, v193\n" \
  "v_mfma_f32_16x16x32_bf16 v[112:115], v[68:71], a[4:7], v[112:115]\n" \
  "v_mfma_f32_16x16x32_bf16 v[116:119], v[68:71], a[36:39], v[116:119]\n" \
  "buffer_load_dwordx4 v194, %[rx], s48 offen lds\n" \
  "s_add_u32 m0, m0, 1024\n" \
  "v_xor_b32 v194, 64, v194\n" \
  "buffer_load_dwordx4 v195, %[rx], s48 offen lds\n" \
  "s_add_u32 m0, m0, 1024\n" \
  "v_xor_b32 v195, 64, v195\n" \
  "v_mfma_f32_16x16x32_bf16 v[120:123], v[68:71], a[68:71], v[120:123]\n" \
  "v_cvt_pk_bf16_f32 v160, v96, v97\n" \
  "v_mfma_f32_16x16x32_bf16 v[124:127], v[68:71], a[100:103], v[124:127]\n" \
  "buffer_load_dwordx4 v196, %[rx], s48 offen lds\n" \
  "s_add_u32 m0, m0, 1024\n" \
  "v_xor_b32 v196, 64, v196\n" \
  "buffer_load_dwordx4 v197, %[rx], s48 offen lds\n" \
  "s_add_u32 m0, m0, 1024\n" \
  "v_xor_b32 v197, 64, v197\n" \
  "v_mfma_f32_16x16x32_bf16 v[112:115], v[72:75], a[8:11], v[112:115]\n" \
  "v_cvt_pk_bf16_f32 v161, v98, v99\n" \
  "v_mfma_f32_16x16x32_bf16 v[116:119], v[72:75], a[40:43], v[116:119]\n" \
  "buffer_load_dwordx4 v198, %[rx], s48 offen lds\n" \
  "s_add_u32 m0, m0, 1024\n" \
  "v_xor_b32 v198, 64, v198\n" \
  "s_add_u32 s48, s48, 7168\n" \
  "buffer_load_dwordx4 v192, %[rx], s48 offen lds\n" \
  "s_add_u32 m0, m0, 1024\n" \
  "v_xor_b32 v192, 64, v192\n" \
  "v_mfma_f32_16x16x32_bf16 v[120:123], v[72:75], a[72:75], v[120:123]\n" \
  "v_cvt_pk_bf16_f32 v164, v100, v101\n" \
  "v_mfma_f32_16x16x32_bf16 v[124:127], v[72:75], a[104:107], v[124:127]\n" \
  "buffer_load_dwordx4 v193, %[rx], s48 offen lds\n" \
  "s_add_u32 m0, m0, 1024\n" \
  "v_xor_b32 v193, 64, v193\n" \
  "buffer_load_dwordx4 v194, %[rx], s48 offen lds\n" \
  "s_add_u32 m0, m0, 1024\n" \
  "v_xor_b32 v194, 64, v194\n" \
  "v_mfma_f32_16x16x32_bf16 v[112:115], v[76:79], a[12:15], v[112:115]\n" \
  "v_cvt_pk_bf16_f32 v165, v102, v103\n" \
  "v_mfma_f32_16x16x32_bf16 v[116:119], v[76:79], a[44:47], v[116:119]\n" \
  "buffer_load_dwordx4 v195, %[rx], s48 offen lds\n" \
  "s_add_u32 m0, m0, 1024\n" \
  "v_xor_b32 v195, 64, v195\n" \
  "buffer_load_dwordx4 v196, %[rx], s48 offen lds\n" \
  "s_add_u32 m0, m0, 1024\n" \
  "v_xor_b32 v196, 64, v196\n" \
  "v_mfma_f32_16x16x32_bf16 v[120:123], v[76:79], a[76:79], v[120:123]\n" \
  "v_cvt_pk_bf16_f32 v168, v104, v105\n" \
  "v_mfma_f32_16x16x32_bf16 v[124:127], v[76:79], a[108:111], v[124:127]\n" \
  "buffer_load_dwordx4 v197, %[rx], s48 offen lds\n" \
  "s_add_u32 m0, m0, 1024\n" \
  "v_xor_b32 v197, 64, v197\n" \
  "buffer_load_dwordx4 v198, %[rx], s48 offen lds\n" \
  "s_add_u32 m0, m0, 1024\n" \
  "v_xor_b32 v198, 64, v198\n" \
  "s_add_u32 s48, s48, 7168\n" \
  "v_mfma_f32_16x16x32_bf16 v[112:115], v[80:83], a[16:19], v[112:115]\n" \
  "v_cvt_pk_bf16_f32 v169, v106, v107\n" \
  "v_mfma_f32_16x16x32_bf16 v[116:119], v[80:83], a[48:51], v[116:119]\n" \
  "buffer_load_dwordx4 v192, %[rx], s48 offen lds\n" \
  "s_add_u32 m0, m0, 1024\n" \
  "v_xor_b32 v192, 64, v192\n" \
  "buffer_load_dwordx4 v193, %[rx], s48 offen lds\n" \
  "s_add_u32 m0, m0, 1024\n" \
  "v_xor_b32 v193, 64, v193\n" \
  "v_mfma_f32_16x16x32_bf16 v[120:123], v[80:83], a[80:83], v[120:123]\n" \
  "v_cvt_pk_bf16_f32 v172, v108, v109\n" \
  "v_mfma_f32_16x16x32_bf16 v[124:127], v[80:83], a[112:115], v[124:127]\n" \
  "buffer_load_dwordx4 v194, %[rx], s48 offen lds\n" \
  "s_add_u32 m0, m0, 1024\n" \
  "v_xor_b32 v194, 64, v194\n" \
  "buffer_load_dwordx4 v195, %[rx], s48 offen lds\n" \
  "s_add_u32 m0, m0, 1024\n" \
  "v_xor_b32 v195, 64, v195\n" \
  "v_mfma_f32_16x16x32_bf16 v[112:115], v[84:87], a[20:23], v[112:115]\n" \
  "v_cvt_pk_bf16_f32 v173, v110, v111\n" \
  "v_mfma_f32_16x16x32_bf16 v[116:119], v[84:87], a[52:55], v[116:119]\n" \
  "buffer_load_dwordx4 v196, %[rx], s48 offen lds\n" \
  "s_add_u32 m0, m0, 1024\n" \
  "v_xor_b32 v196, 64, v196\n" \
  "buffer_load_dwordx4 v197, %[rx], s48 offen lds\n" \
  "s_add_u32 m0, m0, 1024\n" \
  "v_xor_b32 v197, 64, v197\n" \
  "v_mfma_f32_16x16x32_bf16 v[120:123], v[84:87], a[84:87], v[120:123]\n" \
  "v_mfma_f32_16x16x32_bf16 v[124:127], v[84:87], a[116:119], v[124:127]\n" \
  "buffer_load_dwordx4 v198, %[rx], s48 offen lds\n" \
  "s_add_u32 m0, m0, 1024\n" \
  "v_xor_b32 v198, 64, v198\n" \
  "s_add_u32 s48, s48, 7168\n" \
  "buffer_load_dwordx4 v192, %[rx], s48 offen lds\n" \
  "s_add_u32 m0, m0, 1024\n" \
  "v_xor_b32 v192, 64, v192\n" \
  "v_mfma_f32_16x16x32_bf16 v[112:115], v[88:91], a[24:27], v[112:115]\n" \
  "v_mfma_f32_16x16x32_bf16 v[116:119], v[88:91], a[56:59], v[116:119]\n" \
  "buffer_load_dwordx4 v193, %[rx], s48 offen lds\n" \
  "s_add_u32 m0, m0, 1024\n" \
  "v_xor_b32 v193, 64, v193\n" \
  "buffer_load_dwordx4 v194, %[rx], s48 offen lds\n" \
  "s_add_u32 m0, m0, 1024\n" \
  "v_xor_b32 v194, 64, v194\n" \
  "v_mfma_f32_16x16x32_bf16 v[120:123], v[88:91], a[88:91], v[120:123]\n" \
  "v_mfma_f32_16x16x32_bf16 v[124:127], v[88:91], a[120:123], v[124:127]\n" \
  "buffer_load_dwordx4 v195, %[rx], s48 offen lds\n" \
  "s_add_u32 m0, m0, 1024\n" \
  "v_xor_b32 v195, 64, v195\n" \
  "buffer_load_dwordx4 v196, %[rx], s48 offen lds\n" \
  "s_add_u32 m0, m0, 1024\n" \
  "v_xor_b32 v196, 64, v196\n" \
  "v_mfma_f32_16x16x32_bf16 v[112:115], v[92:95], a[28:31], v[112:115]\n" \
  "v_mfma_f32_16x16x32_bf16 v[116:119], v[92:95], a[60:63], v[116:119]\n" \
  "buffer_load_dwordx4 v197, %[rx], s48 offen lds\n" \
  "s_add_u32 m0, m0, 1024\n" \
  "v_xor_b32 v197, 64, v197\n" \
  "buffer_load_dwordx4 v198, %[rx], s48 offen lds\n" \
  "s_add_u32 m0, m0, 1024\n" \
  "v_xor_b32 v198, 64, v198\n" \
  "s_add_u32 s48, s48, 7168\n" \
  "v_mfma_f32_16x16x32_bf16 v[120:123], v[92:95], a[92:95], v[120:123]\n" \
  "v_mfma_f32_16x16x32_bf16 v[124:127], v[92:95], a[124:127], v[124:127]\n" \
  "s_lshl_b32 s57, s54, 10\n" \
  "s_add_u32 s57, s57, %[sO]\n" \
  "v_cvt_pk_bf16_f32 v162, v112, v113\n" \
  "v_cvt_pk_bf16_f32 v163, v114, v115\n" \
  "buffer_store_dwordx4 v[160:163], %[l16], %[rs], s57 offen sc1\n" \
  "s_add_u32 s57, s57, 14336\n" \
  "v_cvt_pk_bf16_f32 v166, v116, v117\n" \
  "v_cvt_pk_bf16_f32 v167, v118, v119\n" \
  "buffer_store_dwordx4 v[164:167], %[l16], %[rs], s57 offen sc1\n" \
  "s_add_u32 s57, s57, 14336\n" \
  "v_cvt_pk_bf16_f32 v170, v120, v121\n" \
  "v_cvt_pk_bf16_f32 v171, v122, v123\n" \
  "buffer_store_dwordx4 v[168:171], %[l16], %[rs], s57 offen sc1\n" \
  "s_add_u32 s57, s57, 14336\n" \
  "v_cvt_pk_bf16_f32 v174, v124, v125\n" \
  "v_cvt_pk_bf16_f32 v175, v126, v127\n" \
  "buffer_store_dwordx4 v[172:175], %[l16], %[rs], s57 offen sc1\n" \
  "s_add_u32 s57, s57, 14336\n" \
  "s_barrier\n" \
  "s_lshl_b32 s47, s52, 10\n" \
  "v_add_u32 v232, s47, %[l16]\n" \
  "ds_write_b128 v232, v[128:131]\n" \
  "ds_write_b128 v232, v[132:135] offset:14336\n" \
  "ds_write_b128 v232, v[136:139] offset:28672\n" \
  "ds_write_b128 v232, v[140:143] offset:43008\n" \
  "s_lshl_b32 s47, s53, 10\n" \
  "v_add_u32 v232, s47, %[l16]\n" \
  "ds_write_b128 v232, v[144:147]\n" \
  "ds_write_b128 v232, v[148:151] offset:14336\n" \
  "ds_write_b128 v232, v[152:155] offset:28672\n" \
  "ds_write_b128 v232, v[156:159] offset:43008\n" \
  "s_lshl_b32 s47, s54, 10\n" \
  "v_add_u32 v232, s47, %[l16]\n" \
  "ds_write_b128 v232, v[160:163]\n" \
  "ds_write_b128 v232, v[164:167] offset:14336\n" \
  "ds_write_b128 v232, v[168:171] offset:28672\n" \
  "ds_write_b128 v232, v[172:175] offset:43008\n" \
  "s_waitcnt vmcnt(4)\n" \
  ".LQ_fin%=:\n" \
  "s_and_b32 s46, %[w], 1\n" \
  "s_mul_i32 s46, s46, 28672\n" \
  "v_add_u32 v232, s46, %[l16]\n" \
  "v_xor_b32 v233, 64, %[rX]\n" \
  "s_waitcnt lgkmcnt(0)\n" \
  "s_barrier\n" \
  "ds_read_b128 v[32:35], v232\n" \
  "ds_read_b128 v[36:39], v232 offset:14336\n" \
  "ds_read_b128 v[40:43], %[rX]\n" \
  "ds_read_b128 v[44:47], %[rX] offset:14336\n" \
  "ds_read_b128 v[48:51], v232 offset:1024\n" \
  "ds_read_b128 v[52:55], v232 offset:15360\n" \
  "ds_read_b128 v[56:59], v233\n" \
  "ds_read_b128 v[60:63], v233 offset:14336\n" \
  "ds_read_b128 v[64:67], v232 offset:2048\n" \
  "ds_read_b128 v[68:71], v232 offset:16384\n" \
  "ds_read_b128 v[72:75], %[rX] offset:128\n" \
  "ds_read_b128 v[76:79], %[rX] offset:14464\n" \
  "ds_read_b128 v[80:83], v232 offset:3072\n" \
  "ds_read_b128 v[84:87], v232 offset:17408\n" \
  "ds_read_b128 v[88:91], v233 offset:128\n" \
  "ds_read_b128 v[92:95], v233 offset:14464\n" \
  "s_waitcnt lgkmcnt(13)\n" \
  "v_mfma_f32_16x16x32_bf16 v[96:99], v[40:43], v[32:35], 0\n" \
  "s_waitcnt lgkmcnt(12)\n" \
  "v_mfma_f32_16x16x32_bf16 v[100:103], v[44:47], v[32:35], 0\n" \
  "v_mfma_f32_16x16x32_bf16 v[104:107], v[40:43], v[36:39], 0\n" \
  "v_mfma_f32_16x16x32_bf16 v[108:111], v[44:47], v[36:39], 0\n" \
  "ds_read_b128 v[32:35], v232 offset:4096\n" \
  "ds_read_b128 v[36:39], v232 offset:18432\n" \
  "ds_read_b128 v[40:43], %[rX] offset:256\n" \
  "ds_read_b128 v[44:47], %[rX] offset:14592\n" \
  "s_waitcnt lgkmcnt(13)\n" \
  "v_mfma_f32_16x16x32_bf16 v[96:99], v[56:59], v[48:51], v[96:99]\n" \
  "s_waitcnt lgkmcnt(12)\n" \
  "v_mfma_f32_16x16x32_bf16 v[100:103], v[60:63], v[48:51], v[100:103]\n" \
  "v_mfma_f32_16x16x32_bf16 v[104:107], v[56:59], v[52:55], v[104:107]\n" \
  "v_mfma_f32_16x16x32_bf16 v[108:111], v[60:63], v[52:55], v[108:111]\n" \
  "ds_read_b128 v[48:51], v232 offset:5120\n" \
  "ds_read_b128 v[52:55], v232 offset:19456\n" \
  "ds_read_b128 v[56:59], v233 offset:256\n" \
  "ds_read_b128 v[60:63], v233 offset:14592\n" \
  "s_waitcnt lgkmcnt(13)\n" \
  "v_mfma_f32_16x16x32_bf16 v[96:99], v[72:75], v[64:67], v[96:99]\n" \
  "s_waitcnt lgkmcnt(12)\n" \
  "v_mfma_f32_16x16x32_bf16 v[100:103], v[76:79], v[64:67], v[100:103]\n" \
  "v_mfma_f32_16x16x32_bf16 v[104:107], v[72:75], v[68:71], v[104:107]\n" \
  "v_mfma_f32_16x16x32_bf16 v[108:111], v[76:79], v[68:71], v[108:111]\n" \
  "ds_read_b128 v[64:67], v232 offset:6144\n" \
  "ds_read_b128 v[68:71], v232 offset:20480\n" \
  "ds_read_b128 v[72:75], %[rX] offset:384\n" \
  "ds_read_b128 v[76:79], %[rX] offset:14720\n" \
  "s_waitcnt lgkmcnt(13)\n" \
  "v_mfma_f32_16x16x32_bf16 v[96:99], v[88:91], v[80:83], v[96:99]\n" \
  "s_waitcnt lgkmcnt(12)\n" \
  "v_mfma_f32_16x16x32_bf16 v[100:103], v[92:95], v[80:83], v[100:103]\n" \
  "v_mfma_f32_16x16x32_bf16 v[104:107], v[88:91], v[84:87], v[104:107]\n" \
  "v_mfma_f32_16x16x32_bf16 v[108:111], v[92:95], v[84:87], v[108:111]\n" \
  "ds_read_b128 v[80:83], v232 offset:7168\n" \
  "ds_read_b128 v[84:87], v232 offset:21504\n" \
  "ds_read_b128 v[88:91], v233 offset:384\n" \
  "ds_read_b128 v[92:95], v233 offset:14720\n" \
  "s_waitcnt lgkmcnt(13)\n" \
  "v_mfma_f32_16x16x32_bf16 v[96:99], v[40:43], v[32:35], v[96:99]\n" \
  "s_waitcnt lgkmcnt(12)\n" \
  "v_mfma_f32_16x16x32_bf16 v[100:103], v[44:47], v[32:35], v[100:103]\n" \
  "v_mfma_f32_16x16x32_bf16 v[104:107], v[40:43], v[36:39], v[104:107]\n" \
  "v_mfma_f32_16x16x32_bf16 v[108:111], v[44:47], v[36:39], v[108:111]\n" \
  "ds_read_b128 v[32:35], v232 offset:8192\n" \
  "ds_read_b128 v[36:39], v232 offset:22528\n" \
  "ds_read_b128 v[40:43], %[rX] offset:512\n" \
  "ds_read_b128 v[44:47], %[rX] offset:14848\n" \
  "s_waitcnt lgkmcnt(13)\n" \
  "v_mfma_f32_16x16x32_bf16 v[96:99], v[56:59], v[48:51], v[96:99]\n" \
  "s_waitcnt lgkmcnt(12)\n" \
  "v_mfma_f32_16x16x32_bf16 v[100:103], v[60:63], v[48:51], v[100:103]\n" \
  "v_mfma_f32_16x16x32_bf16 v[104:107], v[56:59], v[52:55], v[104:107]\n" \
  "v_mfma_f32_16x16x32_bf16 v[108:111], v[60:63], v[52:55], v[108:111]\n" \
  "ds_read_b128 v[48:51], v232 offset:9216\n" \
  "ds_read_b128 v[52:55], v232 offset:23552\n" \
  "ds_read_b128 v[56:59], v233 offset:512\n" \
  "ds_read_b128 v[60:63], v233 offset:14848\n" \
  "s_waitcnt lgkmcnt(13)\n" \
  "v_mfma_f32_16x16x32_bf16 v[96:99], v[72:75], v[64:67], v[96:99]\n" \
  "s_waitcnt lgkmcnt(12)\n" \
  "v_mfma_f32_16x16x32_bf16 v[100:103], v[76:79], v[64:67], v[100:103]\n" \
  "v_mfma_f32_16x16x32_bf16 v[104:107], v[72:75], v[68:71], v[104:107]\n" \
  "v_mfma_f32_16x16x32_bf16 v[108:111], v[76:79], v[68:71], v[108:111]\n" \
  "ds_read_b128 v[64:67], v232 offset:10240\n" \
  "ds_read_b128 v[68:71], v232 offset:24576\n" \
  "ds_read_b128 v[72:75], %[rX] offset:640\n" \
  "ds_read_b128 v[76:79], %[rX] offset:14976\n" \
  "s_waitcnt lgkmcnt(13)\n" \
  "v_mfma_f32_16x16x32_bf16 v[96:99], v[88:91], v[80:83], v[96:99]\n" \
  "s_waitcnt lgkmcnt(12)\n" \
  "v_mfma_f32_16x16x32_bf16 v[100:103], v[92:95], v[80:83], v[100:103]\n" \
  "v_mfma_f32_16x16x32_bf16 v[104:107], v[88:91], v[84:87], v[104:107]\n" \
  "v_mfma_f32_16x16x32_bf16 v[108:111], v[92:95], v[84:87], v[108:111]\n" \
  "ds_read_b128 v[80:83], v232 offset:11264\n" \
  "ds_read_b128 v[84:87], v232 offset:25600\n" \
  "ds_read_b128 v[88:91], v233 offset:640\n" \
  "ds_read_b128 v[92:95], v233 offset:14976\n" \
  "s_waitcnt lgkmcnt(13)\n" \
  "v_mfma_f32_16x16x32_bf16 v[96:99], v[40:43], v[32:35], v[96:99]\n" \
  "s_waitcnt lgkmcnt(12)\n" \
  "v_mfma_f32_16x16x32_bf16 v[100:103], v[44:47], v[32:35], v[100:103]\n" \
  "v_mfma_f32_16x16x32_bf16 v[104:107], v[40:43], v[36:39], v[104:107]\n" \
  "v_mfma_f32_16x16x32_bf16 v[108:111], v[44:47], v[36:39], v[108:111]\n" \
  "ds_read_b128 v[32:35], v232 offset:12288\n" \
  "ds_read_b128 v[36:39], v232 offset:26624\n" \
  "ds_read_b128 v[40:43], %[rX] offset:768\n" \
  "ds_read_b128 v[44:47], %[rX] offset:15104\n" \
  "s_waitcnt lgkmcnt(13)\n" \
  "v_mfma_f32_16x16x32_bf16 v[96:99], v[56:59], v[48:51], v[96:99]\n" \
  "s_waitcnt lgkmcnt(12)\n" \
  "v_mfma_f32_16x16x32_bf16 v[100:103], v[60:63], v[48:51], v[100:103]\n" \
  "v_mfma_f32_16x16x32_bf16 v[104:107], v[56:59], v[52:55], v[104:107]\n" \
  "v_mfma_f32_16x16x32_bf16 v[108:111], v[60:63], v[52:55], v[108:111]\n" \
  "ds_read_b128 v[48:51], v232 offset:13312\n" \
  "ds_read_b128 v[52:55], v232 offset:27648\n" \
  "ds_read_b128 v[56:59], v233 offset:768\n" \
  "ds_read_b128 v[60:63], v233 offset:15104\n" \
  "s_waitcnt lgkmcnt(13)\n" \
  "v_mfma_f32_16x16x32_bf16 v[96:99], v[72:75], v[64:67], v[96:99]\n" \
  "s_waitcnt lgkmcnt(12)\n" \
  "v_mfma_f32_16x16x32_bf16 v[100:103], v[76:79], v[64:67], v[100:103]\n" \
  "v_mfma_f32_16x16x32_bf16 v[104:107], v[72:75], v[68:71], v[104:107]\n" \
  "v_mfma_f32_16x16x32_bf16 v[108:111], v[76:79], v[68:71], v[108:111]\n" \
  "s_waitcnt lgkmcnt(9)\n" \
  "v_mfma_f32_16x16x32_bf16 v[96:99], v[88:91], v[80:83], v[96:99]\n" \
  "s_waitcnt lgkmcnt(8)\n" \
  "v_mfma_f32_16x16x32_bf16 v[100:103], v[92:95], v[80:83], v[100:103]\n" \
  "v_mfma_f32_16x16x32_bf16 v[104:107], v[88:91], v[84:87], v[104:107]\n" \
  "v_mfma_f32_16x16x32_bf16 v[108:111], v[92:95], v[84:87], v[108:111]\n" \
  "s_waitcnt lgkmcnt(5)\n" \
  "v_mfma_f32_16x16x32_bf16 v[96:99], v[40:43], v[32:35], v[96:99]\n" \
  "s_waitcnt lgkmcnt(4)\n" \
  "v_mfma_f32_16x16x32_bf16 v[100:103], v[44:47], v[32:35], v[100:103]\n" \
  "v_mfma_f32_16x16x32_bf16 v[104:107], v[40:43], v[36:39], v[104:107]\n" \
  "v_mfma_f32_16x16x32_bf16 v[108:111], v[44:47], v[36:39], v[108:111]\n" \
  "s_waitcnt lgkmcnt(1)\n" \
  "v_mfma_f32_16x16x32_bf16 v[96:99], v[56:59], v[48:51], v[96:99]\n" \
  "s_waitcnt lgkmcnt(0)\n" \
  "v_mfma_f32_16x16x32_bf16 v[100:103], v[60:63], v[48:51], v[100:103]\n" \
  "v_mfma_f32_16x16x32_bf16 v[104:107], v[56:59], v[52:55], v[104:107]\n" \
  "v_mfma_f32_16x16x32_bf16 v[108:111], v[60:63], v[52:55], v[108:111]\n" \
  "s_nop 3\n" \
  "v_cvt_pk_bf16_f32 v128, v96, v97\n" \
  "v_cvt_pk_bf16_f32 v129, v98, v99\n" \
  "v_cvt_pk_bf16_f32 v130, v100, v101\n" \
  "v_cvt_pk_bf16_f32 v131, v102, v103\n" \
  "v_cvt_pk_bf16_f32 v132, v104, v105\n" \
  "v_cvt_pk_bf16_f32 v133, v106, v107\n" \
  "v_cvt_pk_bf16_f32 v134, v108, v109\n" \
  "v_cvt_pk_bf16_f32 v135, v110, v111\n" \
  "s_add_u32 s46, %[sF], 0\n" \
  "buffer_store_dwordx4 v[128:131], %[l16], %[rs], s46 offen sc1\n" \
  "s_add_u32 s46, %[sF], 32768\n" \
  "buffer_store_dwordx4 v[132:135], %[l16], %[rs], s46 offen sc1\n" \
  "s_waitcnt vmcnt(0) lgkmcnt(0)\n" \
  ".LQ_zend%=:\n" \
  ""
#define ASM_PB_CLOB "memory", "scc", "vcc", "a0", "a1", "a2", "a3", "a4", "a5", "a6", "a7", "a8", "a9", "a10", "a11", "a12", "a13", "a14", "a15", "a16", "a17", "a18", "a19", "a20", "a21", "a22", "a23", "a24", "a25", "a26", "a27", "a28", "a29", "a30", "a31", "a32", "a33", "a34", "a35", "a36", "a37", "a38", "a39", "a40", "a41", "a42", "a43", "a44", "a45", "a46", "a47", "a48", "a49", "a50", "a51", "a52", "a53", "a54", "a55", "a56", "a57", "a58", "a59", "a60", "a61", "a62", "a63", "a64", "a65", "a66", "a67", "a68", "a69", "a70", "a71", "a72", "a73", "a74", "a75", "a76", "a77", "a78", "a79", "a80", "a81", "a82", "a83", "a84", "a85", "a86", "a87", "a88", "a89", "a90", "a91", "a92", "a93", "a94", "a95", "a96", "a97", "a98", "a99", "a100", "a101", "a102", "a103", "a104", "a105", "a106", "a107", "a108", "a109", "a110", "a111", "a112", "a113", "a114", "a115", "a116", "a117", "a118", "a119", "a120", "a121", "a122", "a123", "a124", "a125", "a126", "a127", "s46", "s47", "s48", "s52", "s53", "s54", "s55", "s56", "s57", "v32", "v33", "v34", "v35", "v36", "v37", "v38", "v39", "v40", "v41", "v42", "v43", "v44", "v45", "v46", "v47", "v48", "v49", "v50", "v51", "v52", "v53", "v54", "v55", "v56", "v57", "v58", "v59", "v60", "v61", "v62", "v63", "v64", "v65", "v66", "v67", "v68", "v69", "v70", "v71", "v72", "v73", "v74", "v75", "v76", "v77", "v78", "v79", "v80", "v81", "v82", "v83", "v84", "v85", "v86", "v87", "v88", "v89", "v90", "v91", "v92", "v93", "v94", "v95", "v96", "v97", "v98", "v99", "v100", "v101", "v102", "v103", "v104", "v105", "v106", "v107", "v108", "v109", "v110", "v111", "v112", "v113", "v114", "v115", "v116", "v117", "v118", "v119", "v120", "v121", "v122", "v123", "v124", "v125", "v126", "v127", "v128", "v129", "v130", "v131", "v132", "v133", "v134", "v135", "v136", "v137", "v138", "v139", "v140", "v141", "v142", "v143", "v144", "v145", "v146", "v147", "v148", "v149", "v150", "v151", "v152", "v153", "v154", "v155", "v156", "v157", "v158", "v159", "v160", "v161", "v162", "v163", "v164", "v165", "v166", "v167", "v168", "v169", "v170", "v171", "v172", "v173", "v174", "v175", "v176", "v177", "v178", "v179", "v180", "v181", "v182", "v183", "v184", "v185", "v186", "v187", "v188", "v189", "v190", "v191", "v192", "v193", "v194", "v195", "v196", "v197", "v198", "v199", "v200", "v201", "v202", "v203", "v204", "v205", "v206", "v207", "v232", "v233"

DEV v4u mkrs4(const void* q, unsigned bytes) { const size_t u = (size_t)q; return v4u{(unsigned)u, (unsigned)(u >> 32) & 0xffffu, bytes, 0x00020000u}; }
#define TS_OP
DEV int kx_off(int h, int f) { return h * 65536 + (f >> 3) * 16384 + ((f >> 3) & 1) * ASM_MIX + (f & 7) * 1024; }
DEV int vx_off(int ct, int h) { return ct * 32768 + (h >> 3) * (16384 + ASM_MIX) + (h & 7) * 1024; }
DEV int can_line(int x, int k) { return ((x >> 1) * 2 + (k < 2)) * 32768 + ((x & 1) << 14) + 7168 + ((k + 2) & 3) * 256; }
__global__ void __launch_bounds__(256, 1) k_prep(Params p) {
  extern __shared__ __attribute__((aligned(16))) char smem[];
  LAS char* sm = (LAS char*)smem;
  const int b = blockIdx.x, t = threadIdx.x, l = t & 63, w = rfl(t >> 6), g = l >> 4, n = l & 15;
  const int x = b & 7, j = b >> 3, lb = (int)(unsigned)(size_t)sm;
  TS(0);
  if (j < 28) {
    const int mat = x * 4 + j / 7, grp = j % 7, isB = mat & 1, h = mat >> 1;
#define P_OPS [rs] "s"(mkrs4(p.ws, WS_USED)), [rx] "s"(mkrs4(p.x, 2048 * 448 * 2)), \
    [ra] "s"(mkrs4(isB ? p.wv : p.wk, 448 * 4096 * 2)), [rb] "s"(mkrs4(isB ? p.wo : p.wq, 448 * 4096 * 2)), \
    [ma] "s"(rfl(lb + 32768 + w * 32768)), [k0] "s"(rfl((w + ROT * grp) % 14)), [w] "s"(rfl(w)), \
    [sO] "s"(rfl((isB ? O_BFR : O_AFR) + (h * 28 + 4 * grp) * 14336)), \
    [sF] "s"(rfl((w >> 1) * MB + (isB ? O_VX + vx_off(4 * grp + 2 * (w & 1), h) : O_KX + kx_off(h, 4 * grp + 2 * (w & 1))))), \
    [rX] "v"(lb + (3 + (w >> 1)) * 32768 + n * 896 + (g ^ (n >> 1)) * 16), \
    [l16] "v"(l * 16), [oA] "v"((l >> 5) * 8192 + h * 512 + ((l & 31) ^ (l >> 5)) * 16), [rA] "v"(lb + n * 512 + (g ^ n) * 16) TS_OP
    if (!isB) {
      const int it = 4 * grp + w;
      asm volatile(ASM_PA :: P_OPS, [sB] "s"(rfl((32 * (it >> 1) + 4 * (it & 1)) * 8192)) : ASM_PA_CLOB);
    } else {
      const int e = 64 * w + (l >> 3), sl = (l >> 1) & 3, base = ((h * 256 + e) * 448 + grp * 64) * 2 + (l & 1) * 16;
      const int rr = (8 * g + (n >> 2)) * 128 + (n & 3) * 8, sw = ((n >> 3) & 1) | ((g & 1) << 1);
      asm volatile(ASM_PB :: P_OPS, [oB] "v"(base + (sl ^ ((e >> 1) & 1)) * 32), [oB1] "v"(base + (sl ^ ((e >> 1) & 1) ^ 2) * 32),
                   [rT0] "v"(lb + rr + (0 ^ sw) * 32), [rT1] "v"(lb + rr + (1 ^ sw) * 32), [rT2] "v"(lb + rr + (2 ^ sw) * 32), [rT3] "v"(lb + rr + (3 ^ sw) * 32) : ASM_PB_CLOB);
    }
  } else {
    const rsrc_t ws = mkrs(p.ws, WS_USED);
    const int k = j - 28, i = x * 4 + k, page = (x + 2 * k) & 7;
    if (w != 0) {
      unsigned off = ((k * 3 + w - 1) * 64 + l) * 128, tmp;
      asm volatile("s_getpc_b64 s[90:91]\n.Lpf%=:\n\ts_sub_u32 s90, s90, .Lpf%=-_Z6k_prep6Params\n\ts_subb_u32 s91, s91, 0\n\t"
                   "v_min_i32 %1, .Lcode_hi-_Z6k_prep6Params-4, %1\n\tv_max_i32 %1, 0, %1\n\tglobal_load_dword %0, %1, s[90:91]\n\ts_waitcnt vmcnt(0)"
                   : "=&v"(tmp), "+v"(off) :: "s90", "s91", "scc", "memory");
      const unsigned xa = ld4<0>(mkrs(p.x, 64 * 896), ((k * 3 + w - 1) * 64 + l) * 128, 0);
      const unsigned xb = ld4<0>(mkrs(p.x, XPF * 32 * 896), l < 16 ? (448 + (i * 3 + w - 1) * 16 + l) * 128 : OOB, 0);
      asm volatile("" :: "v"(xa), "v"(xb));
    }
    const v4u zv = {0u, 0u, 0u, 0u}, sv = {SENT, SENT, SENT, SENT};
    const rsrc_t rm = mkrs(p.mem0, 8 * 448 * 2);
#pragma unroll
    for (int q = 0; q < 2; ++q) {
      const int c = t + 256 * q, pc = ((i >> 2) & 1) * 28 + (c >> 4), ch = c & 15;
      const bool m0 = i < 16 && c < 448 && ch < 8;
      const int mo = m0 ? ((4 * (pc & 1) + (ch & 3)) * 448 + (pc >> 1) * 16 + (ch >> 2) * 2) * 2 : OOB;
      const v4u d = {ld4<0>(rm, mo, 0), ld4<0>(rm, mo, 8), ld4<0>(rm, mo, 16), ld4<0>(rm, mo, 24)};
      const bool sp = i < 16 && c >= 448;
      st16<SC1>(m0 ? v4u{san(d.x), san(d.y), san(d.z), san(d.w)} : sp ? zv : sv, ws, (sp && i < 4) ? OOB : c * 16, O_CRIT + i * 8192);
      st16<SC1>(sv, ws, c * 16, O_CRIT + 262144 + i * 8192);
      st16<SC1>(sv, ws, i < 16 ? c * 16 : OOB, O_CRIT + 2 * 262144 + i * 8192);
    }
    if (t < 80) { const int fi = i * 10 + (t >> 3); st16<SC1>(v4u{fi < F_PA ? 2u : 0u, 0u, 0u, 0u}, ws, O_FLG + fi * 256 + (t & 7) * 16, 0); }
    if (w == 0) {
      const int cx = (x >> 2) | ((x & 1) << 1), off = l < 8 ? O_CRIT + page * 2 * MB + 7 * 1024 + k * 256 + l * 16 : OOB;
      unsigned best[4] = {1u << 20, 1u << 20, 1u << 20, 1u << 20};
      for (int rd = 0; rd < NSAMP; ++rd)
#pragma unroll
        for (int c = 0; c < 4; ++c) {
          unsigned long long t0, t1;
          asm volatile("s_waitcnt vmcnt(0)\n\ts_memtime %0\n\ts_waitcnt lgkmcnt(0)" : "=s"(t0) :: "memory");
          st16<SC1>(zv, ws, off, (c ^ cx) * 8192);
          asm volatile("s_waitcnt vmcnt(0)\n\ts_memtime %0\n\ts_waitcnt lgkmcnt(0)" : "=s"(t1) :: "memory");
          best[c] = min(best[c], (unsigned)(t1 - t0));
        }
      st16<SC1>(v4u{best[0], best[1], best[2], best[3]}, ws, l < 8 ? O_TAB + page * 512 + k * 128 + l * 16 : OOB, 0);
    }
    TS(2);
  }
  TS(3);
}

#define DBG_OFF (O_DBG + ((int)blockIdx.x * 4 + w) * 2048 + l * 4)
#define DBG_OP
constexpr int NS = ~(1 << 14);

DEV void role_A(const Params& p, LAS char* sm, int rep, int idx) {
  const int t = threadIdx.x, l = t & 63, w = rfl(t >> 6), g = l >> 4, n = l & 15, r = n & 7;
  const int lb = (int)(unsigned)(size_t)sm;
  const bool mid = idx % 7 == 3;
  const int wb = (w + ASM_WR) & 3;
  const int sM = lb + 24576, sS = lb + 16384;
  int rdL[2];
#pragma unroll
  for (int h2 = 0; h2 < 2; ++h2) {
    const int pp = (4 * idx + 2 * ((n >> 1) & 1) + h2) % 14, rr = 2 * (n >> 2) + (n & 1);
    rdL[h2] = sM + (2 * (2 * pp + (g >> 1)) + (rr >> 2)) * 128 + (rr & 3) * 16 + (g & 1) * 8;
  }
  const int G = mid ? l >> 5 : 0, d = mid ? (l & 31) * 2 : l;
  const int gam = 2 * idx + G, hd = gam / 7, jj = (gam % 7 + (hd & 1)) >> 1;
  const int rr = 4 * (d >> 5) + 2 * ((jj ^ (d >> 1)) & 1) + ((d >> 4) & 1), rp = ((d >> 1) & 6) | (d & 1);
  const int P = 4 * idx + w, ph = P / 14, pp = P % 14;
  const int oKM = O_KM + (((ph >> 2) * 2 + (ph & 1)) * 14 + pp) * 1024 + (g * 16 + ((ph >> 1) & 1) * 8) * 16 + n * 8;
  const unsigned one = (r >> 2) == (g >> 1) ? ((r & 1) ? 0x3f800000u : 0x3f80u) : 0u;
  if (w == 0) *(LAS unsigned*)(sm + 20480 + l * 4) = 0;
  __syncthreads();
#define A_OPS \
    [rs] "s"(mkrs4(p.ws, WS_USED)), [side] "s"(rfl((rep & 1) << 14)), [sW] "s"(rfl(O_AFR + (8 * idx * 14 + 4 * wb) * 1024)), [w] "s"(rfl(w)), \
    [l16] "v"(l * 16), [l16b] "v"(wb < 3 ? l * 16 : OOB), [oCal] "v"(O_TAB + (l >> 3) * 512), [oMa] "v"(m_blk(rep, 2 * wb, l)), [oMb] "v"(m_blk(rep, 2 * wb + 1, l)), \
    [cIn] "v"(t < 2 ? can_line(rep, l) & NS : OOB), [sel0] "v"((r & 2) ? 0u : one), [sel1] "v"((r & 2) ? one : 0u), \
    [wrM] "v"(sM + wb * 2048 + l * 16), [rdL0] "v"(rdL[0]), [rdL1] "v"(rdL[1]), \
    [wrS] "v"(sS + (n >> 3) * 1024 + (2 * g * 8 + r) * 16 + ((w + 2 * (g & 1) + (n >> 3)) & 3) * 4), \
    [cnt] "v"(lb + 20480 + l * 4), [rdS] "v"(sS + G * 1024 + (rr * 8 + rp) * 16), [oSt] "v"(s_piece(0, hd * 4 + jj) + d * 4), \
    [wrK] "v"(lb + w * 4096 + l * 16), [rdK] "v"(lb + (2 * (w & 1) + (n & 1)) * 1024 + (g * 16 + 8 * (w >> 1) + (n >> 1)) * 16), [oKM] "v"(oKM), \
    [oPr] "v"(t == 0 ? O_FLG + (F_PA + idx) * 256 : OOB) DBG_OP TS_OP
  if (mid) asm volatile(ASM_M :: A_OPS, [cOut] "v"(idx % 28 == 3 && t >= 192 && l < 2 ? can_line(2 + l, 2 + rep) : OOB) : ASM_M_CLOB);
  else asm volatile(ASM_A :: A_OPS : ASM_A_CLOB);
}

DEV void role_X(const Params& p, LAS char* sm, int rep, int h) {
  const int t = threadIdx.x, l = t & 63, w = rfl(t >> 6), g = l >> 4, n = l & 15;
  const int lb = (int)(unsigned)(size_t)sm;
  const int wb = (w + rep) & 3;
  const int rr = 4 * (l >> 5) + ((l >> 1) & 3), rg = (l >> 3) & 3, rk = l & 1;
  if (w == 0) *(LAS unsigned*)(sm + 8192 + t * 4) = 0;
  __syncthreads();
  asm volatile(ASM_X ".Lcode_hi:\n" ::
    [rs] "s"(mkrs4(p.ws, WS_USED)), [side] "s"(rfl((rep & 1) << 14)), [sK] "s"(rfl(O_KX + kx_off(h, 8 * wb))), [w] "s"(rfl(w)),
    [oCal] "v"(O_TAB + (l >> 3) * 512), [oMa] "v"(m_blk(rep, 2 * wb, l)), [oMb] "v"(m_blk(rep, 2 * wb + 1, l)), [l16] "v"(l * 16), [l16b] "v"(wb < 3 ? l * 16 : OOB),
    [cIn] "v"(t < 2 ? can_line(rep, l) & NS : OOB), [late] "s"(rfl(rep < 2 ? OOB : 0)), [cOut] "v"(rep < ASM_NM && rep >= 2 * ASM_AH && (h & 3) == 0 && t < 2 ? can_line(2 + l, 2 + rep) : OOB),
    [oPrV] "v"(l < 4 ? O_FLG + (F_KS + (28 * h) / 8 + l) * 256 : OOB), [wrS] "v"(lb + w * 1024 + g * 256 + n * 4), [cnt] "v"(lb + 8192 + l * 4),
    [rdE] "v"(lb + (2 * (rr >> 2) + rk) * 256 + (rr & 3) * 64 + rg * 16),
    [oE] "v"(e_piece(0, h) + l * 8), [oFz] "v"(w >= 2 && l < 40 ? e_piece(0, h) + l * 16 : OOB),
    [oT] "v"((l & 17) == 0 ? e_piece((l >> 3) & 1, h) + 512 + rr * 4 : OOB) DBG_OP TS_OP
    : ASM_X_CLOB);
}

DEV void role_C(const Params& p, LAS char* sm, int y, int hf, int ct) {
  const int t = threadIdx.x, l = t & 63, w = rfl(t >> 6), g = l >> 4, n = l & 15, rep = 2 + y;
  const int lb = (int)(unsigned)(size_t)sm;
  const int rl = n >> 2;
  if (w == 0) *(LAS unsigned*)(sm + 20480 + l * 4) = 0;
  __syncthreads();
  asm volatile(ASM_C ::
    [rs] "s"(mkrs4(p.ws, WS_USED)), [side] "s"(rfl(y << 14)), [sW] "s"(rfl(O_BFR + (4 * w * 28 + ct) * 14336)),
    [sU] "s"(rfl(O_U + (ct * 4 + w) * 1024)), [hf] "s"(rfl(hf)), [sV] "s"(rfl(O_VX + vx_off(ct, 4 * w))),
    [l16] "v"(l * 16), [oCal] "v"(O_TAB + (l >> 3) * 512), [w] "s"(rfl(w)), [oM0] "v"(m_blk(rep, 2 * w, l)), [oM1] "v"(m_blk(rep, 2 * w + 1, l)),
    [wrM] "v"(lb + w * 2048 + l * 16), [rdM] "v"(lb + l * 16),
    [oS] "v"(s_piece(0, (4 * w + g) * 4 + (n >> 3)) + hf * 128 + (n & 7) * 16), [oE] "v"(e_piece(0, 4 * w + (n >> 2)) + hf * 256 + g * 64 + (n & 3) * 16),
    [oT] "v"(e_piece(0, 4 * w + g) + 512 + (4 * hf + rl) * 4),
    [mk0] "v"(rl == 0 ? 0xFFFFu : rl == 1 ? 0xFFFF0000u : 0u), [mk1] "v"(rl == 2 ? 0xFFFFu : rl == 3 ? 0xFFFF0000u : 0u),
    [oPrV] "v"(l < 4 ? O_FLG + (F_VS + ((4 * w + l) * 28 + ct) / 8) * 256 : OOB),
    [oO0] "v"(m_piece(l >> 5, 2 * ct + hf) + (l & 31) * 4), [oO1] "v"(m_piece(2 + (l >> 5), 2 * ct + hf) + (l & 31) * 4),
    [wrR] "v"(lb + 16384 + l * 16 + ((w + (l >> 3)) & 3) * 4), [cnt] "v"(lb + 20480 + l * 4),
    [rdR] "v"(lb + 16384 + (((l >> 2) & 3) * 16 + ((l >> 4) & 1) * 2 + (l & 3) * 4) * 16),
    [cIn] "v"(t < ASM_NM ? can_line(rep, 2 + l) & NS : OOB), [cOut] "v"(ct % 14 == 0 && hf == 0 && t < 4 ? can_line(l, y) : OOB),
    [oU] "v"((w >> 1) == hf ? l * 16 : OOB), [oPr] "v"(t == 4 ? O_FLG + (F_PC + hf * 28 + ct) * 256 : OOB) DBG_OP TS_OP
    : ASM_C_CLOB);
}

#define X_OPS [rx] "s"(mkrs4(p.x, 2048 * 448 * 2)), [rX] "v"(lb + (l & 15) * 896 + ((l >> 4) ^ ((l & 15) >> 1)) * 16)
DEV int f_l0(int h) { return O_FLG + (h < 8 ? F_PA + 56 + h : F_PC + 48 + h) * 256; }
template <int ISV>
DEV void role_server(const Params& p, LAS char* sm, int idx) {
  const int t = threadIdx.x, l = t & 63, w = rfl(t >> 6), lb = (int)(unsigned)(size_t)sm;
  const int T = 8 * idx + 2 * w, h = T / 28, it = T % 28;
#define S_OPS [rs] "s"(mkrs4(p.ws, WS_USED)), [sW] "s"(rfl((ISV ? O_BFR : O_AFR) + T * 14336)), [sX] "s"(rfl((2 * 28 + 7 * w) * 1024)), [odd] "s"(rfl((w & 1) * 64)), X_OPS, \
    [sO] "s"(rfl(ISV ? O_VX + vx_off(it, h) : O_KX + kx_off(h, it))), [sO2] "s"(rfl(ISV ? O_VX + vx_off(it + 1, h) : O_KX + kx_off(h, it + 1))), [oCal] "v"(O_TAB + (l >> 3) * 512), [mb] "s"(rfl(lb + 7 * w * 1024)), \
    [l16] "v"(l * 16), [oPr] "v"(t == 0 ? O_FLG + ((ISV ? F_VS : F_KS) + idx) * 256 : OOB) TS_OP
  if (ISV) asm volatile(ASM_VS :: S_OPS : ASM_VS_CLOB);
  else asm volatile(ASM_KS :: S_OPS : ASM_KS_CLOB);
}

DEV void role_L(const Params& p, LAS char* sm, int li) {
  const int t = threadIdx.x, l = t & 63, w = rfl(t >> 6), lb = (int)(unsigned)(size_t)sm;
  const int g = l >> 4, n = l & 15;
  asm volatile(ASM_L ::
    [rs] "s"(mkrs4(p.ws, WS_USED)), [mb] "s"(rfl(lb + w * 28672)), [sX] "s"(rfl((li * 4 + w) * 28672)), X_OPS, [w] "s"(rfl(w)),
    [rq] "s"(mkrs4(p.wqkv, 448 * 1344 * 2)), [ro] "s"(mkrs4(p.awo, 448 * 448 * 2)), [sWo] "s"(rfl(li * 28 * 896)),
    [sLW] "s"(rfl(O_LW + ((li * 2 + (w & 1)) * 14 + 7 * (w >> 1)) * 1024)), [sLO] "s"(rfl(O_LWO + (7 * w * 16 + li) * 1024)),
    [oWq] "v"((l >> 2) * 2688 + li * 56 + (l & 3) * 16), [rWq] "v"(lb + (7 * (w >> 1) * 32 + 8 * g + (n >> 2)) * 64 + (w & 1) * 32 + (n & 3) * 8),
    [rWo] "v"(lb + 86016 + (4 * g + (n >> 2)) * 896 + 7 * w * 32 + (n & 3) * 8),
    [sWq] "s"(rfl(O_LW + 8 * w * 14336)), [sKVP] "s"(rfl(O_KVP + (64 * w + li) * 4096)), [sKVR] "s"(rfl(O_KVP + (li * 64 + (w & 1)) * 1024)),
    [sKVF] "s"(rfl(O_KVF + 4 * w * 3072)), [sAO] "s"(rfl(O_AOUT + (li * 128 + 4 * w) * 1024)),
    [l16] "v"(l * 16), [oL0] "v"(t == 0 ? f_l0(li) : OOB), [pL0] "v"(f_l0(4 * w + (l & 3))), [oL1] "v"(t == 0 ? O_FLG + (F_L1 + li) * 256 : OOB), [oL2] "v"(t == 0 ? O_FLG + (F_L2 + li) * 256 : OOB),
    [oL3] "v"(t == 0 ? O_FLG + (F_L3 + li) * 256 : OOB), [pL1] "v"(O_FLG + (F_L1 + (l & 15)) * 256), [pL2] "v"(O_FLG + (F_L2 + 4 * w + (l & 3)) * 256),
    [oK0] "v"(w < 2 ? O_KVF + (li * 3 + w) * 1024 + l * 16 : OOB), [oKz] "v"(w == 1 ? O_KVF + (li * 3 + 2) * 1024 + l * 16 : OOB) TS_OP
    : ASM_L_CLOB);
}

DEV void role_F(const Params& p, LAS char* sm, int s0, int q4) {
  const int t = threadIdx.x, l = t & 63, w = rfl(t >> 6), g = l >> 4, n = l & 15, lb = (int)(unsigned)(size_t)sm;
  const int ct = 7 * q4 + 2 * w, cl = ct + (w < 3 ? (l >> 4) & 1 : 0), l14 = 14 * w + (n < 14 ? n : 0);
  const int oO = (n * 448 + ct * 16 + 4 * g) * 2;
  asm volatile(ASM_F ::
    [rs] "s"(mkrs4(p.ws, WS_USED)), [rg] "s"(mkrs4(p.gw, 448 * 2)), [ro] "s"(mkrs4(p.out, 2048 * 448 * 2)),
    [s0] "s"(rfl(s0)), [sX] "s"(rfl(7168 * w)), [odd] "s"(rfl((w & 1) * 64)), X_OPS, [sKX] "s"(rfl(O_KX + 4 * w * 65536)), [sVX] "s"(rfl(O_VX + ct * 32768)), [oCal] "v"(O_TAB + (l >> 3) * 512), [sWo] "s"(rfl(O_LWO + ct * 16384)),
    [sKM] "s"(rfl(O_KM + 2 * w * 14336)), [sU] "s"(rfl(O_U + ct * 4096)),
    [l16] "v"(l * 16), [l16b] "v"(w < 3 ? l * 16 : OOB), [g16] "v"(g * 16), [cm] "v"(n - 4 * g),
    [pKS] "v"(O_FLG + (F_KS + l14) * 256), [pVS] "v"(O_FLG + (F_VS + (n * 28 + cl) / 8) * 256),
    [pA] "v"(O_FLG + (l < 14 ? F_PA + l14 : F_PC + (l & 1) * 28 + cl) * 256), [wrP] "v"(lb + w * 8192 + l * 16), [wrM] "v"(lb + 32768 + w * 2048 + l * 16),
    [lr] "v"(lb + l * 16), [oO] "v"(oO), [oOb] "v"(w < 3 ? oO + 32 : OOB) TS_OP
    : ASM_F_CLOB);
}

constexpr int LDS_BYTES = 144 * 1024;
__global__ void __launch_bounds__(256, 1) k_main(Params p) {
  extern __shared__ __attribute__((aligned(16))) char smem[];
  LAS char* sm = (LAS char*)smem;
  const int b = blockIdx.x, x = b & 7, j = b >> 3;
  TS(8);
  if (x < 4) {
    if (!(p.mask & 2u)) return;
    if (j >= 28) role_X(p, sm, x, x * 4 + (j - 28));
    else if (x < 2) role_A(p, sm, x, x * 28 + j);
    else role_C(p, sm, x - 2, j & 1, (x - 2) * 14 + (j >> 1));
  } else {
    if (p.mask & 1u) {
      if (j >= 28) role_L(p, sm, (x - 4) * 4 + (j - 28));
      else if (ASM_SM ? j < 14 : x < 6) role_server<0>(p, sm, (x - 4) * (ASM_SM ? 14 : 28) + j);
      else role_server<1>(p, sm, ASM_SM ? (x - 4) * 14 + j - 14 : (x - 6) * 28 + j);
    }
    TS(9);
    if (p.mask & 4u) role_F(p, sm, (j >> 2) * 4 + (x - 4), j & 3);
  }
  TS(10);
}

#define SPLIT 0
extern "C" void kernel_launch(void* const* d_in, const int* in_sizes, int n_in, void* d_out, int out_size, void* d_ws, size_t ws_size,
                              hipStream_t stream) {
  (void)in_sizes; (void)n_in; (void)out_size; (void)ws_size;
  Params p;
  p.x = (const unsigned short*)d_in[0]; p.wqkv = (const unsigned short*)d_in[1]; p.awo = (const unsigned short*)d_in[2];
  p.mem0 = (const unsigned short*)d_in[4]; p.wq = (const unsigned short*)d_in[5]; p.wk = (const unsigned short*)d_in[6];
  p.wv = (const unsigned short*)d_in[7]; p.wo = (const unsigned short*)d_in[8]; p.gw = (const unsigned short*)d_in[10];
  p.out = (unsigned short*)d_out;
  p.ws = (char*)(((size_t)d_ws + (2u << 20) - 1) & ~(size_t)((2u << 20) - 1)) + (size_t)WSOFF * MB;
  p.mask = 7u; p.pad = 0u;
  (void)hipFuncSetAttribute((const void*)k_main, hipFuncAttributeMaxDynamicSharedMemorySize, LDS_BYTES);
  (void)hipFuncSetAttribute((const void*)k_prep, hipFuncAttributeMaxDynamicSharedMemorySize, 160 * 1024);
  k_prep<<<256, 256, 160 * 1024, stream>>>(p);
  k_main<<<256, 256, LDS_BYTES, stream>>>(p);
}
```
